```python
import torch

def _softmax(x, axis=-1):
    return torch.softmax(x, dim=axis)
def _gelu(x):
    return torch.nn.functional.gelu(x, approximate="tanh")
def _silu(x):
    return torch.nn.functional.silu(x)
def _sigmoid(x):
    return torch.sigmoid(x)
def _layernorm(x, axis=-1, eps=1e-5):
    m = x.mean(dim=axis, keepdim=True)
    v = x.var(dim=axis, keepdim=True, unbiased=False)
    return (x - m) / torch.sqrt(v + eps)
def _scatter_add(tgt, idx, src, axis=0):
    return tgt.clone().index_add_(axis, idx, src)
def _segment_sum(src, seg, n):
    out = torch.zeros((n,) + src.shape[1:], dtype=src.dtype)
    return out.index_add_(0, seg, src)
def _pad(x, pw):
    flat = tuple(v for pair in reversed(pw) for v in pair)
    return torch.nn.functional.pad(x, flat)

kernel_name = 'sequential_preLN__compressed_kv_cla__glu_gated_bilinear__prompt'

def setup_inputs():

    _w = (lambda a: (lambda t: t.to(torch.bfloat16) if t.is_floating_point() else t)(torch.as_tensor(a)))
    def _A_setup():
        B, H, Sq, Sk, D, M = 1, 16, 2048, 2048, 256, 448
        Dc = 4 * D
        return {
            "x": _w((lambda *s: torch.randn(*s, dtype=torch.bfloat16) * 0.05)(B, Sq, M)),
            "c_kv": _w((lambda *s: torch.randn(*s, dtype=torch.bfloat16) * 0.4)(B, Sk, Dc)),
            "w_q": _w((lambda *s: torch.randn(*s, dtype=torch.bfloat16) * 1.6)(M, H, D) * (M ** -0.5)),
            "w_uk": _w((lambda *s: torch.randn(*s, dtype=torch.bfloat16) * 1.6)(Dc, H, D) * (Dc ** -0.5)),
            "w_uv": _w((lambda *s: torch.randn(*s, dtype=torch.bfloat16) * 0.4)(Dc, H, D) * (Dc ** -0.5)),
            "w_o": _w((lambda *s: torch.randn(*s, dtype=torch.bfloat16) * 0.4)(H, D, M) * ((H * D) ** -0.5)),
        }
    _A_in = _A_setup()

    def _B_setup():
        B, S, D, F = 1, 2048, 448, 1792
        return {
            "x": _w((lambda *s: torch.randn(*s, dtype=torch.bfloat16) * 0.05)(B, S, D)),
            "w_gate": _w((lambda *s: torch.randn(*s, dtype=torch.bfloat16) * 0.4)(D, F) * (D ** -0.5)),
            "w_up": _w((lambda *s: torch.randn(*s, dtype=torch.bfloat16) * 0.4)(D, F) * (D ** -0.5)),
            "w_down": _w((lambda *s: torch.randn(*s, dtype=torch.bfloat16) * 0.4)(F, D) * (F ** -0.5)),
        }
    _B_in = _B_setup()

    return {
        "A_x": _A_in["x"],
        "A_c_kv": _A_in["c_kv"],
        "A_w_q": _A_in["w_q"],
        "A_w_uk": _A_in["w_uk"],
        "A_w_uv": _A_in["w_uv"],
        "A_w_o": _A_in["w_o"],
        "B_x": _B_in["x"],
        "B_w_gate": _B_in["w_gate"],
        "B_w_up": _B_in["w_up"],
        "B_w_down": _B_in["w_down"],
    }

def reference(A_x, A_c_kv, A_w_q, A_w_uk, A_w_uv, A_w_o, B_x, B_w_gate, B_w_up, B_w_down):
    def _A_ref(x, c_kv, w_q, w_uk, w_uv, w_o):
        B, Sq, M = x.shape[0], x.shape[1], x.shape[2]
        Sk = c_kv.shape[1]
        H, D = w_q.shape[1], w_q.shape[2]
        q = torch.einsum("bqm,mhd->bhqd", x, w_q)
        k = torch.einsum("bkc,chd->bhkd", c_kv, w_uk)
        v = torch.einsum("bkc,chd->bhkd", c_kv, w_uv)
        scores = torch.einsum("bhqd,bhkd->bhqk", q, k) * (D ** -0.5)
        off = Sk - Sq
        qi = torch.arange(Sq)[:, None]
        kj = torch.arange(Sk)[None, :]
        cmask = kj <= (qi + off)
        scores = torch.where(cmask[None, None], scores, -1e9)
        p = _softmax(scores, axis=-1)
        ctx = torch.einsum("bhqk,bhkd->bhqd", p, v)
        return torch.einsum("bhqd,hdm->bqm", ctx, w_o)

    def _B_ref(x, w_gate, w_up, w_down):
        g = torch.einsum("bsd,df->bsf", x, w_gate)
        u = torch.einsum("bsd,df->bsf", x, w_up)
        h = g * u
        return torch.einsum("bsf,fd->bsd", h, w_down)

    x = A_x
    h = x + _A_ref(_layernorm(x, axis=-1), A_c_kv, A_w_q, A_w_uk, A_w_uv, A_w_o)
    h2 = h + _B_ref(_layernorm(h, axis=-1), B_w_gate, B_w_up, B_w_down)
    return h2
```

```cpp
#include <hip/hip_runtime.h>
#include <hip/hip_bf16.h>
#include <stdint.h>
typedef unsigned short u16;
typedef short bf16x8 __attribute__((ext_vector_type(8)));
typedef float f32x4 __attribute__((ext_vector_type(4)));
#define DI __device__ __forceinline__
DI float bf2f(u16 x){ return __uint_as_float(((unsigned)x)<<16); }
DI u16 f2bf(float x){ __bf16 b = (__bf16)x; return __builtin_bit_cast(u16,b); }

constexpr int S=2048, M=448, DC=1024, H=16, D=256, F=1792;

__global__ void ln_kernel(const u16* x, u16* y){
  int row = blockIdx.x; int lane = threadIdx.x;
  float v[7]; float s=0;
  for(int i=0;i<7;i++){ v[i]=bf2f(x[row*M+lane+64*i]); s+=v[i]; }
  for(int o=32;o>0;o>>=1) s+=__shfl_xor(s,o);
  float mean=s/M; float q=0;
  for(int i=0;i<7;i++){ float d=v[i]-mean; q+=d*d; }
  for(int o=32;o>0;o>>=1) q+=__shfl_xor(q,o);
  float rstd=rsqrtf(q/M+1e-5f);
  for(int i=0;i<7;i++) y[row*M+lane+64*i]=f2bf((v[i]-mean)*rstd);
}
template<int MODE>
__global__ void gemm_kernel(const u16* A, const u16* B, u16* C, const u16* aux, int Mm, int N, int K){
  __shared__ u16 As[64][40]; __shared__ u16 Bs[64][40];
  int tid=threadIdx.x, lane=tid&63, w=tid>>6;
  int m0=blockIdx.y*64, n0=blockIdx.x*64;
  f32x4 acc[4]={};
  for(int k0=0;k0<K;k0+=32){
    for(int i=tid;i<64*32;i+=256){ int r=i>>5,c=i&31; As[r][c]=A[(size_t)(m0+r)*K+k0+c]; }
    for(int i=tid;i<64*32;i+=256){ int c=i>>6,r=i&63; Bs[r][c]=B[(size_t)(k0+c)*N+n0+r]; }
    __syncthreads();
    bf16x8 a=*(bf16x8*)&As[w*16+(lane&15)][8*(lane>>4)];
    for(int n=0;n<4;n++){ bf16x8 b=*(bf16x8*)&Bs[n*16+(lane&15)][8*(lane>>4)];
      acc[n]=__builtin_amdgcn_mfma_f32_16x16x32_bf16(a,b,acc[n],0,0,0);}
    __syncthreads();
  }
  for(int n=0;n<4;n++)for(int j=0;j<4;j++){
    int r=m0+w*16+(lane>>4)*4+j, c=n0+n*16+(lane&15);
    float v=acc[n][j];
    if(MODE==1) v = bf2f(f2bf(v)) + bf2f(aux[(size_t)r*N+c]);
    if(MODE==2) v = bf2f(f2bf(v)) * bf2f(aux[(size_t)r*N+c]);
    C[(size_t)r*N+c]=f2bf(v);
  }
}
__global__ __launch_bounds__(256) void attn_kernel(const u16* q,const u16* k,const u16* v,u16* o){
  __shared__ float qs[D]; __shared__ float p[S]; __shared__ float red[4];
  int row=blockIdx.x,h=blockIdx.y,tid=threadIdx.x;
  qs[tid]=bf2f(q[(size_t)row*H*D+h*D+tid]); __syncthreads();
  float mx=-1e30f;
  for(int j=tid;j<=row;j+=256){ const u16* kr=k+(size_t)j*H*D+h*D; float s=0;
    _Pragma("unroll 2") for(int d=0;d<D;d+=8){ bf16x8 kk=*(const bf16x8*)(kr+d); for(int e=0;e<8;e++) s+=qs[d+e]*bf2f((u16)kk[e]); }
    s*=0.0625f; p[j]=s; mx=fmaxf(mx,s); }
  for(int o2=32;o2>0;o2>>=1) mx=fmaxf(mx,__shfl_xor(mx,o2));
  if((tid&63)==0) red[tid>>6]=mx; __syncthreads();
  mx=fmaxf(fmaxf(red[0],red[1]),fmaxf(red[2],red[3])); __syncthreads();
  float sum=0;
  for(int j=tid;j<=row;j+=256){ float e=__expf(p[j]-mx); p[j]=e; sum+=e; }
  for(int o2=32;o2>0;o2>>=1) sum+=__shfl_xor(sum,o2);
  if((tid&63)==0) red[tid>>6]=sum; __syncthreads();
  sum=red[0]+red[1]+red[2]+red[3];
  float acc=0;
  for(int j=0;j<=row;j++) acc+=p[j]*bf2f(v[(size_t)j*H*D+h*D+tid]);
  o[(size_t)row*H*D+h*D+tid]=f2bf(acc/sum);
}
extern "C" void kernel_launch(void* const* d_in, const int* in_sizes, int n_in, void* d_out, int out_size, void* d_ws, size_t ws_size, hipStream_t stream) {
  const u16* x=(const u16*)d_in[0]; const u16* ckv=(const u16*)d_in[1]; const u16* wq=(const u16*)d_in[2];
  const u16* wuk=(const u16*)d_in[3]; const u16* wuv=(const u16*)d_in[4]; const u16* wo=(const u16*)d_in[5];
  const u16* wg=(const u16*)d_in[7]; const u16* wu=(const u16*)d_in[8]; const u16* wd=(const u16*)d_in[9];
  u16* ws=(u16*)d_ws;
  u16* xn=ws; u16* q=xn+S*M; u16* k=q+S*H*D; u16* v=k+S*H*D; u16* ctx=v+S*H*D; u16* h=ctx+S*H*D; u16* hn=h+S*M; u16* g=hn+S*M; u16* gu=g+S*F;
  ln_kernel<<<S,64,0,stream>>>(x,xn);
  gemm_kernel<0><<<dim3(H*D/64,S/64),256,0,stream>>>(xn,wq,q,nullptr,S,H*D,M);
  gemm_kernel<0><<<dim3(H*D/64,S/64),256,0,stream>>>(ckv,wuk,k,nullptr,S,H*D,DC);
  gemm_kernel<0><<<dim3(H*D/64,S/64),256,0,stream>>>(ckv,wuv,v,nullptr,S,H*D,DC);
  attn_kernel<<<dim3(S,H),256,0,stream>>>(q,k,v,ctx);
  gemm_kernel<1><<<dim3(M/64,S/64),256,0,stream>>>(ctx,wo,h,x,S,M,H*D);
  ln_kernel<<<S,64,0,stream>>>(h,hn);
  gemm_kernel<0><<<dim3(F/64,S/64),256,0,stream>>>(hn,wg,g,nullptr,S,F,M);
  gemm_kernel<2><<<dim3(F/64,S/64),256,0,stream>>>(hn,wu,gu,g,S,F,M);
  gemm_kernel<1><<<dim3(M/64,S/64),256,0,stream>>>(gu,wd,(u16*)d_out,h,S,M,F);
}
```

```cpp
#include <hip/hip_runtime.h>
#include <stdint.h>
struct Params { const void* x; const void* ckv; const void* wq; const void* wuk; const void* wuv; const void* wo; const void* wg; const void* wu; const void* wd; void* out; void* ws; };
extern "C" __global__ __launch_bounds__(256,1) void fused_block(Params p) {
  __shared__ __attribute__((aligned(1024))) char lds[163840];
  asm volatile(
"s_mov_b64 s[4:5], %0\ns_mov_b32 s6, %1\nL_fused_block_start_1:\ns_load_dwordx2 s[8:9],s[4:5],80\ns_load_dwordx16 s[80:95],s[4:5],0\ns_load_dwordx2 s[76:77],s[4:5],64\nv_and_b32 v253,63,v0\nv_lshrrev_b32 v224,6,v0\nv_lshlrev_b32 v252,4,v253\nv_readfirstlane_b32 s14,v224\ns_and_b32 s18,s6,7\ns_lshr_b32 s17,s6,3\ns_and_b32 s7,s17,1\ns_lshl1_add_u32 s16,s18,s7\ns_bfe_u32 s19,s17,0x10001\ns_lshr_b32 s20,s17,2\n"
"s_and_b32 s40,s14,1\ns_lshr_b32 s41,s14,1\ns_lshl_b32 s15,s14,12\ns_lshl_b32 s7,s14,17\ns_add_u32 s32,s7,0\ns_add_u32 s33,s7,15360\ns_add_u32 s34,s7,30720\ns_add_u32 s35,s7,46080\ns_add_u32 s36,s7,65536\ns_add_u32 s37,s7,80896\ns_add_u32 s38,s7,96256\ns_add_u32 s39,s7,111616\ns_lshl_b32 s7,s14,16\ns_add_u32 s21,s7,0\ns_add_u32 s22,s7,15360\ns_add_u32 s23,s7,30720\ns_add_u32 s44,s7,46080\ns_lshl_b32 s7,s14,13\n"
"s_add_u32 s43,s7,65536\ns_lshl_b32 s7,s40,2\nv_lshrrev_b32 v224,5,v253\nv_and_b32 v226,31,v253\nv_add3_u32 v225,v224,s7,0\nv_lshlrev_b32 v225,1,v225\nv_xor_b32 v225,v225,v226\nv_lshlrev_b32 v225,4,v225\nv_lshl_add_u32 v140,v224,13,v225\nv_add3_u32 v225,v224,s7,2\nv_lshlrev_b32 v225,1,v225\nv_xor_b32 v225,v225,v226\nv_lshlrev_b32 v225,4,v225\nv_lshl_add_u32 v141,v224,13,v225\nv_lshrrev_b32 v226,4,v253\n"
"v_and_b32 v225,7,v253\nv_lshrrev_b32 v145,3,v253\nv_add_u32 v224,0,v226\nv_xor_b32 v224,v224,v225\nv_lshlrev_b32 v224,4,v224\nv_lshl_add_u32 v138,v145,11,v224\nv_add_u32 v224,4,v226\nv_xor_b32 v224,v224,v225\nv_lshlrev_b32 v224,4,v224\nv_lshl_add_u32 v139,v145,11,v224\ns_mov_b32 s30,254464\ns_mov_b32 s31,0x20000\ns_mov_b32 s26,524288\ns_mov_b32 s27,0x20000\ns_lshl_b32 s28,s16,9\ns_lshl_b32 s24,s20,19\n"
"s_waitcnt lgkmcnt(0)\ns_cmp_eq_u32 s19,1\ns_cselect_b32 s86,s88,s86\ns_cselect_b32 s87,s89,s87\ns_add_u32 s28,s28,s86\ns_addc_u32 s29,s87,0\ns_add_u32 s24,s24,s82\ns_addc_u32 s25,s83,0\ns_add_u32 s0,s8,117440512\ns_addc_u32 s1,s9,0\ns_load_dwordx2 s[12:13],s[0:1],0\ns_add_u32 s86,s28,0x100000\ns_addc_u32 s87,s29,0\ns_add_u32 s82,s24,0x100\ns_addc_u32 s83,s25,0\ns_add_u32 m0,s15,0\ns_nop 0\nbuffer_load_dwordx4 v140,s[28:31],s21 offen lds\n"
"s_mov_b32 s10,134217728\ns_mov_b32 s11,0x20000\ns_lshl2_add_u32 s55,s6,s14\ns_mul_i32 s7,s55,1792\ns_add_u32 s48,s80,s7\ns_addc_u32 s49,s81,0\ns_mov_b32 s50,1792\ns_mov_b32 s51,0x20000\nv_lshrrev_b32 v224,5,v253\nv_and_b32 v225,31,v253\nv_lshrrev_b32 v226,1,v225\nv_and_b32 v227,1,v253\nv_mul_u32_u24 v228,896,v224\nv_lshl_add_u32 v228,v226,6,v228\nbuffer_load_dwordx4 v141,s[28:31],s22 offen offset:1024 lds\n"
"v_lshl_add_u32 v228,v227,4,v228\nv_cmp_gt_u32 s[2:3],28,v225\nv_mov_b32 v169,1073741824\nv_lshlrev_b32 v226,10,v226\nv_cndmask_b32 v228,v169,v228,s[2:3]\nv_lshl_add_u32 v226,v227,9,v226\nv_lshl_add_u32 v226,v224,4,v226\nv_cndmask_b32 v168,v169,v226,s[2:3]\nbuffer_load_dwordx4 v[160:163],v228,s[48:51],0 offen\nbuffer_load_dwordx4 v[164:167],v228,s[48:51],0 offen offset:32\nv_and_b32 v224,15,v253\nv_lshrrev_b32 v225,4,v253\n"
"v_lshrrev_b32 v226,1,v224\ns_lshl_b32 s7,s40,14\nbuffer_load_dwordx4 v140,s[28:31],s23 offen offset:2048 lds\ns_add_u32 s7,s7,65536\nv_add_u32 v128,0,v225\nv_xor_b32 v128,v128,v226\nv_lshlrev_b32 v128,4,v128\nv_lshl_add_u32 v128,v224,7,v128\nv_add_u32 v128,s7,v128\nv_add_u32 v129,4,v225\nv_xor_b32 v129,v129,v226\nv_lshlrev_b32 v129,4,v129\nv_lshl_add_u32 v129,v224,7,v129\nv_add_u32 v129,s7,v129\nv_bfe_u32 v224,v253,2,2\n"
"v_lshl_add_u32 v226,v225,3,v224\nv_lshlrev_b32 v226,9,v226\nbuffer_load_dwordx4 v141,s[28:31],s44 offen offset:3072 lds\ns_add_u32 s28,s28,0x40000\ns_addc_u32 s29,s29,0\nv_and_b32 v227,3,v253\nv_lshl_add_u32 v226,v227,3,v226\ns_lshl_b32 s7,s41,8\nv_add_u32 v226,s7,v226\nv_and_b32 v228,1,v225\nv_lshl_add_u32 v228,v228,2,v224\nv_xor_b32 v130,0,v228\nv_lshl_add_u32 v130,v130,5,v226\nv_xor_b32 v131,1,v228\n"
"v_lshl_add_u32 v131,v131,5,v226\nv_xor_b32 v132,2,v228\nv_lshl_add_u32 v132,v132,5,v226\nv_xor_b32 v133,3,v228\nv_lshl_add_u32 v133,v133,5,v226\ns_add_u32 m0,s43,0\ns_nop 0\nbuffer_load_dwordx4 v138,s[24:27],s32 offen lds\nv_xor_b32 v134,4,v228\nv_lshl_add_u32 v134,v134,5,v226\nv_xor_b32 v135,5,v228\nv_lshl_add_u32 v135,v135,5,v226\nv_xor_b32 v136,6,v228\nv_lshl_add_u32 v136,v136,5,v226\nv_xor_b32 v137,7,v228\n"
"v_lshl_add_u32 v137,v137,5,v226\nv_lshl_add_u32 v226,v225,2,v224\nv_bfe_u32 v224,v226,1,2\nv_lshlrev_b32 v226,7,v226\nv_lshl_add_u32 v226,v227,3,v226\ns_add_u32 s65,s15,0x20000\nv_add_u32 v226,s65,v226\nbuffer_load_dwordx4 v139,s[24:27],s33 offen offset:1024 lds\nv_xor_b32 v244,0,v224\nv_lshl_add_u32 v244,v244,5,v226\nv_xor_b32 v245,1,v224\nv_lshl_add_u32 v245,v245,5,v226\nv_xor_b32 v246,2,v224\nv_lshl_add_u32 v246,v246,5,v226\n"
"v_xor_b32 v247,3,v224\nv_lshl_add_u32 v247,v247,5,v226\nv_lshlrev_b32 v225,1,v225\nv_and_b32 v224,7,v253\nv_xor_b32 v224,v224,v225\nv_lshlrev_b32 v148,4,v224\nv_add_u32 v146,0x10000,v252\nv_add_u32 v147,0x20000,v252\nbuffer_load_dwordx4 v138,s[24:27],s34 offen offset:2048 lds\nv_mov_b32 v144,0\ns_and_b32 s7,s17,3\ns_lshl1_add_u32 s7,s7,s41\ns_lshl_b32 s7,s7,16\nv_lshrrev_b32 v224,1,v253\nv_lshlrev_b32 v224,11,v224\n"
"v_and_b32 v225,1,v253\nv_lshl_add_u32 v224,v225,7,v224\nv_add_u32 v224,s7,v224\ns_lshl_b32 s7,s20,17\nv_lshrrev_b32 v225,2,v253\nv_lshlrev_b32 v225,13,v225\nv_lshl_add_u32 v225,v227,7,v225\nv_add_u32 v225,s7,v225\nbuffer_load_dwordx4 v139,s[24:27],s35 offen offset:3072 lds\ns_cmp_eq_u32 s40,s41\ns_cselect_b64 vcc,-1,0\ns_cselect_b32 s60,s86,s82\ns_cselect_b32 s61,s87,s83\ns_mov_b32 s7,0x100000\ns_cselect_b32 s64,s7,0x100\n"
"s_mov_b32 s7,7340032\ns_cselect_b32 s62,s7,524032\ns_mov_b32 s63,0x20000\ns_mul_i32 s78,s64,0\nv_cndmask_b32 v150,v224,v225,vcc\ns_cmp_eq_u32 s14,3\ns_cselect_b32 s62,0,s62\ns_lshr_b32 s7,s17,1\ns_add_u32 m0,s43,4096\ns_nop 0\nbuffer_load_dwordx4 v138,s[24:27],s36 offen lds\ns_lshl2_add_u32 s45,s7,s14\ns_mul_i32 s47,s16,229376\ns_lshr_b32 s56,s45,2\ns_and_b32 s57,s45,3\ns_lshl_b32 s58,s56,18\ns_lshl_b32 s7,s16,9\n"
"s_add_u32 s58,s58,s7\ns_lshl_b32 s7,s57,7\ns_add_u32 s58,s58,s7\ns_add_u32 s84,s84,s58\ns_addc_u32 s85,s85,0\ns_lshl_b32 s59,s45,12\ns_add_u32 s59,s59,s47\ns_add_u32 s59,s59,118489088\nbuffer_load_dwordx4 v139,s[24:27],s37 offen offset:1024 lds\ns_cmp_lt_u32 s45,56\ns_cselect_b32 s7,-1,0\ns_cselect_b32 s59,s59,1073741824\nv_writelane_b32 v144,s84,0\nv_writelane_b32 v144,s85,1\nv_writelane_b32 v144,s7,2\n"
"s_mov_b32 s52,8192\nv_writelane_b32 v144,s52,3\ns_mov_b32 s52,131072\nv_writelane_b32 v144,s52,4\nv_writelane_b32 v144,s59,5\ns_mov_b32 s52,1024\nv_writelane_b32 v144,s52,6\ns_mul_i32 s56,s45,9363\nbuffer_load_dwordx4 v138,s[24:27],s38 offen offset:2048 lds\ns_lshr_b32 s56,s56,16\ns_mul_i32 s7,s56,7\ns_sub_u32 s57,s45,s7\ns_mul_i32 s58,s56,28672\ns_add_u32 s58,s58,s47\ns_lshl_b32 s7,s57,7\ns_add_u32 s58,s58,s7\n"
"s_add_u32 s90,s90,s58\ns_addc_u32 s91,s91,0\ns_lshl_b32 s59,s57,15\ns_lshl_b32 s7,s56,10\ns_add_u32 s59,s59,s7\ns_add_u32 s59,s59,s47\ns_add_u32 s59,s59,122683392\nbuffer_load_dwordx4 v139,s[24:27],s39 offen offset:3072 lds\ns_add_u32 s24,s24,128\ns_addc_u32 s25,s25,0\ns_cmp_lt_u32 s45,56\ns_cselect_b32 s7,-1,0\ns_cselect_b32 s59,s59,1073741824\nv_writelane_b32 v144,s90,8\nv_writelane_b32 v144,s91,9\n"
"v_writelane_b32 v144,s7,10\ns_mov_b32 s52,896\nv_writelane_b32 v144,s52,11\ns_mov_b32 s52,14336\nv_writelane_b32 v144,s52,12\nv_writelane_b32 v144,s59,13\ns_mov_b32 s52,8192\nv_writelane_b32 v144,s52,14\ns_sub_u32 s7,s55,392\ns_add_u32 m0,s15,16384\ns_nop 0\nbuffer_load_dwordx4 v140,s[28:31],s21 offen lds\ns_cmp_ge_u32 s55,392\ns_cselect_b32 s46,s7,s55\ns_cselect_b32 s92,s94,s92\ns_cselect_b32 s93,s95,s93\n"
"s_cselect_b32 s53,0x400,0\ns_mul_i32 s56,s46,2341\ns_lshr_b32 s56,s56,16\ns_mul_i32 s7,s56,28\ns_sub_u32 s57,s46,s7\ns_mul_i32 s58,s56,114688\ns_lshl_b32 s7,s57,7\ns_add_u32 s58,s58,s7\ns_add_u32 s92,s92,s58\ns_addc_u32 s93,s93,0\nbuffer_load_dwordx4 v141,s[28:31],s22 offen offset:1024 lds\ns_mul_i32 s7,s56,229376\ns_add_u32 s53,s53,s7\ns_lshl_b32 s7,s57,13\ns_add_u32 s53,s53,s7\ns_add_u32 s53,s53,128974848\n"
"s_max_u32 s47,s55,784\ns_sub_u32 s47,s47,784\ns_mul_i32 s56,s47,9363\ns_lshr_b32 s56,s56,16\ns_mul_i32 s7,s56,7\ns_sub_u32 s57,s47,s7\ns_mul_i32 s58,s56,9363\ns_lshr_b32 s58,s58,16\ns_mul_i32 s7,s58,7\nbuffer_load_dwordx4 v140,s[28:31],s23 offen offset:2048 lds\ns_sub_u32 s46,s56,s7\ns_mul_i32 s59,s57,229376\ns_lshl_b32 s7,s56,12\ns_add_u32 s59,s59,s7\ns_add_u32 s59,s59,127369216\ns_lshl_b32 s56,s46,5\n"
"s_add_u32 s7,s56,16\ns_cmp_ge_u32 s46,3\ns_cselect_b32 s56,s7,s56\ns_movk_i32 s7,14336\ns_cmp_eq_u32 s46,6\ns_cselect_b32 s56,0x60,s56\ns_cselect_b32 s46,100352,s7\ns_mul_i32 s58,s58,224\nbuffer_load_dwordx4 v141,s[28:31],s44 offen offset:3072 lds\ns_add_u32 s28,s28,0x40000\ns_addc_u32 s29,s29,0\ns_add_u32 s56,s56,s58\ns_mul_i32 s56,s56,896\ns_lshl_b32 s7,s57,7\ns_add_u32 s56,s56,s7\ns_add_u32 s57,s76,s56\n"
"s_addc_u32 s58,s77,0\ns_movk_i32 s45,896\ns_movk_i32 s54,0x400\ns_cmp_lt_u32 s55,784\ns_cselect_b32 s57,s92,s57\ns_cselect_b32 s58,s93,s58\ns_cselect_b32 s59,s53,s59\ns_cselect_b32 s46,57344,s46\ns_cselect_b32 s45,0xe00,s45\ns_cselect_b32 s54,0x800,s54\ns_add_u32 m0,s15,32768\ns_nop 0\nbuffer_load_dwordx4 v140,s[28:31],s21 offen lds\ns_mov_b32 s7,-1\nv_writelane_b32 v144,s57,16\nv_writelane_b32 v144,s58,17\n"
"v_writelane_b32 v144,s7,18\nv_writelane_b32 v144,s45,19\nv_writelane_b32 v144,s46,20\nv_writelane_b32 v144,s59,21\nv_writelane_b32 v144,s54,22\ns_min_u32 s47,s55,151\ns_add_u32 s47,s47,240\ns_mul_i32 s56,s47,9363\ns_lshr_b32 s56,s56,16\ns_mul_i32 s7,s56,7\ns_sub_u32 s57,s47,s7\nbuffer_load_dwordx4 v141,s[28:31],s22 offen offset:1024 lds\ns_mul_i32 s58,s56,9363\ns_lshr_b32 s58,s58,16\ns_mul_i32 s7,s58,7\n"
"s_sub_u32 s46,s56,s7\ns_mul_i32 s59,s57,229376\ns_lshl_b32 s7,s56,12\ns_add_u32 s59,s59,s7\ns_add_u32 s59,s59,127369216\ns_lshl_b32 s56,s46,5\ns_add_u32 s7,s56,16\ns_cmp_ge_u32 s46,3\ns_cselect_b32 s56,s7,s56\ns_movk_i32 s7,14336\ns_cmp_eq_u32 s46,6\ns_cselect_b32 s56,0x60,s56\ns_cselect_b32 s46,100352,s7\nbuffer_load_dwordx4 v140,s[28:31],s23 offen offset:2048 lds\ns_mul_i32 s58,s58,224\ns_add_u32 s56,s56,s58\n"
"s_mul_i32 s56,s56,896\ns_lshl_b32 s7,s57,7\ns_add_u32 s56,s56,s7\ns_add_u32 s57,s76,s56\ns_addc_u32 s58,s77,0\ns_cmp_lt_u32 s55,152\ns_cselect_b32 s7,-1,0\ns_cselect_b32 s59,s59,1073741824\nv_writelane_b32 v144,s57,24\nv_writelane_b32 v144,s58,25\nv_writelane_b32 v144,s7,26\ns_mov_b32 s52,896\nbuffer_load_dwordx4 v141,s[28:31],s44 offen offset:3072 lds\ns_add_u32 s28,s28,0x40000\ns_addc_u32 s29,s29,0\n"
"v_writelane_b32 v144,s52,27\nv_writelane_b32 v144,s46,28\nv_writelane_b32 v144,s59,29\ns_mov_b32 s52,1024\nv_writelane_b32 v144,s52,30\ns_mov_b32 s7,1073741824\nv_writelane_b32 v144,s7,37\nv_writelane_b32 v144,s7,45\nv_writelane_b32 v144,s7,53\ns_mov_b32 s55,0\ns_mov_b32 s56,1073741824\ns_mov_b32 s57,1073741824\ns_mov_b32 s58,1073741824\ns_mov_b32 s59,1073741824\ns_add_u32 m0,s43,32768\ns_nop 0\nbuffer_load_dwordx4 v138,s[24:27],s32 offen lds\n"
"s_sub_u32 s7,15,s20\ns_cmp_eq_u32 s19,0\ns_cselect_b32 s74,s7,s20\ns_mul_i32 s66,s16,229376\ns_add_u32 s66,s66,s15\ns_add_u32 s66,s66,118489088\ns_lshl3_add_u32 s7,s74,s14\ns_mul_i32 s67,s7,14336\ns_add_u32 s67,s67,100663296\ns_add_u32 s68,s67,57344\ns_lshl_b32 s69,s16,21\ns_lshl_b32 s7,s20,18\ns_add_u32 s69,s69,s7\ns_lshl_b32 s7,s40,17\nbuffer_load_dwordx4 v139,s[24:27],s33 offen offset:1024 lds\n"
"s_add_u32 s69,s69,s7\ns_lshl_b32 s7,s41,12\ns_lshl_b32 s7,s7,s19\ns_add_u32 s69,s69,s7\ns_lshl_b32 s7,s19,14\ns_add_u32 s69,s69,s7\ns_lshl_b32 s75,s14,13\ns_add_u32 s75,s75,117735424\ns_cmp_eq_u32 s19,1\ns_movk_i32 s7,0x400\ns_cselect_b32 s70,0x8000,s7\ns_cselect_b32 s72,s7,0x2000\ns_cselect_b32 s71,s7,0x6000\ns_lshr_b32 s7,s6,1\nbuffer_load_dwordx4 v138,s[24:27],s34 offen offset:2048 lds\ns_mul_i32 s79,s7,14336\n"
"s_and_b32 s7,s6,1\ns_lshl2_add_u32 s7,s7,s14\ns_lshl_b32 s7,s7,5\ns_add_u32 s79,s79,s7\ns_add_u32 s79,s79,100663296\ns_waitcnt lgkmcnt(0)\ns_mul_i32 s7,s12,0x9e3779b1\ns_xor_b32 s13,s13,s12\ns_add_u32 s12,s7,0x7f4a7c15\ns_mul_i32 s13,s13,0x85ebca6d\ns_add_u32 s13,s13,0x27d4eb2f\nv_mov_b32 v250,s12\nv_mov_b32 v251,s13\nbuffer_load_dwordx4 v139,s[24:27],s35 offen offset:3072 lds\ns_getpc_b64 s[0:1]\n"
"s_and_b32 s7,s17,3\ns_lshl2_add_u32 s7,s7,s14\ns_lshl_b32 s7,s7,13\nv_lshl_add_u32 v224,v253,7,s7\nv_min_u32 v142,L_fused_block_end_2-L_fused_block_start_1-4096,v224\ns_add_u32 m0,s43,36864\ns_nop 0\nbuffer_load_dwordx4 v138,s[24:27],s36 offen lds\nbuffer_load_dwordx4 v139,s[24:27],s37 offen offset:1024 lds\nbuffer_load_dwordx4 v138,s[24:27],s38 offen offset:2048 lds\nbuffer_load_dwordx4 v139,s[24:27],s39 offen offset:3072 lds\n"
"s_add_u32 s24,s24,128\ns_addc_u32 s25,s25,0\ns_add_u32 m0,s15,49152\ns_nop 0\nbuffer_load_dwordx4 v140,s[28:31],s21 offen lds\nbuffer_load_dwordx4 v141,s[28:31],s22 offen offset:1024 lds\nbuffer_load_dwordx4 v140,s[28:31],s23 offen offset:2048 lds\nbuffer_load_dwordx4 v141,s[28:31],s44 offen offset:3072 lds\ns_add_u32 s28,s28,0x40000\ns_addc_u32 s29,s29,0\nbuffer_load_dword v151,v150,s[60:63],s78 offen\n"
"s_add_u32 s78,s78,s64\ns_waitcnt vmcnt(17)\ns_barrier\nds_read_b64_tr_b16 v[64:65],v130 offset:0\nds_read_b64_tr_b16 v[66:67],v130 offset:2048\nds_read_b64_tr_b16 v[68:69],v131 offset:0\nds_read_b64_tr_b16 v[70:71],v131 offset:2048\nds_read_b128 v[0:3],v128 offset:0\nds_read_b128 v[4:7],v128 offset:2048\nds_read_b128 v[8:11],v128 offset:4096\nds_read_b128 v[12:15],v128 offset:6144\nds_read_b128 v[16:19],v128 offset:8192\n"
"ds_read_b128 v[20:23],v128 offset:10240\nds_read_b128 v[24:27],v128 offset:12288\nds_read_b128 v[28:31],v128 offset:14336\nds_read_b64_tr_b16 v[72:73],v132 offset:0\nds_read_b64_tr_b16 v[74:75],v132 offset:2048\ns_waitcnt lgkmcnt(4)\nds_read_b64_tr_b16 v[76:77],v133 offset:0\nds_read_b64_tr_b16 v[78:79],v133 offset:2048\nds_read_b64_tr_b16 v[80:81],v134 offset:0\nds_read_b64_tr_b16 v[82:83],v134 offset:2048\n"
"ds_read_b64_tr_b16 v[84:85],v135 offset:0\nds_read_b64_tr_b16 v[86:87],v135 offset:2048\nds_read_b64_tr_b16 v[88:89],v136 offset:0\nds_read_b64_tr_b16 v[90:91],v136 offset:2048\nds_read_b64_tr_b16 v[92:93],v137 offset:0\nds_read_b64_tr_b16 v[94:95],v137 offset:2048\ns_cmp_eq_u32 s19,1\ns_cbranch_scc1 L_fused_block_vtype_3\nv_mfma_f32_16x16x32_bf16 a[0:3],v[64:67],v[0:3],0\ns_add_u32 m0,s15,0\nds_read_b64_tr_b16 v[96:97],v130 offset:16384\n"
"v_lshlrev_b32 v208,16,v160\nv_mfma_f32_16x16x32_bf16 a[32:35],v[68:71],v[0:3],0\nbuffer_load_dwordx4 v140,s[28:31],s21 offen lds\nv_mfma_f32_16x16x32_bf16 a[36:39],v[68:71],v[4:7],0\nds_read_b64_tr_b16 v[98:99],v130 offset:18432\nv_and_b32 v209,0xffff0000,v160\nv_mfma_f32_16x16x32_bf16 a[4:7],v[64:67],v[4:7],0\nv_lshlrev_b32 v210,16,v161\nv_and_b32 v211,0xffff0000,v161\nv_mfma_f32_16x16x32_bf16 a[8:11],v[64:67],v[8:11],0\n"
"ds_read_b64_tr_b16 v[100:101],v131 offset:16384\nv_lshlrev_b32 v212,16,v162\nv_mfma_f32_16x16x32_bf16 a[40:43],v[68:71],v[8:11],0\nbuffer_load_dwordx4 v141,s[28:31],s22 offen offset:1024 lds\nv_mfma_f32_16x16x32_bf16 a[44:47],v[68:71],v[12:15],0\nds_read_b64_tr_b16 v[102:103],v131 offset:18432\nv_and_b32 v213,0xffff0000,v162\nv_mfma_f32_16x16x32_bf16 a[12:15],v[64:67],v[12:15],0\nv_lshlrev_b32 v214,16,v163\n"
"v_and_b32 v215,0xffff0000,v163\nv_mfma_f32_16x16x32_bf16 a[16:19],v[64:67],v[16:19],0\nds_read_b128 v[32:35],v129 offset:0\nv_lshlrev_b32 v216,16,v164\nv_mfma_f32_16x16x32_bf16 a[48:51],v[68:71],v[16:19],0\nbuffer_load_dwordx4 v140,s[28:31],s23 offen offset:2048 lds\nv_mfma_f32_16x16x32_bf16 a[52:55],v[68:71],v[20:23],0\nds_read_b128 v[36:39],v129 offset:2048\nv_and_b32 v217,0xffff0000,v164\nv_mfma_f32_16x16x32_bf16 a[20:23],v[64:67],v[20:23],0\n"
"v_lshlrev_b32 v218,16,v165\nv_and_b32 v219,0xffff0000,v165\ns_waitcnt lgkmcnt(15)\nv_mfma_f32_16x16x32_bf16 a[24:27],v[64:67],v[24:27],0\nds_read_b128 v[40:43],v129 offset:4096\nv_lshlrev_b32 v220,16,v166\nv_mfma_f32_16x16x32_bf16 a[56:59],v[68:71],v[24:27],0\nbuffer_load_dwordx4 v141,s[28:31],s44 offen offset:3072 lds\ns_waitcnt lgkmcnt(15)\nv_mfma_f32_16x16x32_bf16 a[60:63],v[68:71],v[28:31],0\ns_add_u32 s28,s28,0x40000\n"
"ds_read_b128 v[44:47],v129 offset:6144\nv_and_b32 v221,0xffff0000,v166\nv_mfma_f32_16x16x32_bf16 a[28:31],v[64:67],v[28:31],0\ns_addc_u32 s29,s29,0\nv_lshlrev_b32 v222,16,v167\nv_and_b32 v223,0xffff0000,v167\ns_waitcnt lgkmcnt(15)\nv_mfma_f32_16x16x32_bf16 a[92:95],v[72:75],v[28:31],0\nds_read_b128 v[48:51],v129 offset:8192\nv_add_f32 v169,v208,v209\ns_waitcnt lgkmcnt(15)\nv_mfma_f32_16x16x32_bf16 a[124:127],v[76:79],v[28:31],0\n"
"global_load_dword v151,v142,s[0:1]\nv_mfma_f32_16x16x32_bf16 a[120:123],v[76:79],v[24:27],0\nds_read_b128 v[52:55],v129 offset:10240\nv_add_f32 v169,v169,v210\nv_mfma_f32_16x16x32_bf16 a[88:91],v[72:75],v[24:27],0\nv_add_f32 v169,v169,v211\nv_add_f32 v169,v169,v212\nv_mfma_f32_16x16x32_bf16 a[84:87],v[72:75],v[20:23],0\nds_read_b128 v[56:59],v129 offset:12288\nv_add_f32 v169,v169,v213\nv_mfma_f32_16x16x32_bf16 a[116:119],v[76:79],v[20:23],0\n"
"v_add_f32 v169,v169,v214\nv_add_f32 v169,v169,v215\nv_mfma_f32_16x16x32_bf16 a[112:115],v[76:79],v[16:19],0\nds_read_b128 v[60:63],v129 offset:14336\nv_add_f32 v169,v169,v216\nv_mfma_f32_16x16x32_bf16 a[80:83],v[72:75],v[16:19],0\nv_add_f32 v169,v169,v217\nv_add_f32 v169,v169,v218\nv_mfma_f32_16x16x32_bf16 a[76:79],v[72:75],v[12:15],0\nds_read_b64_tr_b16 v[104:105],v132 offset:16384\nv_add_f32 v169,v169,v219\n"
"v_mfma_f32_16x16x32_bf16 a[108:111],v[76:79],v[12:15],0\nv_add_f32 v169,v169,v220\nv_add_f32 v169,v169,v221\nv_mfma_f32_16x16x32_bf16 a[104:107],v[76:79],v[8:11],0\nds_read_b64_tr_b16 v[106:107],v132 offset:18432\nv_add_f32 v169,v169,v222\nv_mfma_f32_16x16x32_bf16 a[72:75],v[72:75],v[8:11],0\nv_add_f32 v169,v169,v223\nv_mfma_f32_16x16x32_bf16 a[68:71],v[72:75],v[4:7],0\nds_read_b64_tr_b16 v[108:109],v133 offset:16384\n"
"v_add_f32_dpp v169,v169,v169 row_ror:8 row_mask:0xf bank_mask:0xf\nv_mfma_f32_16x16x32_bf16 a[100:103],v[76:79],v[4:7],0\ns_nop 0\nv_add_f32_dpp v169,v169,v169 row_ror:4 row_mask:0xf bank_mask:0xf\nv_mfma_f32_16x16x32_bf16 a[96:99],v[76:79],v[0:3],0\nds_read_b64_tr_b16 v[110:111],v133 offset:18432\nv_add_f32_dpp v169,v169,v169 row_ror:2 row_mask:0xf bank_mask:0xf\nv_mfma_f32_16x16x32_bf16 a[64:67],v[72:75],v[0:3],0\n"
"s_nop 0\nv_add_f32_dpp v169,v169,v169 row_ror:1 row_mask:0xf bank_mask:0xf\nv_mov_b32 v172,v169\ns_waitcnt lgkmcnt(15)\nv_mfma_f32_16x16x32_bf16 a[128:131],v[80:83],v[0:3],0\nds_read_b64_tr_b16 v[112:113],v134 offset:16384\nv_permlane16_swap_b32 v172,v169\ns_waitcnt lgkmcnt(15)\nv_mfma_f32_16x16x32_bf16 a[160:163],v[84:87],v[0:3],0\nv_add_f32 v169,v169,v172\nv_mul_f32 v170,0x3b124925,v169\nv_mfma_f32_16x16x32_bf16 a[164:167],v[84:87],v[4:7],0\n"
"ds_read_b64_tr_b16 v[114:115],v134 offset:18432\nv_cndmask_b32 v170,0,v170,s[2:3]\nv_mfma_f32_16x16x32_bf16 a[132:135],v[80:83],v[4:7],0\nv_sub_f32 v208,v208,v170\nv_sub_f32 v209,v209,v170\nv_mfma_f32_16x16x32_bf16 a[136:139],v[80:83],v[8:11],0\nds_read_b64_tr_b16 v[116:117],v135 offset:16384\nv_sub_f32 v210,v210,v170\nv_mfma_f32_16x16x32_bf16 a[168:171],v[84:87],v[8:11],0\nv_sub_f32 v211,v211,v170\n"
"v_sub_f32 v212,v212,v170\nv_mfma_f32_16x16x32_bf16 a[172:175],v[84:87],v[12:15],0\nds_read_b64_tr_b16 v[118:119],v135 offset:18432\nv_sub_f32 v213,v213,v170\nv_mfma_f32_16x16x32_bf16 a[140:143],v[80:83],v[12:15],0\nv_sub_f32 v214,v214,v170\nv_sub_f32 v215,v215,v170\nv_mfma_f32_16x16x32_bf16 a[144:147],v[80:83],v[16:19],0\nds_read_b64_tr_b16 v[120:121],v136 offset:16384\nv_sub_f32 v216,v216,v170\nv_mfma_f32_16x16x32_bf16 a[176:179],v[84:87],v[16:19],0\n"
"v_sub_f32 v217,v217,v170\nv_sub_f32 v218,v218,v170\nv_mfma_f32_16x16x32_bf16 a[180:183],v[84:87],v[20:23],0\nds_read_b64_tr_b16 v[122:123],v136 offset:18432\nv_sub_f32 v219,v219,v170\nv_mfma_f32_16x16x32_bf16 a[148:151],v[80:83],v[20:23],0\nv_sub_f32 v220,v220,v170\nv_sub_f32 v221,v221,v170\nv_mfma_f32_16x16x32_bf16 a[152:155],v[80:83],v[24:27],0\nds_read_b64_tr_b16 v[124:125],v137 offset:16384\nv_sub_f32 v222,v222,v170\n"
"v_mfma_f32_16x16x32_bf16 a[184:187],v[84:87],v[24:27],0\nv_sub_f32 v223,v223,v170\nv_mul_f32 v171,v208,v208\nv_mfma_f32_16x16x32_bf16 a[188:191],v[84:87],v[28:31],0\nds_read_b64_tr_b16 v[126:127],v137 offset:18432\nv_fmac_f32 v171,v209,v209\nv_mfma_f32_16x16x32_bf16 a[156:159],v[80:83],v[28:31],0\nv_fmac_f32 v171,v210,v210\nv_fmac_f32 v171,v211,v211\ns_waitcnt lgkmcnt(15)\nv_mfma_f32_16x16x32_bf16 a[220:223],v[88:91],v[28:31],0\n"
"v_fmac_f32 v171,v212,v212\nv_fmac_f32 v171,v213,v213\ns_waitcnt lgkmcnt(15)\nv_mfma_f32_16x16x32_bf16 a[252:255],v[92:95],v[28:31],0\nv_fmac_f32 v171,v214,v214\nv_fmac_f32 v171,v215,v215\nv_mfma_f32_16x16x32_bf16 a[248:251],v[92:95],v[24:27],0\nv_fmac_f32 v171,v216,v216\nv_fmac_f32 v171,v217,v217\nv_mfma_f32_16x16x32_bf16 a[216:219],v[88:91],v[24:27],0\nv_fmac_f32 v171,v218,v218\nv_fmac_f32 v171,v219,v219\n"
"v_mfma_f32_16x16x32_bf16 a[212:215],v[88:91],v[20:23],0\nv_fmac_f32 v171,v220,v220\nv_fmac_f32 v171,v221,v221\nv_mfma_f32_16x16x32_bf16 a[244:247],v[92:95],v[20:23],0\nv_fmac_f32 v171,v222,v222\nv_fmac_f32 v171,v223,v223\nv_mfma_f32_16x16x32_bf16 a[240:243],v[92:95],v[16:19],0\ns_nop 0\nv_add_f32_dpp v171,v171,v171 row_ror:8 row_mask:0xf bank_mask:0xf\nv_mfma_f32_16x16x32_bf16 a[208:211],v[88:91],v[16:19],0\n"
"s_nop 0\nv_add_f32_dpp v171,v171,v171 row_ror:4 row_mask:0xf bank_mask:0xf\nv_mfma_f32_16x16x32_bf16 a[204:207],v[88:91],v[12:15],0\ns_nop 0\nv_add_f32_dpp v171,v171,v171 row_ror:2 row_mask:0xf bank_mask:0xf\nv_mfma_f32_16x16x32_bf16 a[236:239],v[92:95],v[12:15],0\ns_nop 0\nv_add_f32_dpp v171,v171,v171 row_ror:1 row_mask:0xf bank_mask:0xf\nv_mov_b32 v172,v171\nv_mfma_f32_16x16x32_bf16 a[232:235],v[92:95],v[8:11],0\n"
"s_waitcnt vmcnt(10) lgkmcnt(0)\ns_barrier\nv_permlane16_swap_b32 v172,v171\nv_add_f32 v171,v171,v172\nv_mfma_f32_16x16x32_bf16 a[200:203],v[88:91],v[8:11],0\nv_mov_b32 v172,0x3727c5ac\nv_fmac_f32 v172,0x3b124925,v171\nv_mfma_f32_16x16x32_bf16 a[196:199],v[88:91],v[4:7],0\nv_rsq_f32 v172,v172\nv_mfma_f32_16x16x32_bf16 a[228:231],v[92:95],v[4:7],0\nv_mul_f32 v172,0x3db8aa3b,v172\nv_mul_f32 v208,v172,v208\n"
"v_mfma_f32_16x16x32_bf16 a[224:227],v[92:95],v[0:3],0\nv_mul_f32 v209,v172,v209\nv_mul_f32 v210,v172,v210\nv_mfma_f32_16x16x32_bf16 a[192:195],v[88:91],v[0:3],0\nv_mul_f32 v211,v172,v211\nv_mul_f32 v212,v172,v212\nv_mfma_f32_16x16x32_bf16 a[0:3],v[96:99],v[32:35],a[0:3]\ns_add_u32 m0,s43,0\nds_read_b64_tr_b16 v[64:65],v130 offset:32768\nv_mul_f32 v213,v172,v213\nv_mfma_f32_16x16x32_bf16 a[32:35],v[100:103],v[32:35],a[32:35]\n"
"buffer_load_dwordx4 v138,s[24:27],s32 offen lds\nv_mfma_f32_16x16x32_bf16 a[36:39],v[100:103],v[36:39],a[36:39]\nds_read_b64_tr_b16 v[66:67],v130 offset:34816\nv_mul_f32 v214,v172,v214\nv_mfma_f32_16x16x32_bf16 a[4:7],v[96:99],v[36:39],a[4:7]\nv_mul_f32 v215,v172,v215\nv_mul_f32 v216,v172,v216\nv_mfma_f32_16x16x32_bf16 a[8:11],v[96:99],v[40:43],a[8:11]\nds_read_b64_tr_b16 v[68:69],v131 offset:32768\n"
"v_mul_f32 v217,v172,v217\nv_mfma_f32_16x16x32_bf16 a[40:43],v[100:103],v[40:43],a[40:43]\nbuffer_load_dwordx4 v139,s[24:27],s33 offen offset:1024 lds\nv_mfma_f32_16x16x32_bf16 a[44:47],v[100:103],v[44:47],a[44:47]\nds_read_b64_tr_b16 v[70:71],v131 offset:34816\nv_mul_f32 v218,v172,v218\nv_mfma_f32_16x16x32_bf16 a[12:15],v[96:99],v[44:47],a[12:15]\nv_mul_f32 v219,v172,v219\nv_mul_f32 v220,v172,v220\n"
"v_mfma_f32_16x16x32_bf16 a[16:19],v[96:99],v[48:51],a[16:19]\nds_read_b128 v[0:3],v128 offset:32768\nv_mul_f32 v221,v172,v221\nv_mfma_f32_16x16x32_bf16 a[48:51],v[100:103],v[48:51],a[48:51]\nbuffer_load_dwordx4 v138,s[24:27],s34 offen offset:2048 lds\nv_mfma_f32_16x16x32_bf16 a[52:55],v[100:103],v[52:55],a[52:55]\nds_read_b128 v[4:7],v128 offset:34816\nv_mul_f32 v222,v172,v222\nv_mfma_f32_16x16x32_bf16 a[20:23],v[96:99],v[52:55],a[20:23]\n"
"v_mul_f32 v223,v172,v223\nv_cvt_pk_bf16_f32 v160,v208,v209\nv_mfma_f32_16x16x32_bf16 a[24:27],v[96:99],v[56:59],a[24:27]\nds_read_b128 v[8:11],v128 offset:36864\nv_cvt_pk_bf16_f32 v161,v210,v211\nv_mfma_f32_16x16x32_bf16 a[56:59],v[100:103],v[56:59],a[56:59]\nbuffer_load_dwordx4 v139,s[24:27],s35 offen offset:3072 lds\nv_mfma_f32_16x16x32_bf16 a[60:63],v[100:103],v[60:63],a[60:63]\nds_read_b128 v[12:15],v128 offset:38912\n"
"v_cvt_pk_bf16_f32 v162,v216,v217\nv_mfma_f32_16x16x32_bf16 a[28:31],v[96:99],v[60:63],a[28:31]\nv_cvt_pk_bf16_f32 v163,v218,v219\nv_cvt_pk_bf16_f32 v164,v212,v213\nv_mfma_f32_16x16x32_bf16 a[92:95],v[104:107],v[60:63],a[92:95]\ns_add_u32 m0,s43,4096\nds_read_b128 v[16:19],v128 offset:40960\nv_cvt_pk_bf16_f32 v165,v214,v215\nv_mfma_f32_16x16x32_bf16 a[124:127],v[108:111],v[60:63],a[124:127]\nbuffer_load_dwordx4 v138,s[24:27],s36 offen lds\n"
"v_mfma_f32_16x16x32_bf16 a[120:123],v[108:111],v[56:59],a[120:123]\nds_read_b128 v[20:23],v128 offset:43008\nv_cvt_pk_bf16_f32 v166,v220,v221\nv_mfma_f32_16x16x32_bf16 a[88:91],v[104:107],v[56:59],a[88:91]\nv_cvt_pk_bf16_f32 v167,v222,v223\nv_mfma_f32_16x16x32_bf16 a[84:87],v[104:107],v[52:55],a[84:87]\nds_read_b128 v[24:27],v128 offset:45056\nv_mfma_f32_16x16x32_bf16 a[116:119],v[108:111],v[52:55],a[116:119]\n"
"buffer_load_dwordx4 v139,s[24:27],s37 offen offset:1024 lds\nv_mfma_f32_16x16x32_bf16 a[112:115],v[108:111],v[48:51],a[112:115]\nds_read_b128 v[28:31],v128 offset:47104\nv_mfma_f32_16x16x32_bf16 a[80:83],v[104:107],v[48:51],a[80:83]\nbuffer_store_dwordx4 v[160:163],v168,s[8:11],s79 offen sc1\nv_mfma_f32_16x16x32_bf16 a[76:79],v[104:107],v[44:47],a[76:79]\nds_read_b64_tr_b16 v[72:73],v132 offset:32768\n"
"v_mfma_f32_16x16x32_bf16 a[108:111],v[108:111],v[44:47],a[108:111]\nbuffer_load_dwordx4 v138,s[24:27],s38 offen offset:2048 lds\nv_mfma_f32_16x16x32_bf16 a[104:107],v[108:111],v[40:43],a[104:107]\nds_read_b64_tr_b16 v[74:75],v132 offset:34816\nv_mfma_f32_16x16x32_bf16 a[72:75],v[104:107],v[40:43],a[72:75]\nbuffer_store_dwordx4 v[164:167],v168,s[8:11],s79 offen offset:256 sc1\nv_mfma_f32_16x16x32_bf16 a[68:71],v[104:107],v[36:39],a[68:71]\n"
"ds_read_b64_tr_b16 v[76:77],v133 offset:32768\nv_mfma_f32_16x16x32_bf16 a[100:103],v[108:111],v[36:39],a[100:103]\nbuffer_load_dwordx4 v139,s[24:27],s39 offen offset:3072 lds\nv_mfma_f32_16x16x32_bf16 a[96:99],v[108:111],v[32:35],a[96:99]\ns_add_u32 s24,s24,128\nds_read_b64_tr_b16 v[78:79],v133 offset:34816\nv_mfma_f32_16x16x32_bf16 a[64:67],v[104:107],v[32:35],a[64:67]\ns_addc_u32 s25,s25,0\nv_mfma_f32_16x16x32_bf16 a[128:131],v[112:115],v[32:35],a[128:131]\n"
"s_add_u32 m0,s15,16384\nds_read_b64_tr_b16 v[80:81],v134 offset:32768\nv_mfma_f32_16x16x32_bf16 a[160:163],v[116:119],v[32:35],a[160:163]\nbuffer_load_dwordx4 v140,s[28:31],s21 offen lds\nv_mfma_f32_16x16x32_bf16 a[164:167],v[116:119],v[36:39],a[164:167]\nds_read_b64_tr_b16 v[82:83],v134 offset:34816\nv_mfma_f32_16x16x32_bf16 a[132:135],v[112:115],v[36:39],a[132:135]\nv_mfma_f32_16x16x32_bf16 a[136:139],v[112:115],v[40:43],a[136:139]\n"
"ds_read_b64_tr_b16 v[84:85],v135 offset:32768\nv_mfma_f32_16x16x32_bf16 a[168:171],v[116:119],v[40:43],a[168:171]\nbuffer_load_dwordx4 v141,s[28:31],s22 offen offset:1024 lds\nv_mfma_f32_16x16x32_bf16 a[172:175],v[116:119],v[44:47],a[172:175]\nds_read_b64_tr_b16 v[86:87],v135 offset:34816\nv_mfma_f32_16x16x32_bf16 a[140:143],v[112:115],v[44:47],a[140:143]\nv_mfma_f32_16x16x32_bf16 a[144:147],v[112:115],v[48:51],a[144:147]\n"
"ds_read_b64_tr_b16 v[88:89],v136 offset:32768\nv_mfma_f32_16x16x32_bf16 a[176:179],v[116:119],v[48:51],a[176:179]\nbuffer_load_dwordx4 v140,s[28:31],s23 offen offset:2048 lds\nv_mfma_f32_16x16x32_bf16 a[180:183],v[116:119],v[52:55],a[180:183]\nds_read_b64_tr_b16 v[90:91],v136 offset:34816\nv_mfma_f32_16x16x32_bf16 a[148:151],v[112:115],v[52:55],a[148:151]\nv_mfma_f32_16x16x32_bf16 a[152:155],v[112:115],v[56:59],a[152:155]\n"
"ds_read_b64_tr_b16 v[92:93],v137 offset:32768\nv_readlane_b32 s48,v144,s55\ns_add_u32 s55,s55,1\nv_mfma_f32_16x16x32_bf16 a[184:187],v[116:119],v[56:59],a[184:187]\nbuffer_load_dwordx4 v141,s[28:31],s44 offen offset:3072 lds\nv_mfma_f32_16x16x32_bf16 a[188:191],v[116:119],v[60:63],a[188:191]\ns_add_u32 s28,s28,0x40000\nds_read_b64_tr_b16 v[94:95],v137 offset:34816\nv_readlane_b32 s49,v144,s55\nv_mfma_f32_16x16x32_bf16 a[156:159],v[112:115],v[60:63],a[156:159]\n"
"s_addc_u32 s29,s29,0\ns_add_u32 s55,s55,1\nv_mfma_f32_16x16x32_bf16 a[220:223],v[120:123],v[60:63],a[220:223]\nv_readlane_b32 s50,v144,s55\ns_add_u32 s55,s55,1\nv_mfma_f32_16x16x32_bf16 a[252:255],v[124:127],v[60:63],a[252:255]\nv_readlane_b32 s45,v144,s55\ns_add_u32 s55,s55,1\nv_mfma_f32_16x16x32_bf16 a[248:251],v[124:127],v[56:59],a[248:251]\nv_readlane_b32 s47,v144,s55\ns_add_u32 s55,s55,1\nv_mfma_f32_16x16x32_bf16 a[216:219],v[120:123],v[56:59],a[216:219]\n"
"v_readlane_b32 s53,v144,s55\ns_add_u32 s55,s55,1\nv_mfma_f32_16x16x32_bf16 a[212:215],v[120:123],v[52:55],a[212:215]\nv_readlane_b32 s54,v144,s55\ns_add_u32 s55,s55,2\nv_mfma_f32_16x16x32_bf16 a[244:247],v[124:127],v[52:55],a[244:247]\ns_lshl_b32 s46,s45,3\nv_mad_u32_u24 v149,v145,s45,v148\ns_sub_u32 s46,s46,0x400\nv_mfma_f32_16x16x32_bf16 a[240:243],v[124:127],v[48:51],a[240:243]\ns_sub_u32 s47,s47,0x800\n"
"s_add_u32 s52,s47,s46\nv_mfma_f32_16x16x32_bf16 a[208:211],v[120:123],v[48:51],a[208:211]\nv_mfma_f32_16x16x32_bf16 a[204:207],v[120:123],v[44:47],a[204:207]\nv_mfma_f32_16x16x32_bf16 a[236:239],v[124:127],v[44:47],a[236:239]\nv_mfma_f32_16x16x32_bf16 a[232:235],v[124:127],v[40:43],a[232:235]\ns_waitcnt vmcnt(20) lgkmcnt(0)\ns_barrier\nv_mfma_f32_16x16x32_bf16 a[200:203],v[120:123],v[40:43],a[200:203]\n"
"v_mfma_f32_16x16x32_bf16 a[196:199],v[120:123],v[36:39],a[196:199]\nv_mfma_f32_16x16x32_bf16 a[228:231],v[124:127],v[36:39],a[228:231]\nv_mfma_f32_16x16x32_bf16 a[224:227],v[124:127],v[32:35],a[224:227]\nv_mfma_f32_16x16x32_bf16 a[192:195],v[120:123],v[32:35],a[192:195]\ns_mov_b32 s42,6\nL_fused_block_loop_5:\nv_mfma_f32_16x16x32_bf16 a[0:3],v[64:67],v[0:3],a[0:3]\ns_add_u32 m0,s15,32768\nds_read_b64_tr_b16 v[96:97],v130 offset:49152\n"
"v_mfma_f32_16x16x32_bf16 a[32:35],v[68:71],v[0:3],a[32:35]\nbuffer_load_dwordx4 v140,s[28:31],s21 offen lds\nv_mfma_f32_16x16x32_bf16 a[36:39],v[68:71],v[4:7],a[36:39]\nds_read_b64_tr_b16 v[98:99],v130 offset:51200\nv_mfma_f32_16x16x32_bf16 a[4:7],v[64:67],v[4:7],a[4:7]\nv_mfma_f32_16x16x32_bf16 a[8:11],v[64:67],v[8:11],a[8:11]\nds_read_b64_tr_b16 v[100:101],v131 offset:49152\nv_mfma_f32_16x16x32_bf16 a[40:43],v[68:71],v[8:11],a[40:43]\n"
"buffer_load_dwordx4 v141,s[28:31],s22 offen offset:1024 lds\nv_mfma_f32_16x16x32_bf16 a[44:47],v[68:71],v[12:15],a[44:47]\nds_read_b64_tr_b16 v[102:103],v131 offset:51200\nv_mfma_f32_16x16x32_bf16 a[12:15],v[64:67],v[12:15],a[12:15]\nv_mfma_f32_16x16x32_bf16 a[16:19],v[64:67],v[16:19],a[16:19]\nds_read_b128 v[32:35],v129 offset:32768\nv_mfma_f32_16x16x32_bf16 a[48:51],v[68:71],v[16:19],a[48:51]\nbuffer_load_dwordx4 v140,s[28:31],s23 offen offset:2048 lds\n"
"v_mfma_f32_16x16x32_bf16 a[52:55],v[68:71],v[20:23],a[52:55]\nds_read_b128 v[36:39],v129 offset:34816\nv_mfma_f32_16x16x32_bf16 a[20:23],v[64:67],v[20:23],a[20:23]\nv_mfma_f32_16x16x32_bf16 a[24:27],v[64:67],v[24:27],a[24:27]\nds_read_b128 v[40:43],v129 offset:36864\nv_mfma_f32_16x16x32_bf16 a[56:59],v[68:71],v[24:27],a[56:59]\nbuffer_load_dwordx4 v141,s[28:31],s44 offen offset:3072 lds\nv_mfma_f32_16x16x32_bf16 a[60:63],v[68:71],v[28:31],a[60:63]\n"
"s_add_u32 s28,s28,0x40000\nds_read_b128 v[44:47],v129 offset:38912\nv_mfma_f32_16x16x32_bf16 a[28:31],v[64:67],v[28:31],a[28:31]\ns_addc_u32 s29,s29,0\ns_mov_b32 m0,s65\nv_mfma_f32_16x16x32_bf16 a[92:95],v[72:75],v[28:31],a[92:95]\nds_read_b128 v[48:51],v129 offset:40960\nv_mfma_f32_16x16x32_bf16 a[124:127],v[76:79],v[28:31],a[124:127]\nbuffer_load_dwordx4 v149,s[48:51],0 offen lds\nv_mfma_f32_16x16x32_bf16 a[120:123],v[76:79],v[24:27],a[120:123]\n"
"ds_read_b128 v[52:55],v129 offset:43008\nv_mfma_f32_16x16x32_bf16 a[88:91],v[72:75],v[24:27],a[88:91]\nv_mfma_f32_16x16x32_bf16 a[84:87],v[72:75],v[20:23],a[84:87]\nds_read_b128 v[56:59],v129 offset:45056\nv_mfma_f32_16x16x32_bf16 a[116:119],v[76:79],v[20:23],a[116:119]\nbuffer_load_dwordx4 v149,s[48:51],s46 offen offset:1024 lds\nv_mfma_f32_16x16x32_bf16 a[112:115],v[76:79],v[16:19],a[112:115]\nds_read_b128 v[60:63],v129 offset:47104\n"
"v_mfma_f32_16x16x32_bf16 a[80:83],v[72:75],v[16:19],a[80:83]\nv_mfma_f32_16x16x32_bf16 a[76:79],v[72:75],v[12:15],a[76:79]\nds_read_b64_tr_b16 v[104:105],v132 offset:49152\nv_mfma_f32_16x16x32_bf16 a[108:111],v[76:79],v[12:15],a[108:111]\nbuffer_load_dwordx4 v149,s[48:51],s47 offen offset:2048 lds\nv_mfma_f32_16x16x32_bf16 a[104:107],v[76:79],v[8:11],a[104:107]\nds_read_b64_tr_b16 v[106:107],v132 offset:51200\n"
"v_mfma_f32_16x16x32_bf16 a[72:75],v[72:75],v[8:11],a[72:75]\nv_mfma_f32_16x16x32_bf16 a[68:71],v[72:75],v[4:7],a[68:71]\nds_read_b64_tr_b16 v[108:109],v133 offset:49152\nv_mfma_f32_16x16x32_bf16 a[100:103],v[76:79],v[4:7],a[100:103]\nbuffer_load_dwordx4 v149,s[48:51],s52 offen offset:3072 lds\nv_mfma_f32_16x16x32_bf16 a[96:99],v[76:79],v[0:3],a[96:99]\nds_read_b64_tr_b16 v[110:111],v133 offset:51200\n"
"v_mfma_f32_16x16x32_bf16 a[64:67],v[72:75],v[0:3],a[64:67]\nv_mfma_f32_16x16x32_bf16 a[128:131],v[80:83],v[0:3],a[128:131]\nds_read_b64_tr_b16 v[112:113],v134 offset:49152\nv_mfma_f32_16x16x32_bf16 a[160:163],v[84:87],v[0:3],a[160:163]\nbuffer_store_dwordx4 v[152:155],v252,s[8:11],s56 offen sc1\nv_mfma_f32_16x16x32_bf16 a[164:167],v[84:87],v[4:7],a[164:167]\nds_read_b64_tr_b16 v[114:115],v134 offset:51200\n"
"v_mfma_f32_16x16x32_bf16 a[132:135],v[80:83],v[4:7],a[132:135]\nv_mfma_f32_16x16x32_bf16 a[136:139],v[80:83],v[8:11],a[136:139]\nds_read_b64_tr_b16 v[116:117],v135 offset:49152\nv_mfma_f32_16x16x32_bf16 a[168:171],v[84:87],v[8:11],a[168:171]\nv_mfma_f32_16x16x32_bf16 a[172:175],v[84:87],v[12:15],a[172:175]\nds_read_b64_tr_b16 v[118:119],v135 offset:51200\nv_mfma_f32_16x16x32_bf16 a[140:143],v[80:83],v[12:15],a[140:143]\n"
"buffer_store_dwordx4 v[156:159],v252,s[8:11],s57 offen sc1\nv_mfma_f32_16x16x32_bf16 a[144:147],v[80:83],v[16:19],a[144:147]\nds_read_b64_tr_b16 v[120:121],v136 offset:49152\nv_mfma_f32_16x16x32_bf16 a[176:179],v[84:87],v[16:19],a[176:179]\nv_mfma_f32_16x16x32_bf16 a[180:183],v[84:87],v[20:23],a[180:183]\nds_read_b64_tr_b16 v[122:123],v136 offset:51200\nv_mfma_f32_16x16x32_bf16 a[148:151],v[80:83],v[20:23],a[148:151]\n"
"v_mfma_f32_16x16x32_bf16 a[152:155],v[80:83],v[24:27],a[152:155]\nds_read_b64_tr_b16 v[124:125],v137 offset:49152\nv_mfma_f32_16x16x32_bf16 a[184:187],v[84:87],v[24:27],a[184:187]\nbuffer_store_dwordx4 v[160:163],v252,s[8:11],s58 offen sc1\nv_mfma_f32_16x16x32_bf16 a[188:191],v[84:87],v[28:31],a[188:191]\nds_read_b64_tr_b16 v[126:127],v137 offset:51200\nv_mfma_f32_16x16x32_bf16 a[156:159],v[80:83],v[28:31],a[156:159]\n"
"v_mfma_f32_16x16x32_bf16 a[220:223],v[88:91],v[28:31],a[220:223]\nv_mfma_f32_16x16x32_bf16 a[252:255],v[92:95],v[28:31],a[252:255]\nv_mfma_f32_16x16x32_bf16 a[248:251],v[92:95],v[24:27],a[248:251]\nv_mfma_f32_16x16x32_bf16 a[216:219],v[88:91],v[24:27],a[216:219]\nbuffer_store_dwordx4 v[164:167],v252,s[8:11],s59 offen sc1\nv_mfma_f32_16x16x32_bf16 a[212:215],v[88:91],v[20:23],a[212:215]\nv_mfma_f32_16x16x32_bf16 a[244:247],v[92:95],v[20:23],a[244:247]\n"
"v_mfma_f32_16x16x32_bf16 a[240:243],v[92:95],v[16:19],a[240:243]\nv_mfma_f32_16x16x32_bf16 a[208:211],v[88:91],v[16:19],a[208:211]\nv_mfma_f32_16x16x32_bf16 a[204:207],v[88:91],v[12:15],a[204:207]\nv_mfma_f32_16x16x32_bf16 a[236:239],v[92:95],v[12:15],a[236:239]\nv_mfma_f32_16x16x32_bf16 a[232:235],v[92:95],v[8:11],a[232:235]\ns_waitcnt vmcnt(16) lgkmcnt(0)\ns_barrier\nv_mfma_f32_16x16x32_bf16 a[200:203],v[88:91],v[8:11],a[200:203]\n"
"v_mfma_f32_16x16x32_bf16 a[196:199],v[88:91],v[4:7],a[196:199]\nv_mfma_f32_16x16x32_bf16 a[228:231],v[92:95],v[4:7],a[228:231]\nv_mfma_f32_16x16x32_bf16 a[224:227],v[92:95],v[0:3],a[224:227]\nv_mfma_f32_16x16x32_bf16 a[192:195],v[88:91],v[0:3],a[192:195]\nv_mfma_f32_16x16x32_bf16 a[0:3],v[96:99],v[32:35],a[0:3]\ns_add_u32 m0,s43,32768\nds_read_b64_tr_b16 v[64:65],v130 offset:0\nv_mfma_f32_16x16x32_bf16 a[32:35],v[100:103],v[32:35],a[32:35]\n"
"buffer_load_dwordx4 v138,s[24:27],s32 offen lds\nv_mfma_f32_16x16x32_bf16 a[36:39],v[100:103],v[36:39],a[36:39]\nds_read_b64_tr_b16 v[66:67],v130 offset:2048\nv_mfma_f32_16x16x32_bf16 a[4:7],v[96:99],v[36:39],a[4:7]\nv_mfma_f32_16x16x32_bf16 a[8:11],v[96:99],v[40:43],a[8:11]\nds_read_b64_tr_b16 v[68:69],v131 offset:0\nv_mfma_f32_16x16x32_bf16 a[40:43],v[100:103],v[40:43],a[40:43]\nbuffer_load_dwordx4 v139,s[24:27],s33 offen offset:1024 lds\n"
"v_mfma_f32_16x16x32_bf16 a[44:47],v[100:103],v[44:47],a[44:47]\nds_read_b64_tr_b16 v[70:71],v131 offset:2048\nv_mfma_f32_16x16x32_bf16 a[12:15],v[96:99],v[44:47],a[12:15]\nv_mfma_f32_16x16x32_bf16 a[16:19],v[96:99],v[48:51],a[16:19]\nds_read_b128 v[0:3],v128 offset:0\nv_mfma_f32_16x16x32_bf16 a[48:51],v[100:103],v[48:51],a[48:51]\nbuffer_load_dwordx4 v138,s[24:27],s34 offen offset:2048 lds\nv_mfma_f32_16x16x32_bf16 a[52:55],v[100:103],v[52:55],a[52:55]\n"
"ds_read_b128 v[4:7],v128 offset:2048\nv_mfma_f32_16x16x32_bf16 a[20:23],v[96:99],v[52:55],a[20:23]\nv_mfma_f32_16x16x32_bf16 a[24:27],v[96:99],v[56:59],a[24:27]\nds_read_b128 v[8:11],v128 offset:4096\nv_mfma_f32_16x16x32_bf16 a[56:59],v[100:103],v[56:59],a[56:59]\nbuffer_load_dwordx4 v139,s[24:27],s35 offen offset:3072 lds\nv_mfma_f32_16x16x32_bf16 a[60:63],v[100:103],v[60:63],a[60:63]\nds_read_b128 v[12:15],v128 offset:6144\n"
"v_mfma_f32_16x16x32_bf16 a[28:31],v[96:99],v[60:63],a[28:31]\nv_mfma_f32_16x16x32_bf16 a[92:95],v[104:107],v[60:63],a[92:95]\ns_add_u32 m0,s43,36864\nds_read_b128 v[16:19],v128 offset:8192\nv_mfma_f32_16x16x32_bf16 a[124:127],v[108:111],v[60:63],a[124:127]\nbuffer_load_dwordx4 v138,s[24:27],s36 offen lds\nv_mfma_f32_16x16x32_bf16 a[120:123],v[108:111],v[56:59],a[120:123]\nds_read_b128 v[20:23],v128 offset:10240\n"
"v_mfma_f32_16x16x32_bf16 a[88:91],v[104:107],v[56:59],a[88:91]\nv_mfma_f32_16x16x32_bf16 a[84:87],v[104:107],v[52:55],a[84:87]\nds_read_b128 v[24:27],v128 offset:12288\nv_mfma_f32_16x16x32_bf16 a[116:119],v[108:111],v[52:55],a[116:119]\nbuffer_load_dwordx4 v139,s[24:27],s37 offen offset:1024 lds\nv_mfma_f32_16x16x32_bf16 a[112:115],v[108:111],v[48:51],a[112:115]\nds_read_b128 v[28:31],v128 offset:14336\n"
"v_mfma_f32_16x16x32_bf16 a[80:83],v[104:107],v[48:51],a[80:83]\nv_mfma_f32_16x16x32_bf16 a[76:79],v[104:107],v[44:47],a[76:79]\nds_read_b64_tr_b16 v[72:73],v132 offset:0\nv_mfma_f32_16x16x32_bf16 a[108:111],v[108:111],v[44:47],a[108:111]\nbuffer_load_dwordx4 v138,s[24:27],s38 offen offset:2048 lds\nv_mfma_f32_16x16x32_bf16 a[104:107],v[108:111],v[40:43],a[104:107]\nds_read_b64_tr_b16 v[74:75],v132 offset:2048\n"
"v_mfma_f32_16x16x32_bf16 a[72:75],v[104:107],v[40:43],a[72:75]\nv_mfma_f32_16x16x32_bf16 a[68:71],v[104:107],v[36:39],a[68:71]\nds_read_b64_tr_b16 v[76:77],v133 offset:0\nv_mfma_f32_16x16x32_bf16 a[100:103],v[108:111],v[36:39],a[100:103]\nbuffer_load_dwordx4 v139,s[24:27],s39 offen offset:3072 lds\nv_mfma_f32_16x16x32_bf16 a[96:99],v[108:111],v[32:35],a[96:99]\ns_add_u32 s24,s24,128\nds_read_b64_tr_b16 v[78:79],v133 offset:2048\n"
"v_mfma_f32_16x16x32_bf16 a[64:67],v[104:107],v[32:35],a[64:67]\ns_addc_u32 s25,s25,0\nv_mfma_f32_16x16x32_bf16 a[128:131],v[112:115],v[32:35],a[128:131]\ns_add_u32 m0,s15,49152\nds_read_b64_tr_b16 v[80:81],v134 offset:0\nv_mfma_f32_16x16x32_bf16 a[160:163],v[116:119],v[32:35],a[160:163]\nbuffer_load_dwordx4 v140,s[28:31],s21 offen lds\nv_mfma_f32_16x16x32_bf16 a[164:167],v[116:119],v[36:39],a[164:167]\n"
"ds_read_b64_tr_b16 v[82:83],v134 offset:2048\nv_mfma_f32_16x16x32_bf16 a[132:135],v[112:115],v[36:39],a[132:135]\nv_mfma_f32_16x16x32_bf16 a[136:139],v[112:115],v[40:43],a[136:139]\nds_read_b64_tr_b16 v[84:85],v135 offset:0\nv_mfma_f32_16x16x32_bf16 a[168:171],v[116:119],v[40:43],a[168:171]\nbuffer_load_dwordx4 v141,s[28:31],s22 offen offset:1024 lds\nv_mfma_f32_16x16x32_bf16 a[172:175],v[116:119],v[44:47],a[172:175]\n"
"ds_read_b64_tr_b16 v[86:87],v135 offset:2048\nv_mfma_f32_16x16x32_bf16 a[140:143],v[112:115],v[44:47],a[140:143]\nv_mfma_f32_16x16x32_bf16 a[144:147],v[112:115],v[48:51],a[144:147]\nds_read_b64_tr_b16 v[88:89],v136 offset:0\nv_mfma_f32_16x16x32_bf16 a[176:179],v[116:119],v[48:51],a[176:179]\nbuffer_load_dwordx4 v140,s[28:31],s23 offen offset:2048 lds\nv_mfma_f32_16x16x32_bf16 a[180:183],v[116:119],v[52:55],a[180:183]\n"
"ds_read_b64_tr_b16 v[90:91],v136 offset:2048\nv_mfma_f32_16x16x32_bf16 a[148:151],v[112:115],v[52:55],a[148:151]\nv_mfma_f32_16x16x32_bf16 a[152:155],v[112:115],v[56:59],a[152:155]\nds_read_b64_tr_b16 v[92:93],v137 offset:0\nv_mfma_f32_16x16x32_bf16 a[184:187],v[116:119],v[56:59],a[184:187]\nbuffer_load_dwordx4 v141,s[28:31],s44 offen offset:3072 lds\nv_mfma_f32_16x16x32_bf16 a[188:191],v[116:119],v[60:63],a[188:191]\n"
"s_add_u32 s28,s28,0x40000\nds_read_b64_tr_b16 v[94:95],v137 offset:2048\nv_mfma_f32_16x16x32_bf16 a[156:159],v[112:115],v[60:63],a[156:159]\ns_addc_u32 s29,s29,0\nv_mfma_f32_16x16x32_bf16 a[220:223],v[120:123],v[60:63],a[220:223]\nv_mfma_f32_16x16x32_bf16 a[252:255],v[124:127],v[60:63],a[252:255]\nv_mfma_f32_16x16x32_bf16 a[248:251],v[124:127],v[56:59],a[248:251]\nv_mfma_f32_16x16x32_bf16 a[216:219],v[120:123],v[56:59],a[216:219]\n"
"v_mfma_f32_16x16x32_bf16 a[212:215],v[120:123],v[52:55],a[212:215]\nv_mfma_f32_16x16x32_bf16 a[244:247],v[124:127],v[52:55],a[244:247]\nv_mfma_f32_16x16x32_bf16 a[240:243],v[124:127],v[48:51],a[240:243]\nv_mfma_f32_16x16x32_bf16 a[208:211],v[120:123],v[48:51],a[208:211]\nv_mfma_f32_16x16x32_bf16 a[204:207],v[120:123],v[44:47],a[204:207]\nv_mfma_f32_16x16x32_bf16 a[236:239],v[124:127],v[44:47],a[236:239]\n"
"v_mfma_f32_16x16x32_bf16 a[232:235],v[124:127],v[40:43],a[232:235]\ns_waitcnt vmcnt(24) lgkmcnt(0)\ns_barrier\nv_mfma_f32_16x16x32_bf16 a[200:203],v[120:123],v[40:43],a[200:203]\nv_mfma_f32_16x16x32_bf16 a[196:199],v[120:123],v[36:39],a[196:199]\nv_mfma_f32_16x16x32_bf16 a[228:231],v[124:127],v[36:39],a[228:231]\nv_mfma_f32_16x16x32_bf16 a[224:227],v[124:127],v[32:35],a[224:227]\nv_mfma_f32_16x16x32_bf16 a[192:195],v[120:123],v[32:35],a[192:195]\n"
"v_mfma_f32_16x16x32_bf16 a[0:3],v[64:67],v[0:3],a[0:3]\ns_add_u32 m0,s15,0\nds_read_b64_tr_b16 v[96:97],v130 offset:16384\nv_mfma_f32_16x16x32_bf16 a[32:35],v[68:71],v[0:3],a[32:35]\nbuffer_load_dwordx4 v140,s[28:31],s21 offen lds\nv_mfma_f32_16x16x32_bf16 a[36:39],v[68:71],v[4:7],a[36:39]\nds_read_b64_tr_b16 v[98:99],v130 offset:18432\nv_mfma_f32_16x16x32_bf16 a[4:7],v[64:67],v[4:7],a[4:7]\nv_mfma_f32_16x16x32_bf16 a[8:11],v[64:67],v[8:11],a[8:11]\n"
"ds_read_b64_tr_b16 v[100:101],v131 offset:16384\nv_mfma_f32_16x16x32_bf16 a[40:43],v[68:71],v[8:11],a[40:43]\nbuffer_load_dwordx4 v141,s[28:31],s22 offen offset:1024 lds\nv_mfma_f32_16x16x32_bf16 a[44:47],v[68:71],v[12:15],a[44:47]\nds_read_b64_tr_b16 v[102:103],v131 offset:18432\nv_mfma_f32_16x16x32_bf16 a[12:15],v[64:67],v[12:15],a[12:15]\nv_mfma_f32_16x16x32_bf16 a[16:19],v[64:67],v[16:19],a[16:19]\n"
"ds_read_b128 v[32:35],v129 offset:0\nv_mfma_f32_16x16x32_bf16 a[48:51],v[68:71],v[16:19],a[48:51]\nbuffer_load_dwordx4 v140,s[28:31],s23 offen offset:2048 lds\nv_mfma_f32_16x16x32_bf16 a[52:55],v[68:71],v[20:23],a[52:55]\nds_read_b128 v[36:39],v129 offset:2048\nv_mfma_f32_16x16x32_bf16 a[20:23],v[64:67],v[20:23],a[20:23]\nv_mfma_f32_16x16x32_bf16 a[24:27],v[64:67],v[24:27],a[24:27]\nds_read_b128 v[40:43],v129 offset:4096\n"
"v_mfma_f32_16x16x32_bf16 a[56:59],v[68:71],v[24:27],a[56:59]\nbuffer_load_dwordx4 v141,s[28:31],s44 offen offset:3072 lds\nv_mfma_f32_16x16x32_bf16 a[60:63],v[68:71],v[28:31],a[60:63]\ns_add_u32 s28,s28,0x40000\nds_read_b128 v[44:47],v129 offset:6144\nv_mfma_f32_16x16x32_bf16 a[28:31],v[64:67],v[28:31],a[28:31]\ns_addc_u32 s29,s29,0\nv_mfma_f32_16x16x32_bf16 a[92:95],v[72:75],v[28:31],a[92:95]\nds_read_b128 v[48:51],v129 offset:8192\n"
"v_mfma_f32_16x16x32_bf16 a[124:127],v[76:79],v[28:31],a[124:127]\nbuffer_load_dword v151,v150,s[60:63],s78 offen\ns_add_u32 s78,s78,s64\nv_mfma_f32_16x16x32_bf16 a[120:123],v[76:79],v[24:27],a[120:123]\nds_read_b128 v[52:55],v129 offset:10240\nv_mfma_f32_16x16x32_bf16 a[88:91],v[72:75],v[24:27],a[88:91]\nv_mfma_f32_16x16x32_bf16 a[84:87],v[72:75],v[20:23],a[84:87]\nds_read_b128 v[56:59],v129 offset:12288\n"
"v_mfma_f32_16x16x32_bf16 a[116:119],v[76:79],v[20:23],a[116:119]\nv_mfma_f32_16x16x32_bf16 a[112:115],v[76:79],v[16:19],a[112:115]\nds_read_b128 v[60:63],v129 offset:14336\nv_mfma_f32_16x16x32_bf16 a[80:83],v[72:75],v[16:19],a[80:83]\nv_mfma_f32_16x16x32_bf16 a[76:79],v[72:75],v[12:15],a[76:79]\nds_read_b64_tr_b16 v[104:105],v132 offset:16384\nv_mfma_f32_16x16x32_bf16 a[108:111],v[76:79],v[12:15],a[108:111]\n"
"v_mfma_f32_16x16x32_bf16 a[104:107],v[76:79],v[8:11],a[104:107]\nds_read_b64_tr_b16 v[106:107],v132 offset:18432\nv_mfma_f32_16x16x32_bf16 a[72:75],v[72:75],v[8:11],a[72:75]\nv_mfma_f32_16x16x32_bf16 a[68:71],v[72:75],v[4:7],a[68:71]\nds_read_b64_tr_b16 v[108:109],v133 offset:16384\nv_mfma_f32_16x16x32_bf16 a[100:103],v[76:79],v[4:7],a[100:103]\nv_mfma_f32_16x16x32_bf16 a[96:99],v[76:79],v[0:3],a[96:99]\n"
"ds_read_b64_tr_b16 v[110:111],v133 offset:18432\nv_mfma_f32_16x16x32_bf16 a[64:67],v[72:75],v[0:3],a[64:67]\nv_mfma_f32_16x16x32_bf16 a[128:131],v[80:83],v[0:3],a[128:131]\nds_read_b64_tr_b16 v[112:113],v134 offset:16384\nv_mfma_f32_16x16x32_bf16 a[160:163],v[84:87],v[0:3],a[160:163]\nv_mfma_f32_16x16x32_bf16 a[164:167],v[84:87],v[4:7],a[164:167]\nds_read_b64_tr_b16 v[114:115],v134 offset:18432\nv_mfma_f32_16x16x32_bf16 a[132:135],v[80:83],v[4:7],a[132:135]\n"
"v_mfma_f32_16x16x32_bf16 a[136:139],v[80:83],v[8:11],a[136:139]\nds_read_b64_tr_b16 v[116:117],v135 offset:16384\nv_mfma_f32_16x16x32_bf16 a[168:171],v[84:87],v[8:11],a[168:171]\nv_mfma_f32_16x16x32_bf16 a[172:175],v[84:87],v[12:15],a[172:175]\nds_read_b64_tr_b16 v[118:119],v135 offset:18432\nv_mfma_f32_16x16x32_bf16 a[140:143],v[80:83],v[12:15],a[140:143]\nv_mfma_f32_16x16x32_bf16 a[144:147],v[80:83],v[16:19],a[144:147]\n"
"ds_read_b64_tr_b16 v[120:121],v136 offset:16384\nv_mfma_f32_16x16x32_bf16 a[176:179],v[84:87],v[16:19],a[176:179]\nv_mfma_f32_16x16x32_bf16 a[180:183],v[84:87],v[20:23],a[180:183]\nds_read_b64_tr_b16 v[122:123],v136 offset:18432\nv_mfma_f32_16x16x32_bf16 a[148:151],v[80:83],v[20:23],a[148:151]\nv_mfma_f32_16x16x32_bf16 a[152:155],v[80:83],v[24:27],a[152:155]\nds_read_b64_tr_b16 v[124:125],v137 offset:16384\n"
"v_mfma_f32_16x16x32_bf16 a[184:187],v[84:87],v[24:27],a[184:187]\nv_mfma_f32_16x16x32_bf16 a[188:191],v[84:87],v[28:31],a[188:191]\nds_read_b64_tr_b16 v[126:127],v137 offset:18432\nv_mfma_f32_16x16x32_bf16 a[156:159],v[80:83],v[28:31],a[156:159]\nv_mfma_f32_16x16x32_bf16 a[220:223],v[88:91],v[28:31],a[220:223]\nv_mfma_f32_16x16x32_bf16 a[252:255],v[92:95],v[28:31],a[252:255]\nv_mfma_f32_16x16x32_bf16 a[248:251],v[92:95],v[24:27],a[248:251]\n"
"v_mfma_f32_16x16x32_bf16 a[216:219],v[88:91],v[24:27],a[216:219]\nv_mfma_f32_16x16x32_bf16 a[212:215],v[88:91],v[20:23],a[212:215]\nv_mfma_f32_16x16x32_bf16 a[244:247],v[92:95],v[20:23],a[244:247]\nv_mfma_f32_16x16x32_bf16 a[240:243],v[92:95],v[16:19],a[240:243]\nv_mfma_f32_16x16x32_bf16 a[208:211],v[88:91],v[16:19],a[208:211]\nv_mfma_f32_16x16x32_bf16 a[204:207],v[88:91],v[12:15],a[204:207]\nv_mfma_f32_16x16x32_bf16 a[236:239],v[92:95],v[12:15],a[236:239]\n"
"v_mfma_f32_16x16x32_bf16 a[232:235],v[92:95],v[8:11],a[232:235]\ns_waitcnt vmcnt(9) lgkmcnt(0)\ns_barrier\nv_mfma_f32_16x16x32_bf16 a[200:203],v[88:91],v[8:11],a[200:203]\nv_mfma_f32_16x16x32_bf16 a[196:199],v[88:91],v[4:7],a[196:199]\nv_mfma_f32_16x16x32_bf16 a[228:231],v[92:95],v[4:7],a[228:231]\nv_mfma_f32_16x16x32_bf16 a[224:227],v[92:95],v[0:3],a[224:227]\nv_mfma_f32_16x16x32_bf16 a[192:195],v[88:91],v[0:3],a[192:195]\n"
"v_mfma_f32_16x16x32_bf16 a[0:3],v[96:99],v[32:35],a[0:3]\ns_add_u32 m0,s43,0\nds_read_b64_tr_b16 v[64:65],v130 offset:32768\nv_mfma_f32_16x16x32_bf16 a[32:35],v[100:103],v[32:35],a[32:35]\nbuffer_load_dwordx4 v138,s[24:27],s32 offen lds\nv_mfma_f32_16x16x32_bf16 a[36:39],v[100:103],v[36:39],a[36:39]\ns_mov_b32 s56,s53\nds_read_b64_tr_b16 v[66:67],v130 offset:34816\ns_add_u32 s57,s56,s54\nv_mfma_f32_16x16x32_bf16 a[4:7],v[96:99],v[36:39],a[4:7]\n"
"s_add_u32 s58,s57,s54\ns_add_u32 s59,s58,s54\nds_read_b64_tr_b16 v[152:153],v244 offset:0\nv_mfma_f32_16x16x32_bf16 a[8:11],v[96:99],v[40:43],a[8:11]\nds_read_b64_tr_b16 v[68:69],v131 offset:32768\nv_mfma_f32_16x16x32_bf16 a[40:43],v[100:103],v[40:43],a[40:43]\nbuffer_load_dwordx4 v139,s[24:27],s33 offen offset:1024 lds\nv_mfma_f32_16x16x32_bf16 a[44:47],v[100:103],v[44:47],a[44:47]\nds_read_b64_tr_b16 v[70:71],v131 offset:34816\n"
"v_mfma_f32_16x16x32_bf16 a[12:15],v[96:99],v[44:47],a[12:15]\nds_read_b64_tr_b16 v[154:155],v244 offset:2048\nv_mfma_f32_16x16x32_bf16 a[16:19],v[96:99],v[48:51],a[16:19]\nds_read_b128 v[0:3],v128 offset:32768\nv_mfma_f32_16x16x32_bf16 a[48:51],v[100:103],v[48:51],a[48:51]\nbuffer_load_dwordx4 v138,s[24:27],s34 offen offset:2048 lds\nv_mfma_f32_16x16x32_bf16 a[52:55],v[100:103],v[52:55],a[52:55]\nds_read_b128 v[4:7],v128 offset:34816\n"
"v_mfma_f32_16x16x32_bf16 a[20:23],v[96:99],v[52:55],a[20:23]\nds_read_b64_tr_b16 v[156:157],v245 offset:0\nv_mfma_f32_16x16x32_bf16 a[24:27],v[96:99],v[56:59],a[24:27]\nds_read_b128 v[8:11],v128 offset:36864\nv_readlane_b32 s48,v144,s55\ns_add_u32 s55,s55,1\nv_mfma_f32_16x16x32_bf16 a[56:59],v[100:103],v[56:59],a[56:59]\nbuffer_load_dwordx4 v139,s[24:27],s35 offen offset:3072 lds\nv_mfma_f32_16x16x32_bf16 a[60:63],v[100:103],v[60:63],a[60:63]\n"
"ds_read_b128 v[12:15],v128 offset:38912\nv_readlane_b32 s49,v144,s55\ns_add_u32 s55,s55,1\nv_mfma_f32_16x16x32_bf16 a[28:31],v[96:99],v[60:63],a[28:31]\nv_readlane_b32 s50,v144,s55\ns_add_u32 s55,s55,1\nds_read_b64_tr_b16 v[158:159],v245 offset:2048\nv_mfma_f32_16x16x32_bf16 a[92:95],v[104:107],v[60:63],a[92:95]\ns_add_u32 m0,s43,4096\nds_read_b128 v[16:19],v128 offset:40960\nv_readlane_b32 s45,v144,s55\n"
"v_mfma_f32_16x16x32_bf16 a[124:127],v[108:111],v[60:63],a[124:127]\ns_add_u32 s55,s55,1\nbuffer_load_dwordx4 v138,s[24:27],s36 offen lds\nv_mfma_f32_16x16x32_bf16 a[120:123],v[108:111],v[56:59],a[120:123]\nds_read_b128 v[20:23],v128 offset:43008\nv_readlane_b32 s47,v144,s55\ns_add_u32 s55,s55,1\nv_mfma_f32_16x16x32_bf16 a[88:91],v[104:107],v[56:59],a[88:91]\nv_readlane_b32 s53,v144,s55\ns_add_u32 s55,s55,1\n"
"ds_read_b64_tr_b16 v[160:161],v246 offset:0\nv_mfma_f32_16x16x32_bf16 a[84:87],v[104:107],v[52:55],a[84:87]\nds_read_b128 v[24:27],v128 offset:45056\nv_readlane_b32 s54,v144,s55\ns_add_u32 s55,s55,2\nv_mfma_f32_16x16x32_bf16 a[116:119],v[108:111],v[52:55],a[116:119]\nbuffer_load_dwordx4 v139,s[24:27],s37 offen offset:1024 lds\nv_mfma_f32_16x16x32_bf16 a[112:115],v[108:111],v[48:51],a[112:115]\nds_read_b128 v[28:31],v128 offset:47104\n"
"v_mad_u32_u24 v149,v145,s45,v148\nv_mfma_f32_16x16x32_bf16 a[80:83],v[104:107],v[48:51],a[80:83]\ns_lshl_b32 s46,s45,3\nds_read_b64_tr_b16 v[162:163],v246 offset:2048\nv_mfma_f32_16x16x32_bf16 a[76:79],v[104:107],v[44:47],a[76:79]\ns_sub_u32 s46,s46,0x400\nds_read_b64_tr_b16 v[72:73],v132 offset:32768\nv_mfma_f32_16x16x32_bf16 a[108:111],v[108:111],v[44:47],a[108:111]\ns_sub_u32 s47,s47,0x800\nbuffer_load_dwordx4 v138,s[24:27],s38 offen offset:2048 lds\n"
"v_mfma_f32_16x16x32_bf16 a[104:107],v[108:111],v[40:43],a[104:107]\ns_add_u32 s52,s47,s46\nds_read_b64_tr_b16 v[74:75],v132 offset:34816\nv_mfma_f32_16x16x32_bf16 a[72:75],v[104:107],v[40:43],a[72:75]\nds_read_b64_tr_b16 v[164:165],v247 offset:0\nv_mfma_f32_16x16x32_bf16 a[68:71],v[104:107],v[36:39],a[68:71]\nds_read_b64_tr_b16 v[76:77],v133 offset:32768\nv_mfma_f32_16x16x32_bf16 a[100:103],v[108:111],v[36:39],a[100:103]\n"
"buffer_load_dwordx4 v139,s[24:27],s39 offen offset:3072 lds\nv_mfma_f32_16x16x32_bf16 a[96:99],v[108:111],v[32:35],a[96:99]\ns_add_u32 s24,s24,128\nds_read_b64_tr_b16 v[78:79],v133 offset:34816\nv_mfma_f32_16x16x32_bf16 a[64:67],v[104:107],v[32:35],a[64:67]\ns_addc_u32 s25,s25,0\nds_read_b64_tr_b16 v[166:167],v247 offset:2048\nv_mfma_f32_16x16x32_bf16 a[128:131],v[112:115],v[32:35],a[128:131]\ns_add_u32 m0,s15,16384\n"
"ds_read_b64_tr_b16 v[80:81],v134 offset:32768\nv_mfma_f32_16x16x32_bf16 a[160:163],v[116:119],v[32:35],a[160:163]\nbuffer_load_dwordx4 v140,s[28:31],s21 offen lds\nv_mfma_f32_16x16x32_bf16 a[164:167],v[116:119],v[36:39],a[164:167]\nds_read_b64_tr_b16 v[82:83],v134 offset:34816\nv_mfma_f32_16x16x32_bf16 a[132:135],v[112:115],v[36:39],a[132:135]\nv_mfma_f32_16x16x32_bf16 a[136:139],v[112:115],v[40:43],a[136:139]\n"
"ds_read_b64_tr_b16 v[84:85],v135 offset:32768\nv_mfma_f32_16x16x32_bf16 a[168:171],v[116:119],v[40:43],a[168:171]\nbuffer_load_dwordx4 v141,s[28:31],s22 offen offset:1024 lds\nv_mfma_f32_16x16x32_bf16 a[172:175],v[116:119],v[44:47],a[172:175]\nds_read_b64_tr_b16 v[86:87],v135 offset:34816\nv_mfma_f32_16x16x32_bf16 a[140:143],v[112:115],v[44:47],a[140:143]\nv_mfma_f32_16x16x32_bf16 a[144:147],v[112:115],v[48:51],a[144:147]\n"
"s_cmp_eq_u32 s42,2\nds_read_b64_tr_b16 v[88:89],v136 offset:32768\nv_mfma_f32_16x16x32_bf16 a[176:179],v[116:119],v[48:51],a[176:179]\nbuffer_load_dwordx4 v140,s[28:31],s23 offen offset:2048 lds\ns_cbranch_scc1 L_fused_block_pub0_6\nL_fused_block_pub0r_7:\nv_mfma_f32_16x16x32_bf16 a[180:183],v[116:119],v[52:55],a[180:183]\nds_read_b64_tr_b16 v[90:91],v136 offset:34816\nv_mfma_f32_16x16x32_bf16 a[148:151],v[112:115],v[52:55],a[148:151]\n"
"v_mfma_f32_16x16x32_bf16 a[152:155],v[112:115],v[56:59],a[152:155]\nds_read_b64_tr_b16 v[92:93],v137 offset:32768\nv_mfma_f32_16x16x32_bf16 a[184:187],v[116:119],v[56:59],a[184:187]\nbuffer_load_dwordx4 v141,s[28:31],s44 offen offset:3072 lds\nv_mfma_f32_16x16x32_bf16 a[188:191],v[116:119],v[60:63],a[188:191]\ns_add_u32 s28,s28,0x40000\nds_read_b64_tr_b16 v[94:95],v137 offset:34816\nv_mfma_f32_16x16x32_bf16 a[156:159],v[112:115],v[60:63],a[156:159]\n"
"s_addc_u32 s29,s29,0\nv_mfma_f32_16x16x32_bf16 a[220:223],v[120:123],v[60:63],a[220:223]\nv_mfma_f32_16x16x32_bf16 a[252:255],v[124:127],v[60:63],a[252:255]\nv_mfma_f32_16x16x32_bf16 a[248:251],v[124:127],v[56:59],a[248:251]\nv_mfma_f32_16x16x32_bf16 a[216:219],v[120:123],v[56:59],a[216:219]\nv_mfma_f32_16x16x32_bf16 a[212:215],v[120:123],v[52:55],a[212:215]\nv_mfma_f32_16x16x32_bf16 a[244:247],v[124:127],v[52:55],a[244:247]\n"
"v_mfma_f32_16x16x32_bf16 a[240:243],v[124:127],v[48:51],a[240:243]\nv_mfma_f32_16x16x32_bf16 a[208:211],v[120:123],v[48:51],a[208:211]\nv_mfma_f32_16x16x32_bf16 a[204:207],v[120:123],v[44:47],a[204:207]\nv_mfma_f32_16x16x32_bf16 a[236:239],v[124:127],v[44:47],a[236:239]\nv_mfma_f32_16x16x32_bf16 a[232:235],v[124:127],v[40:43],a[232:235]\ns_waitcnt vmcnt(17) lgkmcnt(0)\ns_barrier\nv_mfma_f32_16x16x32_bf16 a[200:203],v[120:123],v[40:43],a[200:203]\n"
"v_mfma_f32_16x16x32_bf16 a[196:199],v[120:123],v[36:39],a[196:199]\nv_mfma_f32_16x16x32_bf16 a[228:231],v[124:127],v[36:39],a[228:231]\nv_mfma_f32_16x16x32_bf16 a[224:227],v[124:127],v[32:35],a[224:227]\nv_mfma_f32_16x16x32_bf16 a[192:195],v[120:123],v[32:35],a[192:195]\ns_sub_u32 s42,s42,1\ns_cmp_lg_u32 s42,0\ns_cbranch_scc1 L_fused_block_loop_5\nv_mfma_f32_16x16x32_bf16 a[0:3],v[64:67],v[0:3],a[0:3]\n"
"s_add_u32 m0,s15,32768\nds_read_b64_tr_b16 v[96:97],v130 offset:49152\nv_mfma_f32_16x16x32_bf16 a[32:35],v[68:71],v[0:3],a[32:35]\nbuffer_load_dwordx4 v140,s[28:31],s21 offen lds\nv_mfma_f32_16x16x32_bf16 a[36:39],v[68:71],v[4:7],a[36:39]\nds_read_b64_tr_b16 v[98:99],v130 offset:51200\nv_mfma_f32_16x16x32_bf16 a[4:7],v[64:67],v[4:7],a[4:7]\nv_lshlrev_b32 v226,7,v253\nv_mfma_f32_16x16x32_bf16 a[8:11],v[64:67],v[8:11],a[8:11]\n"
"ds_read_b64_tr_b16 v[100:101],v131 offset:49152\nv_mfma_f32_16x16x32_bf16 a[40:43],v[68:71],v[8:11],a[40:43]\nbuffer_load_dwordx4 v141,s[28:31],s22 offen offset:1024 lds\nv_mfma_f32_16x16x32_bf16 a[44:47],v[68:71],v[12:15],a[44:47]\nds_read_b64_tr_b16 v[102:103],v131 offset:51200\nv_mfma_f32_16x16x32_bf16 a[12:15],v[64:67],v[12:15],a[12:15]\nv_mfma_f32_16x16x32_bf16 a[16:19],v[64:67],v[16:19],a[16:19]\n"
"ds_read_b128 v[32:35],v129 offset:32768\nv_mfma_f32_16x16x32_bf16 a[48:51],v[68:71],v[16:19],a[48:51]\nbuffer_load_dwordx4 v140,s[28:31],s23 offen offset:2048 lds\nv_mfma_f32_16x16x32_bf16 a[52:55],v[68:71],v[20:23],a[52:55]\nds_read_b128 v[36:39],v129 offset:34816\nv_mfma_f32_16x16x32_bf16 a[20:23],v[64:67],v[20:23],a[20:23]\nv_mfma_f32_16x16x32_bf16 a[24:27],v[64:67],v[24:27],a[24:27]\nds_read_b128 v[40:43],v129 offset:36864\n"
"v_mfma_f32_16x16x32_bf16 a[56:59],v[68:71],v[24:27],a[56:59]\nbuffer_load_dwordx4 v141,s[28:31],s44 offen offset:3072 lds\nv_mfma_f32_16x16x32_bf16 a[60:63],v[68:71],v[28:31],a[60:63]\ns_add_u32 s28,s28,0x40000\nds_read_b128 v[44:47],v129 offset:38912\nv_mfma_f32_16x16x32_bf16 a[28:31],v[64:67],v[28:31],a[28:31]\ns_addc_u32 s29,s29,0\nv_mfma_f32_16x16x32_bf16 a[92:95],v[72:75],v[28:31],a[92:95]\nds_read_b128 v[48:51],v129 offset:40960\n"
"v_mfma_f32_16x16x32_bf16 a[124:127],v[76:79],v[28:31],a[124:127]\nv_mfma_f32_16x16x32_bf16 a[120:123],v[76:79],v[24:27],a[120:123]\nds_read_b128 v[52:55],v129 offset:43008\nv_mfma_f32_16x16x32_bf16 a[88:91],v[72:75],v[24:27],a[88:91]\nv_mfma_f32_16x16x32_bf16 a[84:87],v[72:75],v[20:23],a[84:87]\nds_read_b128 v[56:59],v129 offset:45056\nv_mfma_f32_16x16x32_bf16 a[116:119],v[76:79],v[20:23],a[116:119]\n"
"v_mfma_f32_16x16x32_bf16 a[112:115],v[76:79],v[16:19],a[112:115]\nds_read_b128 v[60:63],v129 offset:47104\nv_mfma_f32_16x16x32_bf16 a[80:83],v[72:75],v[16:19],a[80:83]\nv_mfma_f32_16x16x32_bf16 a[76:79],v[72:75],v[12:15],a[76:79]\nds_read_b64_tr_b16 v[104:105],v132 offset:49152\nv_mfma_f32_16x16x32_bf16 a[108:111],v[76:79],v[12:15],a[108:111]\nv_mfma_f32_16x16x32_bf16 a[104:107],v[76:79],v[8:11],a[104:107]\n"
"ds_read_b64_tr_b16 v[106:107],v132 offset:51200\nv_mfma_f32_16x16x32_bf16 a[72:75],v[72:75],v[8:11],a[72:75]\nv_mfma_f32_16x16x32_bf16 a[68:71],v[72:75],v[4:7],a[68:71]\nds_read_b64_tr_b16 v[108:109],v133 offset:49152\nv_mfma_f32_16x16x32_bf16 a[100:103],v[76:79],v[4:7],a[100:103]\nv_mfma_f32_16x16x32_bf16 a[96:99],v[76:79],v[0:3],a[96:99]\nds_read_b64_tr_b16 v[110:111],v133 offset:51200\nv_mfma_f32_16x16x32_bf16 a[64:67],v[72:75],v[0:3],a[64:67]\n"
"v_mfma_f32_16x16x32_bf16 a[128:131],v[80:83],v[0:3],a[128:131]\nds_read_b64_tr_b16 v[112:113],v134 offset:49152\nv_mfma_f32_16x16x32_bf16 a[160:163],v[84:87],v[0:3],a[160:163]\nv_mfma_f32_16x16x32_bf16 a[164:167],v[84:87],v[4:7],a[164:167]\nds_read_b64_tr_b16 v[114:115],v134 offset:51200\nv_mfma_f32_16x16x32_bf16 a[132:135],v[80:83],v[4:7],a[132:135]\nv_mfma_f32_16x16x32_bf16 a[136:139],v[80:83],v[8:11],a[136:139]\n"
"ds_read_b64_tr_b16 v[116:117],v135 offset:49152\nv_mfma_f32_16x16x32_bf16 a[168:171],v[84:87],v[8:11],a[168:171]\nv_mfma_f32_16x16x32_bf16 a[172:175],v[84:87],v[12:15],a[172:175]\nds_read_b64_tr_b16 v[118:119],v135 offset:51200\nv_mfma_f32_16x16x32_bf16 a[140:143],v[80:83],v[12:15],a[140:143]\nv_mfma_f32_16x16x32_bf16 a[144:147],v[80:83],v[16:19],a[144:147]\nds_read_b64_tr_b16 v[120:121],v136 offset:49152\n"
"v_mfma_f32_16x16x32_bf16 a[176:179],v[84:87],v[16:19],a[176:179]\nv_mfma_f32_16x16x32_bf16 a[180:183],v[84:87],v[20:23],a[180:183]\nds_read_b64_tr_b16 v[122:123],v136 offset:51200\nv_mfma_f32_16x16x32_bf16 a[148:151],v[80:83],v[20:23],a[148:151]\nv_mfma_f32_16x16x32_bf16 a[152:155],v[80:83],v[24:27],a[152:155]\nds_read_b64_tr_b16 v[124:125],v137 offset:49152\nv_mfma_f32_16x16x32_bf16 a[184:187],v[84:87],v[24:27],a[184:187]\n"
"v_mfma_f32_16x16x32_bf16 a[188:191],v[84:87],v[28:31],a[188:191]\nds_read_b64_tr_b16 v[126:127],v137 offset:51200\nv_mfma_f32_16x16x32_bf16 a[156:159],v[80:83],v[28:31],a[156:159]\nv_mfma_f32_16x16x32_bf16 a[220:223],v[88:91],v[28:31],a[220:223]\nv_mfma_f32_16x16x32_bf16 a[252:255],v[92:95],v[28:31],a[252:255]\nv_mfma_f32_16x16x32_bf16 a[248:251],v[92:95],v[24:27],a[248:251]\nv_mfma_f32_16x16x32_bf16 a[216:219],v[88:91],v[24:27],a[216:219]\n"
"v_mfma_f32_16x16x32_bf16 a[212:215],v[88:91],v[20:23],a[212:215]\nv_mfma_f32_16x16x32_bf16 a[244:247],v[92:95],v[20:23],a[244:247]\nv_mfma_f32_16x16x32_bf16 a[240:243],v[92:95],v[16:19],a[240:243]\nv_mfma_f32_16x16x32_bf16 a[208:211],v[88:91],v[16:19],a[208:211]\nv_mfma_f32_16x16x32_bf16 a[204:207],v[88:91],v[12:15],a[204:207]\nv_mfma_f32_16x16x32_bf16 a[236:239],v[92:95],v[12:15],a[236:239]\nv_mfma_f32_16x16x32_bf16 a[232:235],v[92:95],v[8:11],a[232:235]\n"
"s_waitcnt vmcnt(8) lgkmcnt(0)\ns_barrier\nv_mfma_f32_16x16x32_bf16 a[200:203],v[88:91],v[8:11],a[200:203]\nv_mfma_f32_16x16x32_bf16 a[196:199],v[88:91],v[4:7],a[196:199]\nv_mfma_f32_16x16x32_bf16 a[228:231],v[92:95],v[4:7],a[228:231]\nv_mfma_f32_16x16x32_bf16 a[224:227],v[92:95],v[0:3],a[224:227]\nv_mfma_f32_16x16x32_bf16 a[192:195],v[88:91],v[0:3],a[192:195]\nv_mfma_f32_16x16x32_bf16 a[0:3],v[96:99],v[32:35],a[0:3]\n"
"s_add_u32 m0,s43,32768\nds_read_b64_tr_b16 v[64:65],v130 offset:0\nv_mfma_f32_16x16x32_bf16 a[32:35],v[100:103],v[32:35],a[32:35]\nbuffer_load_dwordx4 v138,s[24:27],s32 offen lds\nv_mfma_f32_16x16x32_bf16 a[36:39],v[100:103],v[36:39],a[36:39]\nds_read_b64_tr_b16 v[66:67],v130 offset:2048\nv_mfma_f32_16x16x32_bf16 a[4:7],v[96:99],v[36:39],a[4:7]\nv_mfma_f32_16x16x32_bf16 a[8:11],v[96:99],v[40:43],a[8:11]\n"
"ds_read_b64_tr_b16 v[68:69],v131 offset:0\nv_mfma_f32_16x16x32_bf16 a[40:43],v[100:103],v[40:43],a[40:43]\nbuffer_load_dwordx4 v139,s[24:27],s33 offen offset:1024 lds\nv_mfma_f32_16x16x32_bf16 a[44:47],v[100:103],v[44:47],a[44:47]\nds_read_b64_tr_b16 v[70:71],v131 offset:2048\nv_mfma_f32_16x16x32_bf16 a[12:15],v[96:99],v[44:47],a[12:15]\nv_mfma_f32_16x16x32_bf16 a[16:19],v[96:99],v[48:51],a[16:19]\n"
"ds_read_b128 v[0:3],v128 offset:0\nv_mfma_f32_16x16x32_bf16 a[48:51],v[100:103],v[48:51],a[48:51]\nbuffer_load_dwordx4 v138,s[24:27],s34 offen offset:2048 lds\nv_mfma_f32_16x16x32_bf16 a[52:55],v[100:103],v[52:55],a[52:55]\nds_read_b128 v[4:7],v128 offset:2048\nv_mfma_f32_16x16x32_bf16 a[20:23],v[96:99],v[52:55],a[20:23]\nv_mfma_f32_16x16x32_bf16 a[24:27],v[96:99],v[56:59],a[24:27]\nds_read_b128 v[8:11],v128 offset:4096\n"
"v_mfma_f32_16x16x32_bf16 a[56:59],v[100:103],v[56:59],a[56:59]\nbuffer_load_dwordx4 v139,s[24:27],s35 offen offset:3072 lds\nv_mfma_f32_16x16x32_bf16 a[60:63],v[100:103],v[60:63],a[60:63]\nds_read_b128 v[12:15],v128 offset:6144\nv_mfma_f32_16x16x32_bf16 a[28:31],v[96:99],v[60:63],a[28:31]\nv_mfma_f32_16x16x32_bf16 a[92:95],v[104:107],v[60:63],a[92:95]\ns_add_u32 m0,s43,36864\nds_read_b128 v[16:19],v128 offset:8192\n"
"v_mfma_f32_16x16x32_bf16 a[124:127],v[108:111],v[60:63],a[124:127]\nbuffer_load_dwordx4 v138,s[24:27],s36 offen lds\nv_mfma_f32_16x16x32_bf16 a[120:123],v[108:111],v[56:59],a[120:123]\nds_read_b128 v[20:23],v128 offset:10240\nv_mfma_f32_16x16x32_bf16 a[88:91],v[104:107],v[56:59],a[88:91]\nv_mfma_f32_16x16x32_bf16 a[84:87],v[104:107],v[52:55],a[84:87]\nds_read_b128 v[24:27],v128 offset:12288\nv_mfma_f32_16x16x32_bf16 a[116:119],v[108:111],v[52:55],a[116:119]\n"
"buffer_load_dwordx4 v139,s[24:27],s37 offen offset:1024 lds\nv_mfma_f32_16x16x32_bf16 a[112:115],v[108:111],v[48:51],a[112:115]\nds_read_b128 v[28:31],v128 offset:14336\nv_mfma_f32_16x16x32_bf16 a[80:83],v[104:107],v[48:51],a[80:83]\nv_mfma_f32_16x16x32_bf16 a[76:79],v[104:107],v[44:47],a[76:79]\nds_read_b64_tr_b16 v[72:73],v132 offset:0\nv_mfma_f32_16x16x32_bf16 a[108:111],v[108:111],v[44:47],a[108:111]\n"
"buffer_load_dwordx4 v138,s[24:27],s38 offen offset:2048 lds\nv_mfma_f32_16x16x32_bf16 a[104:107],v[108:111],v[40:43],a[104:107]\nds_read_b64_tr_b16 v[74:75],v132 offset:2048\nv_mfma_f32_16x16x32_bf16 a[72:75],v[104:107],v[40:43],a[72:75]\nv_mfma_f32_16x16x32_bf16 a[68:71],v[104:107],v[36:39],a[68:71]\nds_read_b64_tr_b16 v[76:77],v133 offset:0\nv_mfma_f32_16x16x32_bf16 a[100:103],v[108:111],v[36:39],a[100:103]\n"
"buffer_load_dwordx4 v139,s[24:27],s39 offen offset:3072 lds\nv_mfma_f32_16x16x32_bf16 a[96:99],v[108:111],v[32:35],a[96:99]\ns_add_u32 s24,s24,128\nds_read_b64_tr_b16 v[78:79],v133 offset:2048\nv_mfma_f32_16x16x32_bf16 a[64:67],v[104:107],v[32:35],a[64:67]\ns_addc_u32 s25,s25,0\nv_mfma_f32_16x16x32_bf16 a[128:131],v[112:115],v[32:35],a[128:131]\ns_add_u32 m0,s15,49152\nds_read_b64_tr_b16 v[80:81],v134 offset:0\n"
"v_mfma_f32_16x16x32_bf16 a[160:163],v[116:119],v[32:35],a[160:163]\nbuffer_load_dwordx4 v140,s[28:31],s21 offen lds\nv_mfma_f32_16x16x32_bf16 a[164:167],v[116:119],v[36:39],a[164:167]\nds_read_b64_tr_b16 v[82:83],v134 offset:2048\nv_mfma_f32_16x16x32_bf16 a[132:135],v[112:115],v[36:39],a[132:135]\nv_mfma_f32_16x16x32_bf16 a[136:139],v[112:115],v[40:43],a[136:139]\nds_read_b64_tr_b16 v[84:85],v135 offset:0\n"
"v_mfma_f32_16x16x32_bf16 a[168:171],v[116:119],v[40:43],a[168:171]\nbuffer_load_dwordx4 v141,s[28:31],s22 offen offset:1024 lds\nv_mfma_f32_16x16x32_bf16 a[172:175],v[116:119],v[44:47],a[172:175]\nds_read_b64_tr_b16 v[86:87],v135 offset:2048\nv_mfma_f32_16x16x32_bf16 a[140:143],v[112:115],v[44:47],a[140:143]\nv_mfma_f32_16x16x32_bf16 a[144:147],v[112:115],v[48:51],a[144:147]\nds_read_b64_tr_b16 v[88:89],v136 offset:0\n"
"v_mfma_f32_16x16x32_bf16 a[176:179],v[116:119],v[48:51],a[176:179]\nbuffer_load_dwordx4 v140,s[28:31],s23 offen offset:2048 lds\nv_mfma_f32_16x16x32_bf16 a[180:183],v[116:119],v[52:55],a[180:183]\nds_read_b64_tr_b16 v[90:91],v136 offset:2048\nv_mfma_f32_16x16x32_bf16 a[148:151],v[112:115],v[52:55],a[148:151]\nv_mfma_f32_16x16x32_bf16 a[152:155],v[112:115],v[56:59],a[152:155]\nds_read_b64_tr_b16 v[92:93],v137 offset:0\n"
"v_mfma_f32_16x16x32_bf16 a[184:187],v[116:119],v[56:59],a[184:187]\nbuffer_load_dwordx4 v141,s[28:31],s44 offen offset:3072 lds\nv_mfma_f32_16x16x32_bf16 a[188:191],v[116:119],v[60:63],a[188:191]\ns_add_u32 s28,s28,0x40000\nds_read_b64_tr_b16 v[94:95],v137 offset:2048\nv_mfma_f32_16x16x32_bf16 a[156:159],v[112:115],v[60:63],a[156:159]\ns_addc_u32 s29,s29,0\nv_mfma_f32_16x16x32_bf16 a[220:223],v[120:123],v[60:63],a[220:223]\n"
"v_mfma_f32_16x16x32_bf16 a[252:255],v[124:127],v[60:63],a[252:255]\nbuffer_load_dwordx2 v[224:225],v226,s[8:11],s75 offen sc1\nv_mfma_f32_16x16x32_bf16 a[248:251],v[124:127],v[56:59],a[248:251]\nv_mfma_f32_16x16x32_bf16 a[216:219],v[120:123],v[56:59],a[216:219]\nv_mfma_f32_16x16x32_bf16 a[212:215],v[120:123],v[52:55],a[212:215]\nv_mfma_f32_16x16x32_bf16 a[244:247],v[124:127],v[52:55],a[244:247]\nv_mfma_f32_16x16x32_bf16 a[240:243],v[124:127],v[48:51],a[240:243]\n"
"v_mfma_f32_16x16x32_bf16 a[208:211],v[120:123],v[48:51],a[208:211]\nv_mfma_f32_16x16x32_bf16 a[204:207],v[120:123],v[44:47],a[204:207]\nv_mfma_f32_16x16x32_bf16 a[236:239],v[124:127],v[44:47],a[236:239]\nv_mfma_f32_16x16x32_bf16 a[232:235],v[124:127],v[40:43],a[232:235]\ns_waitcnt vmcnt(17) lgkmcnt(0)\ns_barrier\nv_mfma_f32_16x16x32_bf16 a[200:203],v[120:123],v[40:43],a[200:203]\nv_mfma_f32_16x16x32_bf16 a[196:199],v[120:123],v[36:39],a[196:199]\n"
"v_mfma_f32_16x16x32_bf16 a[228:231],v[124:127],v[36:39],a[228:231]\nv_mfma_f32_16x16x32_bf16 a[224:227],v[124:127],v[32:35],a[224:227]\nv_mfma_f32_16x16x32_bf16 a[192:195],v[120:123],v[32:35],a[192:195]\nv_mfma_f32_16x16x32_bf16 a[0:3],v[64:67],v[0:3],a[0:3]\nds_read_b64_tr_b16 v[96:97],v130 offset:16384\nv_mfma_f32_16x16x32_bf16 a[32:35],v[68:71],v[0:3],a[32:35]\nv_mfma_f32_16x16x32_bf16 a[36:39],v[68:71],v[4:7],a[36:39]\n"
"ds_read_b64_tr_b16 v[98:99],v130 offset:18432\nv_mfma_f32_16x16x32_bf16 a[4:7],v[64:67],v[4:7],a[4:7]\nv_mfma_f32_16x16x32_bf16 a[8:11],v[64:67],v[8:11],a[8:11]\nds_read_b64_tr_b16 v[100:101],v131 offset:16384\nv_mfma_f32_16x16x32_bf16 a[40:43],v[68:71],v[8:11],a[40:43]\nv_mfma_f32_16x16x32_bf16 a[44:47],v[68:71],v[12:15],a[44:47]\nds_read_b64_tr_b16 v[102:103],v131 offset:18432\nv_mfma_f32_16x16x32_bf16 a[12:15],v[64:67],v[12:15],a[12:15]\n"
"v_mfma_f32_16x16x32_bf16 a[16:19],v[64:67],v[16:19],a[16:19]\nds_read_b128 v[32:35],v129 offset:0\nv_mfma_f32_16x16x32_bf16 a[48:51],v[68:71],v[16:19],a[48:51]\nv_mfma_f32_16x16x32_bf16 a[52:55],v[68:71],v[20:23],a[52:55]\nds_read_b128 v[36:39],v129 offset:2048\nv_mfma_f32_16x16x32_bf16 a[20:23],v[64:67],v[20:23],a[20:23]\nv_mfma_f32_16x16x32_bf16 a[24:27],v[64:67],v[24:27],a[24:27]\nds_read_b128 v[40:43],v129 offset:4096\n"
"v_mfma_f32_16x16x32_bf16 a[56:59],v[68:71],v[24:27],a[56:59]\nv_mfma_f32_16x16x32_bf16 a[60:63],v[68:71],v[28:31],a[60:63]\nds_read_b128 v[44:47],v129 offset:6144\nv_mfma_f32_16x16x32_bf16 a[28:31],v[64:67],v[28:31],a[28:31]\nv_mfma_f32_16x16x32_bf16 a[92:95],v[72:75],v[28:31],a[92:95]\nds_read_b128 v[48:51],v129 offset:8192\nv_mfma_f32_16x16x32_bf16 a[124:127],v[76:79],v[28:31],a[124:127]\nv_mfma_f32_16x16x32_bf16 a[120:123],v[76:79],v[24:27],a[120:123]\n"
"ds_read_b128 v[52:55],v129 offset:10240\nv_mfma_f32_16x16x32_bf16 a[88:91],v[72:75],v[24:27],a[88:91]\nv_mfma_f32_16x16x32_bf16 a[84:87],v[72:75],v[20:23],a[84:87]\nds_read_b128 v[56:59],v129 offset:12288\nv_mfma_f32_16x16x32_bf16 a[116:119],v[76:79],v[20:23],a[116:119]\nv_mfma_f32_16x16x32_bf16 a[112:115],v[76:79],v[16:19],a[112:115]\nds_read_b128 v[60:63],v129 offset:14336\nv_mfma_f32_16x16x32_bf16 a[80:83],v[72:75],v[16:19],a[80:83]\n"
"v_mfma_f32_16x16x32_bf16 a[76:79],v[72:75],v[12:15],a[76:79]\nds_read_b64_tr_b16 v[104:105],v132 offset:16384\nv_mfma_f32_16x16x32_bf16 a[108:111],v[76:79],v[12:15],a[108:111]\nv_mfma_f32_16x16x32_bf16 a[104:107],v[76:79],v[8:11],a[104:107]\nds_read_b64_tr_b16 v[106:107],v132 offset:18432\nv_mfma_f32_16x16x32_bf16 a[72:75],v[72:75],v[8:11],a[72:75]\nv_mfma_f32_16x16x32_bf16 a[68:71],v[72:75],v[4:7],a[68:71]\n"
"ds_read_b64_tr_b16 v[108:109],v133 offset:16384\nv_mfma_f32_16x16x32_bf16 a[100:103],v[76:79],v[4:7],a[100:103]\nv_mfma_f32_16x16x32_bf16 a[96:99],v[76:79],v[0:3],a[96:99]\nds_read_b64_tr_b16 v[110:111],v133 offset:18432\nv_mfma_f32_16x16x32_bf16 a[64:67],v[72:75],v[0:3],a[64:67]\nv_mfma_f32_16x16x32_bf16 a[128:131],v[80:83],v[0:3],a[128:131]\nds_read_b64_tr_b16 v[112:113],v134 offset:16384\nv_mfma_f32_16x16x32_bf16 a[160:163],v[84:87],v[0:3],a[160:163]\n"
"v_mfma_f32_16x16x32_bf16 a[164:167],v[84:87],v[4:7],a[164:167]\nds_read_b64_tr_b16 v[114:115],v134 offset:18432\nv_mfma_f32_16x16x32_bf16 a[132:135],v[80:83],v[4:7],a[132:135]\nv_mfma_f32_16x16x32_bf16 a[136:139],v[80:83],v[8:11],a[136:139]\nds_read_b64_tr_b16 v[116:117],v135 offset:16384\nv_mfma_f32_16x16x32_bf16 a[168:171],v[84:87],v[8:11],a[168:171]\nv_mfma_f32_16x16x32_bf16 a[172:175],v[84:87],v[12:15],a[172:175]\n"
"ds_read_b64_tr_b16 v[118:119],v135 offset:18432\nv_mfma_f32_16x16x32_bf16 a[140:143],v[80:83],v[12:15],a[140:143]\nv_mfma_f32_16x16x32_bf16 a[144:147],v[80:83],v[16:19],a[144:147]\nds_read_b64_tr_b16 v[120:121],v136 offset:16384\nv_mfma_f32_16x16x32_bf16 a[176:179],v[84:87],v[16:19],a[176:179]\nv_mfma_f32_16x16x32_bf16 a[180:183],v[84:87],v[20:23],a[180:183]\nds_read_b64_tr_b16 v[122:123],v136 offset:18432\n"
"v_mfma_f32_16x16x32_bf16 a[148:151],v[80:83],v[20:23],a[148:151]\nv_mfma_f32_16x16x32_bf16 a[152:155],v[80:83],v[24:27],a[152:155]\nds_read_b64_tr_b16 v[124:125],v137 offset:16384\nv_mfma_f32_16x16x32_bf16 a[184:187],v[84:87],v[24:27],a[184:187]\nv_mfma_f32_16x16x32_bf16 a[188:191],v[84:87],v[28:31],a[188:191]\nds_read_b64_tr_b16 v[126:127],v137 offset:18432\nv_mfma_f32_16x16x32_bf16 a[156:159],v[80:83],v[28:31],a[156:159]\n"
"v_mfma_f32_16x16x32_bf16 a[220:223],v[88:91],v[28:31],a[220:223]\nv_mfma_f32_16x16x32_bf16 a[252:255],v[92:95],v[28:31],a[252:255]\nv_mfma_f32_16x16x32_bf16 a[248:251],v[92:95],v[24:27],a[248:251]\nv_mfma_f32_16x16x32_bf16 a[216:219],v[88:91],v[24:27],a[216:219]\nv_mfma_f32_16x16x32_bf16 a[212:215],v[88:91],v[20:23],a[212:215]\nv_mfma_f32_16x16x32_bf16 a[244:247],v[92:95],v[20:23],a[244:247]\ns_waitcnt vmcnt(0)\n"
"v_cmp_eq_u64 vcc,s[12:13],v[224:225]\nv_mfma_f32_16x16x32_bf16 a[240:243],v[92:95],v[16:19],a[240:243]\nv_mfma_f32_16x16x32_bf16 a[208:211],v[88:91],v[16:19],a[208:211]\ns_cmp_eq_u64 vcc,exec\nv_mfma_f32_16x16x32_bf16 a[204:207],v[88:91],v[12:15],a[204:207]\ns_cbranch_scc0 L_fused_block_slow0_8\nL_fused_block_slow0r_9:\nv_mfma_f32_16x16x32_bf16 a[236:239],v[92:95],v[12:15],a[236:239]\nv_mfma_f32_16x16x32_bf16 a[232:235],v[92:95],v[8:11],a[232:235]\n"
"s_waitcnt lgkmcnt(0)\ns_barrier\nv_mfma_f32_16x16x32_bf16 a[200:203],v[88:91],v[8:11],a[200:203]\nv_mfma_f32_16x16x32_bf16 a[196:199],v[88:91],v[4:7],a[196:199]\nv_mfma_f32_16x16x32_bf16 a[228:231],v[92:95],v[4:7],a[228:231]\nv_mfma_f32_16x16x32_bf16 a[224:227],v[92:95],v[0:3],a[224:227]\nv_mfma_f32_16x16x32_bf16 a[192:195],v[88:91],v[0:3],a[192:195]\nv_mfma_f32_16x16x32_bf16 a[0:3],v[96:99],v[32:35],a[0:3]\n"
"s_add_u32 m0,s15,0\nds_read_b64_tr_b16 v[64:65],v130 offset:32768\nv_mfma_f32_16x16x32_bf16 a[32:35],v[100:103],v[32:35],a[32:35]\nbuffer_load_dwordx4 v252,s[8:11],s66 offen sc1 lds\nv_mfma_f32_16x16x32_bf16 a[36:39],v[100:103],v[36:39],a[36:39]\nds_read_b64_tr_b16 v[66:67],v130 offset:34816\nv_mfma_f32_16x16x32_bf16 a[4:7],v[96:99],v[36:39],a[4:7]\nv_mfma_f32_16x16x32_bf16 a[8:11],v[96:99],v[40:43],a[8:11]\n"
"ds_read_b64_tr_b16 v[68:69],v131 offset:32768\nv_mfma_f32_16x16x32_bf16 a[40:43],v[100:103],v[40:43],a[40:43]\nbuffer_load_dwordx4 v252,s[8:11],s66 offen offset:1024 sc1 lds\nv_mfma_f32_16x16x32_bf16 a[44:47],v[100:103],v[44:47],a[44:47]\nds_read_b64_tr_b16 v[70:71],v131 offset:34816\nv_mfma_f32_16x16x32_bf16 a[12:15],v[96:99],v[44:47],a[12:15]\nv_mfma_f32_16x16x32_bf16 a[16:19],v[96:99],v[48:51],a[16:19]\n"
"ds_read_b128 v[0:3],v128 offset:32768\nv_mfma_f32_16x16x32_bf16 a[48:51],v[100:103],v[48:51],a[48:51]\nbuffer_load_dwordx4 v252,s[8:11],s66 offen offset:2048 sc1 lds\nv_mfma_f32_16x16x32_bf16 a[52:55],v[100:103],v[52:55],a[52:55]\nds_read_b128 v[4:7],v128 offset:34816\nv_mfma_f32_16x16x32_bf16 a[20:23],v[96:99],v[52:55],a[20:23]\nv_mfma_f32_16x16x32_bf16 a[24:27],v[96:99],v[56:59],a[24:27]\nds_read_b128 v[8:11],v128 offset:36864\n"
"v_mfma_f32_16x16x32_bf16 a[56:59],v[100:103],v[56:59],a[56:59]\nbuffer_load_dwordx4 v252,s[8:11],s66 offen offset:3072 sc1 lds\nv_mfma_f32_16x16x32_bf16 a[60:63],v[100:103],v[60:63],a[60:63]\nds_read_b128 v[12:15],v128 offset:38912\nv_mfma_f32_16x16x32_bf16 a[28:31],v[96:99],v[60:63],a[28:31]\nv_mfma_f32_16x16x32_bf16 a[92:95],v[104:107],v[60:63],a[92:95]\ns_add_u32 s66,s66,0x4000\nds_read_b128 v[16:19],v128 offset:40960\n"
"s_add_u32 m0,s15,16384\nv_mfma_f32_16x16x32_bf16 a[124:127],v[108:111],v[60:63],a[124:127]\nbuffer_load_dwordx4 v252,s[8:11],s66 offen sc1 lds\nv_mfma_f32_16x16x32_bf16 a[120:123],v[108:111],v[56:59],a[120:123]\nds_read_b128 v[20:23],v128 offset:43008\nv_mfma_f32_16x16x32_bf16 a[88:91],v[104:107],v[56:59],a[88:91]\nv_mfma_f32_16x16x32_bf16 a[84:87],v[104:107],v[52:55],a[84:87]\nds_read_b128 v[24:27],v128 offset:45056\n"
"v_mfma_f32_16x16x32_bf16 a[116:119],v[108:111],v[52:55],a[116:119]\nbuffer_load_dwordx4 v252,s[8:11],s66 offen offset:1024 sc1 lds\nv_mfma_f32_16x16x32_bf16 a[112:115],v[108:111],v[48:51],a[112:115]\nds_read_b128 v[28:31],v128 offset:47104\nv_mfma_f32_16x16x32_bf16 a[80:83],v[104:107],v[48:51],a[80:83]\nv_mfma_f32_16x16x32_bf16 a[76:79],v[104:107],v[44:47],a[76:79]\nds_read_b64_tr_b16 v[72:73],v132 offset:32768\n"
"v_mfma_f32_16x16x32_bf16 a[108:111],v[108:111],v[44:47],a[108:111]\nbuffer_load_dwordx4 v252,s[8:11],s66 offen offset:2048 sc1 lds\nv_mfma_f32_16x16x32_bf16 a[104:107],v[108:111],v[40:43],a[104:107]\nds_read_b64_tr_b16 v[74:75],v132 offset:34816\nv_mfma_f32_16x16x32_bf16 a[72:75],v[104:107],v[40:43],a[72:75]\nv_mfma_f32_16x16x32_bf16 a[68:71],v[104:107],v[36:39],a[68:71]\nds_read_b64_tr_b16 v[76:77],v133 offset:32768\n"
"v_mfma_f32_16x16x32_bf16 a[100:103],v[108:111],v[36:39],a[100:103]\nbuffer_load_dwordx4 v252,s[8:11],s66 offen offset:3072 sc1 lds\nv_mfma_f32_16x16x32_bf16 a[96:99],v[108:111],v[32:35],a[96:99]\nds_read_b64_tr_b16 v[78:79],v133 offset:34816\nv_mfma_f32_16x16x32_bf16 a[64:67],v[104:107],v[32:35],a[64:67]\nv_mfma_f32_16x16x32_bf16 a[128:131],v[112:115],v[32:35],a[128:131]\ns_add_u32 s66,s66,0x4000\n"
"ds_read_b64_tr_b16 v[80:81],v134 offset:32768\ns_add_u32 m0,s15,65536\nv_mfma_f32_16x16x32_bf16 a[160:163],v[116:119],v[32:35],a[160:163]\nbuffer_load_dwordx4 v252,s[8:11],s66 offen sc1 lds\nv_mfma_f32_16x16x32_bf16 a[164:167],v[116:119],v[36:39],a[164:167]\nds_read_b64_tr_b16 v[82:83],v134 offset:34816\nv_mfma_f32_16x16x32_bf16 a[132:135],v[112:115],v[36:39],a[132:135]\nv_mfma_f32_16x16x32_bf16 a[136:139],v[112:115],v[40:43],a[136:139]\n"
"ds_read_b64_tr_b16 v[84:85],v135 offset:32768\nv_mfma_f32_16x16x32_bf16 a[168:171],v[116:119],v[40:43],a[168:171]\nbuffer_load_dwordx4 v252,s[8:11],s66 offen offset:1024 sc1 lds\nv_mfma_f32_16x16x32_bf16 a[172:175],v[116:119],v[44:47],a[172:175]\nds_read_b64_tr_b16 v[86:87],v135 offset:34816\nv_mfma_f32_16x16x32_bf16 a[140:143],v[112:115],v[44:47],a[140:143]\nv_mfma_f32_16x16x32_bf16 a[144:147],v[112:115],v[48:51],a[144:147]\n"
"ds_read_b64_tr_b16 v[88:89],v136 offset:32768\nv_mfma_f32_16x16x32_bf16 a[176:179],v[116:119],v[48:51],a[176:179]\nbuffer_load_dwordx4 v252,s[8:11],s66 offen offset:2048 sc1 lds\nv_mfma_f32_16x16x32_bf16 a[180:183],v[116:119],v[52:55],a[180:183]\nds_read_b64_tr_b16 v[90:91],v136 offset:34816\nv_mfma_f32_16x16x32_bf16 a[148:151],v[112:115],v[52:55],a[148:151]\nv_mfma_f32_16x16x32_bf16 a[152:155],v[112:115],v[56:59],a[152:155]\n"
"ds_read_b64_tr_b16 v[92:93],v137 offset:32768\nv_mfma_f32_16x16x32_bf16 a[184:187],v[116:119],v[56:59],a[184:187]\nbuffer_load_dwordx4 v252,s[8:11],s66 offen offset:3072 sc1 lds\nv_mfma_f32_16x16x32_bf16 a[188:191],v[116:119],v[60:63],a[188:191]\nds_read_b64_tr_b16 v[94:95],v137 offset:34816\nv_mfma_f32_16x16x32_bf16 a[156:159],v[112:115],v[60:63],a[156:159]\nv_mfma_f32_16x16x32_bf16 a[220:223],v[120:123],v[60:63],a[220:223]\n"
"s_add_u32 s66,s66,0x4000\ns_add_u32 m0,s15,81920\nv_mfma_f32_16x16x32_bf16 a[252:255],v[124:127],v[60:63],a[252:255]\nbuffer_load_dwordx4 v252,s[8:11],s66 offen sc1 lds\nv_mfma_f32_16x16x32_bf16 a[248:251],v[124:127],v[56:59],a[248:251]\nv_mfma_f32_16x16x32_bf16 a[216:219],v[120:123],v[56:59],a[216:219]\nv_mfma_f32_16x16x32_bf16 a[212:215],v[120:123],v[52:55],a[212:215]\nbuffer_load_dwordx4 v252,s[8:11],s66 offen offset:1024 sc1 lds\n"
"v_mfma_f32_16x16x32_bf16 a[244:247],v[124:127],v[52:55],a[244:247]\nv_mfma_f32_16x16x32_bf16 a[240:243],v[124:127],v[48:51],a[240:243]\nv_mfma_f32_16x16x32_bf16 a[208:211],v[120:123],v[48:51],a[208:211]\nbuffer_load_dwordx4 v252,s[8:11],s66 offen offset:2048 sc1 lds\nv_mfma_f32_16x16x32_bf16 a[204:207],v[120:123],v[44:47],a[204:207]\nv_mfma_f32_16x16x32_bf16 a[236:239],v[124:127],v[44:47],a[236:239]\n"
"v_mfma_f32_16x16x32_bf16 a[232:235],v[124:127],v[40:43],a[232:235]\ns_waitcnt lgkmcnt(0)\ns_barrier\nbuffer_load_dwordx4 v252,s[8:11],s66 offen offset:3072 sc1 lds\nv_mfma_f32_16x16x32_bf16 a[200:203],v[120:123],v[40:43],a[200:203]\nv_mfma_f32_16x16x32_bf16 a[196:199],v[120:123],v[36:39],a[196:199]\nv_mfma_f32_16x16x32_bf16 a[228:231],v[124:127],v[36:39],a[228:231]\ns_add_u32 s66,s66,0x4000\nv_mfma_f32_16x16x32_bf16 a[224:227],v[124:127],v[32:35],a[224:227]\n"
"v_mfma_f32_16x16x32_bf16 a[192:195],v[120:123],v[32:35],a[192:195]\nv_mfma_f32_16x16x32_bf16 a[0:3],v[64:67],v[0:3],a[0:3]\ns_add_u32 m0,s15,131072\nds_read_b64_tr_b16 v[96:97],v130 offset:49152\nv_mfma_f32_16x16x32_bf16 a[32:35],v[68:71],v[0:3],a[32:35]\nbuffer_load_dwordx4 v252,s[8:11],s66 offen sc1 lds\nv_mfma_f32_16x16x32_bf16 a[36:39],v[68:71],v[4:7],a[36:39]\nds_read_b64_tr_b16 v[98:99],v130 offset:51200\n"
"v_mfma_f32_16x16x32_bf16 a[4:7],v[64:67],v[4:7],a[4:7]\nv_mfma_f32_16x16x32_bf16 a[8:11],v[64:67],v[8:11],a[8:11]\nds_read_b64_tr_b16 v[100:101],v131 offset:49152\nv_mfma_f32_16x16x32_bf16 a[40:43],v[68:71],v[8:11],a[40:43]\nv_mfma_f32_16x16x32_bf16 a[44:47],v[68:71],v[12:15],a[44:47]\nds_read_b64_tr_b16 v[102:103],v131 offset:51200\nv_mfma_f32_16x16x32_bf16 a[12:15],v[64:67],v[12:15],a[12:15]\nbuffer_load_dwordx4 v252,s[8:11],s66 offen offset:1024 sc1 lds\n"
"v_mfma_f32_16x16x32_bf16 a[16:19],v[64:67],v[16:19],a[16:19]\nds_read_b128 v[32:35],v129 offset:32768\nv_mfma_f32_16x16x32_bf16 a[48:51],v[68:71],v[16:19],a[48:51]\nv_mfma_f32_16x16x32_bf16 a[52:55],v[68:71],v[20:23],a[52:55]\nds_read_b128 v[36:39],v129 offset:34816\nv_mfma_f32_16x16x32_bf16 a[20:23],v[64:67],v[20:23],a[20:23]\nv_mfma_f32_16x16x32_bf16 a[24:27],v[64:67],v[24:27],a[24:27]\nds_read_b128 v[40:43],v129 offset:36864\n"
"v_mfma_f32_16x16x32_bf16 a[56:59],v[68:71],v[24:27],a[56:59]\nbuffer_load_dwordx4 v252,s[8:11],s66 offen offset:2048 sc1 lds\nv_mfma_f32_16x16x32_bf16 a[60:63],v[68:71],v[28:31],a[60:63]\nds_read_b128 v[44:47],v129 offset:38912\nv_mfma_f32_16x16x32_bf16 a[28:31],v[64:67],v[28:31],a[28:31]\nv_mfma_f32_16x16x32_bf16 a[92:95],v[72:75],v[28:31],a[92:95]\nds_read_b128 v[48:51],v129 offset:40960\nv_mfma_f32_16x16x32_bf16 a[124:127],v[76:79],v[28:31],a[124:127]\n"
"v_mfma_f32_16x16x32_bf16 a[120:123],v[76:79],v[24:27],a[120:123]\nds_read_b128 v[52:55],v129 offset:43008\nv_mfma_f32_16x16x32_bf16 a[88:91],v[72:75],v[24:27],a[88:91]\nbuffer_load_dwordx4 v252,s[8:11],s66 offen offset:3072 sc1 lds\nv_mfma_f32_16x16x32_bf16 a[84:87],v[72:75],v[20:23],a[84:87]\nds_read_b128 v[56:59],v129 offset:45056\nv_mfma_f32_16x16x32_bf16 a[116:119],v[76:79],v[20:23],a[116:119]\n"
"v_mfma_f32_16x16x32_bf16 a[112:115],v[76:79],v[16:19],a[112:115]\nds_read_b128 v[60:63],v129 offset:47104\nv_mfma_f32_16x16x32_bf16 a[80:83],v[72:75],v[16:19],a[80:83]\nv_mfma_f32_16x16x32_bf16 a[76:79],v[72:75],v[12:15],a[76:79]\ns_add_u32 s66,s66,0x4000\nds_read_b64_tr_b16 v[104:105],v132 offset:49152\ns_add_u32 m0,s15,147456\nv_mfma_f32_16x16x32_bf16 a[108:111],v[76:79],v[12:15],a[108:111]\nbuffer_load_dwordx4 v252,s[8:11],s66 offen sc1 lds\n"
"v_mfma_f32_16x16x32_bf16 a[104:107],v[76:79],v[8:11],a[104:107]\nds_read_b64_tr_b16 v[106:107],v132 offset:51200\nv_mfma_f32_16x16x32_bf16 a[72:75],v[72:75],v[8:11],a[72:75]\nv_mfma_f32_16x16x32_bf16 a[68:71],v[72:75],v[4:7],a[68:71]\nds_read_b64_tr_b16 v[108:109],v133 offset:49152\nv_mfma_f32_16x16x32_bf16 a[100:103],v[76:79],v[4:7],a[100:103]\nv_mfma_f32_16x16x32_bf16 a[96:99],v[76:79],v[0:3],a[96:99]\n"
"ds_read_b64_tr_b16 v[110:111],v133 offset:51200\nv_mfma_f32_16x16x32_bf16 a[64:67],v[72:75],v[0:3],a[64:67]\nbuffer_load_dwordx4 v252,s[8:11],s66 offen offset:1024 sc1 lds\nv_mfma_f32_16x16x32_bf16 a[128:131],v[80:83],v[0:3],a[128:131]\nds_read_b64_tr_b16 v[112:113],v134 offset:49152\nv_mfma_f32_16x16x32_bf16 a[160:163],v[84:87],v[0:3],a[160:163]\nv_mfma_f32_16x16x32_bf16 a[164:167],v[84:87],v[4:7],a[164:167]\n"
"ds_read_b64_tr_b16 v[114:115],v134 offset:51200\nv_mfma_f32_16x16x32_bf16 a[132:135],v[80:83],v[4:7],a[132:135]\nv_mfma_f32_16x16x32_bf16 a[136:139],v[80:83],v[8:11],a[136:139]\nds_read_b64_tr_b16 v[116:117],v135 offset:49152\nv_mfma_f32_16x16x32_bf16 a[168:171],v[84:87],v[8:11],a[168:171]\nbuffer_load_dwordx4 v252,s[8:11],s66 offen offset:2048 sc1 lds\nv_mfma_f32_16x16x32_bf16 a[172:175],v[84:87],v[12:15],a[172:175]\n"
"ds_read_b64_tr_b16 v[118:119],v135 offset:51200\nv_mfma_f32_16x16x32_bf16 a[140:143],v[80:83],v[12:15],a[140:143]\nv_mfma_f32_16x16x32_bf16 a[144:147],v[80:83],v[16:19],a[144:147]\nds_read_b64_tr_b16 v[120:121],v136 offset:49152\nv_mfma_f32_16x16x32_bf16 a[176:179],v[84:87],v[16:19],a[176:179]\nv_mfma_f32_16x16x32_bf16 a[180:183],v[84:87],v[20:23],a[180:183]\nds_read_b64_tr_b16 v[122:123],v136 offset:51200\n"
"v_mfma_f32_16x16x32_bf16 a[148:151],v[80:83],v[20:23],a[148:151]\nbuffer_load_dwordx4 v252,s[8:11],s66 offen offset:3072 sc1 lds\nv_mfma_f32_16x16x32_bf16 a[152:155],v[80:83],v[24:27],a[152:155]\nds_read_b64_tr_b16 v[124:125],v137 offset:49152\nv_mfma_f32_16x16x32_bf16 a[184:187],v[84:87],v[24:27],a[184:187]\nv_mfma_f32_16x16x32_bf16 a[188:191],v[84:87],v[28:31],a[188:191]\nds_read_b64_tr_b16 v[126:127],v137 offset:51200\n"
"v_mfma_f32_16x16x32_bf16 a[156:159],v[80:83],v[28:31],a[156:159]\nv_mfma_f32_16x16x32_bf16 a[220:223],v[88:91],v[28:31],a[220:223]\ns_add_u32 s66,s66,0x4000\nv_mfma_f32_16x16x32_bf16 a[252:255],v[92:95],v[28:31],a[252:255]\nv_mfma_f32_16x16x32_bf16 a[248:251],v[92:95],v[24:27],a[248:251]\nv_mfma_f32_16x16x32_bf16 a[216:219],v[88:91],v[24:27],a[216:219]\nv_mfma_f32_16x16x32_bf16 a[212:215],v[88:91],v[20:23],a[212:215]\n"
"v_mfma_f32_16x16x32_bf16 a[244:247],v[92:95],v[20:23],a[244:247]\nv_mfma_f32_16x16x32_bf16 a[240:243],v[92:95],v[16:19],a[240:243]\nv_mfma_f32_16x16x32_bf16 a[208:211],v[88:91],v[16:19],a[208:211]\nv_mfma_f32_16x16x32_bf16 a[204:207],v[88:91],v[12:15],a[204:207]\nv_mfma_f32_16x16x32_bf16 a[236:239],v[92:95],v[12:15],a[236:239]\nv_mfma_f32_16x16x32_bf16 a[232:235],v[92:95],v[8:11],a[232:235]\ns_waitcnt vmcnt(8) lgkmcnt(0)\n"
"s_barrier\nv_mfma_f32_16x16x32_bf16 a[200:203],v[88:91],v[8:11],a[200:203]\nv_mfma_f32_16x16x32_bf16 a[196:199],v[88:91],v[4:7],a[196:199]\nv_mfma_f32_16x16x32_bf16 a[228:231],v[92:95],v[4:7],a[228:231]\nv_mfma_f32_16x16x32_bf16 a[224:227],v[92:95],v[0:3],a[224:227]\nv_mfma_f32_16x16x32_bf16 a[192:195],v[88:91],v[0:3],a[192:195]\nv_mfma_f32_16x16x32_bf16 a[0:3],v[96:99],v[32:35],a[0:3]\nv_mfma_f32_16x16x32_bf16 a[32:35],v[100:103],v[32:35],a[32:35]\n"
"buffer_load_dwordx4 v[184:187],v252,s[8:11],s67 offen offset:0 sc1\nv_mfma_f32_16x16x32_bf16 a[36:39],v[100:103],v[36:39],a[36:39]\nv_mfma_f32_16x16x32_bf16 a[4:7],v[96:99],v[36:39],a[4:7]\nbuffer_load_dwordx4 v[188:191],v252,s[8:11],s68 offen offset:0 sc1\nv_mfma_f32_16x16x32_bf16 a[8:11],v[96:99],v[40:43],a[8:11]\nv_mfma_f32_16x16x32_bf16 a[40:43],v[100:103],v[40:43],a[40:43]\nbuffer_load_dwordx4 v[192:195],v252,s[8:11],s67 offen offset:1024 sc1\n"
"v_mfma_f32_16x16x32_bf16 a[44:47],v[100:103],v[44:47],a[44:47]\nv_mfma_f32_16x16x32_bf16 a[12:15],v[96:99],v[44:47],a[12:15]\nbuffer_load_dwordx4 v[196:199],v252,s[8:11],s68 offen offset:1024 sc1\nv_mfma_f32_16x16x32_bf16 a[16:19],v[96:99],v[48:51],a[16:19]\nv_mfma_f32_16x16x32_bf16 a[48:51],v[100:103],v[48:51],a[48:51]\nbuffer_load_dwordx4 v[200:203],v252,s[8:11],s67 offen offset:2048 sc1\nv_mfma_f32_16x16x32_bf16 a[52:55],v[100:103],v[52:55],a[52:55]\n"
"v_mfma_f32_16x16x32_bf16 a[20:23],v[96:99],v[52:55],a[20:23]\nbuffer_load_dwordx4 v[204:207],v252,s[8:11],s68 offen offset:2048 sc1\nv_mfma_f32_16x16x32_bf16 a[24:27],v[96:99],v[56:59],a[24:27]\nv_mfma_f32_16x16x32_bf16 a[56:59],v[100:103],v[56:59],a[56:59]\nbuffer_load_dwordx4 v[128:131],v252,s[8:11],s67 offen offset:3072 sc1\nv_mfma_f32_16x16x32_bf16 a[60:63],v[100:103],v[60:63],a[60:63]\nv_mfma_f32_16x16x32_bf16 a[28:31],v[96:99],v[60:63],a[28:31]\n"
"s_add_u32 s67,s67,0x1000\nbuffer_load_dwordx4 v[132:135],v252,s[8:11],s68 offen offset:3072 sc1\nv_mfma_f32_16x16x32_bf16 a[92:95],v[104:107],v[60:63],a[92:95]\nv_mfma_f32_16x16x32_bf16 a[124:127],v[108:111],v[60:63],a[124:127]\ns_add_u32 s68,s68,0x1000\nbuffer_load_dwordx4 v[136:139],v252,s[8:11],s67 offen offset:0 sc1\nv_mfma_f32_16x16x32_bf16 a[120:123],v[108:111],v[56:59],a[120:123]\nv_mfma_f32_16x16x32_bf16 a[88:91],v[104:107],v[56:59],a[88:91]\n"
"buffer_load_dwordx4 v[140:143],v252,s[8:11],s68 offen offset:0 sc1\nv_mfma_f32_16x16x32_bf16 a[84:87],v[104:107],v[52:55],a[84:87]\nv_mfma_f32_16x16x32_bf16 a[116:119],v[108:111],v[52:55],a[116:119]\ns_add_u32 m0,s15,32768\nv_mfma_f32_16x16x32_bf16 a[112:115],v[108:111],v[48:51],a[112:115]\nbuffer_load_dwordx4 v252,s[8:11],s66 offen sc1 lds\nv_mfma_f32_16x16x32_bf16 a[80:83],v[104:107],v[48:51],a[80:83]\n"
"v_mfma_f32_16x16x32_bf16 a[76:79],v[104:107],v[44:47],a[76:79]\nbuffer_load_dwordx4 v252,s[8:11],s66 offen offset:1024 sc1 lds\nv_mfma_f32_16x16x32_bf16 a[108:111],v[108:111],v[44:47],a[108:111]\nv_mfma_f32_16x16x32_bf16 a[104:107],v[108:111],v[40:43],a[104:107]\nbuffer_load_dwordx4 v252,s[8:11],s66 offen offset:2048 sc1 lds\nv_mfma_f32_16x16x32_bf16 a[72:75],v[104:107],v[40:43],a[72:75]\nv_mfma_f32_16x16x32_bf16 a[68:71],v[104:107],v[36:39],a[68:71]\n"
"buffer_load_dwordx4 v252,s[8:11],s66 offen offset:3072 sc1 lds\nv_mfma_f32_16x16x32_bf16 a[100:103],v[108:111],v[36:39],a[100:103]\nv_mfma_f32_16x16x32_bf16 a[96:99],v[108:111],v[32:35],a[96:99]\ns_add_u32 s66,s66,0x4000\ns_add_u32 m0,s15,49152\nv_mfma_f32_16x16x32_bf16 a[64:67],v[104:107],v[32:35],a[64:67]\nbuffer_load_dwordx4 v252,s[8:11],s66 offen sc1 lds\nv_mfma_f32_16x16x32_bf16 a[128:131],v[112:115],v[32:35],a[128:131]\n"
"v_mfma_f32_16x16x32_bf16 a[160:163],v[116:119],v[32:35],a[160:163]\nbuffer_load_dwordx4 v252,s[8:11],s66 offen offset:1024 sc1 lds\nv_mfma_f32_16x16x32_bf16 a[164:167],v[116:119],v[36:39],a[164:167]\nv_mfma_f32_16x16x32_bf16 a[132:135],v[112:115],v[36:39],a[132:135]\nbuffer_load_dwordx4 v252,s[8:11],s66 offen offset:2048 sc1 lds\nv_mfma_f32_16x16x32_bf16 a[136:139],v[112:115],v[40:43],a[136:139]\nv_mfma_f32_16x16x32_bf16 a[168:171],v[116:119],v[40:43],a[168:171]\n"
"buffer_load_dwordx4 v252,s[8:11],s66 offen offset:3072 sc1 lds\nv_mfma_f32_16x16x32_bf16 a[172:175],v[116:119],v[44:47],a[172:175]\nv_mfma_f32_16x16x32_bf16 a[140:143],v[112:115],v[44:47],a[140:143]\ns_add_u32 s66,s66,0x4000\ns_add_u32 m0,s15,98304\nv_mfma_f32_16x16x32_bf16 a[144:147],v[112:115],v[48:51],a[144:147]\nbuffer_load_dwordx4 v252,s[8:11],s66 offen sc1 lds\nv_mfma_f32_16x16x32_bf16 a[176:179],v[116:119],v[48:51],a[176:179]\n"
"v_mfma_f32_16x16x32_bf16 a[180:183],v[116:119],v[52:55],a[180:183]\nbuffer_load_dwordx4 v252,s[8:11],s66 offen offset:1024 sc1 lds\nv_mfma_f32_16x16x32_bf16 a[148:151],v[112:115],v[52:55],a[148:151]\nv_mfma_f32_16x16x32_bf16 a[152:155],v[112:115],v[56:59],a[152:155]\nbuffer_load_dwordx4 v252,s[8:11],s66 offen offset:2048 sc1 lds\nv_mfma_f32_16x16x32_bf16 a[184:187],v[116:119],v[56:59],a[184:187]\nv_mfma_f32_16x16x32_bf16 a[188:191],v[116:119],v[60:63],a[188:191]\n"
"buffer_load_dwordx4 v252,s[8:11],s66 offen offset:3072 sc1 lds\nv_mfma_f32_16x16x32_bf16 a[156:159],v[112:115],v[60:63],a[156:159]\nv_mfma_f32_16x16x32_bf16 a[220:223],v[120:123],v[60:63],a[220:223]\ns_add_u32 s66,s66,0x4000\nv_mfma_f32_16x16x32_bf16 a[252:255],v[124:127],v[60:63],a[252:255]\nv_mfma_f32_16x16x32_bf16 a[248:251],v[124:127],v[56:59],a[248:251]\nds_read_b128 v[152:155],v252 offset:0\n"
"v_mfma_f32_16x16x32_bf16 a[216:219],v[120:123],v[56:59],a[216:219]\nv_mfma_f32_16x16x32_bf16 a[212:215],v[120:123],v[52:55],a[212:215]\nds_read_b128 v[156:159],v252 offset:1024\nv_mfma_f32_16x16x32_bf16 a[244:247],v[124:127],v[52:55],a[244:247]\nv_mfma_f32_16x16x32_bf16 a[240:243],v[124:127],v[48:51],a[240:243]\nds_read_b128 v[160:163],v252 offset:2048\nv_mfma_f32_16x16x32_bf16 a[208:211],v[120:123],v[48:51],a[208:211]\n"
"v_mfma_f32_16x16x32_bf16 a[204:207],v[120:123],v[44:47],a[204:207]\nds_read_b128 v[164:167],v252 offset:3072\nv_mfma_f32_16x16x32_bf16 a[236:239],v[124:127],v[44:47],a[236:239]\nv_mfma_f32_16x16x32_bf16 a[232:235],v[124:127],v[40:43],a[232:235]\nds_read_b128 v[168:171],v252 offset:4096\nv_mfma_f32_16x16x32_bf16 a[200:203],v[120:123],v[40:43],a[200:203]\nv_mfma_f32_16x16x32_bf16 a[196:199],v[120:123],v[36:39],a[196:199]\n"
"ds_read_b128 v[172:175],v252 offset:5120\nv_mfma_f32_16x16x32_bf16 a[228:231],v[124:127],v[36:39],a[228:231]\nv_mfma_f32_16x16x32_bf16 a[224:227],v[124:127],v[32:35],a[224:227]\ns_waitcnt vmcnt(20)\nds_read_b128 v[176:179],v252 offset:6144\nv_mfma_f32_16x16x32_bf16 a[192:195],v[120:123],v[32:35],a[192:195]\ns_waitcnt lgkmcnt(5)\ns_branch L_fused_block_q_4\nL_fused_block_vtype_3:\nv_mfma_f32_16x16x32_bf16 a[0:3],v[0:3],v[64:67],0\n"
"s_add_u32 m0,s15,0\nds_read_b64_tr_b16 v[96:97],v130 offset:16384\nv_lshlrev_b32 v208,16,v160\nv_mfma_f32_16x16x32_bf16 a[4:7],v[0:3],v[68:71],0\nbuffer_load_dwordx4 v140,s[28:31],s21 offen lds\nv_mfma_f32_16x16x32_bf16 a[36:39],v[4:7],v[68:71],0\nds_read_b64_tr_b16 v[98:99],v130 offset:18432\nv_and_b32 v209,0xffff0000,v160\nv_mfma_f32_16x16x32_bf16 a[32:35],v[4:7],v[64:67],0\nv_lshlrev_b32 v210,16,v161\n"
"v_and_b32 v211,0xffff0000,v161\nv_mfma_f32_16x16x32_bf16 a[64:67],v[8:11],v[64:67],0\nds_read_b64_tr_b16 v[100:101],v131 offset:16384\nv_lshlrev_b32 v212,16,v162\nv_mfma_f32_16x16x32_bf16 a[68:71],v[8:11],v[68:71],0\nbuffer_load_dwordx4 v141,s[28:31],s22 offen offset:1024 lds\nv_mfma_f32_16x16x32_bf16 a[100:103],v[12:15],v[68:71],0\nds_read_b64_tr_b16 v[102:103],v131 offset:18432\nv_and_b32 v213,0xffff0000,v162\n"
"v_mfma_f32_16x16x32_bf16 a[96:99],v[12:15],v[64:67],0\nv_lshlrev_b32 v214,16,v163\nv_and_b32 v215,0xffff0000,v163\nv_mfma_f32_16x16x32_bf16 a[128:131],v[16:19],v[64:67],0\nds_read_b128 v[32:35],v129 offset:0\nv_lshlrev_b32 v216,16,v164\nv_mfma_f32_16x16x32_bf16 a[132:135],v[16:19],v[68:71],0\nbuffer_load_dwordx4 v140,s[28:31],s23 offen offset:2048 lds\nv_mfma_f32_16x16x32_bf16 a[164:167],v[20:23],v[68:71],0\n"
"ds_read_b128 v[36:39],v129 offset:2048\nv_and_b32 v217,0xffff0000,v164\nv_mfma_f32_16x16x32_bf16 a[160:163],v[20:23],v[64:67],0\nv_lshlrev_b32 v218,16,v165\nv_and_b32 v219,0xffff0000,v165\ns_waitcnt lgkmcnt(15)\nv_mfma_f32_16x16x32_bf16 a[192:195],v[24:27],v[64:67],0\nds_read_b128 v[40:43],v129 offset:4096\nv_lshlrev_b32 v220,16,v166\nv_mfma_f32_16x16x32_bf16 a[196:199],v[24:27],v[68:71],0\nbuffer_load_dwordx4 v141,s[28:31],s44 offen offset:3072 lds\n"
"s_waitcnt lgkmcnt(15)\nv_mfma_f32_16x16x32_bf16 a[228:231],v[28:31],v[68:71],0\ns_add_u32 s28,s28,0x40000\nds_read_b128 v[44:47],v129 offset:6144\nv_and_b32 v221,0xffff0000,v166\nv_mfma_f32_16x16x32_bf16 a[224:227],v[28:31],v[64:67],0\ns_addc_u32 s29,s29,0\nv_lshlrev_b32 v222,16,v167\nv_and_b32 v223,0xffff0000,v167\ns_waitcnt lgkmcnt(15)\nv_mfma_f32_16x16x32_bf16 a[232:235],v[28:31],v[72:75],0\nds_read_b128 v[48:51],v129 offset:8192\n"
"v_add_f32 v169,v208,v209\ns_waitcnt lgkmcnt(15)\nv_mfma_f32_16x16x32_bf16 a[236:239],v[28:31],v[76:79],0\nglobal_load_dword v151,v142,s[0:1]\nv_mfma_f32_16x16x32_bf16 a[204:207],v[24:27],v[76:79],0\nds_read_b128 v[52:55],v129 offset:10240\nv_add_f32 v169,v169,v210\nv_mfma_f32_16x16x32_bf16 a[200:203],v[24:27],v[72:75],0\nv_add_f32 v169,v169,v211\nv_add_f32 v169,v169,v212\nv_mfma_f32_16x16x32_bf16 a[168:171],v[20:23],v[72:75],0\n"
"ds_read_b128 v[56:59],v129 offset:12288\nv_add_f32 v169,v169,v213\nv_mfma_f32_16x16x32_bf16 a[172:175],v[20:23],v[76:79],0\nv_add_f32 v169,v169,v214\nv_add_f32 v169,v169,v215\nv_mfma_f32_16x16x32_bf16 a[140:143],v[16:19],v[76:79],0\nds_read_b128 v[60:63],v129 offset:14336\nv_add_f32 v169,v169,v216\nv_mfma_f32_16x16x32_bf16 a[136:139],v[16:19],v[72:75],0\nv_add_f32 v169,v169,v217\nv_add_f32 v169,v169,v218\n"
"v_mfma_f32_16x16x32_bf16 a[104:107],v[12:15],v[72:75],0\nds_read_b64_tr_b16 v[104:105],v132 offset:16384\nv_add_f32 v169,v169,v219\nv_mfma_f32_16x16x32_bf16 a[108:111],v[12:15],v[76:79],0\nv_add_f32 v169,v169,v220\nv_add_f32 v169,v169,v221\nv_mfma_f32_16x16x32_bf16 a[76:79],v[8:11],v[76:79],0\nds_read_b64_tr_b16 v[106:107],v132 offset:18432\nv_add_f32 v169,v169,v222\nv_mfma_f32_16x16x32_bf16 a[72:75],v[8:11],v[72:75],0\n"
"v_add_f32 v169,v169,v223\nv_mfma_f32_16x16x32_bf16 a[40:43],v[4:7],v[72:75],0\nds_read_b64_tr_b16 v[108:109],v133 offset:16384\nv_add_f32_dpp v169,v169,v169 row_ror:8 row_mask:0xf bank_mask:0xf\nv_mfma_f32_16x16x32_bf16 a[44:47],v[4:7],v[76:79],0\ns_nop 0\nv_add_f32_dpp v169,v169,v169 row_ror:4 row_mask:0xf bank_mask:0xf\nv_mfma_f32_16x16x32_bf16 a[12:15],v[0:3],v[76:79],0\nds_read_b64_tr_b16 v[110:111],v133 offset:18432\n"
"v_add_f32_dpp v169,v169,v169 row_ror:2 row_mask:0xf bank_mask:0xf\nv_mfma_f32_16x16x32_bf16 a[8:11],v[0:3],v[72:75],0\ns_nop 0\nv_add_f32_dpp v169,v169,v169 row_ror:1 row_mask:0xf bank_mask:0xf\nv_mov_b32 v172,v169\ns_waitcnt lgkmcnt(15)\nv_mfma_f32_16x16x32_bf16 a[16:19],v[0:3],v[80:83],0\nds_read_b64_tr_b16 v[112:113],v134 offset:16384\nv_permlane16_swap_b32 v172,v169\ns_waitcnt lgkmcnt(15)\nv_mfma_f32_16x16x32_bf16 a[20:23],v[0:3],v[84:87],0\n"
"v_add_f32 v169,v169,v172\nv_mul_f32 v170,0x3b124925,v169\nv_mfma_f32_16x16x32_bf16 a[52:55],v[4:7],v[84:87],0\nds_read_b64_tr_b16 v[114:115],v134 offset:18432\nv_cndmask_b32 v170,0,v170,s[2:3]\nv_mfma_f32_16x16x32_bf16 a[48:51],v[4:7],v[80:83],0\nv_sub_f32 v208,v208,v170\nv_sub_f32 v209,v209,v170\nv_mfma_f32_16x16x32_bf16 a[80:83],v[8:11],v[80:83],0\nds_read_b64_tr_b16 v[116:117],v135 offset:16384\n"
"v_sub_f32 v210,v210,v170\nv_mfma_f32_16x16x32_bf16 a[84:87],v[8:11],v[84:87],0\nv_sub_f32 v211,v211,v170\nv_sub_f32 v212,v212,v170\nv_mfma_f32_16x16x32_bf16 a[116:119],v[12:15],v[84:87],0\nds_read_b64_tr_b16 v[118:119],v135 offset:18432\nv_sub_f32 v213,v213,v170\nv_mfma_f32_16x16x32_bf16 a[112:115],v[12:15],v[80:83],0\nv_sub_f32 v214,v214,v170\nv_sub_f32 v215,v215,v170\nv_mfma_f32_16x16x32_bf16 a[144:147],v[16:19],v[80:83],0\n"
"ds_read_b64_tr_b16 v[120:121],v136 offset:16384\nv_sub_f32 v216,v216,v170\nv_mfma_f32_16x16x32_bf16 a[148:151],v[16:19],v[84:87],0\nv_sub_f32 v217,v217,v170\nv_sub_f32 v218,v218,v170\nv_mfma_f32_16x16x32_bf16 a[180:183],v[20:23],v[84:87],0\nds_read_b64_tr_b16 v[122:123],v136 offset:18432\nv_sub_f32 v219,v219,v170\nv_mfma_f32_16x16x32_bf16 a[176:179],v[20:23],v[80:83],0\nv_sub_f32 v220,v220,v170\nv_sub_f32 v221,v221,v170\n"
"v_mfma_f32_16x16x32_bf16 a[208:211],v[24:27],v[80:83],0\nds_read_b64_tr_b16 v[124:125],v137 offset:16384\nv_sub_f32 v222,v222,v170\nv_mfma_f32_16x16x32_bf16 a[212:215],v[24:27],v[84:87],0\nv_sub_f32 v223,v223,v170\nv_mul_f32 v171,v208,v208\nv_mfma_f32_16x16x32_bf16 a[244:247],v[28:31],v[84:87],0\nds_read_b64_tr_b16 v[126:127],v137 offset:18432\nv_fmac_f32 v171,v209,v209\nv_mfma_f32_16x16x32_bf16 a[240:243],v[28:31],v[80:83],0\n"
"v_fmac_f32 v171,v210,v210\nv_fmac_f32 v171,v211,v211\ns_waitcnt lgkmcnt(15)\nv_mfma_f32_16x16x32_bf16 a[248:251],v[28:31],v[88:91],0\nv_fmac_f32 v171,v212,v212\nv_fmac_f32 v171,v213,v213\ns_waitcnt lgkmcnt(15)\nv_mfma_f32_16x16x32_bf16 a[252:255],v[28:31],v[92:95],0\nv_fmac_f32 v171,v214,v214\nv_fmac_f32 v171,v215,v215\nv_mfma_f32_16x16x32_bf16 a[220:223],v[24:27],v[92:95],0\nv_fmac_f32 v171,v216,v216\n"
"v_fmac_f32 v171,v217,v217\nv_mfma_f32_16x16x32_bf16 a[216:219],v[24:27],v[88:91],0\nv_fmac_f32 v171,v218,v218\nv_fmac_f32 v171,v219,v219\nv_mfma_f32_16x16x32_bf16 a[184:187],v[20:23],v[88:91],0\nv_fmac_f32 v171,v220,v220\nv_fmac_f32 v171,v221,v221\nv_mfma_f32_16x16x32_bf16 a[188:191],v[20:23],v[92:95],0\nv_fmac_f32 v171,v222,v222\nv_fmac_f32 v171,v223,v223\nv_mfma_f32_16x16x32_bf16 a[156:159],v[16:19],v[92:95],0\n"
"s_nop 0\nv_add_f32_dpp v171,v171,v171 row_ror:8 row_mask:0xf bank_mask:0xf\nv_mfma_f32_16x16x32_bf16 a[152:155],v[16:19],v[88:91],0\ns_nop 0\nv_add_f32_dpp v171,v171,v171 row_ror:4 row_mask:0xf bank_mask:0xf\nv_mfma_f32_16x16x32_bf16 a[120:123],v[12:15],v[88:91],0\ns_nop 0\nv_add_f32_dpp v171,v171,v171 row_ror:2 row_mask:0xf bank_mask:0xf\nv_mfma_f32_16x16x32_bf16 a[124:127],v[12:15],v[92:95],0\ns_nop 0\n"
"v_add_f32_dpp v171,v171,v171 row_ror:1 row_mask:0xf bank_mask:0xf\nv_mov_b32 v172,v171\nv_mfma_f32_16x16x32_bf16 a[92:95],v[8:11],v[92:95],0\ns_waitcnt vmcnt(10) lgkmcnt(0)\ns_barrier\nv_permlane16_swap_b32 v172,v171\nv_add_f32 v171,v171,v172\nv_mfma_f32_16x16x32_bf16 a[88:91],v[8:11],v[88:91],0\nv_mov_b32 v172,0x3727c5ac\nv_fmac_f32 v172,0x3b124925,v171\nv_mfma_f32_16x16x32_bf16 a[56:59],v[4:7],v[88:91],0\n"
"v_rsq_f32 v172,v172\nv_mfma_f32_16x16x32_bf16 a[60:63],v[4:7],v[92:95],0\nv_mul_f32 v172,0x3db8aa3b,v172\nv_mul_f32 v208,v172,v208\nv_mfma_f32_16x16x32_bf16 a[28:31],v[0:3],v[92:95],0\nv_mul_f32 v209,v172,v209\nv_mul_f32 v210,v172,v210\nv_mfma_f32_16x16x32_bf16 a[24:27],v[0:3],v[88:91],0\nv_mul_f32 v211,v172,v211\nv_mul_f32 v212,v172,v212\nv_mfma_f32_16x16x32_bf16 a[0:3],v[32:35],v[96:99],a[0:3]\ns_add_u32 m0,s43,0\n"
"ds_read_b64_tr_b16 v[64:65],v130 offset:32768\nv_mul_f32 v213,v172,v213\nv_mfma_f32_16x16x32_bf16 a[4:7],v[32:35],v[100:103],a[4:7]\nbuffer_load_dwordx4 v138,s[24:27],s32 offen lds\nv_mfma_f32_16x16x32_bf16 a[36:39],v[36:39],v[100:103],a[36:39]\nds_read_b64_tr_b16 v[66:67],v130 offset:34816\nv_mul_f32 v214,v172,v214\nv_mfma_f32_16x16x32_bf16 a[32:35],v[36:39],v[96:99],a[32:35]\nv_mul_f32 v215,v172,v215\n"
"v_mul_f32 v216,v172,v216\nv_mfma_f32_16x16x32_bf16 a[64:67],v[40:43],v[96:99],a[64:67]\nds_read_b64_tr_b16 v[68:69],v131 offset:32768\nv_mul_f32 v217,v172,v217\nv_mfma_f32_16x16x32_bf16 a[68:71],v[40:43],v[100:103],a[68:71]\nbuffer_load_dwordx4 v139,s[24:27],s33 offen offset:1024 lds\nv_mfma_f32_16x16x32_bf16 a[100:103],v[44:47],v[100:103],a[100:103]\nds_read_b64_tr_b16 v[70:71],v131 offset:34816\n"
"v_mul_f32 v218,v172,v218\nv_mfma_f32_16x16x32_bf16 a[96:99],v[44:47],v[96:99],a[96:99]\nv_mul_f32 v219,v172,v219\nv_mul_f32 v220,v172,v220\nv_mfma_f32_16x16x32_bf16 a[128:131],v[48:51],v[96:99],a[128:131]\nds_read_b128 v[0:3],v128 offset:32768\nv_mul_f32 v221,v172,v221\nv_mfma_f32_16x16x32_bf16 a[132:135],v[48:51],v[100:103],a[132:135]\nbuffer_load_dwordx4 v138,s[24:27],s34 offen offset:2048 lds\nv_mfma_f32_16x16x32_bf16 a[164:167],v[52:55],v[100:103],a[164:167]\n"
"ds_read_b128 v[4:7],v128 offset:34816\nv_mul_f32 v222,v172,v222\nv_mfma_f32_16x16x32_bf16 a[160:163],v[52:55],v[96:99],a[160:163]\nv_mul_f32 v223,v172,v223\nv_cvt_pk_bf16_f32 v160,v208,v209\nv_mfma_f32_16x16x32_bf16 a[192:195],v[56:59],v[96:99],a[192:195]\nds_read_b128 v[8:11],v128 offset:36864\nv_cvt_pk_bf16_f32 v161,v210,v211\nv_mfma_f32_16x16x32_bf16 a[196:199],v[56:59],v[100:103],a[196:199]\nbuffer_load_dwordx4 v139,s[24:27],s35 offen offset:3072 lds\n"
"v_mfma_f32_16x16x32_bf16 a[228:231],v[60:63],v[100:103],a[228:231]\nds_read_b128 v[12:15],v128 offset:38912\nv_cvt_pk_bf16_f32 v162,v216,v217\nv_mfma_f32_16x16x32_bf16 a[224:227],v[60:63],v[96:99],a[224:227]\nv_cvt_pk_bf16_f32 v163,v218,v219\nv_cvt_pk_bf16_f32 v164,v212,v213\nv_mfma_f32_16x16x32_bf16 a[232:235],v[60:63],v[104:107],a[232:235]\ns_add_u32 m0,s43,4096\nds_read_b128 v[16:19],v128 offset:40960\n"
"v_cvt_pk_bf16_f32 v165,v214,v215\nv_mfma_f32_16x16x32_bf16 a[236:239],v[60:63],v[108:111],a[236:239]\nbuffer_load_dwordx4 v138,s[24:27],s36 offen lds\nv_mfma_f32_16x16x32_bf16 a[204:207],v[56:59],v[108:111],a[204:207]\nds_read_b128 v[20:23],v128 offset:43008\nv_cvt_pk_bf16_f32 v166,v220,v221\nv_mfma_f32_16x16x32_bf16 a[200:203],v[56:59],v[104:107],a[200:203]\nv_cvt_pk_bf16_f32 v167,v222,v223\nv_mfma_f32_16x16x32_bf16 a[168:171],v[52:55],v[104:107],a[168:171]\n"
"ds_read_b128 v[24:27],v128 offset:45056\nv_mfma_f32_16x16x32_bf16 a[172:175],v[52:55],v[108:111],a[172:175]\nbuffer_load_dwordx4 v139,s[24:27],s37 offen offset:1024 lds\nv_mfma_f32_16x16x32_bf16 a[140:143],v[48:51],v[108:111],a[140:143]\nds_read_b128 v[28:31],v128 offset:47104\nv_mfma_f32_16x16x32_bf16 a[136:139],v[48:51],v[104:107],a[136:139]\nbuffer_store_dwordx4 v[160:163],v168,s[8:11],s79 offen sc1\n"
"v_mfma_f32_16x16x32_bf16 a[104:107],v[44:47],v[104:107],a[104:107]\nds_read_b64_tr_b16 v[72:73],v132 offset:32768\nv_mfma_f32_16x16x32_bf16 a[108:111],v[44:47],v[108:111],a[108:111]\nbuffer_load_dwordx4 v138,s[24:27],s38 offen offset:2048 lds\nv_mfma_f32_16x16x32_bf16 a[76:79],v[40:43],v[108:111],a[76:79]\nds_read_b64_tr_b16 v[74:75],v132 offset:34816\nv_mfma_f32_16x16x32_bf16 a[72:75],v[40:43],v[104:107],a[72:75]\n"
"buffer_store_dwordx4 v[164:167],v168,s[8:11],s79 offen offset:256 sc1\nv_mfma_f32_16x16x32_bf16 a[40:43],v[36:39],v[104:107],a[40:43]\nds_read_b64_tr_b16 v[76:77],v133 offset:32768\nv_mfma_f32_16x16x32_bf16 a[44:47],v[36:39],v[108:111],a[44:47]\nbuffer_load_dwordx4 v139,s[24:27],s39 offen offset:3072 lds\nv_mfma_f32_16x16x32_bf16 a[12:15],v[32:35],v[108:111],a[12:15]\ns_add_u32 s24,s24,128\nds_read_b64_tr_b16 v[78:79],v133 offset:34816\n"
"v_mfma_f32_16x16x32_bf16 a[8:11],v[32:35],v[104:107],a[8:11]\ns_addc_u32 s25,s25,0\nv_mfma_f32_16x16x32_bf16 a[16:19],v[32:35],v[112:115],a[16:19]\ns_add_u32 m0,s15,16384\nds_read_b64_tr_b16 v[80:81],v134 offset:32768\nv_mfma_f32_16x16x32_bf16 a[20:23],v[32:35],v[116:119],a[20:23]\nbuffer_load_dwordx4 v140,s[28:31],s21 offen lds\nv_mfma_f32_16x16x32_bf16 a[52:55],v[36:39],v[116:119],a[52:55]\nds_read_b64_tr_b16 v[82:83],v134 offset:34816\n"
"v_mfma_f32_16x16x32_bf16 a[48:51],v[36:39],v[112:115],a[48:51]\nv_mfma_f32_16x16x32_bf16 a[80:83],v[40:43],v[112:115],a[80:83]\nds_read_b64_tr_b16 v[84:85],v135 offset:32768\nv_mfma_f32_16x16x32_bf16 a[84:87],v[40:43],v[116:119],a[84:87]\nbuffer_load_dwordx4 v141,s[28:31],s22 offen offset:1024 lds\nv_mfma_f32_16x16x32_bf16 a[116:119],v[44:47],v[116:119],a[116:119]\nds_read_b64_tr_b16 v[86:87],v135 offset:34816\n"
"v_mfma_f32_16x16x32_bf16 a[112:115],v[44:47],v[112:115],a[112:115]\nv_mfma_f32_16x16x32_bf16 a[144:147],v[48:51],v[112:115],a[144:147]\nds_read_b64_tr_b16 v[88:89],v136 offset:32768\nv_mfma_f32_16x16x32_bf16 a[148:151],v[48:51],v[116:119],a[148:151]\nbuffer_load_dwordx4 v140,s[28:31],s23 offen offset:2048 lds\nv_mfma_f32_16x16x32_bf16 a[180:183],v[52:55],v[116:119],a[180:183]\nds_read_b64_tr_b16 v[90:91],v136 offset:34816\n"
"v_mfma_f32_16x16x32_bf16 a[176:179],v[52:55],v[112:115],a[176:179]\nv_mfma_f32_16x16x32_bf16 a[208:211],v[56:59],v[112:115],a[208:211]\nds_read_b64_tr_b16 v[92:93],v137 offset:32768\nv_readlane_b32 s48,v144,s55\ns_add_u32 s55,s55,1\nv_mfma_f32_16x16x32_bf16 a[212:215],v[56:59],v[116:119],a[212:215]\nbuffer_load_dwordx4 v141,s[28:31],s44 offen offset:3072 lds\nv_mfma_f32_16x16x32_bf16 a[244:247],v[60:63],v[116:119],a[244:247]\n"
"s_add_u32 s28,s28,0x40000\nds_read_b64_tr_b16 v[94:95],v137 offset:34816\nv_readlane_b32 s49,v144,s55\nv_mfma_f32_16x16x32_bf16 a[240:243],v[60:63],v[112:115],a[240:243]\ns_addc_u32 s29,s29,0\ns_add_u32 s55,s55,1\nv_mfma_f32_16x16x32_bf16 a[248:251],v[60:63],v[120:123],a[248:251]\nv_readlane_b32 s50,v144,s55\ns_add_u32 s55,s55,1\nv_mfma_f32_16x16x32_bf16 a[252:255],v[60:63],v[124:127],a[252:255]\nv_readlane_b32 s45,v144,s55\n"
"s_add_u32 s55,s55,1\nv_mfma_f32_16x16x32_bf16 a[220:223],v[56:59],v[124:127],a[220:223]\nv_readlane_b32 s47,v144,s55\ns_add_u32 s55,s55,1\nv_mfma_f32_16x16x32_bf16 a[216:219],v[56:59],v[120:123],a[216:219]\nv_readlane_b32 s53,v144,s55\ns_add_u32 s55,s55,1\nv_mfma_f32_16x16x32_bf16 a[184:187],v[52:55],v[120:123],a[184:187]\nv_readlane_b32 s54,v144,s55\ns_add_u32 s55,s55,2\nv_mfma_f32_16x16x32_bf16 a[188:191],v[52:55],v[124:127],a[188:191]\n"
"s_lshl_b32 s46,s45,3\nv_mad_u32_u24 v149,v145,s45,v148\ns_sub_u32 s46,s46,0x400\nv_mfma_f32_16x16x32_bf16 a[156:159],v[48:51],v[124:127],a[156:159]\ns_sub_u32 s47,s47,0x800\ns_add_u32 s52,s47,s46\nv_mfma_f32_16x16x32_bf16 a[152:155],v[48:51],v[120:123],a[152:155]\nv_mfma_f32_16x16x32_bf16 a[120:123],v[44:47],v[120:123],a[120:123]\nv_mfma_f32_16x16x32_bf16 a[124:127],v[44:47],v[124:127],a[124:127]\n"
"v_mfma_f32_16x16x32_bf16 a[92:95],v[40:43],v[124:127],a[92:95]\ns_waitcnt vmcnt(20) lgkmcnt(0)\ns_barrier\nv_mfma_f32_16x16x32_bf16 a[88:91],v[40:43],v[120:123],a[88:91]\nv_mfma_f32_16x16x32_bf16 a[56:59],v[36:39],v[120:123],a[56:59]\nv_mfma_f32_16x16x32_bf16 a[60:63],v[36:39],v[124:127],a[60:63]\nv_mfma_f32_16x16x32_bf16 a[28:31],v[32:35],v[124:127],a[28:31]\nv_mfma_f32_16x16x32_bf16 a[24:27],v[32:35],v[120:123],a[24:27]\n"
"s_mov_b32 s42,6\nL_fused_block_loop_10:\nv_mfma_f32_16x16x32_bf16 a[0:3],v[0:3],v[64:67],a[0:3]\ns_add_u32 m0,s15,32768\nds_read_b64_tr_b16 v[96:97],v130 offset:49152\nv_mfma_f32_16x16x32_bf16 a[4:7],v[0:3],v[68:71],a[4:7]\nbuffer_load_dwordx4 v140,s[28:31],s21 offen lds\nv_mfma_f32_16x16x32_bf16 a[36:39],v[4:7],v[68:71],a[36:39]\nds_read_b64_tr_b16 v[98:99],v130 offset:51200\nv_mfma_f32_16x16x32_bf16 a[32:35],v[4:7],v[64:67],a[32:35]\n"
"v_mfma_f32_16x16x32_bf16 a[64:67],v[8:11],v[64:67],a[64:67]\nds_read_b64_tr_b16 v[100:101],v131 offset:49152\nv_mfma_f32_16x16x32_bf16 a[68:71],v[8:11],v[68:71],a[68:71]\nbuffer_load_dwordx4 v141,s[28:31],s22 offen offset:1024 lds\nv_mfma_f32_16x16x32_bf16 a[100:103],v[12:15],v[68:71],a[100:103]\nds_read_b64_tr_b16 v[102:103],v131 offset:51200\nv_mfma_f32_16x16x32_bf16 a[96:99],v[12:15],v[64:67],a[96:99]\n"
"v_mfma_f32_16x16x32_bf16 a[128:131],v[16:19],v[64:67],a[128:131]\nds_read_b128 v[32:35],v129 offset:32768\nv_mfma_f32_16x16x32_bf16 a[132:135],v[16:19],v[68:71],a[132:135]\nbuffer_load_dwordx4 v140,s[28:31],s23 offen offset:2048 lds\nv_mfma_f32_16x16x32_bf16 a[164:167],v[20:23],v[68:71],a[164:167]\nds_read_b128 v[36:39],v129 offset:34816\nv_mfma_f32_16x16x32_bf16 a[160:163],v[20:23],v[64:67],a[160:163]\n"
"v_mfma_f32_16x16x32_bf16 a[192:195],v[24:27],v[64:67],a[192:195]\nds_read_b128 v[40:43],v129 offset:36864\nv_mfma_f32_16x16x32_bf16 a[196:199],v[24:27],v[68:71],a[196:199]\nbuffer_load_dwordx4 v141,s[28:31],s44 offen offset:3072 lds\nv_mfma_f32_16x16x32_bf16 a[228:231],v[28:31],v[68:71],a[228:231]\ns_add_u32 s28,s28,0x40000\nds_read_b128 v[44:47],v129 offset:38912\nv_mfma_f32_16x16x32_bf16 a[224:227],v[28:31],v[64:67],a[224:227]\n"
"s_addc_u32 s29,s29,0\ns_mov_b32 m0,s65\nv_mfma_f32_16x16x32_bf16 a[232:235],v[28:31],v[72:75],a[232:235]\nds_read_b128 v[48:51],v129 offset:40960\nv_mfma_f32_16x16x32_bf16 a[236:239],v[28:31],v[76:79],a[236:239]\nbuffer_load_dwordx4 v149,s[48:51],0 offen lds\nv_mfma_f32_16x16x32_bf16 a[204:207],v[24:27],v[76:79],a[204:207]\nds_read_b128 v[52:55],v129 offset:43008\nv_mfma_f32_16x16x32_bf16 a[200:203],v[24:27],v[72:75],a[200:203]\n"
"v_mfma_f32_16x16x32_bf16 a[168:171],v[20:23],v[72:75],a[168:171]\nds_read_b128 v[56:59],v129 offset:45056\nv_mfma_f32_16x16x32_bf16 a[172:175],v[20:23],v[76:79],a[172:175]\nbuffer_load_dwordx4 v149,s[48:51],s46 offen offset:1024 lds\nv_mfma_f32_16x16x32_bf16 a[140:143],v[16:19],v[76:79],a[140:143]\nds_read_b128 v[60:63],v129 offset:47104\nv_mfma_f32_16x16x32_bf16 a[136:139],v[16:19],v[72:75],a[136:139]\n"
"v_mfma_f32_16x16x32_bf16 a[104:107],v[12:15],v[72:75],a[104:107]\nds_read_b64_tr_b16 v[104:105],v132 offset:49152\nv_mfma_f32_16x16x32_bf16 a[108:111],v[12:15],v[76:79],a[108:111]\nbuffer_load_dwordx4 v149,s[48:51],s47 offen offset:2048 lds\nv_mfma_f32_16x16x32_bf16 a[76:79],v[8:11],v[76:79],a[76:79]\nds_read_b64_tr_b16 v[106:107],v132 offset:51200\nv_mfma_f32_16x16x32_bf16 a[72:75],v[8:11],v[72:75],a[72:75]\n"
"v_mfma_f32_16x16x32_bf16 a[40:43],v[4:7],v[72:75],a[40:43]\nds_read_b64_tr_b16 v[108:109],v133 offset:49152\nv_mfma_f32_16x16x32_bf16 a[44:47],v[4:7],v[76:79],a[44:47]\nbuffer_load_dwordx4 v149,s[48:51],s52 offen offset:3072 lds\nv_mfma_f32_16x16x32_bf16 a[12:15],v[0:3],v[76:79],a[12:15]\nds_read_b64_tr_b16 v[110:111],v133 offset:51200\nv_mfma_f32_16x16x32_bf16 a[8:11],v[0:3],v[72:75],a[8:11]\nv_mfma_f32_16x16x32_bf16 a[16:19],v[0:3],v[80:83],a[16:19]\n"
"ds_read_b64_tr_b16 v[112:113],v134 offset:49152\nv_mfma_f32_16x16x32_bf16 a[20:23],v[0:3],v[84:87],a[20:23]\nbuffer_store_dwordx4 v[152:155],v252,s[8:11],s56 offen sc1\nv_mfma_f32_16x16x32_bf16 a[52:55],v[4:7],v[84:87],a[52:55]\nds_read_b64_tr_b16 v[114:115],v134 offset:51200\nv_mfma_f32_16x16x32_bf16 a[48:51],v[4:7],v[80:83],a[48:51]\nv_mfma_f32_16x16x32_bf16 a[80:83],v[8:11],v[80:83],a[80:83]\nds_read_b64_tr_b16 v[116:117],v135 offset:49152\n"
"v_mfma_f32_16x16x32_bf16 a[84:87],v[8:11],v[84:87],a[84:87]\nv_mfma_f32_16x16x32_bf16 a[116:119],v[12:15],v[84:87],a[116:119]\nds_read_b64_tr_b16 v[118:119],v135 offset:51200\nv_mfma_f32_16x16x32_bf16 a[112:115],v[12:15],v[80:83],a[112:115]\nbuffer_store_dwordx4 v[156:159],v252,s[8:11],s57 offen sc1\nv_mfma_f32_16x16x32_bf16 a[144:147],v[16:19],v[80:83],a[144:147]\nds_read_b64_tr_b16 v[120:121],v136 offset:49152\n"
"v_mfma_f32_16x16x32_bf16 a[148:151],v[16:19],v[84:87],a[148:151]\nv_mfma_f32_16x16x32_bf16 a[180:183],v[20:23],v[84:87],a[180:183]\nds_read_b64_tr_b16 v[122:123],v136 offset:51200\nv_mfma_f32_16x16x32_bf16 a[176:179],v[20:23],v[80:83],a[176:179]\nv_mfma_f32_16x16x32_bf16 a[208:211],v[24:27],v[80:83],a[208:211]\nds_read_b64_tr_b16 v[124:125],v137 offset:49152\nv_mfma_f32_16x16x32_bf16 a[212:215],v[24:27],v[84:87],a[212:215]\n"
"buffer_store_dwordx4 v[160:163],v252,s[8:11],s58 offen sc1\nv_mfma_f32_16x16x32_bf16 a[244:247],v[28:31],v[84:87],a[244:247]\nds_read_b64_tr_b16 v[126:127],v137 offset:51200\nv_mfma_f32_16x16x32_bf16 a[240:243],v[28:31],v[80:83],a[240:243]\nv_mfma_f32_16x16x32_bf16 a[248:251],v[28:31],v[88:91],a[248:251]\nv_mfma_f32_16x16x32_bf16 a[252:255],v[28:31],v[92:95],a[252:255]\nv_mfma_f32_16x16x32_bf16 a[220:223],v[24:27],v[92:95],a[220:223]\n"
"v_mfma_f32_16x16x32_bf16 a[216:219],v[24:27],v[88:91],a[216:219]\nbuffer_store_dwordx4 v[164:167],v252,s[8:11],s59 offen sc1\nv_mfma_f32_16x16x32_bf16 a[184:187],v[20:23],v[88:91],a[184:187]\nv_mfma_f32_16x16x32_bf16 a[188:191],v[20:23],v[92:95],a[188:191]\nv_mfma_f32_16x16x32_bf16 a[156:159],v[16:19],v[92:95],a[156:159]\nv_mfma_f32_16x16x32_bf16 a[152:155],v[16:19],v[88:91],a[152:155]\nv_mfma_f32_16x16x32_bf16 a[120:123],v[12:15],v[88:91],a[120:123]\n"
"v_mfma_f32_16x16x32_bf16 a[124:127],v[12:15],v[92:95],a[124:127]\nv_mfma_f32_16x16x32_bf16 a[92:95],v[8:11],v[92:95],a[92:95]\ns_waitcnt vmcnt(16) lgkmcnt(0)\ns_barrier\nv_mfma_f32_16x16x32_bf16 a[88:91],v[8:11],v[88:91],a[88:91]\nv_mfma_f32_16x16x32_bf16 a[56:59],v[4:7],v[88:91],a[56:59]\nv_mfma_f32_16x16x32_bf16 a[60:63],v[4:7],v[92:95],a[60:63]\nv_mfma_f32_16x16x32_bf16 a[28:31],v[0:3],v[92:95],a[28:31]\n"
"v_mfma_f32_16x16x32_bf16 a[24:27],v[0:3],v[88:91],a[24:27]\nv_mfma_f32_16x16x32_bf16 a[0:3],v[32:35],v[96:99],a[0:3]\ns_add_u32 m0,s43,32768\nds_read_b64_tr_b16 v[64:65],v130 offset:0\nv_mfma_f32_16x16x32_bf16 a[4:7],v[32:35],v[100:103],a[4:7]\nbuffer_load_dwordx4 v138,s[24:27],s32 offen lds\nv_mfma_f32_16x16x32_bf16 a[36:39],v[36:39],v[100:103],a[36:39]\nds_read_b64_tr_b16 v[66:67],v130 offset:2048\n"
"v_mfma_f32_16x16x32_bf16 a[32:35],v[36:39],v[96:99],a[32:35]\nv_mfma_f32_16x16x32_bf16 a[64:67],v[40:43],v[96:99],a[64:67]\nds_read_b64_tr_b16 v[68:69],v131 offset:0\nv_mfma_f32_16x16x32_bf16 a[68:71],v[40:43],v[100:103],a[68:71]\nbuffer_load_dwordx4 v139,s[24:27],s33 offen offset:1024 lds\nv_mfma_f32_16x16x32_bf16 a[100:103],v[44:47],v[100:103],a[100:103]\nds_read_b64_tr_b16 v[70:71],v131 offset:2048\n"
"v_mfma_f32_16x16x32_bf16 a[96:99],v[44:47],v[96:99],a[96:99]\nv_mfma_f32_16x16x32_bf16 a[128:131],v[48:51],v[96:99],a[128:131]\nds_read_b128 v[0:3],v128 offset:0\nv_mfma_f32_16x16x32_bf16 a[132:135],v[48:51],v[100:103],a[132:135]\nbuffer_load_dwordx4 v138,s[24:27],s34 offen offset:2048 lds\nv_mfma_f32_16x16x32_bf16 a[164:167],v[52:55],v[100:103],a[164:167]\nds_read_b128 v[4:7],v128 offset:2048\nv_mfma_f32_16x16x32_bf16 a[160:163],v[52:55],v[96:99],a[160:163]\n"
"v_mfma_f32_16x16x32_bf16 a[192:195],v[56:59],v[96:99],a[192:195]\nds_read_b128 v[8:11],v128 offset:4096\nv_mfma_f32_16x16x32_bf16 a[196:199],v[56:59],v[100:103],a[196:199]\nbuffer_load_dwordx4 v139,s[24:27],s35 offen offset:3072 lds\nv_mfma_f32_16x16x32_bf16 a[228:231],v[60:63],v[100:103],a[228:231]\nds_read_b128 v[12:15],v128 offset:6144\nv_mfma_f32_16x16x32_bf16 a[224:227],v[60:63],v[96:99],a[224:227]\n"
"v_mfma_f32_16x16x32_bf16 a[232:235],v[60:63],v[104:107],a[232:235]\ns_add_u32 m0,s43,36864\nds_read_b128 v[16:19],v128 offset:8192\nv_mfma_f32_16x16x32_bf16 a[236:239],v[60:63],v[108:111],a[236:239]\nbuffer_load_dwordx4 v138,s[24:27],s36 offen lds\nv_mfma_f32_16x16x32_bf16 a[204:207],v[56:59],v[108:111],a[204:207]\nds_read_b128 v[20:23],v128 offset:10240\nv_mfma_f32_16x16x32_bf16 a[200:203],v[56:59],v[104:107],a[200:203]\n"
"v_mfma_f32_16x16x32_bf16 a[168:171],v[52:55],v[104:107],a[168:171]\nds_read_b128 v[24:27],v128 offset:12288\nv_mfma_f32_16x16x32_bf16 a[172:175],v[52:55],v[108:111],a[172:175]\nbuffer_load_dwordx4 v139,s[24:27],s37 offen offset:1024 lds\nv_mfma_f32_16x16x32_bf16 a[140:143],v[48:51],v[108:111],a[140:143]\nds_read_b128 v[28:31],v128 offset:14336\nv_mfma_f32_16x16x32_bf16 a[136:139],v[48:51],v[104:107],a[136:139]\n"
"v_mfma_f32_16x16x32_bf16 a[104:107],v[44:47],v[104:107],a[104:107]\nds_read_b64_tr_b16 v[72:73],v132 offset:0\nv_mfma_f32_16x16x32_bf16 a[108:111],v[44:47],v[108:111],a[108:111]\nbuffer_load_dwordx4 v138,s[24:27],s38 offen offset:2048 lds\nv_mfma_f32_16x16x32_bf16 a[76:79],v[40:43],v[108:111],a[76:79]\nds_read_b64_tr_b16 v[74:75],v132 offset:2048\nv_mfma_f32_16x16x32_bf16 a[72:75],v[40:43],v[104:107],a[72:75]\n"
"v_mfma_f32_16x16x32_bf16 a[40:43],v[36:39],v[104:107],a[40:43]\nds_read_b64_tr_b16 v[76:77],v133 offset:0\nv_mfma_f32_16x16x32_bf16 a[44:47],v[36:39],v[108:111],a[44:47]\nbuffer_load_dwordx4 v139,s[24:27],s39 offen offset:3072 lds\nv_mfma_f32_16x16x32_bf16 a[12:15],v[32:35],v[108:111],a[12:15]\ns_add_u32 s24,s24,128\nds_read_b64_tr_b16 v[78:79],v133 offset:2048\nv_mfma_f32_16x16x32_bf16 a[8:11],v[32:35],v[104:107],a[8:11]\n"
"s_addc_u32 s25,s25,0\nv_mfma_f32_16x16x32_bf16 a[16:19],v[32:35],v[112:115],a[16:19]\ns_add_u32 m0,s15,49152\nds_read_b64_tr_b16 v[80:81],v134 offset:0\nv_mfma_f32_16x16x32_bf16 a[20:23],v[32:35],v[116:119],a[20:23]\nbuffer_load_dwordx4 v140,s[28:31],s21 offen lds\nv_mfma_f32_16x16x32_bf16 a[52:55],v[36:39],v[116:119],a[52:55]\nds_read_b64_tr_b16 v[82:83],v134 offset:2048\nv_mfma_f32_16x16x32_bf16 a[48:51],v[36:39],v[112:115],a[48:51]\n"
"v_mfma_f32_16x16x32_bf16 a[80:83],v[40:43],v[112:115],a[80:83]\nds_read_b64_tr_b16 v[84:85],v135 offset:0\nv_mfma_f32_16x16x32_bf16 a[84:87],v[40:43],v[116:119],a[84:87]\nbuffer_load_dwordx4 v141,s[28:31],s22 offen offset:1024 lds\nv_mfma_f32_16x16x32_bf16 a[116:119],v[44:47],v[116:119],a[116:119]\nds_read_b64_tr_b16 v[86:87],v135 offset:2048\nv_mfma_f32_16x16x32_bf16 a[112:115],v[44:47],v[112:115],a[112:115]\n"
"v_mfma_f32_16x16x32_bf16 a[144:147],v[48:51],v[112:115],a[144:147]\nds_read_b64_tr_b16 v[88:89],v136 offset:0\nv_mfma_f32_16x16x32_bf16 a[148:151],v[48:51],v[116:119],a[148:151]\nbuffer_load_dwordx4 v140,s[28:31],s23 offen offset:2048 lds\nv_mfma_f32_16x16x32_bf16 a[180:183],v[52:55],v[116:119],a[180:183]\nds_read_b64_tr_b16 v[90:91],v136 offset:2048\nv_mfma_f32_16x16x32_bf16 a[176:179],v[52:55],v[112:115],a[176:179]\n"
"v_mfma_f32_16x16x32_bf16 a[208:211],v[56:59],v[112:115],a[208:211]\nds_read_b64_tr_b16 v[92:93],v137 offset:0\nv_mfma_f32_16x16x32_bf16 a[212:215],v[56:59],v[116:119],a[212:215]\nbuffer_load_dwordx4 v141,s[28:31],s44 offen offset:3072 lds\nv_mfma_f32_16x16x32_bf16 a[244:247],v[60:63],v[116:119],a[244:247]\ns_add_u32 s28,s28,0x40000\nds_read_b64_tr_b16 v[94:95],v137 offset:2048\nv_mfma_f32_16x16x32_bf16 a[240:243],v[60:63],v[112:115],a[240:243]\n"
"s_addc_u32 s29,s29,0\nv_mfma_f32_16x16x32_bf16 a[248:251],v[60:63],v[120:123],a[248:251]\nv_mfma_f32_16x16x32_bf16 a[252:255],v[60:63],v[124:127],a[252:255]\nv_mfma_f32_16x16x32_bf16 a[220:223],v[56:59],v[124:127],a[220:223]\nv_mfma_f32_16x16x32_bf16 a[216:219],v[56:59],v[120:123],a[216:219]\nv_mfma_f32_16x16x32_bf16 a[184:187],v[52:55],v[120:123],a[184:187]\nv_mfma_f32_16x16x32_bf16 a[188:191],v[52:55],v[124:127],a[188:191]\n"
"v_mfma_f32_16x16x32_bf16 a[156:159],v[48:51],v[124:127],a[156:159]\nv_mfma_f32_16x16x32_bf16 a[152:155],v[48:51],v[120:123],a[152:155]\nv_mfma_f32_16x16x32_bf16 a[120:123],v[44:47],v[120:123],a[120:123]\nv_mfma_f32_16x16x32_bf16 a[124:127],v[44:47],v[124:127],a[124:127]\nv_mfma_f32_16x16x32_bf16 a[92:95],v[40:43],v[124:127],a[92:95]\ns_waitcnt vmcnt(24) lgkmcnt(0)\ns_barrier\nv_mfma_f32_16x16x32_bf16 a[88:91],v[40:43],v[120:123],a[88:91]\n"
"v_mfma_f32_16x16x32_bf16 a[56:59],v[36:39],v[120:123],a[56:59]\nv_mfma_f32_16x16x32_bf16 a[60:63],v[36:39],v[124:127],a[60:63]\nv_mfma_f32_16x16x32_bf16 a[28:31],v[32:35],v[124:127],a[28:31]\nv_mfma_f32_16x16x32_bf16 a[24:27],v[32:35],v[120:123],a[24:27]\nv_mfma_f32_16x16x32_bf16 a[0:3],v[0:3],v[64:67],a[0:3]\ns_add_u32 m0,s15,0\nds_read_b64_tr_b16 v[96:97],v130 offset:16384\nv_mfma_f32_16x16x32_bf16 a[4:7],v[0:3],v[68:71],a[4:7]\n"
"buffer_load_dwordx4 v140,s[28:31],s21 offen lds\nv_mfma_f32_16x16x32_bf16 a[36:39],v[4:7],v[68:71],a[36:39]\nds_read_b64_tr_b16 v[98:99],v130 offset:18432\nv_mfma_f32_16x16x32_bf16 a[32:35],v[4:7],v[64:67],a[32:35]\nv_mfma_f32_16x16x32_bf16 a[64:67],v[8:11],v[64:67],a[64:67]\nds_read_b64_tr_b16 v[100:101],v131 offset:16384\nv_mfma_f32_16x16x32_bf16 a[68:71],v[8:11],v[68:71],a[68:71]\nbuffer_load_dwordx4 v141,s[28:31],s22 offen offset:1024 lds\n"
"v_mfma_f32_16x16x32_bf16 a[100:103],v[12:15],v[68:71],a[100:103]\nds_read_b64_tr_b16 v[102:103],v131 offset:18432\nv_mfma_f32_16x16x32_bf16 a[96:99],v[12:15],v[64:67],a[96:99]\nv_mfma_f32_16x16x32_bf16 a[128:131],v[16:19],v[64:67],a[128:131]\nds_read_b128 v[32:35],v129 offset:0\nv_mfma_f32_16x16x32_bf16 a[132:135],v[16:19],v[68:71],a[132:135]\nbuffer_load_dwordx4 v140,s[28:31],s23 offen offset:2048 lds\n"
"v_mfma_f32_16x16x32_bf16 a[164:167],v[20:23],v[68:71],a[164:167]\nds_read_b128 v[36:39],v129 offset:2048\nv_mfma_f32_16x16x32_bf16 a[160:163],v[20:23],v[64:67],a[160:163]\nv_mfma_f32_16x16x32_bf16 a[192:195],v[24:27],v[64:67],a[192:195]\nds_read_b128 v[40:43],v129 offset:4096\nv_mfma_f32_16x16x32_bf16 a[196:199],v[24:27],v[68:71],a[196:199]\nbuffer_load_dwordx4 v141,s[28:31],s44 offen offset:3072 lds\n"
"v_mfma_f32_16x16x32_bf16 a[228:231],v[28:31],v[68:71],a[228:231]\ns_add_u32 s28,s28,0x40000\nds_read_b128 v[44:47],v129 offset:6144\nv_mfma_f32_16x16x32_bf16 a[224:227],v[28:31],v[64:67],a[224:227]\ns_addc_u32 s29,s29,0\nv_mfma_f32_16x16x32_bf16 a[232:235],v[28:31],v[72:75],a[232:235]\nds_read_b128 v[48:51],v129 offset:8192\nv_mfma_f32_16x16x32_bf16 a[236:239],v[28:31],v[76:79],a[236:239]\nbuffer_load_dword v151,v150,s[60:63],s78 offen\n"
"s_add_u32 s78,s78,s64\nv_mfma_f32_16x16x32_bf16 a[204:207],v[24:27],v[76:79],a[204:207]\nds_read_b128 v[52:55],v129 offset:10240\nv_mfma_f32_16x16x32_bf16 a[200:203],v[24:27],v[72:75],a[200:203]\nv_mfma_f32_16x16x32_bf16 a[168:171],v[20:23],v[72:75],a[168:171]\nds_read_b128 v[56:59],v129 offset:12288\nv_mfma_f32_16x16x32_bf16 a[172:175],v[20:23],v[76:79],a[172:175]\nv_mfma_f32_16x16x32_bf16 a[140:143],v[16:19],v[76:79],a[140:143]\n"
"ds_read_b128 v[60:63],v129 offset:14336\nv_mfma_f32_16x16x32_bf16 a[136:139],v[16:19],v[72:75],a[136:139]\nv_mfma_f32_16x16x32_bf16 a[104:107],v[12:15],v[72:75],a[104:107]\nds_read_b64_tr_b16 v[104:105],v132 offset:16384\nv_mfma_f32_16x16x32_bf16 a[108:111],v[12:15],v[76:79],a[108:111]\nv_mfma_f32_16x16x32_bf16 a[76:79],v[8:11],v[76:79],a[76:79]\nds_read_b64_tr_b16 v[106:107],v132 offset:18432\nv_mfma_f32_16x16x32_bf16 a[72:75],v[8:11],v[72:75],a[72:75]\n"
"v_mfma_f32_16x16x32_bf16 a[40:43],v[4:7],v[72:75],a[40:43]\nds_read_b64_tr_b16 v[108:109],v133 offset:16384\nv_mfma_f32_16x16x32_bf16 a[44:47],v[4:7],v[76:79],a[44:47]\nv_mfma_f32_16x16x32_bf16 a[12:15],v[0:3],v[76:79],a[12:15]\nds_read_b64_tr_b16 v[110:111],v133 offset:18432\nv_mfma_f32_16x16x32_bf16 a[8:11],v[0:3],v[72:75],a[8:11]\nv_mfma_f32_16x16x32_bf16 a[16:19],v[0:3],v[80:83],a[16:19]\nds_read_b64_tr_b16 v[112:113],v134 offset:16384\n"
"v_mfma_f32_16x16x32_bf16 a[20:23],v[0:3],v[84:87],a[20:23]\nv_mfma_f32_16x16x32_bf16 a[52:55],v[4:7],v[84:87],a[52:55]\nds_read_b64_tr_b16 v[114:115],v134 offset:18432\nv_mfma_f32_16x16x32_bf16 a[48:51],v[4:7],v[80:83],a[48:51]\nv_mfma_f32_16x16x32_bf16 a[80:83],v[8:11],v[80:83],a[80:83]\nds_read_b64_tr_b16 v[116:117],v135 offset:16384\nv_mfma_f32_16x16x32_bf16 a[84:87],v[8:11],v[84:87],a[84:87]\nv_mfma_f32_16x16x32_bf16 a[116:119],v[12:15],v[84:87],a[116:119]\n"
"ds_read_b64_tr_b16 v[118:119],v135 offset:18432\nv_mfma_f32_16x16x32_bf16 a[112:115],v[12:15],v[80:83],a[112:115]\nv_mfma_f32_16x16x32_bf16 a[144:147],v[16:19],v[80:83],a[144:147]\nds_read_b64_tr_b16 v[120:121],v136 offset:16384\nv_mfma_f32_16x16x32_bf16 a[148:151],v[16:19],v[84:87],a[148:151]\nv_mfma_f32_16x16x32_bf16 a[180:183],v[20:23],v[84:87],a[180:183]\nds_read_b64_tr_b16 v[122:123],v136 offset:18432\n"
"v_mfma_f32_16x16x32_bf16 a[176:179],v[20:23],v[80:83],a[176:179]\nv_mfma_f32_16x16x32_bf16 a[208:211],v[24:27],v[80:83],a[208:211]\nds_read_b64_tr_b16 v[124:125],v137 offset:16384\nv_mfma_f32_16x16x32_bf16 a[212:215],v[24:27],v[84:87],a[212:215]\nv_mfma_f32_16x16x32_bf16 a[244:247],v[28:31],v[84:87],a[244:247]\nds_read_b64_tr_b16 v[126:127],v137 offset:18432\nv_mfma_f32_16x16x32_bf16 a[240:243],v[28:31],v[80:83],a[240:243]\n"
"v_mfma_f32_16x16x32_bf16 a[248:251],v[28:31],v[88:91],a[248:251]\nv_mfma_f32_16x16x32_bf16 a[252:255],v[28:31],v[92:95],a[252:255]\nv_mfma_f32_16x16x32_bf16 a[220:223],v[24:27],v[92:95],a[220:223]\nv_mfma_f32_16x16x32_bf16 a[216:219],v[24:27],v[88:91],a[216:219]\nv_mfma_f32_16x16x32_bf16 a[184:187],v[20:23],v[88:91],a[184:187]\nv_mfma_f32_16x16x32_bf16 a[188:191],v[20:23],v[92:95],a[188:191]\nv_mfma_f32_16x16x32_bf16 a[156:159],v[16:19],v[92:95],a[156:159]\n"
"v_mfma_f32_16x16x32_bf16 a[152:155],v[16:19],v[88:91],a[152:155]\nv_mfma_f32_16x16x32_bf16 a[120:123],v[12:15],v[88:91],a[120:123]\nv_mfma_f32_16x16x32_bf16 a[124:127],v[12:15],v[92:95],a[124:127]\nv_mfma_f32_16x16x32_bf16 a[92:95],v[8:11],v[92:95],a[92:95]\ns_waitcnt vmcnt(9) lgkmcnt(0)\ns_barrier\nv_mfma_f32_16x16x32_bf16 a[88:91],v[8:11],v[88:91],a[88:91]\nv_mfma_f32_16x16x32_bf16 a[56:59],v[4:7],v[88:91],a[56:59]\n"
"v_mfma_f32_16x16x32_bf16 a[60:63],v[4:7],v[92:95],a[60:63]\nv_mfma_f32_16x16x32_bf16 a[28:31],v[0:3],v[92:95],a[28:31]\nv_mfma_f32_16x16x32_bf16 a[24:27],v[0:3],v[88:91],a[24:27]\nv_mfma_f32_16x16x32_bf16 a[0:3],v[32:35],v[96:99],a[0:3]\ns_add_u32 m0,s43,0\nds_read_b64_tr_b16 v[64:65],v130 offset:32768\nv_mfma_f32_16x16x32_bf16 a[4:7],v[32:35],v[100:103],a[4:7]\nbuffer_load_dwordx4 v138,s[24:27],s32 offen lds\n"
"v_mfma_f32_16x16x32_bf16 a[36:39],v[36:39],v[100:103],a[36:39]\ns_mov_b32 s56,s53\nds_read_b64_tr_b16 v[66:67],v130 offset:34816\ns_add_u32 s57,s56,s54\nv_mfma_f32_16x16x32_bf16 a[32:35],v[36:39],v[96:99],a[32:35]\ns_add_u32 s58,s57,s54\ns_add_u32 s59,s58,s54\nds_read_b64_tr_b16 v[152:153],v244 offset:0\nv_mfma_f32_16x16x32_bf16 a[64:67],v[40:43],v[96:99],a[64:67]\nds_read_b64_tr_b16 v[68:69],v131 offset:32768\n"
"v_mfma_f32_16x16x32_bf16 a[68:71],v[40:43],v[100:103],a[68:71]\nbuffer_load_dwordx4 v139,s[24:27],s33 offen offset:1024 lds\nv_mfma_f32_16x16x32_bf16 a[100:103],v[44:47],v[100:103],a[100:103]\nds_read_b64_tr_b16 v[70:71],v131 offset:34816\nv_mfma_f32_16x16x32_bf16 a[96:99],v[44:47],v[96:99],a[96:99]\nds_read_b64_tr_b16 v[154:155],v244 offset:2048\nv_mfma_f32_16x16x32_bf16 a[128:131],v[48:51],v[96:99],a[128:131]\n"
"ds_read_b128 v[0:3],v128 offset:32768\nv_mfma_f32_16x16x32_bf16 a[132:135],v[48:51],v[100:103],a[132:135]\nbuffer_load_dwordx4 v138,s[24:27],s34 offen offset:2048 lds\nv_mfma_f32_16x16x32_bf16 a[164:167],v[52:55],v[100:103],a[164:167]\nds_read_b128 v[4:7],v128 offset:34816\nv_mfma_f32_16x16x32_bf16 a[160:163],v[52:55],v[96:99],a[160:163]\nds_read_b64_tr_b16 v[156:157],v245 offset:0\nv_mfma_f32_16x16x32_bf16 a[192:195],v[56:59],v[96:99],a[192:195]\n"
"ds_read_b128 v[8:11],v128 offset:36864\nv_readlane_b32 s48,v144,s55\ns_add_u32 s55,s55,1\nv_mfma_f32_16x16x32_bf16 a[196:199],v[56:59],v[100:103],a[196:199]\nbuffer_load_dwordx4 v139,s[24:27],s35 offen offset:3072 lds\nv_mfma_f32_16x16x32_bf16 a[228:231],v[60:63],v[100:103],a[228:231]\nds_read_b128 v[12:15],v128 offset:38912\nv_readlane_b32 s49,v144,s55\ns_add_u32 s55,s55,1\nv_mfma_f32_16x16x32_bf16 a[224:227],v[60:63],v[96:99],a[224:227]\n"
"v_readlane_b32 s50,v144,s55\ns_add_u32 s55,s55,1\nds_read_b64_tr_b16 v[158:159],v245 offset:2048\nv_mfma_f32_16x16x32_bf16 a[232:235],v[60:63],v[104:107],a[232:235]\ns_add_u32 m0,s43,4096\nds_read_b128 v[16:19],v128 offset:40960\nv_readlane_b32 s45,v144,s55\nv_mfma_f32_16x16x32_bf16 a[236:239],v[60:63],v[108:111],a[236:239]\ns_add_u32 s55,s55,1\nbuffer_load_dwordx4 v138,s[24:27],s36 offen lds\nv_mfma_f32_16x16x32_bf16 a[204:207],v[56:59],v[108:111],a[204:207]\n"
"ds_read_b128 v[20:23],v128 offset:43008\nv_readlane_b32 s47,v144,s55\ns_add_u32 s55,s55,1\nv_mfma_f32_16x16x32_bf16 a[200:203],v[56:59],v[104:107],a[200:203]\nv_readlane_b32 s53,v144,s55\ns_add_u32 s55,s55,1\nds_read_b64_tr_b16 v[160:161],v246 offset:0\nv_mfma_f32_16x16x32_bf16 a[168:171],v[52:55],v[104:107],a[168:171]\nds_read_b128 v[24:27],v128 offset:45056\nv_readlane_b32 s54,v144,s55\ns_add_u32 s55,s55,2\n"
"v_mfma_f32_16x16x32_bf16 a[172:175],v[52:55],v[108:111],a[172:175]\nbuffer_load_dwordx4 v139,s[24:27],s37 offen offset:1024 lds\nv_mfma_f32_16x16x32_bf16 a[140:143],v[48:51],v[108:111],a[140:143]\nds_read_b128 v[28:31],v128 offset:47104\nv_mad_u32_u24 v149,v145,s45,v148\nv_mfma_f32_16x16x32_bf16 a[136:139],v[48:51],v[104:107],a[136:139]\ns_lshl_b32 s46,s45,3\nds_read_b64_tr_b16 v[162:163],v246 offset:2048\n"
"v_mfma_f32_16x16x32_bf16 a[104:107],v[44:47],v[104:107],a[104:107]\ns_sub_u32 s46,s46,0x400\nds_read_b64_tr_b16 v[72:73],v132 offset:32768\nv_mfma_f32_16x16x32_bf16 a[108:111],v[44:47],v[108:111],a[108:111]\ns_sub_u32 s47,s47,0x800\nbuffer_load_dwordx4 v138,s[24:27],s38 offen offset:2048 lds\nv_mfma_f32_16x16x32_bf16 a[76:79],v[40:43],v[108:111],a[76:79]\ns_add_u32 s52,s47,s46\nds_read_b64_tr_b16 v[74:75],v132 offset:34816\n"
"v_mfma_f32_16x16x32_bf16 a[72:75],v[40:43],v[104:107],a[72:75]\nds_read_b64_tr_b16 v[164:165],v247 offset:0\nv_mfma_f32_16x16x32_bf16 a[40:43],v[36:39],v[104:107],a[40:43]\nds_read_b64_tr_b16 v[76:77],v133 offset:32768\nv_mfma_f32_16x16x32_bf16 a[44:47],v[36:39],v[108:111],a[44:47]\nbuffer_load_dwordx4 v139,s[24:27],s39 offen offset:3072 lds\nv_mfma_f32_16x16x32_bf16 a[12:15],v[32:35],v[108:111],a[12:15]\n"
"s_add_u32 s24,s24,128\nds_read_b64_tr_b16 v[78:79],v133 offset:34816\nv_mfma_f32_16x16x32_bf16 a[8:11],v[32:35],v[104:107],a[8:11]\ns_addc_u32 s25,s25,0\nds_read_b64_tr_b16 v[166:167],v247 offset:2048\nv_mfma_f32_16x16x32_bf16 a[16:19],v[32:35],v[112:115],a[16:19]\ns_add_u32 m0,s15,16384\nds_read_b64_tr_b16 v[80:81],v134 offset:32768\nv_mfma_f32_16x16x32_bf16 a[20:23],v[32:35],v[116:119],a[20:23]\n"
"buffer_load_dwordx4 v140,s[28:31],s21 offen lds\nv_mfma_f32_16x16x32_bf16 a[52:55],v[36:39],v[116:119],a[52:55]\nds_read_b64_tr_b16 v[82:83],v134 offset:34816\nv_mfma_f32_16x16x32_bf16 a[48:51],v[36:39],v[112:115],a[48:51]\nv_mfma_f32_16x16x32_bf16 a[80:83],v[40:43],v[112:115],a[80:83]\nds_read_b64_tr_b16 v[84:85],v135 offset:32768\nv_mfma_f32_16x16x32_bf16 a[84:87],v[40:43],v[116:119],a[84:87]\nbuffer_load_dwordx4 v141,s[28:31],s22 offen offset:1024 lds\n"
"v_mfma_f32_16x16x32_bf16 a[116:119],v[44:47],v[116:119],a[116:119]\nds_read_b64_tr_b16 v[86:87],v135 offset:34816\nv_mfma_f32_16x16x32_bf16 a[112:115],v[44:47],v[112:115],a[112:115]\nv_mfma_f32_16x16x32_bf16 a[144:147],v[48:51],v[112:115],a[144:147]\ns_cmp_eq_u32 s42,2\nds_read_b64_tr_b16 v[88:89],v136 offset:32768\nv_mfma_f32_16x16x32_bf16 a[148:151],v[48:51],v[116:119],a[148:151]\nbuffer_load_dwordx4 v140,s[28:31],s23 offen offset:2048 lds\n"
"s_cbranch_scc1 L_fused_block_pub0_11\nL_fused_block_pub0r_12:\nv_mfma_f32_16x16x32_bf16 a[180:183],v[52:55],v[116:119],a[180:183]\nds_read_b64_tr_b16 v[90:91],v136 offset:34816\nv_mfma_f32_16x16x32_bf16 a[176:179],v[52:55],v[112:115],a[176:179]\nv_mfma_f32_16x16x32_bf16 a[208:211],v[56:59],v[112:115],a[208:211]\nds_read_b64_tr_b16 v[92:93],v137 offset:32768\nv_mfma_f32_16x16x32_bf16 a[212:215],v[56:59],v[116:119],a[212:215]\n"
"buffer_load_dwordx4 v141,s[28:31],s44 offen offset:3072 lds\nv_mfma_f32_16x16x32_bf16 a[244:247],v[60:63],v[116:119],a[244:247]\ns_add_u32 s28,s28,0x40000\nds_read_b64_tr_b16 v[94:95],v137 offset:34816\nv_mfma_f32_16x16x32_bf16 a[240:243],v[60:63],v[112:115],a[240:243]\ns_addc_u32 s29,s29,0\nv_mfma_f32_16x16x32_bf16 a[248:251],v[60:63],v[120:123],a[248:251]\nv_mfma_f32_16x16x32_bf16 a[252:255],v[60:63],v[124:127],a[252:255]\n"
"v_mfma_f32_16x16x32_bf16 a[220:223],v[56:59],v[124:127],a[220:223]\nv_mfma_f32_16x16x32_bf16 a[216:219],v[56:59],v[120:123],a[216:219]\nv_mfma_f32_16x16x32_bf16 a[184:187],v[52:55],v[120:123],a[184:187]\nv_mfma_f32_16x16x32_bf16 a[188:191],v[52:55],v[124:127],a[188:191]\nv_mfma_f32_16x16x32_bf16 a[156:159],v[48:51],v[124:127],a[156:159]\nv_mfma_f32_16x16x32_bf16 a[152:155],v[48:51],v[120:123],a[152:155]\n"
"v_mfma_f32_16x16x32_bf16 a[120:123],v[44:47],v[120:123],a[120:123]\nv_mfma_f32_16x16x32_bf16 a[124:127],v[44:47],v[124:127],a[124:127]\nv_mfma_f32_16x16x32_bf16 a[92:95],v[40:43],v[124:127],a[92:95]\ns_waitcnt vmcnt(17) lgkmcnt(0)\ns_barrier\nv_mfma_f32_16x16x32_bf16 a[88:91],v[40:43],v[120:123],a[88:91]\nv_mfma_f32_16x16x32_bf16 a[56:59],v[36:39],v[120:123],a[56:59]\nv_mfma_f32_16x16x32_bf16 a[60:63],v[36:39],v[124:127],a[60:63]\n"
"v_mfma_f32_16x16x32_bf16 a[28:31],v[32:35],v[124:127],a[28:31]\nv_mfma_f32_16x16x32_bf16 a[24:27],v[32:35],v[120:123],a[24:27]\ns_sub_u32 s42,s42,1\ns_cmp_lg_u32 s42,0\ns_cbranch_scc1 L_fused_block_loop_10\nv_mfma_f32_16x16x32_bf16 a[0:3],v[0:3],v[64:67],a[0:3]\ns_add_u32 m0,s15,32768\nds_read_b64_tr_b16 v[96:97],v130 offset:49152\nv_mfma_f32_16x16x32_bf16 a[4:7],v[0:3],v[68:71],a[4:7]\nbuffer_load_dwordx4 v140,s[28:31],s21 offen lds\n"
"v_mfma_f32_16x16x32_bf16 a[36:39],v[4:7],v[68:71],a[36:39]\nds_read_b64_tr_b16 v[98:99],v130 offset:51200\nv_mfma_f32_16x16x32_bf16 a[32:35],v[4:7],v[64:67],a[32:35]\nv_lshlrev_b32 v226,7,v253\nv_mfma_f32_16x16x32_bf16 a[64:67],v[8:11],v[64:67],a[64:67]\nds_read_b64_tr_b16 v[100:101],v131 offset:49152\nv_mfma_f32_16x16x32_bf16 a[68:71],v[8:11],v[68:71],a[68:71]\nbuffer_load_dwordx4 v141,s[28:31],s22 offen offset:1024 lds\n"
"v_mfma_f32_16x16x32_bf16 a[100:103],v[12:15],v[68:71],a[100:103]\nds_read_b64_tr_b16 v[102:103],v131 offset:51200\nv_mfma_f32_16x16x32_bf16 a[96:99],v[12:15],v[64:67],a[96:99]\nv_mfma_f32_16x16x32_bf16 a[128:131],v[16:19],v[64:67],a[128:131]\nds_read_b128 v[32:35],v129 offset:32768\nv_mfma_f32_16x16x32_bf16 a[132:135],v[16:19],v[68:71],a[132:135]\nbuffer_load_dwordx4 v140,s[28:31],s23 offen offset:2048 lds\n"
"v_mfma_f32_16x16x32_bf16 a[164:167],v[20:23],v[68:71],a[164:167]\nds_read_b128 v[36:39],v129 offset:34816\nv_mfma_f32_16x16x32_bf16 a[160:163],v[20:23],v[64:67],a[160:163]\nv_mfma_f32_16x16x32_bf16 a[192:195],v[24:27],v[64:67],a[192:195]\nds_read_b128 v[40:43],v129 offset:36864\nv_mfma_f32_16x16x32_bf16 a[196:199],v[24:27],v[68:71],a[196:199]\nbuffer_load_dwordx4 v141,s[28:31],s44 offen offset:3072 lds\n"
"v_mfma_f32_16x16x32_bf16 a[228:231],v[28:31],v[68:71],a[228:231]\ns_add_u32 s28,s28,0x40000\nds_read_b128 v[44:47],v129 offset:38912\nv_mfma_f32_16x16x32_bf16 a[224:227],v[28:31],v[64:67],a[224:227]\ns_addc_u32 s29,s29,0\nv_mfma_f32_16x16x32_bf16 a[232:235],v[28:31],v[72:75],a[232:235]\nds_read_b128 v[48:51],v129 offset:40960\nv_mfma_f32_16x16x32_bf16 a[236:239],v[28:31],v[76:79],a[236:239]\nv_mfma_f32_16x16x32_bf16 a[204:207],v[24:27],v[76:79],a[204:207]\n"
"ds_read_b128 v[52:55],v129 offset:43008\nv_mfma_f32_16x16x32_bf16 a[200:203],v[24:27],v[72:75],a[200:203]\nv_mfma_f32_16x16x32_bf16 a[168:171],v[20:23],v[72:75],a[168:171]\nds_read_b128 v[56:59],v129 offset:45056\nv_mfma_f32_16x16x32_bf16 a[172:175],v[20:23],v[76:79],a[172:175]\nv_mfma_f32_16x16x32_bf16 a[140:143],v[16:19],v[76:79],a[140:143]\nds_read_b128 v[60:63],v129 offset:47104\nv_mfma_f32_16x16x32_bf16 a[136:139],v[16:19],v[72:75],a[136:139]\n"
"v_mfma_f32_16x16x32_bf16 a[104:107],v[12:15],v[72:75],a[104:107]\nds_read_b64_tr_b16 v[104:105],v132 offset:49152\nv_mfma_f32_16x16x32_bf16 a[108:111],v[12:15],v[76:79],a[108:111]\nv_mfma_f32_16x16x32_bf16 a[76:79],v[8:11],v[76:79],a[76:79]\nds_read_b64_tr_b16 v[106:107],v132 offset:51200\nv_mfma_f32_16x16x32_bf16 a[72:75],v[8:11],v[72:75],a[72:75]\nv_mfma_f32_16x16x32_bf16 a[40:43],v[4:7],v[72:75],a[40:43]\n"
"ds_read_b64_tr_b16 v[108:109],v133 offset:49152\nv_mfma_f32_16x16x32_bf16 a[44:47],v[4:7],v[76:79],a[44:47]\nv_mfma_f32_16x16x32_bf16 a[12:15],v[0:3],v[76:79],a[12:15]\nds_read_b64_tr_b16 v[110:111],v133 offset:51200\nv_mfma_f32_16x16x32_bf16 a[8:11],v[0:3],v[72:75],a[8:11]\nv_mfma_f32_16x16x32_bf16 a[16:19],v[0:3],v[80:83],a[16:19]\nds_read_b64_tr_b16 v[112:113],v134 offset:49152\nv_mfma_f32_16x16x32_bf16 a[20:23],v[0:3],v[84:87],a[20:23]\n"
"v_mfma_f32_16x16x32_bf16 a[52:55],v[4:7],v[84:87],a[52:55]\nds_read_b64_tr_b16 v[114:115],v134 offset:51200\nv_mfma_f32_16x16x32_bf16 a[48:51],v[4:7],v[80:83],a[48:51]\nv_mfma_f32_16x16x32_bf16 a[80:83],v[8:11],v[80:83],a[80:83]\nds_read_b64_tr_b16 v[116:117],v135 offset:49152\nv_mfma_f32_16x16x32_bf16 a[84:87],v[8:11],v[84:87],a[84:87]\nv_mfma_f32_16x16x32_bf16 a[116:119],v[12:15],v[84:87],a[116:119]\n"
"ds_read_b64_tr_b16 v[118:119],v135 offset:51200\nv_mfma_f32_16x16x32_bf16 a[112:115],v[12:15],v[80:83],a[112:115]\nv_mfma_f32_16x16x32_bf16 a[144:147],v[16:19],v[80:83],a[144:147]\nds_read_b64_tr_b16 v[120:121],v136 offset:49152\nv_mfma_f32_16x16x32_bf16 a[148:151],v[16:19],v[84:87],a[148:151]\nv_mfma_f32_16x16x32_bf16 a[180:183],v[20:23],v[84:87],a[180:183]\nds_read_b64_tr_b16 v[122:123],v136 offset:51200\n"
"v_mfma_f32_16x16x32_bf16 a[176:179],v[20:23],v[80:83],a[176:179]\nv_mfma_f32_16x16x32_bf16 a[208:211],v[24:27],v[80:83],a[208:211]\nds_read_b64_tr_b16 v[124:125],v137 offset:49152\nv_mfma_f32_16x16x32_bf16 a[212:215],v[24:27],v[84:87],a[212:215]\nv_mfma_f32_16x16x32_bf16 a[244:247],v[28:31],v[84:87],a[244:247]\nds_read_b64_tr_b16 v[126:127],v137 offset:51200\nv_mfma_f32_16x16x32_bf16 a[240:243],v[28:31],v[80:83],a[240:243]\n"
"v_mfma_f32_16x16x32_bf16 a[248:251],v[28:31],v[88:91],a[248:251]\nv_mfma_f32_16x16x32_bf16 a[252:255],v[28:31],v[92:95],a[252:255]\nv_mfma_f32_16x16x32_bf16 a[220:223],v[24:27],v[92:95],a[220:223]\nv_mfma_f32_16x16x32_bf16 a[216:219],v[24:27],v[88:91],a[216:219]\nv_mfma_f32_16x16x32_bf16 a[184:187],v[20:23],v[88:91],a[184:187]\nv_mfma_f32_16x16x32_bf16 a[188:191],v[20:23],v[92:95],a[188:191]\nv_mfma_f32_16x16x32_bf16 a[156:159],v[16:19],v[92:95],a[156:159]\n"
"v_mfma_f32_16x16x32_bf16 a[152:155],v[16:19],v[88:91],a[152:155]\nv_mfma_f32_16x16x32_bf16 a[120:123],v[12:15],v[88:91],a[120:123]\nv_mfma_f32_16x16x32_bf16 a[124:127],v[12:15],v[92:95],a[124:127]\nv_mfma_f32_16x16x32_bf16 a[92:95],v[8:11],v[92:95],a[92:95]\ns_waitcnt vmcnt(8) lgkmcnt(0)\ns_barrier\nv_mfma_f32_16x16x32_bf16 a[88:91],v[8:11],v[88:91],a[88:91]\nv_mfma_f32_16x16x32_bf16 a[56:59],v[4:7],v[88:91],a[56:59]\n"
"v_mfma_f32_16x16x32_bf16 a[60:63],v[4:7],v[92:95],a[60:63]\nv_mfma_f32_16x16x32_bf16 a[28:31],v[0:3],v[92:95],a[28:31]\nv_mfma_f32_16x16x32_bf16 a[24:27],v[0:3],v[88:91],a[24:27]\nv_mfma_f32_16x16x32_bf16 a[0:3],v[32:35],v[96:99],a[0:3]\ns_add_u32 m0,s43,32768\nds_read_b64_tr_b16 v[64:65],v130 offset:0\nv_mfma_f32_16x16x32_bf16 a[4:7],v[32:35],v[100:103],a[4:7]\nbuffer_load_dwordx4 v138,s[24:27],s32 offen lds\n"
"v_mfma_f32_16x16x32_bf16 a[36:39],v[36:39],v[100:103],a[36:39]\nds_read_b64_tr_b16 v[66:67],v130 offset:2048\nv_mfma_f32_16x16x32_bf16 a[32:35],v[36:39],v[96:99],a[32:35]\nv_mfma_f32_16x16x32_bf16 a[64:67],v[40:43],v[96:99],a[64:67]\nds_read_b64_tr_b16 v[68:69],v131 offset:0\nv_mfma_f32_16x16x32_bf16 a[68:71],v[40:43],v[100:103],a[68:71]\nbuffer_load_dwordx4 v139,s[24:27],s33 offen offset:1024 lds\n"
"v_mfma_f32_16x16x32_bf16 a[100:103],v[44:47],v[100:103],a[100:103]\nds_read_b64_tr_b16 v[70:71],v131 offset:2048\nv_mfma_f32_16x16x32_bf16 a[96:99],v[44:47],v[96:99],a[96:99]\nv_mfma_f32_16x16x32_bf16 a[128:131],v[48:51],v[96:99],a[128:131]\nds_read_b128 v[0:3],v128 offset:0\nv_mfma_f32_16x16x32_bf16 a[132:135],v[48:51],v[100:103],a[132:135]\nbuffer_load_dwordx4 v138,s[24:27],s34 offen offset:2048 lds\n"
"v_mfma_f32_16x16x32_bf16 a[164:167],v[52:55],v[100:103],a[164:167]\nds_read_b128 v[4:7],v128 offset:2048\nv_mfma_f32_16x16x32_bf16 a[160:163],v[52:55],v[96:99],a[160:163]\nv_mfma_f32_16x16x32_bf16 a[192:195],v[56:59],v[96:99],a[192:195]\nds_read_b128 v[8:11],v128 offset:4096\nv_mfma_f32_16x16x32_bf16 a[196:199],v[56:59],v[100:103],a[196:199]\nbuffer_load_dwordx4 v139,s[24:27],s35 offen offset:3072 lds\n"
"v_mfma_f32_16x16x32_bf16 a[228:231],v[60:63],v[100:103],a[228:231]\nds_read_b128 v[12:15],v128 offset:6144\nv_mfma_f32_16x16x32_bf16 a[224:227],v[60:63],v[96:99],a[224:227]\nv_mfma_f32_16x16x32_bf16 a[232:235],v[60:63],v[104:107],a[232:235]\ns_add_u32 m0,s43,36864\nds_read_b128 v[16:19],v128 offset:8192\nv_mfma_f32_16x16x32_bf16 a[236:239],v[60:63],v[108:111],a[236:239]\nbuffer_load_dwordx4 v138,s[24:27],s36 offen lds\n"
"v_mfma_f32_16x16x32_bf16 a[204:207],v[56:59],v[108:111],a[204:207]\nds_read_b128 v[20:23],v128 offset:10240\nv_mfma_f32_16x16x32_bf16 a[200:203],v[56:59],v[104:107],a[200:203]\nv_mfma_f32_16x16x32_bf16 a[168:171],v[52:55],v[104:107],a[168:171]\nds_read_b128 v[24:27],v128 offset:12288\nv_mfma_f32_16x16x32_bf16 a[172:175],v[52:55],v[108:111],a[172:175]\nbuffer_load_dwordx4 v139,s[24:27],s37 offen offset:1024 lds\n"
"v_mfma_f32_16x16x32_bf16 a[140:143],v[48:51],v[108:111],a[140:143]\nds_read_b128 v[28:31],v128 offset:14336\nv_mfma_f32_16x16x32_bf16 a[136:139],v[48:51],v[104:107],a[136:139]\nv_mfma_f32_16x16x32_bf16 a[104:107],v[44:47],v[104:107],a[104:107]\nds_read_b64_tr_b16 v[72:73],v132 offset:0\nv_mfma_f32_16x16x32_bf16 a[108:111],v[44:47],v[108:111],a[108:111]\nbuffer_load_dwordx4 v138,s[24:27],s38 offen offset:2048 lds\n"
"v_mfma_f32_16x16x32_bf16 a[76:79],v[40:43],v[108:111],a[76:79]\nds_read_b64_tr_b16 v[74:75],v132 offset:2048\nv_mfma_f32_16x16x32_bf16 a[72:75],v[40:43],v[104:107],a[72:75]\nv_mfma_f32_16x16x32_bf16 a[40:43],v[36:39],v[104:107],a[40:43]\nds_read_b64_tr_b16 v[76:77],v133 offset:0\nv_mfma_f32_16x16x32_bf16 a[44:47],v[36:39],v[108:111],a[44:47]\nbuffer_load_dwordx4 v139,s[24:27],s39 offen offset:3072 lds\n"
"v_mfma_f32_16x16x32_bf16 a[12:15],v[32:35],v[108:111],a[12:15]\ns_add_u32 s24,s24,128\nds_read_b64_tr_b16 v[78:79],v133 offset:2048\nv_mfma_f32_16x16x32_bf16 a[8:11],v[32:35],v[104:107],a[8:11]\ns_addc_u32 s25,s25,0\nv_mfma_f32_16x16x32_bf16 a[16:19],v[32:35],v[112:115],a[16:19]\ns_add_u32 m0,s15,49152\nds_read_b64_tr_b16 v[80:81],v134 offset:0\nv_mfma_f32_16x16x32_bf16 a[20:23],v[32:35],v[116:119],a[20:23]\n"
"buffer_load_dwordx4 v140,s[28:31],s21 offen lds\nv_mfma_f32_16x16x32_bf16 a[52:55],v[36:39],v[116:119],a[52:55]\nds_read_b64_tr_b16 v[82:83],v134 offset:2048\nv_mfma_f32_16x16x32_bf16 a[48:51],v[36:39],v[112:115],a[48:51]\nv_mfma_f32_16x16x32_bf16 a[80:83],v[40:43],v[112:115],a[80:83]\nds_read_b64_tr_b16 v[84:85],v135 offset:0\nv_mfma_f32_16x16x32_bf16 a[84:87],v[40:43],v[116:119],a[84:87]\nbuffer_load_dwordx4 v141,s[28:31],s22 offen offset:1024 lds\n"
"v_mfma_f32_16x16x32_bf16 a[116:119],v[44:47],v[116:119],a[116:119]\nds_read_b64_tr_b16 v[86:87],v135 offset:2048\nv_mfma_f32_16x16x32_bf16 a[112:115],v[44:47],v[112:115],a[112:115]\nv_mfma_f32_16x16x32_bf16 a[144:147],v[48:51],v[112:115],a[144:147]\nds_read_b64_tr_b16 v[88:89],v136 offset:0\nv_mfma_f32_16x16x32_bf16 a[148:151],v[48:51],v[116:119],a[148:151]\nbuffer_load_dwordx4 v140,s[28:31],s23 offen offset:2048 lds\n"
"v_mfma_f32_16x16x32_bf16 a[180:183],v[52:55],v[116:119],a[180:183]\nds_read_b64_tr_b16 v[90:91],v136 offset:2048\nv_mfma_f32_16x16x32_bf16 a[176:179],v[52:55],v[112:115],a[176:179]\nv_mfma_f32_16x16x32_bf16 a[208:211],v[56:59],v[112:115],a[208:211]\nds_read_b64_tr_b16 v[92:93],v137 offset:0\nv_mfma_f32_16x16x32_bf16 a[212:215],v[56:59],v[116:119],a[212:215]\nbuffer_load_dwordx4 v141,s[28:31],s44 offen offset:3072 lds\n"
"v_mfma_f32_16x16x32_bf16 a[244:247],v[60:63],v[116:119],a[244:247]\ns_add_u32 s28,s28,0x40000\nds_read_b64_tr_b16 v[94:95],v137 offset:2048\nv_mfma_f32_16x16x32_bf16 a[240:243],v[60:63],v[112:115],a[240:243]\ns_addc_u32 s29,s29,0\nv_mfma_f32_16x16x32_bf16 a[248:251],v[60:63],v[120:123],a[248:251]\nv_mfma_f32_16x16x32_bf16 a[252:255],v[60:63],v[124:127],a[252:255]\nbuffer_load_dwordx2 v[224:225],v226,s[8:11],s75 offen sc1\n"
"v_mfma_f32_16x16x32_bf16 a[220:223],v[56:59],v[124:127],a[220:223]\nv_mfma_f32_16x16x32_bf16 a[216:219],v[56:59],v[120:123],a[216:219]\nv_mfma_f32_16x16x32_bf16 a[184:187],v[52:55],v[120:123],a[184:187]\nv_mfma_f32_16x16x32_bf16 a[188:191],v[52:55],v[124:127],a[188:191]\nv_mfma_f32_16x16x32_bf16 a[156:159],v[48:51],v[124:127],a[156:159]\nv_mfma_f32_16x16x32_bf16 a[152:155],v[48:51],v[120:123],a[152:155]\n"
"v_mfma_f32_16x16x32_bf16 a[120:123],v[44:47],v[120:123],a[120:123]\nv_mfma_f32_16x16x32_bf16 a[124:127],v[44:47],v[124:127],a[124:127]\nv_mfma_f32_16x16x32_bf16 a[92:95],v[40:43],v[124:127],a[92:95]\ns_waitcnt vmcnt(17) lgkmcnt(0)\ns_barrier\nv_mfma_f32_16x16x32_bf16 a[88:91],v[40:43],v[120:123],a[88:91]\nv_mfma_f32_16x16x32_bf16 a[56:59],v[36:39],v[120:123],a[56:59]\nv_mfma_f32_16x16x32_bf16 a[60:63],v[36:39],v[124:127],a[60:63]\n"
"v_mfma_f32_16x16x32_bf16 a[28:31],v[32:35],v[124:127],a[28:31]\nv_mfma_f32_16x16x32_bf16 a[24:27],v[32:35],v[120:123],a[24:27]\nv_mfma_f32_16x16x32_bf16 a[0:3],v[0:3],v[64:67],a[0:3]\nds_read_b64_tr_b16 v[96:97],v130 offset:16384\nv_mfma_f32_16x16x32_bf16 a[4:7],v[0:3],v[68:71],a[4:7]\nv_mfma_f32_16x16x32_bf16 a[36:39],v[4:7],v[68:71],a[36:39]\nds_read_b64_tr_b16 v[98:99],v130 offset:18432\nv_mfma_f32_16x16x32_bf16 a[32:35],v[4:7],v[64:67],a[32:35]\n"
"v_mfma_f32_16x16x32_bf16 a[64:67],v[8:11],v[64:67],a[64:67]\nds_read_b64_tr_b16 v[100:101],v131 offset:16384\nv_mfma_f32_16x16x32_bf16 a[68:71],v[8:11],v[68:71],a[68:71]\nv_mfma_f32_16x16x32_bf16 a[100:103],v[12:15],v[68:71],a[100:103]\nds_read_b64_tr_b16 v[102:103],v131 offset:18432\nv_mfma_f32_16x16x32_bf16 a[96:99],v[12:15],v[64:67],a[96:99]\nv_mfma_f32_16x16x32_bf16 a[128:131],v[16:19],v[64:67],a[128:131]\n"
"ds_read_b128 v[32:35],v129 offset:0\nv_mfma_f32_16x16x32_bf16 a[132:135],v[16:19],v[68:71],a[132:135]\nv_mfma_f32_16x16x32_bf16 a[164:167],v[20:23],v[68:71],a[164:167]\nds_read_b128 v[36:39],v129 offset:2048\nv_mfma_f32_16x16x32_bf16 a[160:163],v[20:23],v[64:67],a[160:163]\nv_mfma_f32_16x16x32_bf16 a[192:195],v[24:27],v[64:67],a[192:195]\nds_read_b128 v[40:43],v129 offset:4096\nv_mfma_f32_16x16x32_bf16 a[196:199],v[24:27],v[68:71],a[196:199]\n"
"v_mfma_f32_16x16x32_bf16 a[228:231],v[28:31],v[68:71],a[228:231]\nds_read_b128 v[44:47],v129 offset:6144\nv_mfma_f32_16x16x32_bf16 a[224:227],v[28:31],v[64:67],a[224:227]\nv_mfma_f32_16x16x32_bf16 a[232:235],v[28:31],v[72:75],a[232:235]\nds_read_b128 v[48:51],v129 offset:8192\nv_mfma_f32_16x16x32_bf16 a[236:239],v[28:31],v[76:79],a[236:239]\nv_mfma_f32_16x16x32_bf16 a[204:207],v[24:27],v[76:79],a[204:207]\n"
"ds_read_b128 v[52:55],v129 offset:10240\nv_mfma_f32_16x16x32_bf16 a[200:203],v[24:27],v[72:75],a[200:203]\nv_mfma_f32_16x16x32_bf16 a[168:171],v[20:23],v[72:75],a[168:171]\nds_read_b128 v[56:59],v129 offset:12288\nv_mfma_f32_16x16x32_bf16 a[172:175],v[20:23],v[76:79],a[172:175]\nv_mfma_f32_16x16x32_bf16 a[140:143],v[16:19],v[76:79],a[140:143]\nds_read_b128 v[60:63],v129 offset:14336\nv_mfma_f32_16x16x32_bf16 a[136:139],v[16:19],v[72:75],a[136:139]\n"
"v_mfma_f32_16x16x32_bf16 a[104:107],v[12:15],v[72:75],a[104:107]\nds_read_b64_tr_b16 v[104:105],v132 offset:16384\nv_mfma_f32_16x16x32_bf16 a[108:111],v[12:15],v[76:79],a[108:111]\nv_mfma_f32_16x16x32_bf16 a[76:79],v[8:11],v[76:79],a[76:79]\nds_read_b64_tr_b16 v[106:107],v132 offset:18432\nv_mfma_f32_16x16x32_bf16 a[72:75],v[8:11],v[72:75],a[72:75]\nv_mfma_f32_16x16x32_bf16 a[40:43],v[4:7],v[72:75],a[40:43]\n"
"ds_read_b64_tr_b16 v[108:109],v133 offset:16384\nv_mfma_f32_16x16x32_bf16 a[44:47],v[4:7],v[76:79],a[44:47]\nv_mfma_f32_16x16x32_bf16 a[12:15],v[0:3],v[76:79],a[12:15]\nds_read_b64_tr_b16 v[110:111],v133 offset:18432\nv_mfma_f32_16x16x32_bf16 a[8:11],v[0:3],v[72:75],a[8:11]\nv_mfma_f32_16x16x32_bf16 a[16:19],v[0:3],v[80:83],a[16:19]\nds_read_b64_tr_b16 v[112:113],v134 offset:16384\nv_mfma_f32_16x16x32_bf16 a[20:23],v[0:3],v[84:87],a[20:23]\n"
"v_mfma_f32_16x16x32_bf16 a[52:55],v[4:7],v[84:87],a[52:55]\nds_read_b64_tr_b16 v[114:115],v134 offset:18432\nv_mfma_f32_16x16x32_bf16 a[48:51],v[4:7],v[80:83],a[48:51]\nv_mfma_f32_16x16x32_bf16 a[80:83],v[8:11],v[80:83],a[80:83]\nds_read_b64_tr_b16 v[116:117],v135 offset:16384\nv_mfma_f32_16x16x32_bf16 a[84:87],v[8:11],v[84:87],a[84:87]\nv_mfma_f32_16x16x32_bf16 a[116:119],v[12:15],v[84:87],a[116:119]\n"
"ds_read_b64_tr_b16 v[118:119],v135 offset:18432\nv_mfma_f32_16x16x32_bf16 a[112:115],v[12:15],v[80:83],a[112:115]\nv_mfma_f32_16x16x32_bf16 a[144:147],v[16:19],v[80:83],a[144:147]\nds_read_b64_tr_b16 v[120:121],v136 offset:16384\nv_mfma_f32_16x16x32_bf16 a[148:151],v[16:19],v[84:87],a[148:151]\nv_mfma_f32_16x16x32_bf16 a[180:183],v[20:23],v[84:87],a[180:183]\nds_read_b64_tr_b16 v[122:123],v136 offset:18432\n"
"v_mfma_f32_16x16x32_bf16 a[176:179],v[20:23],v[80:83],a[176:179]\nv_mfma_f32_16x16x32_bf16 a[208:211],v[24:27],v[80:83],a[208:211]\nds_read_b64_tr_b16 v[124:125],v137 offset:16384\nv_mfma_f32_16x16x32_bf16 a[212:215],v[24:27],v[84:87],a[212:215]\nv_mfma_f32_16x16x32_bf16 a[244:247],v[28:31],v[84:87],a[244:247]\nds_read_b64_tr_b16 v[126:127],v137 offset:18432\nv_mfma_f32_16x16x32_bf16 a[240:243],v[28:31],v[80:83],a[240:243]\n"
"v_mfma_f32_16x16x32_bf16 a[248:251],v[28:31],v[88:91],a[248:251]\nv_mfma_f32_16x16x32_bf16 a[252:255],v[28:31],v[92:95],a[252:255]\nv_mfma_f32_16x16x32_bf16 a[220:223],v[24:27],v[92:95],a[220:223]\nv_mfma_f32_16x16x32_bf16 a[216:219],v[24:27],v[88:91],a[216:219]\nv_mfma_f32_16x16x32_bf16 a[184:187],v[20:23],v[88:91],a[184:187]\nv_mfma_f32_16x16x32_bf16 a[188:191],v[20:23],v[92:95],a[188:191]\ns_waitcnt vmcnt(0)\n"
"v_cmp_eq_u64 vcc,s[12:13],v[224:225]\nv_mfma_f32_16x16x32_bf16 a[156:159],v[16:19],v[92:95],a[156:159]\nv_mfma_f32_16x16x32_bf16 a[152:155],v[16:19],v[88:91],a[152:155]\ns_cmp_eq_u64 vcc,exec\nv_mfma_f32_16x16x32_bf16 a[120:123],v[12:15],v[88:91],a[120:123]\ns_cbranch_scc0 L_fused_block_slow0_13\nL_fused_block_slow0r_14:\nv_mfma_f32_16x16x32_bf16 a[124:127],v[12:15],v[92:95],a[124:127]\nv_mfma_f32_16x16x32_bf16 a[92:95],v[8:11],v[92:95],a[92:95]\n"
"s_waitcnt lgkmcnt(0)\ns_barrier\nv_mfma_f32_16x16x32_bf16 a[88:91],v[8:11],v[88:91],a[88:91]\nv_mfma_f32_16x16x32_bf16 a[56:59],v[4:7],v[88:91],a[56:59]\nv_mfma_f32_16x16x32_bf16 a[60:63],v[4:7],v[92:95],a[60:63]\nv_mfma_f32_16x16x32_bf16 a[28:31],v[0:3],v[92:95],a[28:31]\nv_mfma_f32_16x16x32_bf16 a[24:27],v[0:3],v[88:91],a[24:27]\nv_mfma_f32_16x16x32_bf16 a[0:3],v[32:35],v[96:99],a[0:3]\ns_add_u32 m0,s15,0\n"
"ds_read_b64_tr_b16 v[64:65],v130 offset:32768\nv_mfma_f32_16x16x32_bf16 a[4:7],v[32:35],v[100:103],a[4:7]\nbuffer_load_dwordx4 v252,s[8:11],s66 offen sc1 lds\nv_mfma_f32_16x16x32_bf16 a[36:39],v[36:39],v[100:103],a[36:39]\nds_read_b64_tr_b16 v[66:67],v130 offset:34816\nv_mfma_f32_16x16x32_bf16 a[32:35],v[36:39],v[96:99],a[32:35]\nv_mfma_f32_16x16x32_bf16 a[64:67],v[40:43],v[96:99],a[64:67]\nds_read_b64_tr_b16 v[68:69],v131 offset:32768\n"
"v_mfma_f32_16x16x32_bf16 a[68:71],v[40:43],v[100:103],a[68:71]\nbuffer_load_dwordx4 v252,s[8:11],s66 offen offset:1024 sc1 lds\nv_mfma_f32_16x16x32_bf16 a[100:103],v[44:47],v[100:103],a[100:103]\nds_read_b64_tr_b16 v[70:71],v131 offset:34816\nv_mfma_f32_16x16x32_bf16 a[96:99],v[44:47],v[96:99],a[96:99]\nv_mfma_f32_16x16x32_bf16 a[128:131],v[48:51],v[96:99],a[128:131]\nds_read_b128 v[0:3],v128 offset:32768\n"
"v_mfma_f32_16x16x32_bf16 a[132:135],v[48:51],v[100:103],a[132:135]\nbuffer_load_dwordx4 v252,s[8:11],s66 offen offset:2048 sc1 lds\nv_mfma_f32_16x16x32_bf16 a[164:167],v[52:55],v[100:103],a[164:167]\nds_read_b128 v[4:7],v128 offset:34816\nv_mfma_f32_16x16x32_bf16 a[160:163],v[52:55],v[96:99],a[160:163]\nv_mfma_f32_16x16x32_bf16 a[192:195],v[56:59],v[96:99],a[192:195]\nds_read_b128 v[8:11],v128 offset:36864\n"
"v_mfma_f32_16x16x32_bf16 a[196:199],v[56:59],v[100:103],a[196:199]\nbuffer_load_dwordx4 v252,s[8:11],s66 offen offset:3072 sc1 lds\nv_mfma_f32_16x16x32_bf16 a[228:231],v[60:63],v[100:103],a[228:231]\nds_read_b128 v[12:15],v128 offset:38912\nv_mfma_f32_16x16x32_bf16 a[224:227],v[60:63],v[96:99],a[224:227]\nv_mfma_f32_16x16x32_bf16 a[232:235],v[60:63],v[104:107],a[232:235]\ns_add_u32 s66,s66,0x4000\n"
"ds_read_b128 v[16:19],v128 offset:40960\ns_add_u32 m0,s15,16384\nv_mfma_f32_16x16x32_bf16 a[236:239],v[60:63],v[108:111],a[236:239]\nbuffer_load_dwordx4 v252,s[8:11],s66 offen sc1 lds\nv_mfma_f32_16x16x32_bf16 a[204:207],v[56:59],v[108:111],a[204:207]\nds_read_b128 v[20:23],v128 offset:43008\nv_mfma_f32_16x16x32_bf16 a[200:203],v[56:59],v[104:107],a[200:203]\nv_mfma_f32_16x16x32_bf16 a[168:171],v[52:55],v[104:107],a[168:171]\n"
"ds_read_b128 v[24:27],v128 offset:45056\nv_mfma_f32_16x16x32_bf16 a[172:175],v[52:55],v[108:111],a[172:175]\nbuffer_load_dwordx4 v252,s[8:11],s66 offen offset:1024 sc1 lds\nv_mfma_f32_16x16x32_bf16 a[140:143],v[48:51],v[108:111],a[140:143]\nds_read_b128 v[28:31],v128 offset:47104\nv_mfma_f32_16x16x32_bf16 a[136:139],v[48:51],v[104:107],a[136:139]\nv_mfma_f32_16x16x32_bf16 a[104:107],v[44:47],v[104:107],a[104:107]\n"
"ds_read_b64_tr_b16 v[72:73],v132 offset:32768\nv_mfma_f32_16x16x32_bf16 a[108:111],v[44:47],v[108:111],a[108:111]\nbuffer_load_dwordx4 v252,s[8:11],s66 offen offset:2048 sc1 lds\nv_mfma_f32_16x16x32_bf16 a[76:79],v[40:43],v[108:111],a[76:79]\nds_read_b64_tr_b16 v[74:75],v132 offset:34816\nv_mfma_f32_16x16x32_bf16 a[72:75],v[40:43],v[104:107],a[72:75]\nv_mfma_f32_16x16x32_bf16 a[40:43],v[36:39],v[104:107],a[40:43]\n"
"ds_read_b64_tr_b16 v[76:77],v133 offset:32768\nv_mfma_f32_16x16x32_bf16 a[44:47],v[36:39],v[108:111],a[44:47]\nbuffer_load_dwordx4 v252,s[8:11],s66 offen offset:3072 sc1 lds\nv_mfma_f32_16x16x32_bf16 a[12:15],v[32:35],v[108:111],a[12:15]\nds_read_b64_tr_b16 v[78:79],v133 offset:34816\nv_mfma_f32_16x16x32_bf16 a[8:11],v[32:35],v[104:107],a[8:11]\nv_mfma_f32_16x16x32_bf16 a[16:19],v[32:35],v[112:115],a[16:19]\n"
"s_add_u32 s66,s66,0x4000\nds_read_b64_tr_b16 v[80:81],v134 offset:32768\ns_add_u32 m0,s15,65536\nv_mfma_f32_16x16x32_bf16 a[20:23],v[32:35],v[116:119],a[20:23]\nbuffer_load_dwordx4 v252,s[8:11],s66 offen sc1 lds\nv_mfma_f32_16x16x32_bf16 a[52:55],v[36:39],v[116:119],a[52:55]\nds_read_b64_tr_b16 v[82:83],v134 offset:34816\nv_mfma_f32_16x16x32_bf16 a[48:51],v[36:39],v[112:115],a[48:51]\nv_mfma_f32_16x16x32_bf16 a[80:83],v[40:43],v[112:115],a[80:83]\n"
"ds_read_b64_tr_b16 v[84:85],v135 offset:32768\nv_mfma_f32_16x16x32_bf16 a[84:87],v[40:43],v[116:119],a[84:87]\nbuffer_load_dwordx4 v252,s[8:11],s66 offen offset:1024 sc1 lds\nv_mfma_f32_16x16x32_bf16 a[116:119],v[44:47],v[116:119],a[116:119]\nds_read_b64_tr_b16 v[86:87],v135 offset:34816\nv_mfma_f32_16x16x32_bf16 a[112:115],v[44:47],v[112:115],a[112:115]\nv_mfma_f32_16x16x32_bf16 a[144:147],v[48:51],v[112:115],a[144:147]\n"
"ds_read_b64_tr_b16 v[88:89],v136 offset:32768\nv_mfma_f32_16x16x32_bf16 a[148:151],v[48:51],v[116:119],a[148:151]\nbuffer_load_dwordx4 v252,s[8:11],s66 offen offset:2048 sc1 lds\nv_mfma_f32_16x16x32_bf16 a[180:183],v[52:55],v[116:119],a[180:183]\nds_read_b64_tr_b16 v[90:91],v136 offset:34816\nv_mfma_f32_16x16x32_bf16 a[176:179],v[52:55],v[112:115],a[176:179]\nv_mfma_f32_16x16x32_bf16 a[208:211],v[56:59],v[112:115],a[208:211]\n"
"ds_read_b64_tr_b16 v[92:93],v137 offset:32768\nv_mfma_f32_16x16x32_bf16 a[212:215],v[56:59],v[116:119],a[212:215]\nbuffer_load_dwordx4 v252,s[8:11],s66 offen offset:3072 sc1 lds\nv_mfma_f32_16x16x32_bf16 a[244:247],v[60:63],v[116:119],a[244:247]\nds_read_b64_tr_b16 v[94:95],v137 offset:34816\nv_mfma_f32_16x16x32_bf16 a[240:243],v[60:63],v[112:115],a[240:243]\nv_mfma_f32_16x16x32_bf16 a[248:251],v[60:63],v[120:123],a[248:251]\n"
"s_add_u32 s66,s66,0x4000\ns_add_u32 m0,s15,81920\nv_mfma_f32_16x16x32_bf16 a[252:255],v[60:63],v[124:127],a[252:255]\nbuffer_load_dwordx4 v252,s[8:11],s66 offen sc1 lds\nv_mfma_f32_16x16x32_bf16 a[220:223],v[56:59],v[124:127],a[220:223]\nv_mfma_f32_16x16x32_bf16 a[216:219],v[56:59],v[120:123],a[216:219]\nv_mfma_f32_16x16x32_bf16 a[184:187],v[52:55],v[120:123],a[184:187]\nbuffer_load_dwordx4 v252,s[8:11],s66 offen offset:1024 sc1 lds\n"
"v_mfma_f32_16x16x32_bf16 a[188:191],v[52:55],v[124:127],a[188:191]\nv_mfma_f32_16x16x32_bf16 a[156:159],v[48:51],v[124:127],a[156:159]\nv_mfma_f32_16x16x32_bf16 a[152:155],v[48:51],v[120:123],a[152:155]\nbuffer_load_dwordx4 v252,s[8:11],s66 offen offset:2048 sc1 lds\nv_mfma_f32_16x16x32_bf16 a[120:123],v[44:47],v[120:123],a[120:123]\nv_mfma_f32_16x16x32_bf16 a[124:127],v[44:47],v[124:127],a[124:127]\n"
"v_mfma_f32_16x16x32_bf16 a[92:95],v[40:43],v[124:127],a[92:95]\ns_waitcnt lgkmcnt(0)\ns_barrier\nbuffer_load_dwordx4 v252,s[8:11],s66 offen offset:3072 sc1 lds\nv_mfma_f32_16x16x32_bf16 a[88:91],v[40:43],v[120:123],a[88:91]\nv_mfma_f32_16x16x32_bf16 a[56:59],v[36:39],v[120:123],a[56:59]\nv_mfma_f32_16x16x32_bf16 a[60:63],v[36:39],v[124:127],a[60:63]\ns_add_u32 s66,s66,0x4000\nv_mfma_f32_16x16x32_bf16 a[28:31],v[32:35],v[124:127],a[28:31]\n"
"v_mfma_f32_16x16x32_bf16 a[24:27],v[32:35],v[120:123],a[24:27]\nv_mfma_f32_16x16x32_bf16 a[0:3],v[0:3],v[64:67],a[0:3]\ns_add_u32 m0,s15,131072\nds_read_b64_tr_b16 v[96:97],v130 offset:49152\nv_mfma_f32_16x16x32_bf16 a[4:7],v[0:3],v[68:71],a[4:7]\nbuffer_load_dwordx4 v252,s[8:11],s66 offen sc1 lds\nv_mfma_f32_16x16x32_bf16 a[36:39],v[4:7],v[68:71],a[36:39]\nds_read_b64_tr_b16 v[98:99],v130 offset:51200\n"
"v_mfma_f32_16x16x32_bf16 a[32:35],v[4:7],v[64:67],a[32:35]\nv_mfma_f32_16x16x32_bf16 a[64:67],v[8:11],v[64:67],a[64:67]\nds_read_b64_tr_b16 v[100:101],v131 offset:49152\nv_mfma_f32_16x16x32_bf16 a[68:71],v[8:11],v[68:71],a[68:71]\nv_mfma_f32_16x16x32_bf16 a[100:103],v[12:15],v[68:71],a[100:103]\nds_read_b64_tr_b16 v[102:103],v131 offset:51200\nv_mfma_f32_16x16x32_bf16 a[96:99],v[12:15],v[64:67],a[96:99]\n"
"buffer_load_dwordx4 v252,s[8:11],s66 offen offset:1024 sc1 lds\nv_mfma_f32_16x16x32_bf16 a[128:131],v[16:19],v[64:67],a[128:131]\nds_read_b128 v[32:35],v129 offset:32768\nv_mfma_f32_16x16x32_bf16 a[132:135],v[16:19],v[68:71],a[132:135]\nv_mfma_f32_16x16x32_bf16 a[164:167],v[20:23],v[68:71],a[164:167]\nds_read_b128 v[36:39],v129 offset:34816\nv_mfma_f32_16x16x32_bf16 a[160:163],v[20:23],v[64:67],a[160:163]\n"
"v_mfma_f32_16x16x32_bf16 a[192:195],v[24:27],v[64:67],a[192:195]\nds_read_b128 v[40:43],v129 offset:36864\nv_mfma_f32_16x16x32_bf16 a[196:199],v[24:27],v[68:71],a[196:199]\nbuffer_load_dwordx4 v252,s[8:11],s66 offen offset:2048 sc1 lds\nv_mfma_f32_16x16x32_bf16 a[228:231],v[28:31],v[68:71],a[228:231]\nds_read_b128 v[44:47],v129 offset:38912\nv_mfma_f32_16x16x32_bf16 a[224:227],v[28:31],v[64:67],a[224:227]\n"
"v_mfma_f32_16x16x32_bf16 a[232:235],v[28:31],v[72:75],a[232:235]\nds_read_b128 v[48:51],v129 offset:40960\nv_mfma_f32_16x16x32_bf16 a[236:239],v[28:31],v[76:79],a[236:239]\nv_mfma_f32_16x16x32_bf16 a[204:207],v[24:27],v[76:79],a[204:207]\nds_read_b128 v[52:55],v129 offset:43008\nv_mfma_f32_16x16x32_bf16 a[200:203],v[24:27],v[72:75],a[200:203]\nbuffer_load_dwordx4 v252,s[8:11],s66 offen offset:3072 sc1 lds\n"
"v_mfma_f32_16x16x32_bf16 a[168:171],v[20:23],v[72:75],a[168:171]\nds_read_b128 v[56:59],v129 offset:45056\nv_mfma_f32_16x16x32_bf16 a[172:175],v[20:23],v[76:79],a[172:175]\nv_mfma_f32_16x16x32_bf16 a[140:143],v[16:19],v[76:79],a[140:143]\nds_read_b128 v[60:63],v129 offset:47104\nv_mfma_f32_16x16x32_bf16 a[136:139],v[16:19],v[72:75],a[136:139]\nv_mfma_f32_16x16x32_bf16 a[104:107],v[12:15],v[72:75],a[104:107]\n"
"s_add_u32 s66,s66,0x4000\nds_read_b64_tr_b16 v[104:105],v132 offset:49152\ns_add_u32 m0,s15,147456\nv_mfma_f32_16x16x32_bf16 a[108:111],v[12:15],v[76:79],a[108:111]\nbuffer_load_dwordx4 v252,s[8:11],s66 offen sc1 lds\nv_mfma_f32_16x16x32_bf16 a[76:79],v[8:11],v[76:79],a[76:79]\nds_read_b64_tr_b16 v[106:107],v132 offset:51200\nv_mfma_f32_16x16x32_bf16 a[72:75],v[8:11],v[72:75],a[72:75]\nv_mfma_f32_16x16x32_bf16 a[40:43],v[4:7],v[72:75],a[40:43]\n"
"ds_read_b64_tr_b16 v[108:109],v133 offset:49152\nv_mfma_f32_16x16x32_bf16 a[44:47],v[4:7],v[76:79],a[44:47]\nv_mfma_f32_16x16x32_bf16 a[12:15],v[0:3],v[76:79],a[12:15]\nds_read_b64_tr_b16 v[110:111],v133 offset:51200\nv_mfma_f32_16x16x32_bf16 a[8:11],v[0:3],v[72:75],a[8:11]\nbuffer_load_dwordx4 v252,s[8:11],s66 offen offset:1024 sc1 lds\nv_mfma_f32_16x16x32_bf16 a[16:19],v[0:3],v[80:83],a[16:19]\nds_read_b64_tr_b16 v[112:113],v134 offset:49152\n"
"v_mfma_f32_16x16x32_bf16 a[20:23],v[0:3],v[84:87],a[20:23]\nv_mfma_f32_16x16x32_bf16 a[52:55],v[4:7],v[84:87],a[52:55]\nds_read_b64_tr_b16 v[114:115],v134 offset:51200\nv_mfma_f32_16x16x32_bf16 a[48:51],v[4:7],v[80:83],a[48:51]\nv_mfma_f32_16x16x32_bf16 a[80:83],v[8:11],v[80:83],a[80:83]\nds_read_b64_tr_b16 v[116:117],v135 offset:49152\nv_mfma_f32_16x16x32_bf16 a[84:87],v[8:11],v[84:87],a[84:87]\nbuffer_load_dwordx4 v252,s[8:11],s66 offen offset:2048 sc1 lds\n"
"v_mfma_f32_16x16x32_bf16 a[116:119],v[12:15],v[84:87],a[116:119]\nds_read_b64_tr_b16 v[118:119],v135 offset:51200\nv_mfma_f32_16x16x32_bf16 a[112:115],v[12:15],v[80:83],a[112:115]\nv_mfma_f32_16x16x32_bf16 a[144:147],v[16:19],v[80:83],a[144:147]\nds_read_b64_tr_b16 v[120:121],v136 offset:49152\nv_mfma_f32_16x16x32_bf16 a[148:151],v[16:19],v[84:87],a[148:151]\nv_mfma_f32_16x16x32_bf16 a[180:183],v[20:23],v[84:87],a[180:183]\n"
"ds_read_b64_tr_b16 v[122:123],v136 offset:51200\nv_mfma_f32_16x16x32_bf16 a[176:179],v[20:23],v[80:83],a[176:179]\nbuffer_load_dwordx4 v252,s[8:11],s66 offen offset:3072 sc1 lds\nv_mfma_f32_16x16x32_bf16 a[208:211],v[24:27],v[80:83],a[208:211]\nds_read_b64_tr_b16 v[124:125],v137 offset:49152\nv_mfma_f32_16x16x32_bf16 a[212:215],v[24:27],v[84:87],a[212:215]\nv_mfma_f32_16x16x32_bf16 a[244:247],v[28:31],v[84:87],a[244:247]\n"
"ds_read_b64_tr_b16 v[126:127],v137 offset:51200\nv_mfma_f32_16x16x32_bf16 a[240:243],v[28:31],v[80:83],a[240:243]\nv_mfma_f32_16x16x32_bf16 a[248:251],v[28:31],v[88:91],a[248:251]\ns_add_u32 s66,s66,0x4000\nv_mfma_f32_16x16x32_bf16 a[252:255],v[28:31],v[92:95],a[252:255]\nv_mfma_f32_16x16x32_bf16 a[220:223],v[24:27],v[92:95],a[220:223]\nv_mfma_f32_16x16x32_bf16 a[216:219],v[24:27],v[88:91],a[216:219]\n"
"v_mfma_f32_16x16x32_bf16 a[184:187],v[20:23],v[88:91],a[184:187]\nv_mfma_f32_16x16x32_bf16 a[188:191],v[20:23],v[92:95],a[188:191]\nv_mfma_f32_16x16x32_bf16 a[156:159],v[16:19],v[92:95],a[156:159]\nv_mfma_f32_16x16x32_bf16 a[152:155],v[16:19],v[88:91],a[152:155]\nv_mfma_f32_16x16x32_bf16 a[120:123],v[12:15],v[88:91],a[120:123]\nv_mfma_f32_16x16x32_bf16 a[124:127],v[12:15],v[92:95],a[124:127]\nv_mfma_f32_16x16x32_bf16 a[92:95],v[8:11],v[92:95],a[92:95]\n"
"s_waitcnt vmcnt(8) lgkmcnt(0)\ns_barrier\nv_mfma_f32_16x16x32_bf16 a[88:91],v[8:11],v[88:91],a[88:91]\nv_mfma_f32_16x16x32_bf16 a[56:59],v[4:7],v[88:91],a[56:59]\nv_mfma_f32_16x16x32_bf16 a[60:63],v[4:7],v[92:95],a[60:63]\nv_mfma_f32_16x16x32_bf16 a[28:31],v[0:3],v[92:95],a[28:31]\nv_mfma_f32_16x16x32_bf16 a[24:27],v[0:3],v[88:91],a[24:27]\nv_mfma_f32_16x16x32_bf16 a[0:3],v[32:35],v[96:99],a[0:3]\n"
"v_mfma_f32_16x16x32_bf16 a[4:7],v[32:35],v[100:103],a[4:7]\nbuffer_load_dwordx4 v[184:187],v252,s[8:11],s67 offen offset:0 sc1\nv_mfma_f32_16x16x32_bf16 a[36:39],v[36:39],v[100:103],a[36:39]\nv_mfma_f32_16x16x32_bf16 a[32:35],v[36:39],v[96:99],a[32:35]\nbuffer_load_dwordx4 v[188:191],v252,s[8:11],s68 offen offset:0 sc1\nv_mfma_f32_16x16x32_bf16 a[64:67],v[40:43],v[96:99],a[64:67]\nv_mfma_f32_16x16x32_bf16 a[68:71],v[40:43],v[100:103],a[68:71]\n"
"buffer_load_dwordx4 v[192:195],v252,s[8:11],s67 offen offset:1024 sc1\nv_mfma_f32_16x16x32_bf16 a[100:103],v[44:47],v[100:103],a[100:103]\nv_mfma_f32_16x16x32_bf16 a[96:99],v[44:47],v[96:99],a[96:99]\nbuffer_load_dwordx4 v[196:199],v252,s[8:11],s68 offen offset:1024 sc1\nv_mfma_f32_16x16x32_bf16 a[128:131],v[48:51],v[96:99],a[128:131]\nv_mfma_f32_16x16x32_bf16 a[132:135],v[48:51],v[100:103],a[132:135]\n"
"buffer_load_dwordx4 v[200:203],v252,s[8:11],s67 offen offset:2048 sc1\nv_mfma_f32_16x16x32_bf16 a[164:167],v[52:55],v[100:103],a[164:167]\nv_mfma_f32_16x16x32_bf16 a[160:163],v[52:55],v[96:99],a[160:163]\nbuffer_load_dwordx4 v[204:207],v252,s[8:11],s68 offen offset:2048 sc1\nv_mfma_f32_16x16x32_bf16 a[192:195],v[56:59],v[96:99],a[192:195]\nv_mfma_f32_16x16x32_bf16 a[196:199],v[56:59],v[100:103],a[196:199]\n"
"buffer_load_dwordx4 v[128:131],v252,s[8:11],s67 offen offset:3072 sc1\nv_mfma_f32_16x16x32_bf16 a[228:231],v[60:63],v[100:103],a[228:231]\nv_mfma_f32_16x16x32_bf16 a[224:227],v[60:63],v[96:99],a[224:227]\ns_add_u32 s67,s67,0x1000\nbuffer_load_dwordx4 v[132:135],v252,s[8:11],s68 offen offset:3072 sc1\nv_mfma_f32_16x16x32_bf16 a[232:235],v[60:63],v[104:107],a[232:235]\nv_mfma_f32_16x16x32_bf16 a[236:239],v[60:63],v[108:111],a[236:239]\n"
"s_add_u32 s68,s68,0x1000\nbuffer_load_dwordx4 v[136:139],v252,s[8:11],s67 offen offset:0 sc1\nv_mfma_f32_16x16x32_bf16 a[204:207],v[56:59],v[108:111],a[204:207]\nv_mfma_f32_16x16x32_bf16 a[200:203],v[56:59],v[104:107],a[200:203]\nbuffer_load_dwordx4 v[140:143],v252,s[8:11],s68 offen offset:0 sc1\nv_mfma_f32_16x16x32_bf16 a[168:171],v[52:55],v[104:107],a[168:171]\nv_mfma_f32_16x16x32_bf16 a[172:175],v[52:55],v[108:111],a[172:175]\n"
"s_add_u32 m0,s15,32768\nv_mfma_f32_16x16x32_bf16 a[140:143],v[48:51],v[108:111],a[140:143]\nbuffer_load_dwordx4 v252,s[8:11],s66 offen sc1 lds\nv_mfma_f32_16x16x32_bf16 a[136:139],v[48:51],v[104:107],a[136:139]\nv_mfma_f32_16x16x32_bf16 a[104:107],v[44:47],v[104:107],a[104:107]\nbuffer_load_dwordx4 v252,s[8:11],s66 offen offset:1024 sc1 lds\nv_mfma_f32_16x16x32_bf16 a[108:111],v[44:47],v[108:111],a[108:111]\n"
"v_mfma_f32_16x16x32_bf16 a[76:79],v[40:43],v[108:111],a[76:79]\nbuffer_load_dwordx4 v252,s[8:11],s66 offen offset:2048 sc1 lds\nv_mfma_f32_16x16x32_bf16 a[72:75],v[40:43],v[104:107],a[72:75]\nv_mfma_f32_16x16x32_bf16 a[40:43],v[36:39],v[104:107],a[40:43]\nbuffer_load_dwordx4 v252,s[8:11],s66 offen offset:3072 sc1 lds\nv_mfma_f32_16x16x32_bf16 a[44:47],v[36:39],v[108:111],a[44:47]\nv_mfma_f32_16x16x32_bf16 a[12:15],v[32:35],v[108:111],a[12:15]\n"
"s_add_u32 s66,s66,0x4000\ns_add_u32 m0,s15,49152\nv_mfma_f32_16x16x32_bf16 a[8:11],v[32:35],v[104:107],a[8:11]\nbuffer_load_dwordx4 v252,s[8:11],s66 offen sc1 lds\nv_mfma_f32_16x16x32_bf16 a[16:19],v[32:35],v[112:115],a[16:19]\nv_mfma_f32_16x16x32_bf16 a[20:23],v[32:35],v[116:119],a[20:23]\nbuffer_load_dwordx4 v252,s[8:11],s66 offen offset:1024 sc1 lds\nv_mfma_f32_16x16x32_bf16 a[52:55],v[36:39],v[116:119],a[52:55]\n"
"v_mfma_f32_16x16x32_bf16 a[48:51],v[36:39],v[112:115],a[48:51]\nbuffer_load_dwordx4 v252,s[8:11],s66 offen offset:2048 sc1 lds\nv_mfma_f32_16x16x32_bf16 a[80:83],v[40:43],v[112:115],a[80:83]\nv_mfma_f32_16x16x32_bf16 a[84:87],v[40:43],v[116:119],a[84:87]\nbuffer_load_dwordx4 v252,s[8:11],s66 offen offset:3072 sc1 lds\nv_mfma_f32_16x16x32_bf16 a[116:119],v[44:47],v[116:119],a[116:119]\nv_mfma_f32_16x16x32_bf16 a[112:115],v[44:47],v[112:115],a[112:115]\n"
"s_add_u32 s66,s66,0x4000\ns_add_u32 m0,s15,98304\nv_mfma_f32_16x16x32_bf16 a[144:147],v[48:51],v[112:115],a[144:147]\nbuffer_load_dwordx4 v252,s[8:11],s66 offen sc1 lds\nv_mfma_f32_16x16x32_bf16 a[148:151],v[48:51],v[116:119],a[148:151]\nv_mfma_f32_16x16x32_bf16 a[180:183],v[52:55],v[116:119],a[180:183]\nbuffer_load_dwordx4 v252,s[8:11],s66 offen offset:1024 sc1 lds\nv_mfma_f32_16x16x32_bf16 a[176:179],v[52:55],v[112:115],a[176:179]\n"
"v_mfma_f32_16x16x32_bf16 a[208:211],v[56:59],v[112:115],a[208:211]\nbuffer_load_dwordx4 v252,s[8:11],s66 offen offset:2048 sc1 lds\nv_mfma_f32_16x16x32_bf16 a[212:215],v[56:59],v[116:119],a[212:215]\nv_mfma_f32_16x16x32_bf16 a[244:247],v[60:63],v[116:119],a[244:247]\nbuffer_load_dwordx4 v252,s[8:11],s66 offen offset:3072 sc1 lds\nv_mfma_f32_16x16x32_bf16 a[240:243],v[60:63],v[112:115],a[240:243]\nv_mfma_f32_16x16x32_bf16 a[248:251],v[60:63],v[120:123],a[248:251]\n"
"s_add_u32 s66,s66,0x4000\nv_mfma_f32_16x16x32_bf16 a[252:255],v[60:63],v[124:127],a[252:255]\nv_mfma_f32_16x16x32_bf16 a[220:223],v[56:59],v[124:127],a[220:223]\nds_read_b128 v[152:155],v252 offset:0\nv_mfma_f32_16x16x32_bf16 a[216:219],v[56:59],v[120:123],a[216:219]\nv_mfma_f32_16x16x32_bf16 a[184:187],v[52:55],v[120:123],a[184:187]\nds_read_b128 v[156:159],v252 offset:1024\nv_mfma_f32_16x16x32_bf16 a[188:191],v[52:55],v[124:127],a[188:191]\n"
"v_mfma_f32_16x16x32_bf16 a[156:159],v[48:51],v[124:127],a[156:159]\nds_read_b128 v[160:163],v252 offset:2048\nv_mfma_f32_16x16x32_bf16 a[152:155],v[48:51],v[120:123],a[152:155]\nv_mfma_f32_16x16x32_bf16 a[120:123],v[44:47],v[120:123],a[120:123]\nds_read_b128 v[164:167],v252 offset:3072\nv_mfma_f32_16x16x32_bf16 a[124:127],v[44:47],v[124:127],a[124:127]\nv_mfma_f32_16x16x32_bf16 a[92:95],v[40:43],v[124:127],a[92:95]\n"
"ds_read_b128 v[168:171],v252 offset:4096\nv_mfma_f32_16x16x32_bf16 a[88:91],v[40:43],v[120:123],a[88:91]\nv_mfma_f32_16x16x32_bf16 a[56:59],v[36:39],v[120:123],a[56:59]\nds_read_b128 v[172:175],v252 offset:5120\nv_mfma_f32_16x16x32_bf16 a[60:63],v[36:39],v[124:127],a[60:63]\nv_mfma_f32_16x16x32_bf16 a[28:31],v[32:35],v[124:127],a[28:31]\ns_waitcnt vmcnt(20)\nds_read_b128 v[176:179],v252 offset:6144\n"
"v_mfma_f32_16x16x32_bf16 a[24:27],v[32:35],v[120:123],a[24:27]\ns_waitcnt lgkmcnt(5)\nL_fused_block_q_4:\nv_mfma_f32_16x16x32_bf16 v[0:3],v[152:155],v[184:187],0\ns_mov_b32 s73,s69\nds_read_b128 v[180:183],v252 offset:7168\nv_mfma_f32_16x16x32_bf16 v[4:7],v[152:155],v[188:191],0\nbuffer_load_dwordx4 v[232:235],v252,s[8:11],s67 offen offset:1024 sc1\nv_mfma_f32_16x16x32_bf16 v[12:15],v[156:159],v[188:191],0\n"
"s_lshl_b32 s55,s20,2\nds_read_b128 v[152:155],v252 offset:8192\ns_sub_u32 s56,64,s55\nv_mfma_f32_16x16x32_bf16 v[8:11],v[156:159],v[184:187],0\ns_waitcnt lgkmcnt(5)\nbuffer_load_dwordx4 v[236:239],v252,s[8:11],s68 offen offset:1024 sc1\nv_mfma_f32_16x16x32_bf16 v[16:19],v[160:163],v[184:187],0\ns_add_u32 s55,s55,4\nds_read_b128 v[156:159],v252 offset:9216\ns_lshl_b32 s57,s16,21\nv_mfma_f32_16x16x32_bf16 v[20:23],v[160:163],v[188:191],0\n"
"s_add_u32 s25,s57,s15\ns_mul_i32 s28,s16,229376\ns_add_u32 s28,s28,122683392\nv_mfma_f32_16x16x32_bf16 v[28:31],v[164:167],v[188:191],0\ns_add_u32 s28,s28,s15\nds_read_b128 v[160:163],v252 offset:10240\ns_add_u32 s29,s28,229376\nv_mfma_f32_16x16x32_bf16 v[24:27],v[164:167],v[184:187],0\ns_waitcnt lgkmcnt(5)\ns_sub_u32 s21,15,s20\nv_accvgpr_read_b32 v208,a0\nv_mfma_f32_16x16x32_bf16 v[32:35],v[168:171],v[184:187],0\n"
"s_add_u32 m0,s15,114688\nds_read_b128 v[164:167],v252 offset:11264\nv_accvgpr_read_b32 v209,a1\nv_mfma_f32_16x16x32_bf16 v[36:39],v[168:171],v[188:191],0\ns_cmp_gt_u32 s56,40\nbuffer_load_dwordx4 v252,s[8:11],s66 offen sc1 lds\nv_mfma_f32_16x16x32_bf16 v[44:47],v[172:175],v[188:191],0\ns_cselect_b32 s22,38,s56\nds_read_b128 v[168:171],v252 offset:12288\nv_accvgpr_read_b32 v210,a2\nv_mfma_f32_16x16x32_bf16 v[40:43],v[172:175],v[184:187],0\n"
"s_waitcnt lgkmcnt(5)\nv_accvgpr_read_b32 v211,a3\nv_accvgpr_read_b32 v212,a32\nv_mfma_f32_16x16x32_bf16 v[48:51],v[176:179],v[184:187],0\ns_cselect_b32 s23,0,1\nds_read_b128 v[172:175],v252 offset:13312\nv_accvgpr_read_b32 v213,a33\nv_mfma_f32_16x16x32_bf16 v[52:55],v[176:179],v[188:191],0\ns_cselect_b32 s24,2,1\nbuffer_load_dwordx4 v252,s[8:11],s66 offen offset:1024 sc1 lds\nv_mfma_f32_16x16x32_bf16 v[60:63],v[180:183],v[188:191],0\n"
"s_cselect_b32 s57,1,0\nds_read_b128 v[176:179],v252 offset:14336\ns_and_b32 s36,s57,s19\nv_mfma_f32_16x16x32_bf16 v[56:59],v[180:183],v[184:187],0\ns_waitcnt lgkmcnt(5)\nv_accvgpr_read_b32 v214,a34\nv_accvgpr_read_b32 v215,a35\nv_mfma_f32_16x16x32_bf16 v[64:67],v[152:155],v[184:187],0\ns_cmp_eq_u32 s19,1\nds_read_b128 v[180:183],v252 offset:15360\nv_cvt_pk_bf16_f32 v216,v208,v209\nv_mfma_f32_16x16x32_bf16 v[68:71],v[152:155],v[188:191],0\n"
"s_cselect_b32 s22,s55,s22\nbuffer_load_dwordx4 v252,s[8:11],s66 offen offset:2048 sc1 lds\nv_mfma_f32_16x16x32_bf16 v[76:79],v[156:159],v[188:191],0\ns_cselect_b32 s23,1,s23\nds_read_b128 v[152:155],v252 offset:16384\nv_cvt_pk_bf16_f32 v217,v210,v211\nv_mfma_f32_16x16x32_bf16 v[72:75],v[156:159],v[184:187],0\ns_waitcnt lgkmcnt(5)\nv_cvt_pk_bf16_f32 v218,v212,v213\nv_cvt_pk_bf16_f32 v219,v214,v215\n"
"v_mfma_f32_16x16x32_bf16 v[80:83],v[160:163],v[184:187],0\ns_cselect_b32 s24,1,s24\nds_read_b128 v[156:159],v252 offset:17408\nv_mfma_f32_16x16x32_bf16 v[84:87],v[160:163],v[188:191],0\ns_cselect_b32 s21,s20,s21\nbuffer_load_dwordx4 v252,s[8:11],s66 offen offset:3072 sc1 lds\nv_mfma_f32_16x16x32_bf16 v[92:95],v[164:167],v[188:191],0\ns_lshl_b32 s27,s22,15\nds_read_b128 v[160:163],v252 offset:18432\n"
"s_add_u32 s27,s27,s25\nv_mfma_f32_16x16x32_bf16 v[88:91],v[164:167],v[184:187],0\ns_waitcnt lgkmcnt(5)\ns_add_u32 s34,s25,1245184\ns_lshl_b32 s35,s56,15\nv_mfma_f32_16x16x32_bf16 v[96:99],v[168:171],v[184:187],0\ns_add_u32 s35,s35,s25\nds_read_b128 v[164:167],v252 offset:19456\ns_sub_u32 s56,s56,38\nv_mfma_f32_16x16x32_bf16 v[100:103],v[168:171],v[188:191],0\ns_add_u32 s66,s66,0x4000\ns_add_u32 s56,s56,s22\n"
"s_add_u32 s69,s69,s70\nv_mfma_f32_16x16x32_bf16 v[108:111],v[172:175],v[188:191],0\ns_cmp_eq_u32 s36,1\nds_read_b128 v[168:171],v252 offset:20480\nv_mfma_f32_16x16x32_bf16 v[104:107],v[172:175],v[184:187],0\ns_waitcnt lgkmcnt(5)\nbuffer_store_dwordx4 v[216:219],v252,s[8:11],s73 offen sc1\nv_mfma_f32_16x16x32_bf16 v[112:115],v[176:179],v[184:187],0\ns_cselect_b32 s24,0,s24\nds_read_b128 v[172:175],v252 offset:21504\n"
"v_accvgpr_read_b32 v220,a4\nv_mfma_f32_16x16x32_bf16 v[116:119],v[176:179],v[188:191],0\ns_cselect_b32 s30,s28,1073741824\nv_accvgpr_read_b32 v221,a5\nv_accvgpr_read_b32 v222,a6\nv_mfma_f32_16x16x32_bf16 v[124:127],v[180:183],v[188:191],0\ns_waitcnt vmcnt(25)\nds_read_b128 v[176:179],v252 offset:22528\ns_cselect_b32 s31,s29,0\nv_accvgpr_read_b32 v223,a7\nv_mfma_f32_16x16x32_bf16 v[120:123],v[180:183],v[184:187],0\n"
"s_waitcnt lgkmcnt(5)\nv_accvgpr_read_b32 v224,a36\nv_accvgpr_read_b32 v225,a37\nv_mfma_f32_16x16x32_bf16 v[0:3],v[152:155],v[192:195],v[0:3]\ns_cselect_b32 s28,s34,s28\nds_read_b128 v[180:183],v252 offset:23552\nv_accvgpr_read_b32 v226,a38\nv_mfma_f32_16x16x32_bf16 v[4:7],v[152:155],v[196:199],v[4:7]\ns_cselect_b32 s29,s35,s29\nbuffer_load_dwordx4 v[184:187],v252,s[8:11],s67 offen offset:2048 sc1\n"
"v_mfma_f32_16x16x32_bf16 v[12:15],v[156:159],v[196:199],v[12:15]\ns_cselect_b32 s56,s56,s22\nds_read_b128 v[152:155],v252 offset:24576\ns_sub_u32 s92,6,s56\nv_mfma_f32_16x16x32_bf16 v[8:11],v[156:159],v[192:195],v[8:11]\ns_waitcnt lgkmcnt(5)\nbuffer_load_dwordx4 v[188:191],v252,s[8:11],s68 offen offset:2048 sc1\nv_mfma_f32_16x16x32_bf16 v[16:19],v[160:163],v[192:195],v[16:19]\ns_lshl_b32 s92,s92,20\n"
"ds_read_b128 v[156:159],v252 offset:25600\ns_lshl3_add_u32 s55,s16,s20\nv_mfma_f32_16x16x32_bf16 v[20:23],v[160:163],v[196:199],v[20:23]\ns_lshl2_add_u32 s55,s55,s14\ns_mul_i32 s40,s55,17408\ns_add_u32 s40,s40,83886080\nv_mfma_f32_16x16x32_bf16 v[28:31],v[164:167],v[196:199],v[28:31]\ns_lshl_b32 s85,s55,14\nds_read_b128 v[160:163],v252 offset:26624\ns_add_u32 s85,s85,33554432\nv_mfma_f32_16x16x32_bf16 v[24:27],v[164:167],v[192:195],v[24:27]\n"
"s_waitcnt lgkmcnt(5)\ns_lshl_b32 s55,s55,7\ns_add_u32 s41,s55,117669888\nv_mfma_f32_16x16x32_bf16 v[32:35],v[168:171],v[192:195],v[32:35]\ns_add_u32 s86,s55,117604352\nds_read_b128 v[164:167],v252 offset:27648\nv_accvgpr_read_b32 v227,a39\nv_mfma_f32_16x16x32_bf16 v[36:39],v[168:171],v[196:199],v[36:39]\ns_cmp_eq_u32 s24,2\nv_cvt_pk_bf16_f32 v228,v220,v221\nv_cvt_pk_bf16_f32 v229,v222,v223\nv_mfma_f32_16x16x32_bf16 v[44:47],v[172:175],v[196:199],v[44:47]\n"
"s_cselect_b32 s93,s85,1073741824\nds_read_b128 v[168:171],v252 offset:28672\nv_cvt_pk_bf16_f32 v230,v224,v225\nv_mfma_f32_16x16x32_bf16 v[40:43],v[172:175],v[192:195],v[40:43]\ns_waitcnt lgkmcnt(5)\nv_cvt_pk_bf16_f32 v231,v226,v227\nv_mfma_f32_16x16x32_bf16 v[48:51],v[176:179],v[192:195],v[48:51]\ns_cselect_b32 s94,s86,1073741824\nds_read_b128 v[172:175],v252 offset:29696\ns_lshl_b32 s95,s16,11\nv_mfma_f32_16x16x32_bf16 v[52:55],v[176:179],v[196:199],v[52:55]\n"
"s_add_u32 s95,s95,117473280\ns_lshr_b32 s55,s17,1\ns_lshl_b32 s55,s55,7\nv_mfma_f32_16x16x32_bf16 v[60:63],v[180:183],v[196:199],v[60:63]\ns_add_u32 s87,s95,s55\nds_read_b128 v[176:179],v252 offset:30720\ns_add_u32 s73,s73,s72\nv_mfma_f32_16x16x32_bf16 v[56:59],v[180:183],v[192:195],v[56:59]\ns_waitcnt lgkmcnt(5)\nbuffer_store_dwordx4 v[228:231],v252,s[8:11],s73 offen sc1\nv_mfma_f32_16x16x32_bf16 v[64:67],v[152:155],v[192:195],v[64:67]\n"
"s_cmp_eq_u32 s14,0\nds_read_b128 v[180:183],v252 offset:31744\nv_accvgpr_read_b32 v208,a8\nv_mfma_f32_16x16x32_bf16 v[68:71],v[152:155],v[196:199],v[68:71]\ns_cselect_b32 s87,s87,1073741824\ns_and_b32 s55,s20,3\nv_accvgpr_read_b32 v209,a9\nv_mfma_f32_16x16x32_bf16 v[76:79],v[156:159],v[196:199],v[76:79]\ns_cmp_eq_u32 s55,0\nds_read_b128 v[152:155],v146 offset:0\nv_accvgpr_read_b32 v210,a10\nv_mfma_f32_16x16x32_bf16 v[72:75],v[156:159],v[192:195],v[72:75]\n"
"s_waitcnt lgkmcnt(5)\nv_accvgpr_read_b32 v211,a11\nv_accvgpr_read_b32 v212,a40\nv_mfma_f32_16x16x32_bf16 v[80:83],v[160:163],v[192:195],v[80:83]\ns_cselect_b32 s54,s87,1073741824\nds_read_b128 v[156:159],v146 offset:1024\ns_or_b32 s55,s24,s20\nv_mfma_f32_16x16x32_bf16 v[84:87],v[160:163],v[196:199],v[84:87]\ns_mov_b32 s53,0\ns_mov_b32 s52,0xffff\nv_accvgpr_read_b32 v213,a41\nv_mfma_f32_16x16x32_bf16 v[92:95],v[164:167],v[196:199],v[92:95]\n"
"s_add_u32 s88,s8,127369216\nds_read_b128 v[160:163],v146 offset:2048\nv_accvgpr_read_b32 v214,a42\nv_mfma_f32_16x16x32_bf16 v[88:91],v[164:167],v[192:195],v[88:91]\ns_waitcnt lgkmcnt(5)\nv_accvgpr_read_b32 v215,a43\nv_cvt_pk_bf16_f32 v216,v208,v209\nv_mfma_f32_16x16x32_bf16 v[96:99],v[168:171],v[192:195],v[96:99]\ns_addc_u32 s89,s9,0\nds_read_b128 v[164:167],v146 offset:3072\ns_mov_b32 s90,0x400000\n"
"v_mfma_f32_16x16x32_bf16 v[100:103],v[168:171],v[196:199],v[100:103]\ns_mov_b32 s91,0x20000\ns_mov_b32 s39,2\ns_mov_b32 s32,0x8000\nv_mfma_f32_16x16x32_bf16 v[108:111],v[172:175],v[196:199],v[108:111]\ns_add_u32 s73,s73,s71\nds_read_b128 v[168:171],v146 offset:4096\nv_cvt_pk_bf16_f32 v217,v210,v211\nv_mfma_f32_16x16x32_bf16 v[104:107],v[172:175],v[192:195],v[104:107]\ns_waitcnt lgkmcnt(5)\nv_cvt_pk_bf16_f32 v218,v212,v213\n"
"v_cvt_pk_bf16_f32 v219,v214,v215\nv_mfma_f32_16x16x32_bf16 v[112:115],v[176:179],v[192:195],v[112:115]\nds_read_b128 v[172:175],v146 offset:5120\nv_mfma_f32_16x16x32_bf16 v[116:119],v[176:179],v[196:199],v[116:119]\nbuffer_store_dwordx4 v[216:219],v252,s[8:11],s73 offen sc1\nv_mfma_f32_16x16x32_bf16 v[124:127],v[180:183],v[196:199],v[124:127]\ns_waitcnt vmcnt(27)\nds_read_b128 v[176:179],v146 offset:6144\n"
"v_accvgpr_read_b32 v220,a12\nv_mfma_f32_16x16x32_bf16 v[120:123],v[180:183],v[192:195],v[120:123]\ns_waitcnt lgkmcnt(5)\nv_accvgpr_read_b32 v221,a13\nv_accvgpr_read_b32 v222,a14\nv_mfma_f32_16x16x32_bf16 v[0:3],v[152:155],v[200:203],v[0:3]\nds_read_b128 v[180:183],v146 offset:7168\nv_accvgpr_read_b32 v223,a15\nv_mfma_f32_16x16x32_bf16 v[4:7],v[152:155],v[204:207],v[4:7]\nbuffer_load_dwordx4 v[192:195],v252,s[8:11],s67 offen offset:3072 sc1\n"
"s_add_u32 s67,s67,0x1000\nv_mfma_f32_16x16x32_bf16 v[12:15],v[156:159],v[204:207],v[12:15]\ns_add_u32 s73,s73,s72\nds_read_b128 v[152:155],v146 offset:8192\nv_accvgpr_read_b32 v224,a44\nv_mfma_f32_16x16x32_bf16 v[8:11],v[156:159],v[200:203],v[8:11]\ns_waitcnt lgkmcnt(5)\nbuffer_load_dwordx4 v[196:199],v252,s[8:11],s68 offen offset:3072 sc1\nv_mfma_f32_16x16x32_bf16 v[16:19],v[160:163],v[200:203],v[16:19]\n"
"s_add_u32 s68,s68,0x1000\nds_read_b128 v[156:159],v146 offset:9216\nv_accvgpr_read_b32 v225,a45\nv_mfma_f32_16x16x32_bf16 v[20:23],v[160:163],v[204:207],v[20:23]\nv_accvgpr_read_b32 v226,a46\nv_accvgpr_read_b32 v227,a47\nv_mfma_f32_16x16x32_bf16 v[28:31],v[164:167],v[204:207],v[28:31]\nds_read_b128 v[160:163],v146 offset:10240\nv_cvt_pk_bf16_f32 v228,v220,v221\nv_mfma_f32_16x16x32_bf16 v[24:27],v[164:167],v[200:203],v[24:27]\n"
"s_waitcnt lgkmcnt(5)\nv_cvt_pk_bf16_f32 v229,v222,v223\nv_cvt_pk_bf16_f32 v230,v224,v225\nv_mfma_f32_16x16x32_bf16 v[32:35],v[168:171],v[200:203],v[32:35]\nds_read_b128 v[164:167],v146 offset:11264\nv_cvt_pk_bf16_f32 v231,v226,v227\nv_mfma_f32_16x16x32_bf16 v[36:39],v[168:171],v[204:207],v[36:39]\nbuffer_store_dwordx4 v[228:231],v252,s[8:11],s73 offen sc1\nv_mfma_f32_16x16x32_bf16 v[44:47],v[172:175],v[204:207],v[44:47]\n"
"ds_read_b128 v[168:171],v146 offset:12288\nv_accvgpr_read_b32 v208,a16\nv_mfma_f32_16x16x32_bf16 v[40:43],v[172:175],v[200:203],v[40:43]\ns_waitcnt lgkmcnt(5)\nv_accvgpr_read_b32 v209,a17\nv_accvgpr_read_b32 v210,a18\nv_mfma_f32_16x16x32_bf16 v[48:51],v[176:179],v[200:203],v[48:51]\ns_add_u32 s73,s73,s71\nds_read_b128 v[172:175],v146 offset:13312\nv_accvgpr_read_b32 v211,a19\nv_mfma_f32_16x16x32_bf16 v[52:55],v[176:179],v[204:207],v[52:55]\n"
"v_accvgpr_read_b32 v212,a48\nv_accvgpr_read_b32 v213,a49\nv_mfma_f32_16x16x32_bf16 v[60:63],v[180:183],v[204:207],v[60:63]\nds_read_b128 v[176:179],v146 offset:14336\nv_accvgpr_read_b32 v214,a50\nv_mfma_f32_16x16x32_bf16 v[56:59],v[180:183],v[200:203],v[56:59]\ns_waitcnt lgkmcnt(5)\nv_accvgpr_read_b32 v215,a51\nv_cvt_pk_bf16_f32 v216,v208,v209\nv_mfma_f32_16x16x32_bf16 v[64:67],v[152:155],v[200:203],v[64:67]\n"
"ds_read_b128 v[180:183],v146 offset:15360\nv_cvt_pk_bf16_f32 v217,v210,v211\nv_mfma_f32_16x16x32_bf16 v[68:71],v[152:155],v[204:207],v[68:71]\nv_cvt_pk_bf16_f32 v218,v212,v213\nv_cvt_pk_bf16_f32 v219,v214,v215\nv_mfma_f32_16x16x32_bf16 v[76:79],v[156:159],v[204:207],v[76:79]\nds_read_b128 v[152:155],v146 offset:16384\nv_mfma_f32_16x16x32_bf16 v[72:75],v[156:159],v[200:203],v[72:75]\ns_waitcnt lgkmcnt(5)\n"
"buffer_store_dwordx4 v[216:219],v252,s[8:11],s73 offen sc1\nv_mfma_f32_16x16x32_bf16 v[80:83],v[160:163],v[200:203],v[80:83]\nds_read_b128 v[156:159],v146 offset:17408\nv_accvgpr_read_b32 v220,a20\nv_mfma_f32_16x16x32_bf16 v[84:87],v[160:163],v[204:207],v[84:87]\ns_add_u32 s73,s73,s72\nv_accvgpr_read_b32 v221,a21\nv_accvgpr_read_b32 v222,a22\nv_mfma_f32_16x16x32_bf16 v[92:95],v[164:167],v[204:207],v[92:95]\n"
"ds_read_b128 v[160:163],v146 offset:18432\nv_accvgpr_read_b32 v223,a23\nv_mfma_f32_16x16x32_bf16 v[88:91],v[164:167],v[200:203],v[88:91]\ns_waitcnt lgkmcnt(5)\nv_accvgpr_read_b32 v224,a52\nv_accvgpr_read_b32 v225,a53\nv_mfma_f32_16x16x32_bf16 v[96:99],v[168:171],v[200:203],v[96:99]\nds_read_b128 v[164:167],v146 offset:19456\nv_accvgpr_read_b32 v226,a54\nv_mfma_f32_16x16x32_bf16 v[100:103],v[168:171],v[204:207],v[100:103]\n"
"v_accvgpr_read_b32 v227,a55\nv_cvt_pk_bf16_f32 v228,v220,v221\nv_mfma_f32_16x16x32_bf16 v[108:111],v[172:175],v[204:207],v[108:111]\nds_read_b128 v[168:171],v146 offset:20480\nv_cvt_pk_bf16_f32 v229,v222,v223\nv_mfma_f32_16x16x32_bf16 v[104:107],v[172:175],v[200:203],v[104:107]\ns_waitcnt lgkmcnt(5)\nv_cvt_pk_bf16_f32 v230,v224,v225\nv_cvt_pk_bf16_f32 v231,v226,v227\nv_mfma_f32_16x16x32_bf16 v[112:115],v[176:179],v[200:203],v[112:115]\n"
"ds_read_b128 v[172:175],v146 offset:21504\nv_mfma_f32_16x16x32_bf16 v[116:119],v[176:179],v[204:207],v[116:119]\nbuffer_store_dwordx4 v[228:231],v252,s[8:11],s73 offen sc1\nv_mfma_f32_16x16x32_bf16 v[124:127],v[180:183],v[204:207],v[124:127]\ns_waitcnt vmcnt(30)\nds_read_b128 v[176:179],v146 offset:22528\nv_accvgpr_read_b32 v208,a24\nv_mfma_f32_16x16x32_bf16 v[120:123],v[180:183],v[200:203],v[120:123]\n"
"s_waitcnt lgkmcnt(5)\nv_accvgpr_read_b32 v209,a25\nv_accvgpr_read_b32 v210,a26\nv_mfma_f32_16x16x32_bf16 v[0:3],v[152:155],v[128:131],v[0:3]\nds_read_b128 v[180:183],v146 offset:23552\nv_accvgpr_read_b32 v211,a27\nv_mfma_f32_16x16x32_bf16 v[4:7],v[152:155],v[132:135],v[4:7]\nbuffer_load_dwordx4 v[200:203],v252,s[8:11],s67 offen offset:0 sc1\nv_mfma_f32_16x16x32_bf16 v[12:15],v[156:159],v[132:135],v[12:15]\n"
"s_add_u32 s73,s73,s71\nds_read_b128 v[152:155],v146 offset:24576\nv_accvgpr_read_b32 v212,a56\nv_mfma_f32_16x16x32_bf16 v[8:11],v[156:159],v[128:131],v[8:11]\ns_waitcnt lgkmcnt(5)\nbuffer_load_dwordx4 v[204:207],v252,s[8:11],s68 offen offset:0 sc1\nv_mfma_f32_16x16x32_bf16 v[16:19],v[160:163],v[128:131],v[16:19]\nds_read_b128 v[156:159],v146 offset:25600\nv_accvgpr_read_b32 v213,a57\nv_mfma_f32_16x16x32_bf16 v[20:23],v[160:163],v[132:135],v[20:23]\n"
"v_accvgpr_read_b32 v214,a58\nv_accvgpr_read_b32 v215,a59\nv_mfma_f32_16x16x32_bf16 v[28:31],v[164:167],v[132:135],v[28:31]\nds_read_b128 v[160:163],v146 offset:26624\nv_cvt_pk_bf16_f32 v216,v208,v209\nv_mfma_f32_16x16x32_bf16 v[24:27],v[164:167],v[128:131],v[24:27]\ns_waitcnt lgkmcnt(5)\nv_cvt_pk_bf16_f32 v217,v210,v211\nv_cvt_pk_bf16_f32 v218,v212,v213\nv_mfma_f32_16x16x32_bf16 v[32:35],v[168:171],v[128:131],v[32:35]\n"
"ds_read_b128 v[164:167],v146 offset:27648\nv_cvt_pk_bf16_f32 v219,v214,v215\nv_mfma_f32_16x16x32_bf16 v[36:39],v[168:171],v[132:135],v[36:39]\nbuffer_store_dwordx4 v[216:219],v252,s[8:11],s73 offen sc1\nv_mfma_f32_16x16x32_bf16 v[44:47],v[172:175],v[132:135],v[44:47]\nds_read_b128 v[168:171],v146 offset:28672\nv_accvgpr_read_b32 v220,a28\nv_mfma_f32_16x16x32_bf16 v[40:43],v[172:175],v[128:131],v[40:43]\n"
"s_waitcnt lgkmcnt(5)\nv_accvgpr_read_b32 v221,a29\nv_accvgpr_read_b32 v222,a30\nv_mfma_f32_16x16x32_bf16 v[48:51],v[176:179],v[128:131],v[48:51]\ns_add_u32 s73,s73,s72\nds_read_b128 v[172:175],v146 offset:29696\nv_accvgpr_read_b32 v223,a31\nv_mfma_f32_16x16x32_bf16 v[52:55],v[176:179],v[132:135],v[52:55]\nv_accvgpr_read_b32 v224,a60\nv_accvgpr_read_b32 v225,a61\nv_mfma_f32_16x16x32_bf16 v[60:63],v[180:183],v[132:135],v[60:63]\n"
"ds_read_b128 v[176:179],v146 offset:30720\nv_accvgpr_read_b32 v226,a62\nv_mfma_f32_16x16x32_bf16 v[56:59],v[180:183],v[128:131],v[56:59]\ns_waitcnt lgkmcnt(5)\nv_accvgpr_read_b32 v227,a63\nv_cvt_pk_bf16_f32 v228,v220,v221\nv_mfma_f32_16x16x32_bf16 v[64:67],v[152:155],v[128:131],v[64:67]\nds_read_b128 v[180:183],v146 offset:31744\nv_cvt_pk_bf16_f32 v229,v222,v223\nv_mfma_f32_16x16x32_bf16 v[68:71],v[152:155],v[132:135],v[68:71]\n"
"s_waitcnt vmcnt(13)\ns_barrier\nv_cvt_pk_bf16_f32 v230,v224,v225\nv_cvt_pk_bf16_f32 v231,v226,v227\nv_mfma_f32_16x16x32_bf16 v[76:79],v[156:159],v[132:135],v[76:79]\ns_add_u32 m0,s15,0\nds_read_b128 v[152:155],v147 offset:0\nv_mfma_f32_16x16x32_bf16 v[72:75],v[156:159],v[128:131],v[72:75]\ns_waitcnt lgkmcnt(5)\nbuffer_load_dwordx4 v252,s[8:11],s66 offen sc1 lds\nv_mfma_f32_16x16x32_bf16 v[80:83],v[160:163],v[128:131],v[80:83]\n"
"ds_read_b128 v[156:159],v147 offset:1024\nv_mfma_f32_16x16x32_bf16 v[84:87],v[160:163],v[132:135],v[84:87]\nbuffer_store_dwordx4 v[228:231],v252,s[8:11],s73 offen sc1\nv_mfma_f32_16x16x32_bf16 v[92:95],v[164:167],v[132:135],v[92:95]\nds_read_b128 v[160:163],v147 offset:2048\nv_accvgpr_read_b32 v208,a64\nv_mfma_f32_16x16x32_bf16 v[88:91],v[164:167],v[128:131],v[88:91]\ns_waitcnt lgkmcnt(5)\nbuffer_load_dwordx4 v252,s[8:11],s66 offen offset:1024 sc1 lds\n"
"v_mfma_f32_16x16x32_bf16 v[96:99],v[168:171],v[128:131],v[96:99]\nds_read_b128 v[164:167],v147 offset:3072\nv_accvgpr_read_b32 v209,a65\nv_mfma_f32_16x16x32_bf16 v[100:103],v[168:171],v[132:135],v[100:103]\nv_accvgpr_read_b32 v210,a66\nv_accvgpr_read_b32 v211,a67\nv_mfma_f32_16x16x32_bf16 v[108:111],v[172:175],v[132:135],v[108:111]\nds_read_b128 v[168:171],v147 offset:4096\nv_accvgpr_read_b32 v212,a96\n"
"v_mfma_f32_16x16x32_bf16 v[104:107],v[172:175],v[128:131],v[104:107]\ns_waitcnt lgkmcnt(5)\nbuffer_load_dwordx4 v252,s[8:11],s66 offen offset:2048 sc1 lds\nv_mfma_f32_16x16x32_bf16 v[112:115],v[176:179],v[128:131],v[112:115]\nds_read_b128 v[172:175],v147 offset:5120\nv_accvgpr_read_b32 v213,a97\nv_mfma_f32_16x16x32_bf16 v[116:119],v[176:179],v[132:135],v[116:119]\nv_accvgpr_read_b32 v214,a98\nv_accvgpr_read_b32 v215,a99\n"
"v_mfma_f32_16x16x32_bf16 v[124:127],v[180:183],v[132:135],v[124:127]\ns_mov_b32 s73,s69\nds_read_b128 v[176:179],v147 offset:6144\nv_cvt_pk_bf16_f32 v216,v208,v209\nv_mfma_f32_16x16x32_bf16 v[120:123],v[180:183],v[128:131],v[120:123]\ns_waitcnt lgkmcnt(5)\nbuffer_load_dwordx4 v252,s[8:11],s66 offen offset:3072 sc1 lds\nv_mfma_f32_16x16x32_bf16 v[0:3],v[152:155],v[136:139],v[0:3]\nds_read_b128 v[180:183],v147 offset:7168\n"
"v_cvt_pk_bf16_f32 v217,v210,v211\nv_mfma_f32_16x16x32_bf16 v[4:7],v[152:155],v[140:143],v[4:7]\nbuffer_load_dwordx4 v[128:131],v252,s[8:11],s67 offen offset:1024 sc1\nv_mfma_f32_16x16x32_bf16 v[12:15],v[156:159],v[140:143],v[12:15]\nds_read_b128 v[152:155],v147 offset:8192\nv_cvt_pk_bf16_f32 v218,v212,v213\nv_mfma_f32_16x16x32_bf16 v[8:11],v[156:159],v[136:139],v[8:11]\ns_waitcnt lgkmcnt(5)\ns_add_u32 s66,s66,0x4000\n"
"s_add_u32 m0,s15,16384\nv_mfma_f32_16x16x32_bf16 v[16:19],v[160:163],v[136:139],v[16:19]\ns_add_u32 s69,s69,s70\nds_read_b128 v[156:159],v147 offset:9216\nv_cvt_pk_bf16_f32 v219,v214,v215\nv_mfma_f32_16x16x32_bf16 v[20:23],v[160:163],v[140:143],v[20:23]\nbuffer_load_dwordx4 v252,s[8:11],s66 offen sc1 lds\nv_mfma_f32_16x16x32_bf16 v[28:31],v[164:167],v[140:143],v[28:31]\nds_read_b128 v[160:163],v147 offset:10240\n"
"v_mfma_f32_16x16x32_bf16 v[24:27],v[164:167],v[136:139],v[24:27]\ns_waitcnt lgkmcnt(5)\nbuffer_load_dwordx4 v[132:135],v252,s[8:11],s68 offen offset:1024 sc1\nv_mfma_f32_16x16x32_bf16 v[32:35],v[168:171],v[136:139],v[32:35]\nds_read_b128 v[164:167],v147 offset:11264\nv_mfma_f32_16x16x32_bf16 v[36:39],v[168:171],v[140:143],v[36:39]\nbuffer_load_dwordx4 v252,s[8:11],s66 offen offset:1024 sc1 lds\nv_mfma_f32_16x16x32_bf16 v[44:47],v[172:175],v[140:143],v[44:47]\n"
"ds_read_b128 v[168:171],v147 offset:12288\nv_mfma_f32_16x16x32_bf16 v[40:43],v[172:175],v[136:139],v[40:43]\ns_waitcnt lgkmcnt(5)\nbuffer_store_dwordx4 v[216:219],v252,s[8:11],s73 offen sc1\nv_mfma_f32_16x16x32_bf16 v[48:51],v[176:179],v[136:139],v[48:51]\nds_read_b128 v[172:175],v147 offset:13312\nv_accvgpr_read_b32 v220,a68\nv_mfma_f32_16x16x32_bf16 v[52:55],v[176:179],v[140:143],v[52:55]\nbuffer_load_dwordx4 v252,s[8:11],s66 offen offset:2048 sc1 lds\n"
"v_mfma_f32_16x16x32_bf16 v[60:63],v[180:183],v[140:143],v[60:63]\nds_read_b128 v[176:179],v147 offset:14336\nv_accvgpr_read_b32 v221,a69\nv_mfma_f32_16x16x32_bf16 v[56:59],v[180:183],v[136:139],v[56:59]\ns_waitcnt lgkmcnt(5)\nv_accvgpr_read_b32 v222,a70\nv_accvgpr_read_b32 v223,a71\nv_mfma_f32_16x16x32_bf16 v[64:67],v[152:155],v[136:139],v[64:67]\nds_read_b128 v[180:183],v147 offset:15360\nv_accvgpr_read_b32 v224,a100\n"
"v_mfma_f32_16x16x32_bf16 v[68:71],v[152:155],v[140:143],v[68:71]\nbuffer_load_dwordx4 v252,s[8:11],s66 offen offset:3072 sc1 lds\nv_mfma_f32_16x16x32_bf16 v[76:79],v[156:159],v[140:143],v[76:79]\ns_add_u32 s73,s73,s72\nds_read_b128 v[152:155],v147 offset:16384\nv_accvgpr_read_b32 v225,a101\nv_mfma_f32_16x16x32_bf16 v[72:75],v[156:159],v[136:139],v[72:75]\ns_waitcnt lgkmcnt(5)\nv_accvgpr_read_b32 v226,a102\n"
"v_accvgpr_read_b32 v227,a103\nv_mfma_f32_16x16x32_bf16 v[80:83],v[160:163],v[136:139],v[80:83]\nds_read_b128 v[156:159],v147 offset:17408\nv_cvt_pk_bf16_f32 v228,v220,v221\nv_mfma_f32_16x16x32_bf16 v[84:87],v[160:163],v[140:143],v[84:87]\ns_add_u32 s66,s66,0x4000\ns_add_u32 m0,s15,65536\nv_cvt_pk_bf16_f32 v229,v222,v223\nv_mfma_f32_16x16x32_bf16 v[92:95],v[164:167],v[140:143],v[92:95]\nds_read_b128 v[160:163],v147 offset:18432\n"
"v_cvt_pk_bf16_f32 v230,v224,v225\nv_mfma_f32_16x16x32_bf16 v[88:91],v[164:167],v[136:139],v[88:91]\ns_waitcnt lgkmcnt(5)\nbuffer_load_dwordx4 v252,s[8:11],s66 offen sc1 lds\nv_mfma_f32_16x16x32_bf16 v[96:99],v[168:171],v[136:139],v[96:99]\nds_read_b128 v[164:167],v147 offset:19456\nv_cvt_pk_bf16_f32 v231,v226,v227\nv_mfma_f32_16x16x32_bf16 v[100:103],v[168:171],v[140:143],v[100:103]\nbuffer_store_dwordx4 v[228:231],v252,s[8:11],s73 offen sc1\n"
"v_mfma_f32_16x16x32_bf16 v[108:111],v[172:175],v[140:143],v[108:111]\nds_read_b128 v[168:171],v147 offset:20480\nv_accvgpr_read_b32 v208,a72\nv_mfma_f32_16x16x32_bf16 v[104:107],v[172:175],v[136:139],v[104:107]\ns_waitcnt lgkmcnt(5)\nbuffer_load_dwordx4 v252,s[8:11],s66 offen offset:1024 sc1 lds\nv_mfma_f32_16x16x32_bf16 v[112:115],v[176:179],v[136:139],v[112:115]\nds_read_b128 v[172:175],v147 offset:21504\n"
"v_accvgpr_read_b32 v209,a73\nv_mfma_f32_16x16x32_bf16 v[116:119],v[176:179],v[140:143],v[116:119]\nv_accvgpr_read_b32 v210,a74\nv_accvgpr_read_b32 v211,a75\nv_mfma_f32_16x16x32_bf16 v[124:127],v[180:183],v[140:143],v[124:127]\nds_read_b128 v[176:179],v147 offset:22528\nv_accvgpr_read_b32 v212,a104\nv_mfma_f32_16x16x32_bf16 v[120:123],v[180:183],v[136:139],v[120:123]\ns_waitcnt lgkmcnt(5)\nbuffer_load_dwordx4 v252,s[8:11],s66 offen offset:2048 sc1 lds\n"
"v_mfma_f32_16x16x32_bf16 v[0:3],v[152:155],v[232:235],v[0:3]\nds_read_b128 v[180:183],v147 offset:23552\nv_accvgpr_read_b32 v213,a105\nv_mfma_f32_16x16x32_bf16 v[4:7],v[152:155],v[236:239],v[4:7]\nbuffer_load_dwordx4 v[136:139],v252,s[8:11],s67 offen offset:2048 sc1\nv_mfma_f32_16x16x32_bf16 v[12:15],v[156:159],v[236:239],v[12:15]\nds_read_b128 v[152:155],v147 offset:24576\nv_accvgpr_read_b32 v214,a106\n"
"v_mfma_f32_16x16x32_bf16 v[8:11],v[156:159],v[232:235],v[8:11]\ns_waitcnt lgkmcnt(5)\nbuffer_load_dwordx4 v252,s[8:11],s66 offen offset:3072 sc1 lds\nv_mfma_f32_16x16x32_bf16 v[16:19],v[160:163],v[232:235],v[16:19]\ns_add_u32 s73,s73,s71\nds_read_b128 v[156:159],v147 offset:25600\nv_accvgpr_read_b32 v215,a107\nv_mfma_f32_16x16x32_bf16 v[20:23],v[160:163],v[236:239],v[20:23]\nbuffer_load_dwordx4 v[140:143],v252,s[8:11],s68 offen offset:2048 sc1\n"
"v_mfma_f32_16x16x32_bf16 v[28:31],v[164:167],v[236:239],v[28:31]\nds_read_b128 v[160:163],v147 offset:26624\nv_cvt_pk_bf16_f32 v216,v208,v209\nv_mfma_f32_16x16x32_bf16 v[24:27],v[164:167],v[232:235],v[24:27]\ns_waitcnt lgkmcnt(5)\ns_add_u32 s66,s66,0x4000\nv_cvt_pk_bf16_f32 v217,v210,v211\nv_mfma_f32_16x16x32_bf16 v[32:35],v[168:171],v[232:235],v[32:35]\nds_read_b128 v[164:167],v147 offset:27648\nv_cvt_pk_bf16_f32 v218,v212,v213\n"
"v_mfma_f32_16x16x32_bf16 v[36:39],v[168:171],v[236:239],v[36:39]\nv_cvt_pk_bf16_f32 v219,v214,v215\nv_mfma_f32_16x16x32_bf16 v[44:47],v[172:175],v[236:239],v[44:47]\nds_read_b128 v[168:171],v147 offset:28672\nv_mfma_f32_16x16x32_bf16 v[40:43],v[172:175],v[232:235],v[40:43]\ns_waitcnt lgkmcnt(5)\nbuffer_store_dwordx4 v[216:219],v252,s[8:11],s73 offen sc1\nv_mfma_f32_16x16x32_bf16 v[48:51],v[176:179],v[232:235],v[48:51]\n"
"ds_read_b128 v[172:175],v147 offset:29696\nv_accvgpr_read_b32 v220,a76\nv_mfma_f32_16x16x32_bf16 v[52:55],v[176:179],v[236:239],v[52:55]\ns_add_u32 s73,s73,s72\nv_accvgpr_read_b32 v221,a77\nv_accvgpr_read_b32 v222,a78\nv_mfma_f32_16x16x32_bf16 v[60:63],v[180:183],v[236:239],v[60:63]\nds_read_b128 v[176:179],v147 offset:30720\nv_accvgpr_read_b32 v223,a79\nv_mfma_f32_16x16x32_bf16 v[56:59],v[180:183],v[232:235],v[56:59]\n"
"s_waitcnt lgkmcnt(5)\nv_accvgpr_read_b32 v224,a108\nv_accvgpr_read_b32 v225,a109\nv_mfma_f32_16x16x32_bf16 v[64:67],v[152:155],v[232:235],v[64:67]\nds_read_b128 v[180:183],v147 offset:31744\nv_accvgpr_read_b32 v226,a110\nv_mfma_f32_16x16x32_bf16 v[68:71],v[152:155],v[236:239],v[68:71]\nv_accvgpr_read_b32 v227,a111\nv_cvt_pk_bf16_f32 v228,v220,v221\nv_mfma_f32_16x16x32_bf16 v[76:79],v[156:159],v[236:239],v[76:79]\n"
"ds_read_b128 v[152:155],v252 offset:32768\nv_cvt_pk_bf16_f32 v229,v222,v223\nv_mfma_f32_16x16x32_bf16 v[72:75],v[156:159],v[232:235],v[72:75]\ns_waitcnt lgkmcnt(5)\nv_cvt_pk_bf16_f32 v230,v224,v225\nv_cvt_pk_bf16_f32 v231,v226,v227\nv_mfma_f32_16x16x32_bf16 v[80:83],v[160:163],v[232:235],v[80:83]\nds_read_b128 v[156:159],v252 offset:33792\nv_mfma_f32_16x16x32_bf16 v[84:87],v[160:163],v[236:239],v[84:87]\n"
"buffer_store_dwordx4 v[228:231],v252,s[8:11],s73 offen sc1\nv_mfma_f32_16x16x32_bf16 v[92:95],v[164:167],v[236:239],v[92:95]\nds_read_b128 v[160:163],v252 offset:34816\nv_accvgpr_read_b32 v208,a80\nv_mfma_f32_16x16x32_bf16 v[88:91],v[164:167],v[232:235],v[88:91]\ns_waitcnt lgkmcnt(5)\nv_accvgpr_read_b32 v209,a81\nv_accvgpr_read_b32 v210,a82\nv_mfma_f32_16x16x32_bf16 v[96:99],v[168:171],v[232:235],v[96:99]\n"
"s_add_u32 s73,s73,s71\nds_read_b128 v[164:167],v252 offset:35840\nv_accvgpr_read_b32 v211,a83\nv_mfma_f32_16x16x32_bf16 v[100:103],v[168:171],v[236:239],v[100:103]\nv_accvgpr_read_b32 v212,a112\nv_accvgpr_read_b32 v213,a113\nv_mfma_f32_16x16x32_bf16 v[108:111],v[172:175],v[236:239],v[108:111]\nds_read_b128 v[168:171],v252 offset:36864\nv_accvgpr_read_b32 v214,a114\nv_mfma_f32_16x16x32_bf16 v[104:107],v[172:175],v[232:235],v[104:107]\n"
"s_waitcnt lgkmcnt(5)\nv_accvgpr_read_b32 v215,a115\nv_cvt_pk_bf16_f32 v216,v208,v209\nv_mfma_f32_16x16x32_bf16 v[112:115],v[176:179],v[232:235],v[112:115]\nds_read_b128 v[172:175],v252 offset:37888\nv_cvt_pk_bf16_f32 v217,v210,v211\nv_mfma_f32_16x16x32_bf16 v[116:119],v[176:179],v[236:239],v[116:119]\nv_cvt_pk_bf16_f32 v218,v212,v213\nv_cvt_pk_bf16_f32 v219,v214,v215\nv_mfma_f32_16x16x32_bf16 v[124:127],v[180:183],v[236:239],v[124:127]\n"
"s_waitcnt vmcnt(31)\nds_read_b128 v[176:179],v252 offset:38912\nv_mfma_f32_16x16x32_bf16 v[120:123],v[180:183],v[232:235],v[120:123]\ns_waitcnt lgkmcnt(5)\nbuffer_store_dwordx4 v[216:219],v252,s[8:11],s73 offen sc1\nv_mfma_f32_16x16x32_bf16 v[0:3],v[152:155],v[184:187],v[0:3]\nds_read_b128 v[180:183],v252 offset:39936\nv_accvgpr_read_b32 v220,a84\nv_mfma_f32_16x16x32_bf16 v[4:7],v[152:155],v[188:191],v[4:7]\n"
"buffer_load_dwordx4 v[232:235],v252,s[8:11],s67 offen offset:3072 sc1\ns_add_u32 s67,s67,0x1000\nv_mfma_f32_16x16x32_bf16 v[12:15],v[156:159],v[188:191],v[12:15]\nds_read_b128 v[152:155],v252 offset:40960\nv_accvgpr_read_b32 v221,a85\nv_mfma_f32_16x16x32_bf16 v[8:11],v[156:159],v[184:187],v[8:11]\ns_waitcnt lgkmcnt(5)\nbuffer_load_dwordx4 v[236:239],v252,s[8:11],s68 offen offset:3072 sc1\nv_mfma_f32_16x16x32_bf16 v[16:19],v[160:163],v[184:187],v[16:19]\n"
"s_add_u32 s68,s68,0x1000\nds_read_b128 v[156:159],v252 offset:41984\nv_accvgpr_read_b32 v222,a86\nv_mfma_f32_16x16x32_bf16 v[20:23],v[160:163],v[188:191],v[20:23]\ns_add_u32 s73,s73,s72\nv_accvgpr_read_b32 v223,a87\nv_accvgpr_read_b32 v224,a116\nv_mfma_f32_16x16x32_bf16 v[28:31],v[164:167],v[188:191],v[28:31]\nds_read_b128 v[160:163],v252 offset:43008\nv_accvgpr_read_b32 v225,a117\nv_mfma_f32_16x16x32_bf16 v[24:27],v[164:167],v[184:187],v[24:27]\n"
"s_waitcnt lgkmcnt(5)\nv_accvgpr_read_b32 v226,a118\nv_accvgpr_read_b32 v227,a119\nv_mfma_f32_16x16x32_bf16 v[32:35],v[168:171],v[184:187],v[32:35]\nds_read_b128 v[164:167],v252 offset:44032\nv_cvt_pk_bf16_f32 v228,v220,v221\nv_mfma_f32_16x16x32_bf16 v[36:39],v[168:171],v[188:191],v[36:39]\nv_cvt_pk_bf16_f32 v229,v222,v223\nv_cvt_pk_bf16_f32 v230,v224,v225\nv_mfma_f32_16x16x32_bf16 v[44:47],v[172:175],v[188:191],v[44:47]\n"
"ds_read_b128 v[168:171],v252 offset:45056\nv_cvt_pk_bf16_f32 v231,v226,v227\nv_mfma_f32_16x16x32_bf16 v[40:43],v[172:175],v[184:187],v[40:43]\ns_waitcnt lgkmcnt(5)\nbuffer_store_dwordx4 v[228:231],v252,s[8:11],s73 offen sc1\nv_mfma_f32_16x16x32_bf16 v[48:51],v[176:179],v[184:187],v[48:51]\nds_read_b128 v[172:175],v252 offset:46080\nv_accvgpr_read_b32 v208,a88\nv_mfma_f32_16x16x32_bf16 v[52:55],v[176:179],v[188:191],v[52:55]\n"
"v_accvgpr_read_b32 v209,a89\nv_accvgpr_read_b32 v210,a90\nv_mfma_f32_16x16x32_bf16 v[60:63],v[180:183],v[188:191],v[60:63]\nds_read_b128 v[176:179],v252 offset:47104\nv_accvgpr_read_b32 v211,a91\nv_mfma_f32_16x16x32_bf16 v[56:59],v[180:183],v[184:187],v[56:59]\ns_waitcnt lgkmcnt(5)\nv_accvgpr_read_b32 v212,a120\nv_accvgpr_read_b32 v213,a121\nv_mfma_f32_16x16x32_bf16 v[64:67],v[152:155],v[184:187],v[64:67]\n"
"s_add_u32 s73,s73,s71\nds_read_b128 v[180:183],v252 offset:48128\nv_accvgpr_read_b32 v214,a122\nv_mfma_f32_16x16x32_bf16 v[68:71],v[152:155],v[188:191],v[68:71]\ns_barrier\nv_accvgpr_read_b32 v215,a123\nv_cvt_pk_bf16_f32 v216,v208,v209\nv_mfma_f32_16x16x32_bf16 v[76:79],v[156:159],v[188:191],v[76:79]\ns_add_u32 m0,s15,81920\nds_read_b128 v[152:155],v252 offset:49152\nv_cvt_pk_bf16_f32 v217,v210,v211\n"
"v_mfma_f32_16x16x32_bf16 v[72:75],v[156:159],v[184:187],v[72:75]\ns_waitcnt lgkmcnt(5)\nbuffer_load_dwordx4 v252,s[8:11],s66 offen sc1 lds\nv_mfma_f32_16x16x32_bf16 v[80:83],v[160:163],v[184:187],v[80:83]\nds_read_b128 v[156:159],v252 offset:50176\nv_cvt_pk_bf16_f32 v218,v212,v213\nv_mfma_f32_16x16x32_bf16 v[84:87],v[160:163],v[188:191],v[84:87]\nv_cvt_pk_bf16_f32 v219,v214,v215\nv_mfma_f32_16x16x32_bf16 v[92:95],v[164:167],v[188:191],v[92:95]\n"
"ds_read_b128 v[160:163],v252 offset:51200\nv_mfma_f32_16x16x32_bf16 v[88:91],v[164:167],v[184:187],v[88:91]\ns_waitcnt lgkmcnt(5)\nbuffer_load_dwordx4 v252,s[8:11],s66 offen offset:1024 sc1 lds\nv_mfma_f32_16x16x32_bf16 v[96:99],v[168:171],v[184:187],v[96:99]\nds_read_b128 v[164:167],v252 offset:52224\nv_mfma_f32_16x16x32_bf16 v[100:103],v[168:171],v[188:191],v[100:103]\nbuffer_store_dwordx4 v[216:219],v252,s[8:11],s73 offen sc1\n"
"v_mfma_f32_16x16x32_bf16 v[108:111],v[172:175],v[188:191],v[108:111]\nds_read_b128 v[168:171],v252 offset:53248\nv_accvgpr_read_b32 v220,a92\nv_mfma_f32_16x16x32_bf16 v[104:107],v[172:175],v[184:187],v[104:107]\ns_waitcnt lgkmcnt(5)\nbuffer_load_dwordx4 v252,s[8:11],s66 offen offset:2048 sc1 lds\nv_mfma_f32_16x16x32_bf16 v[112:115],v[176:179],v[184:187],v[112:115]\nds_read_b128 v[172:175],v252 offset:54272\n"
"v_accvgpr_read_b32 v221,a93\nv_mfma_f32_16x16x32_bf16 v[116:119],v[176:179],v[188:191],v[116:119]\nv_accvgpr_read_b32 v222,a94\nv_accvgpr_read_b32 v223,a95\nv_mfma_f32_16x16x32_bf16 v[124:127],v[180:183],v[188:191],v[124:127]\ns_waitcnt vmcnt(35)\nds_read_b128 v[176:179],v252 offset:55296\nv_accvgpr_read_b32 v224,a124\nv_mfma_f32_16x16x32_bf16 v[120:123],v[180:183],v[184:187],v[120:123]\ns_waitcnt lgkmcnt(5)\n"
"buffer_load_dwordx4 v252,s[8:11],s66 offen offset:3072 sc1 lds\nv_mfma_f32_16x16x32_bf16 v[0:3],v[152:155],v[192:195],v[0:3]\nds_read_b128 v[180:183],v252 offset:56320\nv_accvgpr_read_b32 v225,a125\nv_mfma_f32_16x16x32_bf16 v[4:7],v[152:155],v[196:199],v[4:7]\ns_add_u32 s73,s73,s72\nbuffer_load_dwordx4 v[184:187],v252,s[8:11],s67 offen offset:0 sc1\nv_mfma_f32_16x16x32_bf16 v[12:15],v[156:159],v[196:199],v[12:15]\n"
"ds_read_b128 v[152:155],v252 offset:57344\nv_accvgpr_read_b32 v226,a126\nv_mfma_f32_16x16x32_bf16 v[8:11],v[156:159],v[192:195],v[8:11]\ns_waitcnt lgkmcnt(5)\ns_add_u32 s66,s66,0x4000\nv_accvgpr_read_b32 v227,a127\nv_mfma_f32_16x16x32_bf16 v[16:19],v[160:163],v[192:195],v[16:19]\nds_read_b128 v[156:159],v252 offset:58368\nv_cvt_pk_bf16_f32 v228,v220,v221\nv_mfma_f32_16x16x32_bf16 v[20:23],v[160:163],v[196:199],v[20:23]\n"
"buffer_load_dwordx4 v[188:191],v252,s[8:11],s68 offen offset:0 sc1\nv_mfma_f32_16x16x32_bf16 v[28:31],v[164:167],v[196:199],v[28:31]\nds_read_b128 v[160:163],v252 offset:59392\nv_cvt_pk_bf16_f32 v229,v222,v223\nv_mfma_f32_16x16x32_bf16 v[24:27],v[164:167],v[192:195],v[24:27]\ns_waitcnt lgkmcnt(5)\nv_cvt_pk_bf16_f32 v230,v224,v225\nv_cvt_pk_bf16_f32 v231,v226,v227\nv_mfma_f32_16x16x32_bf16 v[32:35],v[168:171],v[192:195],v[32:35]\n"
"ds_read_b128 v[164:167],v252 offset:60416\nv_mfma_f32_16x16x32_bf16 v[36:39],v[168:171],v[196:199],v[36:39]\nbuffer_store_dwordx4 v[228:231],v252,s[8:11],s73 offen sc1\nv_mfma_f32_16x16x32_bf16 v[44:47],v[172:175],v[196:199],v[44:47]\nds_read_b128 v[168:171],v252 offset:61440\nv_accvgpr_read_b32 v208,a128\nv_mfma_f32_16x16x32_bf16 v[40:43],v[172:175],v[192:195],v[40:43]\ns_waitcnt lgkmcnt(5)\nv_accvgpr_read_b32 v209,a129\n"
"v_accvgpr_read_b32 v210,a130\nv_mfma_f32_16x16x32_bf16 v[48:51],v[176:179],v[192:195],v[48:51]\ns_mov_b32 s73,s69\nds_read_b128 v[172:175],v252 offset:62464\nv_accvgpr_read_b32 v211,a131\nv_mfma_f32_16x16x32_bf16 v[52:55],v[176:179],v[196:199],v[52:55]\nv_accvgpr_read_b32 v212,a160\nv_accvgpr_read_b32 v213,a161\nv_mfma_f32_16x16x32_bf16 v[60:63],v[180:183],v[196:199],v[60:63]\nds_read_b128 v[176:179],v252 offset:63488\n"
"v_accvgpr_read_b32 v214,a162\nv_mfma_f32_16x16x32_bf16 v[56:59],v[180:183],v[192:195],v[56:59]\ns_waitcnt lgkmcnt(5)\nv_accvgpr_read_b32 v215,a163\nv_cvt_pk_bf16_f32 v216,v208,v209\nv_mfma_f32_16x16x32_bf16 v[64:67],v[152:155],v[192:195],v[64:67]\nds_read_b128 v[180:183],v252 offset:64512\nv_cvt_pk_bf16_f32 v217,v210,v211\nv_mfma_f32_16x16x32_bf16 v[68:71],v[152:155],v[196:199],v[68:71]\ns_add_u32 s69,s69,s70\n"
"v_cvt_pk_bf16_f32 v218,v212,v213\nv_cvt_pk_bf16_f32 v219,v214,v215\nv_mfma_f32_16x16x32_bf16 v[76:79],v[156:159],v[196:199],v[76:79]\nds_read_b128 v[152:155],v146 offset:32768\nv_mfma_f32_16x16x32_bf16 v[72:75],v[156:159],v[192:195],v[72:75]\ns_waitcnt lgkmcnt(5)\nbuffer_store_dwordx4 v[216:219],v252,s[8:11],s73 offen sc1\nv_mfma_f32_16x16x32_bf16 v[80:83],v[160:163],v[192:195],v[80:83]\nds_read_b128 v[156:159],v146 offset:33792\n"
"v_accvgpr_read_b32 v220,a132\nv_mfma_f32_16x16x32_bf16 v[84:87],v[160:163],v[196:199],v[84:87]\ns_add_u32 s73,s73,s72\nv_accvgpr_read_b32 v221,a133\nv_accvgpr_read_b32 v222,a134\nv_mfma_f32_16x16x32_bf16 v[92:95],v[164:167],v[196:199],v[92:95]\nds_read_b128 v[160:163],v146 offset:34816\nv_accvgpr_read_b32 v223,a135\nv_mfma_f32_16x16x32_bf16 v[88:91],v[164:167],v[192:195],v[88:91]\ns_waitcnt lgkmcnt(5)\n"
"v_accvgpr_read_b32 v224,a164\nv_accvgpr_read_b32 v225,a165\nv_mfma_f32_16x16x32_bf16 v[96:99],v[168:171],v[192:195],v[96:99]\nds_read_b128 v[164:167],v146 offset:35840\nv_accvgpr_read_b32 v226,a166\nv_mfma_f32_16x16x32_bf16 v[100:103],v[168:171],v[196:199],v[100:103]\nv_accvgpr_read_b32 v227,a167\nv_cvt_pk_bf16_f32 v228,v220,v221\nv_mfma_f32_16x16x32_bf16 v[108:111],v[172:175],v[196:199],v[108:111]\n"
"ds_read_b128 v[168:171],v146 offset:36864\nv_cvt_pk_bf16_f32 v229,v222,v223\nv_mfma_f32_16x16x32_bf16 v[104:107],v[172:175],v[192:195],v[104:107]\ns_waitcnt lgkmcnt(5)\nv_cvt_pk_bf16_f32 v230,v224,v225\nv_cvt_pk_bf16_f32 v231,v226,v227\nv_mfma_f32_16x16x32_bf16 v[112:115],v[176:179],v[192:195],v[112:115]\nds_read_b128 v[172:175],v146 offset:37888\nv_mfma_f32_16x16x32_bf16 v[116:119],v[176:179],v[196:199],v[116:119]\n"
"buffer_store_dwordx4 v[228:231],v252,s[8:11],s73 offen sc1\nv_mfma_f32_16x16x32_bf16 v[124:127],v[180:183],v[196:199],v[124:127]\ns_waitcnt vmcnt(36)\nds_read_b128 v[176:179],v146 offset:38912\nv_accvgpr_read_b32 v208,a136\nv_mfma_f32_16x16x32_bf16 v[120:123],v[180:183],v[192:195],v[120:123]\ns_waitcnt lgkmcnt(5)\nv_accvgpr_read_b32 v209,a137\nv_accvgpr_read_b32 v210,a138\nv_mfma_f32_16x16x32_bf16 v[0:3],v[152:155],v[200:203],v[0:3]\n"
"ds_read_b128 v[180:183],v146 offset:39936\nv_accvgpr_read_b32 v211,a139\nv_mfma_f32_16x16x32_bf16 v[4:7],v[152:155],v[204:207],v[4:7]\nbuffer_load_dwordx4 v[192:195],v252,s[8:11],s67 offen offset:1024 sc1\nv_mfma_f32_16x16x32_bf16 v[12:15],v[156:159],v[204:207],v[12:15]\ns_add_u32 s73,s73,s71\nds_read_b128 v[152:155],v146 offset:40960\nv_accvgpr_read_b32 v212,a168\nv_mfma_f32_16x16x32_bf16 v[8:11],v[156:159],v[200:203],v[8:11]\n"
"s_waitcnt lgkmcnt(5)\nbuffer_load_dwordx4 v[196:199],v252,s[8:11],s68 offen offset:1024 sc1\nv_mfma_f32_16x16x32_bf16 v[16:19],v[160:163],v[200:203],v[16:19]\nds_read_b128 v[156:159],v146 offset:41984\nv_accvgpr_read_b32 v213,a169\nv_mfma_f32_16x16x32_bf16 v[20:23],v[160:163],v[204:207],v[20:23]\nv_accvgpr_read_b32 v214,a170\nv_accvgpr_read_b32 v215,a171\nv_mfma_f32_16x16x32_bf16 v[28:31],v[164:167],v[204:207],v[28:31]\n"
"ds_read_b128 v[160:163],v146 offset:43008\nv_cvt_pk_bf16_f32 v216,v208,v209\nv_mfma_f32_16x16x32_bf16 v[24:27],v[164:167],v[200:203],v[24:27]\ns_waitcnt lgkmcnt(5)\nv_cvt_pk_bf16_f32 v217,v210,v211\nv_cvt_pk_bf16_f32 v218,v212,v213\nv_mfma_f32_16x16x32_bf16 v[32:35],v[168:171],v[200:203],v[32:35]\nds_read_b128 v[164:167],v146 offset:44032\nv_cvt_pk_bf16_f32 v219,v214,v215\nv_mfma_f32_16x16x32_bf16 v[36:39],v[168:171],v[204:207],v[36:39]\n"
"buffer_store_dwordx4 v[216:219],v252,s[8:11],s73 offen sc1\nv_mfma_f32_16x16x32_bf16 v[44:47],v[172:175],v[204:207],v[44:47]\nds_read_b128 v[168:171],v146 offset:45056\nv_accvgpr_read_b32 v220,a140\nv_mfma_f32_16x16x32_bf16 v[40:43],v[172:175],v[200:203],v[40:43]\ns_waitcnt lgkmcnt(5)\nv_accvgpr_read_b32 v221,a141\nv_accvgpr_read_b32 v222,a142\nv_mfma_f32_16x16x32_bf16 v[48:51],v[176:179],v[200:203],v[48:51]\n"
"s_add_u32 s73,s73,s72\nds_read_b128 v[172:175],v146 offset:46080\nv_accvgpr_read_b32 v223,a143\nv_mfma_f32_16x16x32_bf16 v[52:55],v[176:179],v[204:207],v[52:55]\nv_accvgpr_read_b32 v224,a172\nv_accvgpr_read_b32 v225,a173\nv_mfma_f32_16x16x32_bf16 v[60:63],v[180:183],v[204:207],v[60:63]\nds_read_b128 v[176:179],v146 offset:47104\nv_accvgpr_read_b32 v226,a174\nv_mfma_f32_16x16x32_bf16 v[56:59],v[180:183],v[200:203],v[56:59]\n"
"s_waitcnt lgkmcnt(5)\nv_accvgpr_read_b32 v227,a175\nv_cvt_pk_bf16_f32 v228,v220,v221\nv_mfma_f32_16x16x32_bf16 v[64:67],v[152:155],v[200:203],v[64:67]\nds_read_b128 v[180:183],v146 offset:48128\nv_cvt_pk_bf16_f32 v229,v222,v223\nv_mfma_f32_16x16x32_bf16 v[68:71],v[152:155],v[204:207],v[68:71]\nv_cvt_pk_bf16_f32 v230,v224,v225\nv_cvt_pk_bf16_f32 v231,v226,v227\nv_mfma_f32_16x16x32_bf16 v[76:79],v[156:159],v[204:207],v[76:79]\n"
"ds_read_b128 v[152:155],v146 offset:49152\nv_mfma_f32_16x16x32_bf16 v[72:75],v[156:159],v[200:203],v[72:75]\ns_waitcnt lgkmcnt(5)\nbuffer_store_dwordx4 v[228:231],v252,s[8:11],s73 offen sc1\nv_mfma_f32_16x16x32_bf16 v[80:83],v[160:163],v[200:203],v[80:83]\nds_read_b128 v[156:159],v146 offset:50176\nv_accvgpr_read_b32 v208,a144\nv_mfma_f32_16x16x32_bf16 v[84:87],v[160:163],v[204:207],v[84:87]\ns_add_u32 s73,s73,s71\n"
"v_accvgpr_read_b32 v209,a145\nv_accvgpr_read_b32 v210,a146\nv_mfma_f32_16x16x32_bf16 v[92:95],v[164:167],v[204:207],v[92:95]\nds_read_b128 v[160:163],v146 offset:51200\nv_accvgpr_read_b32 v211,a147\nv_mfma_f32_16x16x32_bf16 v[88:91],v[164:167],v[200:203],v[88:91]\ns_waitcnt lgkmcnt(5)\nv_accvgpr_read_b32 v212,a176\nv_accvgpr_read_b32 v213,a177\nv_mfma_f32_16x16x32_bf16 v[96:99],v[168:171],v[200:203],v[96:99]\n"
"ds_read_b128 v[164:167],v146 offset:52224\nv_accvgpr_read_b32 v214,a178\nv_mfma_f32_16x16x32_bf16 v[100:103],v[168:171],v[204:207],v[100:103]\nv_accvgpr_read_b32 v215,a179\nv_cvt_pk_bf16_f32 v216,v208,v209\nv_mfma_f32_16x16x32_bf16 v[108:111],v[172:175],v[204:207],v[108:111]\nds_read_b128 v[168:171],v146 offset:53248\nv_cvt_pk_bf16_f32 v217,v210,v211\nv_mfma_f32_16x16x32_bf16 v[104:107],v[172:175],v[200:203],v[104:107]\n"
"s_waitcnt lgkmcnt(5)\nv_cvt_pk_bf16_f32 v218,v212,v213\nv_cvt_pk_bf16_f32 v219,v214,v215\nv_mfma_f32_16x16x32_bf16 v[112:115],v[176:179],v[200:203],v[112:115]\nds_read_b128 v[172:175],v146 offset:54272\nv_mfma_f32_16x16x32_bf16 v[116:119],v[176:179],v[204:207],v[116:119]\nbuffer_store_dwordx4 v[216:219],v252,s[8:11],s73 offen sc1\nv_mfma_f32_16x16x32_bf16 v[124:127],v[180:183],v[204:207],v[124:127]\n"
"s_waitcnt vmcnt(32)\nds_read_b128 v[176:179],v146 offset:55296\nv_accvgpr_read_b32 v220,a148\nv_mfma_f32_16x16x32_bf16 v[120:123],v[180:183],v[200:203],v[120:123]\ns_waitcnt lgkmcnt(5)\nv_accvgpr_read_b32 v221,a149\nv_accvgpr_read_b32 v222,a150\nv_mfma_f32_16x16x32_bf16 v[0:3],v[152:155],v[128:131],v[0:3]\ns_add_u32 s73,s73,s72\nds_read_b128 v[180:183],v146 offset:56320\nv_accvgpr_read_b32 v223,a151\n"
"v_mfma_f32_16x16x32_bf16 v[4:7],v[152:155],v[132:135],v[4:7]\nv_accvgpr_read_b32 v224,a180\nv_accvgpr_read_b32 v225,a181\nv_mfma_f32_16x16x32_bf16 v[12:15],v[156:159],v[132:135],v[12:15]\nds_read_b128 v[152:155],v146 offset:57344\nv_accvgpr_read_b32 v226,a182\nv_mfma_f32_16x16x32_bf16 v[8:11],v[156:159],v[128:131],v[8:11]\ns_waitcnt lgkmcnt(5)\nv_accvgpr_read_b32 v227,a183\nv_cvt_pk_bf16_f32 v228,v220,v221\n"
"v_mfma_f32_16x16x32_bf16 v[16:19],v[160:163],v[128:131],v[16:19]\nds_read_b128 v[156:159],v146 offset:58368\nv_cvt_pk_bf16_f32 v229,v222,v223\nv_mfma_f32_16x16x32_bf16 v[20:23],v[160:163],v[132:135],v[20:23]\nv_cvt_pk_bf16_f32 v230,v224,v225\nv_cvt_pk_bf16_f32 v231,v226,v227\nv_mfma_f32_16x16x32_bf16 v[28:31],v[164:167],v[132:135],v[28:31]\nds_read_b128 v[160:163],v146 offset:59392\nv_mfma_f32_16x16x32_bf16 v[24:27],v[164:167],v[128:131],v[24:27]\n"
"s_waitcnt lgkmcnt(5)\nbuffer_store_dwordx4 v[228:231],v252,s[8:11],s73 offen sc1\nv_mfma_f32_16x16x32_bf16 v[32:35],v[168:171],v[128:131],v[32:35]\nds_read_b128 v[164:167],v146 offset:60416\nv_accvgpr_read_b32 v208,a152\nv_mfma_f32_16x16x32_bf16 v[36:39],v[168:171],v[132:135],v[36:39]\ns_add_u32 s73,s73,s71\nv_accvgpr_read_b32 v209,a153\nv_accvgpr_read_b32 v210,a154\nv_mfma_f32_16x16x32_bf16 v[44:47],v[172:175],v[132:135],v[44:47]\n"
"ds_read_b128 v[168:171],v146 offset:61440\nv_accvgpr_read_b32 v211,a155\nv_mfma_f32_16x16x32_bf16 v[40:43],v[172:175],v[128:131],v[40:43]\ns_waitcnt lgkmcnt(5)\nv_accvgpr_read_b32 v212,a184\nv_accvgpr_read_b32 v213,a185\nv_mfma_f32_16x16x32_bf16 v[48:51],v[176:179],v[128:131],v[48:51]\nds_read_b128 v[172:175],v146 offset:62464\nv_accvgpr_read_b32 v214,a186\nv_mfma_f32_16x16x32_bf16 v[52:55],v[176:179],v[132:135],v[52:55]\n"
"v_accvgpr_read_b32 v215,a187\nv_cvt_pk_bf16_f32 v216,v208,v209\nv_mfma_f32_16x16x32_bf16 v[60:63],v[180:183],v[132:135],v[60:63]\nds_read_b128 v[176:179],v146 offset:63488\nv_cvt_pk_bf16_f32 v217,v210,v211\nv_mfma_f32_16x16x32_bf16 v[56:59],v[180:183],v[128:131],v[56:59]\ns_waitcnt lgkmcnt(5)\nv_cvt_pk_bf16_f32 v218,v212,v213\nv_cvt_pk_bf16_f32 v219,v214,v215\nv_mfma_f32_16x16x32_bf16 v[64:67],v[152:155],v[128:131],v[64:67]\n"
"ds_read_b128 v[180:183],v146 offset:64512\nv_mfma_f32_16x16x32_bf16 v[68:71],v[152:155],v[132:135],v[68:71]\ns_waitcnt vmcnt(11)\ns_barrier\nbuffer_store_dwordx4 v[216:219],v252,s[8:11],s73 offen sc1\nv_mfma_f32_16x16x32_bf16 v[76:79],v[156:159],v[132:135],v[76:79]\nds_read_b128 v[152:155],v252 offset:0\nv_accvgpr_read_b32 v220,a156\nv_mfma_f32_16x16x32_bf16 v[72:75],v[156:159],v[128:131],v[72:75]\n"
"s_waitcnt lgkmcnt(5)\nv_accvgpr_read_b32 v221,a157\nv_accvgpr_read_b32 v222,a158\nv_mfma_f32_16x16x32_bf16 v[80:83],v[160:163],v[128:131],v[80:83]\ns_add_u32 s73,s73,s72\nds_read_b128 v[156:159],v252 offset:1024\nv_accvgpr_read_b32 v223,a159\nv_mfma_f32_16x16x32_bf16 v[84:87],v[160:163],v[132:135],v[84:87]\nv_accvgpr_read_b32 v224,a188\nv_accvgpr_read_b32 v225,a189\nv_mfma_f32_16x16x32_bf16 v[92:95],v[164:167],v[132:135],v[92:95]\n"
"ds_read_b128 v[160:163],v252 offset:2048\nv_accvgpr_read_b32 v226,a190\nv_mfma_f32_16x16x32_bf16 v[88:91],v[164:167],v[128:131],v[88:91]\ns_waitcnt lgkmcnt(5)\nv_accvgpr_read_b32 v227,a191\nv_cvt_pk_bf16_f32 v228,v220,v221\nv_mfma_f32_16x16x32_bf16 v[96:99],v[168:171],v[128:131],v[96:99]\nds_read_b128 v[164:167],v252 offset:3072\nv_cvt_pk_bf16_f32 v229,v222,v223\nv_mfma_f32_16x16x32_bf16 v[100:103],v[168:171],v[132:135],v[100:103]\n"
"v_cvt_pk_bf16_f32 v230,v224,v225\nv_cvt_pk_bf16_f32 v231,v226,v227\nv_mfma_f32_16x16x32_bf16 v[108:111],v[172:175],v[132:135],v[108:111]\nds_read_b128 v[168:171],v252 offset:4096\nv_mfma_f32_16x16x32_bf16 v[104:107],v[172:175],v[128:131],v[104:107]\ns_waitcnt lgkmcnt(5)\nbuffer_store_dwordx4 v[228:231],v252,s[8:11],s73 offen sc1\nv_mfma_f32_16x16x32_bf16 v[112:115],v[176:179],v[128:131],v[112:115]\n"
"ds_read_b128 v[172:175],v252 offset:5120\nv_accvgpr_read_b32 v208,a192\nv_mfma_f32_16x16x32_bf16 v[116:119],v[176:179],v[132:135],v[116:119]\ns_mov_b32 s73,s69\nv_accvgpr_read_b32 v209,a193\nv_accvgpr_read_b32 v210,a194\nv_mfma_f32_16x16x32_bf16 v[124:127],v[180:183],v[132:135],v[124:127]\nds_read_b128 v[176:179],v252 offset:6144\nv_accvgpr_read_b32 v211,a195\nv_mfma_f32_16x16x32_bf16 v[120:123],v[180:183],v[128:131],v[120:123]\n"
"s_waitcnt lgkmcnt(5)\nv_accvgpr_read_b32 v212,a224\nv_accvgpr_read_b32 v213,a225\nv_mfma_f32_16x16x32_bf16 v[0:3],v[152:155],v[136:139],v[0:3]\nds_read_b128 v[180:183],v252 offset:7168\nv_accvgpr_read_b32 v214,a226\nv_mfma_f32_16x16x32_bf16 v[4:7],v[152:155],v[140:143],v[4:7]\nv_accvgpr_read_b32 v215,a227\nv_cvt_pk_bf16_f32 v216,v208,v209\nv_mfma_f32_16x16x32_bf16 v[12:15],v[156:159],v[140:143],v[12:15]\n"
"ds_read_b128 v[152:155],v252 offset:8192\nv_cvt_pk_bf16_f32 v217,v210,v211\nv_mfma_f32_16x16x32_bf16 v[8:11],v[156:159],v[136:139],v[8:11]\ns_waitcnt lgkmcnt(5)\nv_cvt_pk_bf16_f32 v218,v212,v213\nv_cvt_pk_bf16_f32 v219,v214,v215\nv_mfma_f32_16x16x32_bf16 v[16:19],v[160:163],v[136:139],v[16:19]\ns_add_u32 s69,s69,s70\nds_read_b128 v[156:159],v252 offset:9216\nv_mfma_f32_16x16x32_bf16 v[20:23],v[160:163],v[140:143],v[20:23]\n"
"buffer_store_dwordx4 v[216:219],v252,s[8:11],s73 offen sc1\nv_mfma_f32_16x16x32_bf16 v[28:31],v[164:167],v[140:143],v[28:31]\nds_read_b128 v[160:163],v252 offset:10240\nv_accvgpr_read_b32 v220,a196\nv_mfma_f32_16x16x32_bf16 v[24:27],v[164:167],v[136:139],v[24:27]\ns_waitcnt lgkmcnt(5)\nv_accvgpr_read_b32 v221,a197\nv_accvgpr_read_b32 v222,a198\nv_mfma_f32_16x16x32_bf16 v[32:35],v[168:171],v[136:139],v[32:35]\n"
"s_add_u32 s73,s73,s72\nds_read_b128 v[164:167],v252 offset:11264\nv_accvgpr_read_b32 v223,a199\nv_mfma_f32_16x16x32_bf16 v[36:39],v[168:171],v[140:143],v[36:39]\nv_accvgpr_read_b32 v224,a228\nv_accvgpr_read_b32 v225,a229\nv_mfma_f32_16x16x32_bf16 v[44:47],v[172:175],v[140:143],v[44:47]\nds_read_b128 v[168:171],v252 offset:12288\nv_accvgpr_read_b32 v226,a230\nv_mfma_f32_16x16x32_bf16 v[40:43],v[172:175],v[136:139],v[40:43]\n"
"s_waitcnt lgkmcnt(5)\nv_accvgpr_read_b32 v227,a231\nv_cvt_pk_bf16_f32 v228,v220,v221\nv_mfma_f32_16x16x32_bf16 v[48:51],v[176:179],v[136:139],v[48:51]\nds_read_b128 v[172:175],v252 offset:13312\nv_cvt_pk_bf16_f32 v229,v222,v223\nv_mfma_f32_16x16x32_bf16 v[52:55],v[176:179],v[140:143],v[52:55]\nv_cvt_pk_bf16_f32 v230,v224,v225\nv_cvt_pk_bf16_f32 v231,v226,v227\nv_mfma_f32_16x16x32_bf16 v[60:63],v[180:183],v[140:143],v[60:63]\n"
"ds_read_b128 v[176:179],v252 offset:14336\nv_mfma_f32_16x16x32_bf16 v[56:59],v[180:183],v[136:139],v[56:59]\ns_waitcnt lgkmcnt(5)\nbuffer_store_dwordx4 v[228:231],v252,s[8:11],s73 offen sc1\nv_mfma_f32_16x16x32_bf16 v[64:67],v[152:155],v[136:139],v[64:67]\nds_read_b128 v[180:183],v252 offset:15360\nv_accvgpr_read_b32 v208,a200\nv_mfma_f32_16x16x32_bf16 v[68:71],v[152:155],v[140:143],v[68:71]\ns_add_u32 s73,s73,s71\n"
"v_accvgpr_read_b32 v209,a201\nv_accvgpr_read_b32 v210,a202\nv_mfma_f32_16x16x32_bf16 v[76:79],v[156:159],v[140:143],v[76:79]\nds_read_b128 v[152:155],v252 offset:16384\nv_accvgpr_read_b32 v211,a203\nv_mfma_f32_16x16x32_bf16 v[72:75],v[156:159],v[136:139],v[72:75]\ns_waitcnt lgkmcnt(5)\nv_accvgpr_read_b32 v212,a232\nv_accvgpr_read_b32 v213,a233\nv_mfma_f32_16x16x32_bf16 v[80:83],v[160:163],v[136:139],v[80:83]\n"
"ds_read_b128 v[156:159],v252 offset:17408\nv_accvgpr_read_b32 v214,a234\nv_mfma_f32_16x16x32_bf16 v[84:87],v[160:163],v[140:143],v[84:87]\nv_accvgpr_read_b32 v215,a235\nv_cvt_pk_bf16_f32 v216,v208,v209\nv_mfma_f32_16x16x32_bf16 v[92:95],v[164:167],v[140:143],v[92:95]\nds_read_b128 v[160:163],v252 offset:18432\nv_cvt_pk_bf16_f32 v217,v210,v211\nv_mfma_f32_16x16x32_bf16 v[88:91],v[164:167],v[136:139],v[88:91]\n"
"s_waitcnt lgkmcnt(5)\nv_cvt_pk_bf16_f32 v218,v212,v213\nv_cvt_pk_bf16_f32 v219,v214,v215\nv_mfma_f32_16x16x32_bf16 v[96:99],v[168:171],v[136:139],v[96:99]\nds_read_b128 v[164:167],v252 offset:19456\nv_mfma_f32_16x16x32_bf16 v[100:103],v[168:171],v[140:143],v[100:103]\nbuffer_store_dwordx4 v[216:219],v252,s[8:11],s73 offen sc1\nv_mfma_f32_16x16x32_bf16 v[108:111],v[172:175],v[140:143],v[108:111]\nds_read_b128 v[168:171],v252 offset:20480\n"
"v_accvgpr_read_b32 v220,a204\nv_mfma_f32_16x16x32_bf16 v[104:107],v[172:175],v[136:139],v[104:107]\ns_waitcnt lgkmcnt(5)\nv_accvgpr_read_b32 v221,a205\nv_accvgpr_read_b32 v222,a206\nv_mfma_f32_16x16x32_bf16 v[112:115],v[176:179],v[136:139],v[112:115]\ns_add_u32 s73,s73,s72\nds_read_b128 v[172:175],v252 offset:21504\nv_accvgpr_read_b32 v223,a207\nv_mfma_f32_16x16x32_bf16 v[116:119],v[176:179],v[140:143],v[116:119]\n"
"v_accvgpr_read_b32 v224,a236\nv_accvgpr_read_b32 v225,a237\nv_mfma_f32_16x16x32_bf16 v[124:127],v[180:183],v[140:143],v[124:127]\nds_read_b128 v[176:179],v252 offset:22528\nv_accvgpr_read_b32 v226,a238\nv_mfma_f32_16x16x32_bf16 v[120:123],v[180:183],v[136:139],v[120:123]\ns_waitcnt lgkmcnt(5)\nv_accvgpr_read_b32 v227,a239\nv_cvt_pk_bf16_f32 v228,v220,v221\nv_mfma_f32_16x16x32_bf16 v[0:3],v[152:155],v[232:235],v[0:3]\n"
"ds_read_b128 v[180:183],v252 offset:23552\nv_cvt_pk_bf16_f32 v229,v222,v223\nv_mfma_f32_16x16x32_bf16 v[4:7],v[152:155],v[236:239],v[4:7]\nv_cvt_pk_bf16_f32 v230,v224,v225\nv_cvt_pk_bf16_f32 v231,v226,v227\nv_mfma_f32_16x16x32_bf16 v[12:15],v[156:159],v[236:239],v[12:15]\nds_read_b128 v[152:155],v252 offset:24576\nv_mfma_f32_16x16x32_bf16 v[8:11],v[156:159],v[232:235],v[8:11]\ns_waitcnt lgkmcnt(5)\n"
"buffer_store_dwordx4 v[228:231],v252,s[8:11],s73 offen sc1\nv_mfma_f32_16x16x32_bf16 v[16:19],v[160:163],v[232:235],v[16:19]\nds_read_b128 v[156:159],v252 offset:25600\nv_accvgpr_read_b32 v208,a208\nv_mfma_f32_16x16x32_bf16 v[20:23],v[160:163],v[236:239],v[20:23]\ns_add_u32 s73,s73,s71\nv_accvgpr_read_b32 v209,a209\nv_accvgpr_read_b32 v210,a210\nv_mfma_f32_16x16x32_bf16 v[28:31],v[164:167],v[236:239],v[28:31]\n"
"ds_read_b128 v[160:163],v252 offset:26624\nv_accvgpr_read_b32 v211,a211\nv_mfma_f32_16x16x32_bf16 v[24:27],v[164:167],v[232:235],v[24:27]\ns_waitcnt lgkmcnt(5)\nv_accvgpr_read_b32 v212,a240\nv_accvgpr_read_b32 v213,a241\nv_mfma_f32_16x16x32_bf16 v[32:35],v[168:171],v[232:235],v[32:35]\nds_read_b128 v[164:167],v252 offset:27648\nv_accvgpr_read_b32 v214,a242\nv_mfma_f32_16x16x32_bf16 v[36:39],v[168:171],v[236:239],v[36:39]\n"
"v_accvgpr_read_b32 v215,a243\nv_cvt_pk_bf16_f32 v216,v208,v209\nv_mfma_f32_16x16x32_bf16 v[44:47],v[172:175],v[236:239],v[44:47]\nds_read_b128 v[168:171],v252 offset:28672\nv_cvt_pk_bf16_f32 v217,v210,v211\nv_mfma_f32_16x16x32_bf16 v[40:43],v[172:175],v[232:235],v[40:43]\ns_waitcnt lgkmcnt(5)\nv_cvt_pk_bf16_f32 v218,v212,v213\nv_cvt_pk_bf16_f32 v219,v214,v215\nv_mfma_f32_16x16x32_bf16 v[48:51],v[176:179],v[232:235],v[48:51]\n"
"ds_read_b128 v[172:175],v252 offset:29696\nv_mfma_f32_16x16x32_bf16 v[52:55],v[176:179],v[236:239],v[52:55]\nbuffer_store_dwordx4 v[216:219],v252,s[8:11],s73 offen sc1\nv_mfma_f32_16x16x32_bf16 v[60:63],v[180:183],v[236:239],v[60:63]\nds_read_b128 v[176:179],v252 offset:30720\nv_accvgpr_read_b32 v220,a212\nv_mfma_f32_16x16x32_bf16 v[56:59],v[180:183],v[232:235],v[56:59]\ns_waitcnt lgkmcnt(5)\nv_accvgpr_read_b32 v221,a213\n"
"v_accvgpr_read_b32 v222,a214\nv_mfma_f32_16x16x32_bf16 v[64:67],v[152:155],v[232:235],v[64:67]\ns_add_u32 s73,s73,s72\nds_read_b128 v[180:183],v252 offset:31744\nv_accvgpr_read_b32 v223,a215\nv_mfma_f32_16x16x32_bf16 v[68:71],v[152:155],v[236:239],v[68:71]\nv_accvgpr_read_b32 v224,a244\nv_accvgpr_read_b32 v225,a245\nv_mfma_f32_16x16x32_bf16 v[76:79],v[156:159],v[236:239],v[76:79]\nds_read_b128 v[152:155],v146 offset:0\n"
"v_accvgpr_read_b32 v226,a246\nv_mfma_f32_16x16x32_bf16 v[72:75],v[156:159],v[232:235],v[72:75]\ns_waitcnt lgkmcnt(5)\nv_accvgpr_read_b32 v227,a247\nv_cvt_pk_bf16_f32 v228,v220,v221\nv_mfma_f32_16x16x32_bf16 v[80:83],v[160:163],v[232:235],v[80:83]\nds_read_b128 v[156:159],v146 offset:1024\nv_cvt_pk_bf16_f32 v229,v222,v223\nv_mfma_f32_16x16x32_bf16 v[84:87],v[160:163],v[236:239],v[84:87]\nv_cvt_pk_bf16_f32 v230,v224,v225\n"
"v_cvt_pk_bf16_f32 v231,v226,v227\nv_mfma_f32_16x16x32_bf16 v[92:95],v[164:167],v[236:239],v[92:95]\nds_read_b128 v[160:163],v146 offset:2048\nv_mfma_f32_16x16x32_bf16 v[88:91],v[164:167],v[232:235],v[88:91]\ns_waitcnt lgkmcnt(5)\nbuffer_store_dwordx4 v[228:231],v252,s[8:11],s73 offen sc1\nv_mfma_f32_16x16x32_bf16 v[96:99],v[168:171],v[232:235],v[96:99]\nds_read_b128 v[164:167],v146 offset:3072\nv_accvgpr_read_b32 v208,a216\n"
"v_mfma_f32_16x16x32_bf16 v[100:103],v[168:171],v[236:239],v[100:103]\ns_add_u32 s73,s73,s71\nv_accvgpr_read_b32 v209,a217\nv_accvgpr_read_b32 v210,a218\nv_mfma_f32_16x16x32_bf16 v[108:111],v[172:175],v[236:239],v[108:111]\nds_read_b128 v[168:171],v146 offset:4096\nv_accvgpr_read_b32 v211,a219\nv_mfma_f32_16x16x32_bf16 v[104:107],v[172:175],v[232:235],v[104:107]\ns_waitcnt lgkmcnt(5)\nv_accvgpr_read_b32 v212,a248\n"
"v_accvgpr_read_b32 v213,a249\nv_mfma_f32_16x16x32_bf16 v[112:115],v[176:179],v[232:235],v[112:115]\nds_read_b128 v[172:175],v146 offset:5120\nv_accvgpr_read_b32 v214,a250\nv_mfma_f32_16x16x32_bf16 v[116:119],v[176:179],v[236:239],v[116:119]\nv_accvgpr_read_b32 v215,a251\nv_cvt_pk_bf16_f32 v216,v208,v209\nv_mfma_f32_16x16x32_bf16 v[124:127],v[180:183],v[236:239],v[124:127]\ns_waitcnt vmcnt(17)\nds_read_b128 v[176:179],v146 offset:6144\n"
"v_cvt_pk_bf16_f32 v217,v210,v211\nv_mfma_f32_16x16x32_bf16 v[120:123],v[180:183],v[232:235],v[120:123]\ns_waitcnt lgkmcnt(5)\nv_cvt_pk_bf16_f32 v218,v212,v213\nv_cvt_pk_bf16_f32 v219,v214,v215\nv_mfma_f32_16x16x32_bf16 v[0:3],v[152:155],v[184:187],v[0:3]\nds_read_b128 v[180:183],v146 offset:7168\nv_mfma_f32_16x16x32_bf16 v[4:7],v[152:155],v[188:191],v[4:7]\nbuffer_store_dwordx4 v[216:219],v252,s[8:11],s73 offen sc1\n"
"v_mfma_f32_16x16x32_bf16 v[12:15],v[156:159],v[188:191],v[12:15]\nds_read_b128 v[152:155],v146 offset:8192\nv_accvgpr_read_b32 v220,a220\nv_mfma_f32_16x16x32_bf16 v[8:11],v[156:159],v[184:187],v[8:11]\ns_waitcnt lgkmcnt(5)\nv_accvgpr_read_b32 v221,a221\nv_accvgpr_read_b32 v222,a222\nv_mfma_f32_16x16x32_bf16 v[16:19],v[160:163],v[184:187],v[16:19]\ns_add_u32 s73,s73,s72\nds_read_b128 v[156:159],v146 offset:9216\n"
"v_accvgpr_read_b32 v223,a223\nv_mfma_f32_16x16x32_bf16 v[20:23],v[160:163],v[188:191],v[20:23]\nv_accvgpr_read_b32 v224,a252\nv_accvgpr_read_b32 v225,a253\nv_mfma_f32_16x16x32_bf16 v[28:31],v[164:167],v[188:191],v[28:31]\nds_read_b128 v[160:163],v146 offset:10240\nv_accvgpr_read_b32 v226,a254\nv_mfma_f32_16x16x32_bf16 v[24:27],v[164:167],v[184:187],v[24:27]\ns_waitcnt lgkmcnt(5)\nv_accvgpr_read_b32 v227,a255\n"
"v_cvt_pk_bf16_f32 v228,v220,v221\nv_mfma_f32_16x16x32_bf16 v[32:35],v[168:171],v[184:187],v[32:35]\nds_read_b128 v[164:167],v146 offset:11264\nv_cvt_pk_bf16_f32 v229,v222,v223\nv_mfma_f32_16x16x32_bf16 v[36:39],v[168:171],v[188:191],v[36:39]\nv_cvt_pk_bf16_f32 v230,v224,v225\nv_cvt_pk_bf16_f32 v231,v226,v227\nv_mfma_f32_16x16x32_bf16 v[44:47],v[172:175],v[188:191],v[44:47]\nds_read_b128 v[168:171],v146 offset:12288\n"
"v_mfma_f32_16x16x32_bf16 v[40:43],v[172:175],v[184:187],v[40:43]\ns_waitcnt lgkmcnt(5)\nbuffer_store_dwordx4 v[228:231],v252,s[8:11],s73 offen sc1\nv_mfma_f32_16x16x32_bf16 v[48:51],v[176:179],v[184:187],v[48:51]\nds_read_b128 v[172:175],v146 offset:13312\nv_mfma_f32_16x16x32_bf16 v[52:55],v[176:179],v[188:191],v[52:55]\nv_mfma_f32_16x16x32_bf16 v[60:63],v[180:183],v[188:191],v[60:63]\nds_read_b128 v[176:179],v146 offset:14336\n"
"v_mfma_f32_16x16x32_bf16 v[56:59],v[180:183],v[184:187],v[56:59]\ns_waitcnt lgkmcnt(5)\nv_mfma_f32_16x16x32_bf16 v[64:67],v[152:155],v[184:187],v[64:67]\nds_read_b128 v[180:183],v146 offset:15360\nv_mfma_f32_16x16x32_bf16 v[68:71],v[152:155],v[188:191],v[68:71]\nv_mfma_f32_16x16x32_bf16 v[76:79],v[156:159],v[188:191],v[76:79]\nds_read_b128 v[152:155],v146 offset:16384\nv_mfma_f32_16x16x32_bf16 v[72:75],v[156:159],v[184:187],v[72:75]\n"
"s_waitcnt lgkmcnt(5)\nv_mfma_f32_16x16x32_bf16 v[80:83],v[160:163],v[184:187],v[80:83]\nds_read_b128 v[156:159],v146 offset:17408\nv_mfma_f32_16x16x32_bf16 v[84:87],v[160:163],v[188:191],v[84:87]\nv_mfma_f32_16x16x32_bf16 v[92:95],v[164:167],v[188:191],v[92:95]\nds_read_b128 v[160:163],v146 offset:18432\nv_mfma_f32_16x16x32_bf16 v[88:91],v[164:167],v[184:187],v[88:91]\ns_waitcnt lgkmcnt(5)\nv_mfma_f32_16x16x32_bf16 v[96:99],v[168:171],v[184:187],v[96:99]\n"
"ds_read_b128 v[164:167],v146 offset:19456\nv_mfma_f32_16x16x32_bf16 v[100:103],v[168:171],v[188:191],v[100:103]\nv_mfma_f32_16x16x32_bf16 v[108:111],v[172:175],v[188:191],v[108:111]\nds_read_b128 v[168:171],v146 offset:20480\nv_mfma_f32_16x16x32_bf16 v[104:107],v[172:175],v[184:187],v[104:107]\ns_waitcnt lgkmcnt(5)\nv_mfma_f32_16x16x32_bf16 v[112:115],v[176:179],v[184:187],v[112:115]\nds_read_b128 v[172:175],v146 offset:21504\n"
"v_mfma_f32_16x16x32_bf16 v[116:119],v[176:179],v[188:191],v[116:119]\nv_mfma_f32_16x16x32_bf16 v[124:127],v[180:183],v[188:191],v[124:127]\ns_waitcnt vmcnt(14)\nds_read_b128 v[176:179],v146 offset:22528\nv_mfma_f32_16x16x32_bf16 v[120:123],v[180:183],v[184:187],v[120:123]\ns_waitcnt lgkmcnt(5)\nv_mfma_f32_16x16x32_bf16 v[0:3],v[152:155],v[192:195],v[0:3]\nds_read_b128 v[180:183],v146 offset:23552\n"
"v_mfma_f32_16x16x32_bf16 v[4:7],v[152:155],v[196:199],v[4:7]\nv_mfma_f32_16x16x32_bf16 v[12:15],v[156:159],v[196:199],v[12:15]\nds_read_b128 v[152:155],v146 offset:24576\nv_mfma_f32_16x16x32_bf16 v[8:11],v[156:159],v[192:195],v[8:11]\ns_waitcnt lgkmcnt(5)\nv_mfma_f32_16x16x32_bf16 v[16:19],v[160:163],v[192:195],v[16:19]\nds_read_b128 v[156:159],v146 offset:25600\nv_mfma_f32_16x16x32_bf16 v[20:23],v[160:163],v[196:199],v[20:23]\n"
"v_mfma_f32_16x16x32_bf16 v[28:31],v[164:167],v[196:199],v[28:31]\nds_read_b128 v[160:163],v146 offset:26624\nv_mfma_f32_16x16x32_bf16 v[24:27],v[164:167],v[192:195],v[24:27]\ns_waitcnt lgkmcnt(5)\nv_mfma_f32_16x16x32_bf16 v[32:35],v[168:171],v[192:195],v[32:35]\nds_read_b128 v[164:167],v146 offset:27648\nv_mfma_f32_16x16x32_bf16 v[36:39],v[168:171],v[196:199],v[36:39]\nv_cvt_pk_bf16_f32 v0,v0,v1\nv_cvt_pk_bf16_f32 v1,v2,v3\n"
"v_mfma_f32_16x16x32_bf16 v[44:47],v[172:175],v[196:199],v[44:47]\nds_read_b128 v[168:171],v146 offset:28672\nv_cvt_pk_bf16_f32 v2,v8,v9\nv_mfma_f32_16x16x32_bf16 v[40:43],v[172:175],v[192:195],v[40:43]\ns_waitcnt lgkmcnt(5)\nv_cvt_pk_bf16_f32 v3,v10,v11\nv_cvt_pk_bf16_f32 v4,v4,v5\nv_mfma_f32_16x16x32_bf16 v[48:51],v[176:179],v[192:195],v[48:51]\nds_read_b128 v[172:175],v146 offset:29696\nv_cvt_pk_bf16_f32 v5,v6,v7\n"
"v_mfma_f32_16x16x32_bf16 v[52:55],v[176:179],v[196:199],v[52:55]\nv_cvt_pk_bf16_f32 v6,v12,v13\nv_cvt_pk_bf16_f32 v7,v14,v15\nv_mfma_f32_16x16x32_bf16 v[60:63],v[180:183],v[196:199],v[60:63]\nds_read_b128 v[176:179],v146 offset:30720\nv_cvt_pk_bf16_f32 v16,v16,v17\nv_mfma_f32_16x16x32_bf16 v[56:59],v[180:183],v[192:195],v[56:59]\ns_waitcnt lgkmcnt(5)\nv_cvt_pk_bf16_f32 v17,v18,v19\nv_cvt_pk_bf16_f32 v18,v24,v25\n"
"v_mfma_f32_16x16x32_bf16 v[64:67],v[152:155],v[192:195],v[64:67]\nds_read_b128 v[180:183],v146 offset:31744\nv_cvt_pk_bf16_f32 v19,v26,v27\nv_mfma_f32_16x16x32_bf16 v[68:71],v[152:155],v[196:199],v[68:71]\nv_cvt_pk_bf16_f32 v20,v20,v21\nv_cvt_pk_bf16_f32 v21,v22,v23\nv_mfma_f32_16x16x32_bf16 v[76:79],v[156:159],v[196:199],v[76:79]\nv_cvt_pk_bf16_f32 v22,v28,v29\nv_cvt_pk_bf16_f32 v23,v30,v31\nv_mfma_f32_16x16x32_bf16 v[72:75],v[156:159],v[192:195],v[72:75]\n"
"s_waitcnt lgkmcnt(4)\nv_cvt_pk_bf16_f32 v32,v32,v33\nv_cvt_pk_bf16_f32 v33,v34,v35\nv_mfma_f32_16x16x32_bf16 v[80:83],v[160:163],v[192:195],v[80:83]\nv_cvt_pk_bf16_f32 v34,v40,v41\nv_cvt_pk_bf16_f32 v35,v42,v43\nv_mfma_f32_16x16x32_bf16 v[84:87],v[160:163],v[196:199],v[84:87]\nv_cvt_pk_bf16_f32 v36,v36,v37\nv_cvt_pk_bf16_f32 v37,v38,v39\nv_mfma_f32_16x16x32_bf16 v[92:95],v[164:167],v[196:199],v[92:95]\n"
"v_cvt_pk_bf16_f32 v38,v44,v45\nv_cvt_pk_bf16_f32 v39,v46,v47\nv_mfma_f32_16x16x32_bf16 v[88:91],v[164:167],v[192:195],v[88:91]\ns_waitcnt lgkmcnt(2)\nv_cvt_pk_bf16_f32 v48,v48,v49\nv_cvt_pk_bf16_f32 v49,v50,v51\nv_mfma_f32_16x16x32_bf16 v[96:99],v[168:171],v[192:195],v[96:99]\nv_cvt_pk_bf16_f32 v50,v56,v57\nv_cvt_pk_bf16_f32 v51,v58,v59\nv_mfma_f32_16x16x32_bf16 v[100:103],v[168:171],v[196:199],v[100:103]\n"
"v_cvt_pk_bf16_f32 v52,v52,v53\nv_cvt_pk_bf16_f32 v53,v54,v55\nv_mfma_f32_16x16x32_bf16 v[108:111],v[172:175],v[196:199],v[108:111]\nv_cvt_pk_bf16_f32 v54,v60,v61\nv_cvt_pk_bf16_f32 v55,v62,v63\nv_mfma_f32_16x16x32_bf16 v[104:107],v[172:175],v[192:195],v[104:107]\ns_waitcnt lgkmcnt(0)\nv_cvt_pk_bf16_f32 v64,v64,v65\nv_cvt_pk_bf16_f32 v65,v66,v67\nv_mfma_f32_16x16x32_bf16 v[112:115],v[176:179],v[192:195],v[112:115]\n"
"v_cvt_pk_bf16_f32 v66,v72,v73\nv_cvt_pk_bf16_f32 v67,v74,v75\nv_mfma_f32_16x16x32_bf16 v[116:119],v[176:179],v[196:199],v[116:119]\nv_cvt_pk_bf16_f32 v68,v68,v69\nv_cvt_pk_bf16_f32 v69,v70,v71\nv_mfma_f32_16x16x32_bf16 v[124:127],v[180:183],v[196:199],v[124:127]\nv_cvt_pk_bf16_f32 v70,v76,v77\nv_cvt_pk_bf16_f32 v71,v78,v79\nv_mfma_f32_16x16x32_bf16 v[120:123],v[180:183],v[192:195],v[120:123]\nv_cvt_pk_bf16_f32 v80,v80,v81\n"
"v_cvt_pk_bf16_f32 v81,v82,v83\nv_cvt_pk_bf16_f32 v82,v88,v89\nv_cvt_pk_bf16_f32 v83,v90,v91\nv_cvt_pk_bf16_f32 v84,v84,v85\nv_cvt_pk_bf16_f32 v85,v86,v87\nv_cvt_pk_bf16_f32 v86,v92,v93\nv_cvt_pk_bf16_f32 v87,v94,v95\nv_cvt_pk_bf16_f32 v96,v96,v97\nv_cvt_pk_bf16_f32 v97,v98,v99\nv_cvt_pk_bf16_f32 v98,v104,v105\nv_cvt_pk_bf16_f32 v99,v106,v107\nv_cvt_pk_bf16_f32 v100,v100,v101\nv_cvt_pk_bf16_f32 v101,v102,v103\n"
"v_cvt_pk_bf16_f32 v102,v108,v109\nv_cvt_pk_bf16_f32 v103,v110,v111\nv_cvt_pk_bf16_f32 v112,v112,v113\nv_cvt_pk_bf16_f32 v113,v114,v115\nv_cvt_pk_bf16_f32 v114,v120,v121\nv_cvt_pk_bf16_f32 v115,v122,v123\nv_cvt_pk_bf16_f32 v116,v116,v117\nv_cvt_pk_bf16_f32 v117,v118,v119\nv_cvt_pk_bf16_f32 v118,v124,v125\nv_cvt_pk_bf16_f32 v119,v126,v127\nv_accvgpr_write_b32 a0,v0\nv_accvgpr_write_b32 a1,v1\nv_accvgpr_write_b32 a2,v2\n"
"v_accvgpr_write_b32 a3,v3\nv_accvgpr_write_b32 a4,v16\nv_accvgpr_write_b32 a5,v17\nv_accvgpr_write_b32 a6,v18\nv_accvgpr_write_b32 a7,v19\nv_accvgpr_write_b32 a8,v32\nv_accvgpr_write_b32 a9,v33\nv_accvgpr_write_b32 a10,v34\nv_accvgpr_write_b32 a11,v35\nv_accvgpr_write_b32 a12,v48\nv_accvgpr_write_b32 a13,v49\nv_accvgpr_write_b32 a14,v50\nv_accvgpr_write_b32 a15,v51\nv_accvgpr_write_b32 a16,v64\nv_accvgpr_write_b32 a17,v65\n"
"v_accvgpr_write_b32 a18,v66\nv_accvgpr_write_b32 a19,v67\nv_accvgpr_write_b32 a20,v80\nv_accvgpr_write_b32 a21,v81\nv_accvgpr_write_b32 a22,v82\nv_accvgpr_write_b32 a23,v83\nv_accvgpr_write_b32 a24,v96\nv_accvgpr_write_b32 a25,v97\nv_accvgpr_write_b32 a26,v98\nv_accvgpr_write_b32 a27,v99\nv_accvgpr_write_b32 a28,v112\nv_accvgpr_write_b32 a29,v113\nv_accvgpr_write_b32 a30,v114\nv_accvgpr_write_b32 a31,v115\n"
"v_accvgpr_write_b32 a32,v4\nv_accvgpr_write_b32 a33,v5\nv_accvgpr_write_b32 a34,v6\nv_accvgpr_write_b32 a35,v7\nv_accvgpr_write_b32 a36,v20\nv_accvgpr_write_b32 a37,v21\nv_accvgpr_write_b32 a38,v22\nv_accvgpr_write_b32 a39,v23\nv_accvgpr_write_b32 a40,v36\nv_accvgpr_write_b32 a41,v37\nv_accvgpr_write_b32 a42,v38\nv_accvgpr_write_b32 a43,v39\nv_accvgpr_write_b32 a44,v52\nv_accvgpr_write_b32 a45,v53\n"
"v_accvgpr_write_b32 a46,v54\nv_accvgpr_write_b32 a47,v55\nv_accvgpr_write_b32 a48,v68\nv_accvgpr_write_b32 a49,v69\nv_accvgpr_write_b32 a50,v70\nv_accvgpr_write_b32 a51,v71\nv_accvgpr_write_b32 a52,v84\nv_accvgpr_write_b32 a53,v85\nv_accvgpr_write_b32 a54,v86\nv_accvgpr_write_b32 a55,v87\nv_accvgpr_write_b32 a56,v100\nv_accvgpr_write_b32 a57,v101\nv_accvgpr_write_b32 a58,v102\nv_accvgpr_write_b32 a59,v103\n"
"v_accvgpr_write_b32 a60,v116\nv_accvgpr_write_b32 a61,v117\nv_accvgpr_write_b32 a62,v118\nv_accvgpr_write_b32 a63,v119\nv_lshlrev_b32 v248,7,v253\ns_lshl2_add_u32 s7,s17,s14\ns_lshl_b32 s7,s7,13\nv_add_u32 v200,s7,v248\nv_mov_b32 v198,0xff800000\nv_add_u32 v196,0x8000,v252\nv_add_u32 v197,0x18000,v252\nv_and_b32 v202,15,v253\nv_lshrrev_b32 v203,4,v253\nv_lshlrev_b32 v204,2,v203\nv_add_u32 v205,0,v204\n"
"v_cmp_le_u32 s[44:45],v205,v202\nv_add_u32 v205,1,v204\nv_cmp_le_u32 s[46:47],v205,v202\nv_add_u32 v205,2,v204\nv_cmp_le_u32 s[48:49],v205,v202\nv_add_u32 v205,3,v204\nv_cmp_le_u32 s[50:51],v205,v202\nv_lshrrev_b32 v204,3,v202\nv_mul_u32_u24 v204,114688,v204\nv_lshl_add_u32 v204,v203,7,v204\nv_and_b32 v205,7,v202\nv_lshl_add_u32 v199,v205,4,v204\ns_waitcnt vmcnt(0)\ns_barrier\nbuffer_store_dwordx2 v[250:251],off,s[8:11],s87 sc1\n"
"s_or_b32 s7,s6,s14\ns_cmp_eq_u32 s7,0\ns_mov_b32 s7,1073741824\ns_cselect_b32 s7,117440512,s7\nbuffer_store_dwordx2 v[250:251],off,s[8:11],s7 sc1\nv_mov_b32 v0,0\nv_mov_b32 v1,0\nv_mov_b32 v2,0\nv_mov_b32 v3,0\nv_mov_b32 v4,0\nv_mov_b32 v5,0\nv_mov_b32 v6,0\nv_mov_b32 v7,0\nv_mov_b32 v8,0\nv_mov_b32 v9,0\nv_mov_b32 v10,0\nv_mov_b32 v11,0\nv_mov_b32 v12,0\nv_mov_b32 v13,0\nv_mov_b32 v14,0\nv_mov_b32 v15,0\n"
"v_mov_b32 v16,0\nv_mov_b32 v17,0\nv_mov_b32 v18,0\nv_mov_b32 v19,0\nv_mov_b32 v20,0\nv_mov_b32 v21,0\nv_mov_b32 v22,0\nv_mov_b32 v23,0\nv_mov_b32 v24,0\nv_mov_b32 v25,0\nv_mov_b32 v26,0\nv_mov_b32 v27,0\nv_mov_b32 v28,0\nv_mov_b32 v29,0\nv_mov_b32 v30,0\nv_mov_b32 v31,0\nv_mov_b32 v32,0\nv_mov_b32 v33,0\nv_mov_b32 v34,0\nv_mov_b32 v35,0\nv_mov_b32 v36,0\nv_mov_b32 v37,0\nv_mov_b32 v38,0\nv_mov_b32 v39,0\n"
"v_mov_b32 v40,0\nv_mov_b32 v41,0\nv_mov_b32 v42,0\nv_mov_b32 v43,0\nv_mov_b32 v44,0\nv_mov_b32 v45,0\nv_mov_b32 v46,0\nv_mov_b32 v47,0\nv_mov_b32 v48,0\nv_mov_b32 v49,0\nv_mov_b32 v50,0\nv_mov_b32 v51,0\nv_mov_b32 v52,0\nv_mov_b32 v53,0\nv_mov_b32 v54,0\nv_mov_b32 v55,0\nv_mov_b32 v56,0\nv_mov_b32 v57,0\nv_mov_b32 v58,0\nv_mov_b32 v59,0\nv_mov_b32 v60,0\nv_mov_b32 v61,0\nv_mov_b32 v62,0\nv_mov_b32 v63,0\n"
"v_mov_b32 v64,0\nv_mov_b32 v65,0\nv_mov_b32 v66,0\nv_mov_b32 v67,0\nv_mov_b32 v68,0\nv_mov_b32 v69,0\nv_mov_b32 v70,0\nv_mov_b32 v71,0\nv_mov_b32 v72,0\nv_mov_b32 v73,0\nv_mov_b32 v74,0\nv_mov_b32 v75,0\nv_mov_b32 v76,0\nv_mov_b32 v77,0\nv_mov_b32 v78,0\nv_mov_b32 v79,0\nv_mov_b32 v80,0\nv_mov_b32 v81,0\nv_mov_b32 v82,0\nv_mov_b32 v83,0\nv_mov_b32 v84,0\nv_mov_b32 v85,0\nv_mov_b32 v86,0\nv_mov_b32 v87,0\n"
"v_mov_b32 v88,0\nv_mov_b32 v89,0\nv_mov_b32 v90,0\nv_mov_b32 v91,0\nv_mov_b32 v92,0\nv_mov_b32 v93,0\nv_mov_b32 v94,0\nv_mov_b32 v95,0\nv_mov_b32 v96,0\nv_mov_b32 v97,0\nv_mov_b32 v98,0\nv_mov_b32 v99,0\nv_mov_b32 v100,0\nv_mov_b32 v101,0\nv_mov_b32 v102,0\nv_mov_b32 v103,0\nv_mov_b32 v104,0\nv_mov_b32 v105,0\nv_mov_b32 v106,0\nv_mov_b32 v107,0\nv_mov_b32 v108,0\nv_mov_b32 v109,0\nv_mov_b32 v110,0\n"
"v_mov_b32 v111,0\nv_mov_b32 v112,0\nv_mov_b32 v113,0\nv_mov_b32 v114,0\nv_mov_b32 v115,0\nv_mov_b32 v116,0\nv_mov_b32 v117,0\nv_mov_b32 v118,0\nv_mov_b32 v119,0\nv_mov_b32 v120,0\nv_mov_b32 v121,0\nv_mov_b32 v122,0\nv_mov_b32 v123,0\nv_mov_b32 v124,0\nv_mov_b32 v125,0\nv_mov_b32 v126,0\nv_mov_b32 v127,0\nv_mov_b32 v184,0\nv_mov_b32 v185,0\nv_mov_b32 v186,0\nv_mov_b32 v187,0\nv_mov_b32 v188,0\nv_mov_b32 v189,0\n"
"v_mov_b32 v190,0\nv_mov_b32 v191,0\nv_mov_b32 v192,0\nv_mov_b32 v193,0\nv_mov_b32 v194,0\nv_mov_b32 v195,0\ns_mov_b32 s37,0xbf800000\ns_mov_b32 s38,0xff7fffff\nv_lshlrev_b32 v246,7,v253\ns_mov_b64 exec,0xffff\ns_mov_b32 s7,0\nL_fused_block_poll_15:\nbuffer_load_dwordx2 v[244:245],v246,s[8:11],s95 offen sc1\ns_add_u32 s7,s7,1\ns_waitcnt vmcnt(0)\nv_cmp_eq_u64 vcc,s[12:13],v[244:245]\ns_nop 1\ns_cmp_eq_u64 vcc,exec\n"
"s_cbranch_scc1 L_fused_block_polld_16\ns_cmp_lt_u32 s7,131072\ns_cbranch_scc1 L_fused_block_poll_15\nL_fused_block_polld_16:\ns_mov_b64 exec,-1\ns_add_u32 m0,s15,32768\ns_nop 0\nbuffer_load_dwordx4 v252,s[8:11],s25 offen sc1 lds\nbuffer_load_dwordx4 v252,s[8:11],s25 offen offset:1024 sc1 lds\nbuffer_load_dwordx4 v252,s[8:11],s25 offen offset:2048 sc1 lds\nbuffer_load_dwordx4 v252,s[8:11],s25 offen offset:3072 sc1 lds\n"
"s_add_u32 s26,s25,0x4000\ns_add_u32 s25,s25,0x8000\ns_cmp_eq_u32 s25,s27\ns_cselect_b32 s25,s28,s25\ns_cselect_b32 s27,s29,s27\ns_cselect_b32 s28,s30,s28\ns_cselect_b32 s29,s31,s29\ns_cselect_b32 s30,1073741824,s30\ns_add_u32 s33,s25,0x4000\ns_add_u32 m0,s15,49152\ns_nop 0\nbuffer_load_dwordx4 v252,s[8:11],s25 offen sc1 lds\nbuffer_load_dwordx4 v252,s[8:11],s25 offen offset:1024 sc1 lds\nbuffer_load_dwordx4 v252,s[8:11],s25 offen offset:2048 sc1 lds\n"
"buffer_load_dwordx4 v252,s[8:11],s25 offen offset:3072 sc1 lds\ns_add_u32 s25,s25,0x8000\ns_cmp_eq_u32 s25,s27\ns_cselect_b32 s25,s28,s25\ns_cselect_b32 s27,s29,s27\ns_cselect_b32 s28,s30,s28\ns_cselect_b32 s29,s31,s29\ns_cselect_b32 s30,1073741824,s30\ns_add_u32 m0,s15,98304\ns_nop 0\nbuffer_load_dwordx4 v252,s[8:11],s26 offen sc1 lds\nbuffer_load_dwordx4 v252,s[8:11],s26 offen offset:1024 sc1 lds\n"
"buffer_load_dwordx4 v252,s[8:11],s26 offen offset:2048 sc1 lds\nbuffer_load_dwordx4 v252,s[8:11],s26 offen offset:3072 sc1 lds\ns_mov_b32 s26,s33\ns_add_u32 m0,s15,114688\ns_nop 0\nbuffer_load_dwordx4 v252,s[8:11],s26 offen sc1 lds\nbuffer_load_dwordx4 v252,s[8:11],s26 offen offset:1024 sc1 lds\nbuffer_load_dwordx4 v252,s[8:11],s26 offen offset:2048 sc1 lds\nbuffer_load_dwordx4 v252,s[8:11],s26 offen offset:3072 sc1 lds\n"
"s_add_u32 s26,s25,0x4000\ns_add_u32 m0,s15,0\ns_nop 0\nbuffer_load_dwordx4 v252,s[8:11],s25 offen sc1 lds\nbuffer_load_dwordx4 v252,s[8:11],s25 offen offset:1024 sc1 lds\nbuffer_load_dwordx4 v252,s[8:11],s25 offen offset:2048 sc1 lds\nbuffer_load_dwordx4 v252,s[8:11],s25 offen offset:3072 sc1 lds\ns_add_u32 s25,s25,0x8000\ns_cmp_eq_u32 s25,s27\ns_cselect_b32 s25,s28,s25\ns_cselect_b32 s27,s29,s27\n"
"s_cselect_b32 s28,s30,s28\ns_cselect_b32 s29,s31,s29\ns_cselect_b32 s30,1073741824,s30\ns_add_u32 m0,s15,16384\ns_nop 0\nbuffer_load_dwordx4 v252,s[8:11],s25 offen sc1 lds\nbuffer_load_dwordx4 v252,s[8:11],s25 offen offset:1024 sc1 lds\ns_waitcnt vmcnt(10)\ns_barrier\nds_read_b128 a[224:227],v196 offset:0\nds_read_b128 a[228:231],v196 offset:8192\nds_read_b128 a[232:235],v196 offset:1024\nds_read_b128 a[236:239],v196 offset:9216\n"
"ds_read_b128 a[240:243],v196 offset:2048\nds_read_b128 a[244:247],v196 offset:10240\nds_read_b128 a[248:251],v196 offset:3072\nL_fused_block_pro_17:\ns_waitcnt lgkmcnt(5)\nv_mfma_f32_16x16x32_bf16 v[128:131],a[224:227],a[0:3],v[184:187]\ns_lshl_b32 s36,s14,4\nds_read_b128 a[252:255],v196 offset:11264\nv_mfma_f32_16x16x32_bf16 v[132:135],a[224:227],a[32:35],v[188:191]\nbuffer_store_dwordx4 a[0:3],v252,s[8:11],s93 offen offset:0 sc1\n"
"v_mfma_f32_16x16x32_bf16 v[140:143],a[228:231],a[32:35],v[188:191]\nds_read_b128 a[224:227],v196 offset:4096\nv_mfma_f32_16x16x32_bf16 v[136:139],a[228:231],a[0:3],v[184:187]\ns_waitcnt lgkmcnt(5)\nbuffer_store_dwordx4 a[4:7],v252,s[8:11],s93 offen offset:1024 sc1\nv_mfma_f32_16x16x32_bf16 v[128:131],a[232:235],a[4:7],v[128:131]\nds_read_b128 a[228:231],v196 offset:12288\nv_mfma_f32_16x16x32_bf16 v[132:135],a[232:235],a[36:39],v[132:135]\n"
"buffer_store_dwordx4 a[8:11],v252,s[8:11],s93 offen offset:2048 sc1\nv_mfma_f32_16x16x32_bf16 v[140:143],a[236:239],a[36:39],v[140:143]\nds_read_b128 a[232:235],v196 offset:5120\nv_mfma_f32_16x16x32_bf16 v[136:139],a[236:239],a[4:7],v[136:139]\ns_waitcnt lgkmcnt(5)\nbuffer_store_dwordx4 a[12:15],v252,s[8:11],s93 offen offset:3072 sc1\nv_mfma_f32_16x16x32_bf16 v[128:131],a[240:243],a[8:11],v[128:131]\n"
"s_add_u32 s93,s93,0x1000\nds_read_b128 a[236:239],v196 offset:13312\nv_mfma_f32_16x16x32_bf16 v[132:135],a[240:243],a[40:43],v[132:135]\ns_sub_u32 s34,s22,4\nbuffer_store_dwordx4 a[16:19],v252,s[8:11],s93 offen offset:0 sc1\nv_mfma_f32_16x16x32_bf16 v[140:143],a[244:247],a[40:43],v[140:143]\ns_cmp_eq_u32 s23,0\nds_read_b128 a[240:243],v196 offset:6144\nv_mfma_f32_16x16x32_bf16 v[136:139],a[244:247],a[8:11],v[136:139]\n"
"s_waitcnt lgkmcnt(5)\nbuffer_store_dwordx4 a[20:23],v252,s[8:11],s93 offen offset:1024 sc1\nv_mfma_f32_16x16x32_bf16 v[128:131],a[248:251],a[12:15],v[128:131]\ns_cselect_b32 s34,s22,s34\nds_read_b128 a[244:247],v196 offset:14336\nv_mfma_f32_16x16x32_bf16 v[132:135],a[248:251],a[44:47],v[132:135]\ns_lshr_b32 s34,s34,1\nbuffer_store_dwordx4 a[24:27],v252,s[8:11],s93 offen offset:2048 sc1\nv_mfma_f32_16x16x32_bf16 v[140:143],a[252:255],a[44:47],v[140:143]\n"
"s_mov_b32 s35,2\nds_read_b128 a[248:251],v196 offset:7168\nv_mfma_f32_16x16x32_bf16 v[136:139],a[252:255],a[12:15],v[136:139]\ns_waitcnt lgkmcnt(5)\nbuffer_store_dwordx4 a[28:31],v252,s[8:11],s93 offen offset:3072 sc1\nv_mfma_f32_16x16x32_bf16 v[128:131],a[224:227],a[16:19],v[128:131]\ns_add_u32 s93,s93,0x1000\nds_read_b128 a[252:255],v196 offset:15360\nv_mfma_f32_16x16x32_bf16 v[132:135],a[224:227],a[48:51],v[132:135]\n"
"buffer_store_dwordx4 a[32:35],v252,s[8:11],s93 offen offset:0 sc1\nv_mfma_f32_16x16x32_bf16 v[140:143],a[228:231],a[48:51],v[140:143]\nds_read_b128 a[224:227],v196 offset:16384\nv_mfma_f32_16x16x32_bf16 v[136:139],a[228:231],a[16:19],v[136:139]\ns_waitcnt lgkmcnt(5)\nbuffer_store_dwordx4 a[36:39],v252,s[8:11],s93 offen offset:1024 sc1\nv_mfma_f32_16x16x32_bf16 v[128:131],a[232:235],a[20:23],v[128:131]\n"
"ds_read_b128 a[228:231],v196 offset:24576\nv_mfma_f32_16x16x32_bf16 v[132:135],a[232:235],a[52:55],v[132:135]\nbuffer_store_dwordx4 a[40:43],v252,s[8:11],s93 offen offset:2048 sc1\nv_mfma_f32_16x16x32_bf16 v[140:143],a[236:239],a[52:55],v[140:143]\nds_read_b128 a[232:235],v196 offset:17408\nv_mfma_f32_16x16x32_bf16 v[136:139],a[236:239],a[20:23],v[136:139]\ns_waitcnt lgkmcnt(5)\nbuffer_store_dwordx4 a[44:47],v252,s[8:11],s93 offen offset:3072 sc1\n"
"v_mfma_f32_16x16x32_bf16 v[128:131],a[240:243],a[24:27],v[128:131]\ns_add_u32 s93,s93,0x1000\nds_read_b128 a[236:239],v196 offset:25600\nv_mfma_f32_16x16x32_bf16 v[132:135],a[240:243],a[56:59],v[132:135]\nbuffer_store_dwordx4 a[48:51],v252,s[8:11],s93 offen offset:0 sc1\nv_mfma_f32_16x16x32_bf16 v[140:143],a[244:247],a[56:59],v[140:143]\nds_read_b128 a[240:243],v196 offset:18432\nv_mfma_f32_16x16x32_bf16 v[136:139],a[244:247],a[24:27],v[136:139]\n"
"s_waitcnt lgkmcnt(5)\nbuffer_store_dwordx4 a[52:55],v252,s[8:11],s93 offen offset:1024 sc1\nv_mfma_f32_16x16x32_bf16 v[128:131],a[248:251],a[28:31],v[128:131]\nds_read_b128 a[244:247],v196 offset:26624\nv_mfma_f32_16x16x32_bf16 v[132:135],a[248:251],a[60:63],v[132:135]\nbuffer_store_dwordx4 a[56:59],v252,s[8:11],s93 offen offset:2048 sc1\nv_mfma_f32_16x16x32_bf16 v[140:143],a[252:255],a[60:63],v[140:143]\n"
"ds_read_b128 a[248:251],v196 offset:19456\nv_mfma_f32_16x16x32_bf16 v[136:139],a[252:255],a[28:31],v[136:139]\ns_waitcnt lgkmcnt(5)\nbuffer_store_dwordx4 a[60:63],v252,s[8:11],s93 offen offset:3072 sc1\ns_mov_b32 s93,1073741824\ns_nop 1\ns_cmp_lg_u32 s34,0\ns_cbranch_scc1 L_fused_block_P0_18\ns_cmp_gt_i32 s36,0\ns_cselect_b64 s[42:43],-1,0\ns_cmp_eq_i32 s36,0\ns_cselect_b64 s[52:53],s[44:45],s[42:43]\n"
"s_cselect_b64 s[54:55],s[46:47],s[42:43]\ns_cselect_b64 s[56:57],s[48:49],s[42:43]\ns_cselect_b64 s[58:59],s[50:51],s[42:43]\nv_cndmask_b32 v128,v198,v128,s[52:53]\nv_cndmask_b32 v129,v198,v129,s[54:55]\nv_cndmask_b32 v130,v198,v130,s[56:57]\nv_cndmask_b32 v131,v198,v131,s[58:59]\ns_cmp_gt_i32 s36,-64\ns_cselect_b64 s[42:43],-1,0\ns_cmp_eq_i32 s36,-64\ns_cselect_b64 s[52:53],s[44:45],s[42:43]\ns_cselect_b64 s[54:55],s[46:47],s[42:43]\n"
"s_cselect_b64 s[56:57],s[48:49],s[42:43]\ns_cselect_b64 s[58:59],s[50:51],s[42:43]\nv_cndmask_b32 v132,v198,v132,s[52:53]\nv_cndmask_b32 v133,v198,v133,s[54:55]\nv_cndmask_b32 v134,v198,v134,s[56:57]\nv_cndmask_b32 v135,v198,v135,s[58:59]\ns_cmp_gt_i32 s36,16\ns_cselect_b64 s[42:43],-1,0\ns_cmp_eq_i32 s36,16\ns_cselect_b64 s[52:53],s[44:45],s[42:43]\ns_cselect_b64 s[54:55],s[46:47],s[42:43]\ns_cselect_b64 s[56:57],s[48:49],s[42:43]\n"
"s_cselect_b64 s[58:59],s[50:51],s[42:43]\nv_cndmask_b32 v136,v198,v136,s[52:53]\nv_cndmask_b32 v137,v198,v137,s[54:55]\nv_cndmask_b32 v138,v198,v138,s[56:57]\nv_cndmask_b32 v139,v198,v139,s[58:59]\ns_cmp_gt_i32 s36,-48\ns_cselect_b64 s[42:43],-1,0\ns_cmp_eq_i32 s36,-48\ns_cselect_b64 s[52:53],s[44:45],s[42:43]\ns_cselect_b64 s[54:55],s[46:47],s[42:43]\ns_cselect_b64 s[56:57],s[48:49],s[42:43]\ns_cselect_b64 s[58:59],s[50:51],s[42:43]\n"
"v_cndmask_b32 v140,v198,v140,s[52:53]\nv_cndmask_b32 v141,v198,v141,s[54:55]\nv_cndmask_b32 v142,v198,v142,s[56:57]\nv_cndmask_b32 v143,v198,v143,s[58:59]\ns_sub_u32 s36,s36,32\ns_branch L_fused_block_M0_20\nL_fused_block_P1_19:\nv_mfma_f32_16x16x32_bf16 v[128:131],a[224:227],a[0:3],v[184:187]\nds_read_b128 a[252:255],v196 offset:11264\nv_cvt_pk_bf16_f32 v176,v160,v161\nv_mfma_f32_16x16x32_bf16 v[132:135],a[224:227],a[32:35],v[188:191]\n"
"v_exp_f32 v168,v152\nv_mfma_f32_16x16x32_bf16 v[140:143],a[228:231],a[32:35],v[188:191]\nds_read_b128 a[224:227],v196 offset:4096\nv_add_f32 v194,v194,v168\nv_mfma_f32_16x16x32_bf16 v[136:139],a[228:231],a[0:3],v[184:187]\ns_waitcnt lgkmcnt(5)\nv_exp_f32 v172,v156\nv_mfma_f32_16x16x32_bf16 v[128:131],a[232:235],a[4:7],v[128:131]\nds_read_b128 a[228:231],v196 offset:12288\nv_add_f32 v195,v195,v172\n"
"v_mfma_f32_16x16x32_bf16 v[132:135],a[232:235],a[36:39],v[132:135]\nbuffer_load_dwordx4 v252,s[8:11],s25 offen offset:2048 sc1 lds\nv_mfma_f32_16x16x32_bf16 v[140:143],a[236:239],a[36:39],v[140:143]\nds_read_b128 a[232:235],v196 offset:5120\nv_cvt_pk_bf16_f32 v180,v164,v165\nv_mfma_f32_16x16x32_bf16 v[136:139],a[236:239],a[4:7],v[136:139]\ns_waitcnt lgkmcnt(5)\nv_exp_f32 v169,v153\nv_mfma_f32_16x16x32_bf16 v[128:131],a[240:243],a[8:11],v[128:131]\n"
"ds_read_b128 a[236:239],v196 offset:13312\nv_add_f32 v194,v194,v169\nv_mfma_f32_16x16x32_bf16 v[132:135],a[240:243],a[40:43],v[132:135]\nv_exp_f32 v173,v157\nv_mfma_f32_16x16x32_bf16 v[140:143],a[244:247],a[40:43],v[140:143]\nds_read_b128 a[240:243],v196 offset:6144\nv_add_f32 v195,v195,v173\nv_mfma_f32_16x16x32_bf16 v[136:139],a[244:247],a[8:11],v[136:139]\ns_waitcnt lgkmcnt(5)\nv_exp_f32 v170,v154\n"
"v_mfma_f32_16x16x32_bf16 v[128:131],a[248:251],a[12:15],v[128:131]\nds_read_b128 a[244:247],v196 offset:14336\nv_add_f32 v194,v194,v170\nv_mfma_f32_16x16x32_bf16 v[132:135],a[248:251],a[44:47],v[132:135]\nbuffer_load_dwordx4 v252,s[8:11],s25 offen offset:3072 sc1 lds\nv_mfma_f32_16x16x32_bf16 v[140:143],a[252:255],a[44:47],v[140:143]\ns_add_u32 s84,s32,0x8000\nds_read_b128 a[248:251],v196 offset:7168\n"
"s_and_b32 s84,s84,0xffff\nv_mfma_f32_16x16x32_bf16 v[136:139],a[252:255],a[12:15],v[136:139]\ns_waitcnt lgkmcnt(5)\ns_add_u32 s84,s84,s15\ns_add_u32 m0,s84,0x10000\nv_mfma_f32_16x16x32_bf16 v[128:131],a[224:227],a[16:19],v[128:131]\nds_read_b128 a[252:255],v196 offset:15360\nv_cvt_pk_bf16_f32 v177,v162,v163\nv_mfma_f32_16x16x32_bf16 v[132:135],a[224:227],a[48:51],v[132:135]\nv_exp_f32 v174,v158\nv_mfma_f32_16x16x32_bf16 v[140:143],a[228:231],a[48:51],v[140:143]\n"
"ds_read_b128 a[224:227],v197 offset:16384\nv_add_f32 v195,v195,v174\nv_mfma_f32_16x16x32_bf16 v[136:139],a[228:231],a[16:19],v[136:139]\ns_waitcnt lgkmcnt(5)\nv_exp_f32 v171,v155\nv_mfma_f32_16x16x32_bf16 v[128:131],a[232:235],a[20:23],v[128:131]\nds_read_b128 a[228:231],v197 offset:17408\nv_add_f32 v194,v194,v171\nv_mfma_f32_16x16x32_bf16 v[132:135],a[232:235],a[52:55],v[132:135]\nbuffer_load_dwordx4 v252,s[8:11],s26 offen sc1 lds\n"
"v_mfma_f32_16x16x32_bf16 v[140:143],a[236:239],a[52:55],v[140:143]\nds_read_b128 a[232:235],v197 offset:18432\nv_cvt_pk_bf16_f32 v181,v166,v167\nv_mfma_f32_16x16x32_bf16 v[136:139],a[236:239],a[20:23],v[136:139]\ns_waitcnt lgkmcnt(5)\nv_exp_f32 v175,v159\nv_mfma_f32_16x16x32_bf16 v[128:131],a[240:243],a[24:27],v[128:131]\nds_read_b128 a[236:239],v197 offset:19456\nv_add_f32 v195,v195,v175\nv_mfma_f32_16x16x32_bf16 v[132:135],a[240:243],a[56:59],v[132:135]\n"
"v_cvt_pk_bf16_f32 v178,v168,v169\nv_cvt_pk_bf16_f32 v182,v172,v173\nv_mfma_f32_16x16x32_bf16 v[140:143],a[244:247],a[56:59],v[140:143]\nds_read_b128 a[240:243],v197 offset:20480\nv_cvt_pk_bf16_f32 v179,v170,v171\nv_mfma_f32_16x16x32_bf16 v[136:139],a[244:247],a[24:27],v[136:139]\ns_waitcnt lgkmcnt(5)\nv_cvt_pk_bf16_f32 v183,v174,v175\nv_max_f32 v202,v194,v195\nv_mfma_f32_16x16x32_bf16 v[128:131],a[248:251],a[28:31],v[128:131]\n"
"ds_read_b128 a[244:247],v197 offset:21504\nv_cmp_nge_f32 vcc,s37,v202\nv_mfma_f32_16x16x32_bf16 v[132:135],a[248:251],a[60:63],v[132:135]\nbuffer_load_dwordx4 v252,s[8:11],s26 offen offset:1024 sc1 lds\nv_mfma_f32_16x16x32_bf16 v[140:143],a[252:255],a[60:63],v[140:143]\nds_read_b128 a[248:251],v197 offset:22528\nv_mfma_f32_16x16x32_bf16 v[136:139],a[252:255],a[28:31],v[136:139]\ns_waitcnt lgkmcnt(5)\n"
"s_cbranch_vccnz L_fused_block_rare_23\nL_fused_block_rarer_24:\nv_mfma_f32_16x16x32_bf16 v[0:3],a[224:227],v[176:179],v[0:3]\ns_add_u32 s33,s25,0x4000\nds_read_b128 a[252:255],v197 offset:23552\nv_add_f32 v192,v192,v194\nv_mfma_f32_16x16x32_bf16 v[4:7],a[224:227],v[180:183],v[4:7]\ns_add_u32 s25,s25,0x8000\nv_add_f32 v193,v193,v195\nv_mfma_f32_16x16x32_bf16 v[12:15],a[228:231],v[180:183],v[12:15]\n"
"s_cmp_eq_u32 s25,s27\nds_read_b128 a[224:227],v197 offset:24576\nv_mfma_f32_16x16x32_bf16 v[8:11],a[228:231],v[176:179],v[8:11]\ns_waitcnt lgkmcnt(5)\nv_exp_f32 v160,v128\nv_mfma_f32_16x16x32_bf16 v[16:19],a[232:235],v[176:179],v[16:19]\ns_cselect_b32 s25,s28,s25\nds_read_b128 a[228:231],v197 offset:25600\nv_mfma_f32_16x16x32_bf16 v[20:23],a[232:235],v[180:183],v[20:23]\ns_cselect_b32 s27,s29,s27\n"
"buffer_load_dwordx4 v252,s[8:11],s26 offen offset:2048 sc1 lds\nv_mfma_f32_16x16x32_bf16 v[28:31],a[236:239],v[180:183],v[28:31]\ns_cselect_b32 s28,s30,s28\nds_read_b128 a[232:235],v197 offset:26624\nv_mfma_f32_16x16x32_bf16 v[24:27],a[236:239],v[176:179],v[24:27]\ns_waitcnt lgkmcnt(5)\nv_exp_f32 v164,v132\nv_mfma_f32_16x16x32_bf16 v[32:35],a[240:243],v[176:179],v[32:35]\ns_cselect_b32 s29,s31,s29\n"
"ds_read_b128 a[236:239],v197 offset:27648\nv_mfma_f32_16x16x32_bf16 v[36:39],a[240:243],v[180:183],v[36:39]\ns_cselect_b32 s30,1073741824,s30\nv_exp_f32 v161,v129\nv_mfma_f32_16x16x32_bf16 v[44:47],a[244:247],v[180:183],v[44:47]\nds_read_b128 a[240:243],v197 offset:28672\nv_add_f32 v194,v160,v161\nv_mfma_f32_16x16x32_bf16 v[40:43],a[244:247],v[176:179],v[40:43]\ns_waitcnt lgkmcnt(5)\nv_exp_f32 v165,v133\n"
"v_mfma_f32_16x16x32_bf16 v[48:51],a[248:251],v[176:179],v[48:51]\nds_read_b128 a[244:247],v197 offset:29696\nv_add_f32 v195,v164,v165\nv_mfma_f32_16x16x32_bf16 v[52:55],a[248:251],v[180:183],v[52:55]\nbuffer_load_dwordx4 v252,s[8:11],s26 offen offset:3072 sc1 lds\nv_mfma_f32_16x16x32_bf16 v[60:63],a[252:255],v[180:183],v[60:63]\ns_add_u32 m0,s32,s15\nds_read_b128 a[248:251],v197 offset:30720\ns_mov_b32 s26,s33\n"
"v_mfma_f32_16x16x32_bf16 v[56:59],a[252:255],v[176:179],v[56:59]\ns_waitcnt lgkmcnt(5)\nv_exp_f32 v162,v130\nv_mfma_f32_16x16x32_bf16 v[64:67],a[224:227],v[176:179],v[64:67]\nds_read_b128 a[252:255],v197 offset:31744\nv_add_f32 v194,v194,v162\nv_mfma_f32_16x16x32_bf16 v[68:71],a[224:227],v[180:183],v[68:71]\ns_waitcnt vmcnt(8)\ns_barrier\nv_mfma_f32_16x16x32_bf16 v[76:79],a[228:231],v[180:183],v[76:79]\n"
"ds_read_b128 a[224:227],v196 offset:16384\nv_mfma_f32_16x16x32_bf16 v[72:75],a[228:231],v[176:179],v[72:75]\ns_waitcnt lgkmcnt(5)\nv_xor_b32 v197,0x8000,v197\nv_mfma_f32_16x16x32_bf16 v[80:83],a[232:235],v[176:179],v[80:83]\nds_read_b128 a[228:231],v196 offset:24576\nv_mfma_f32_16x16x32_bf16 v[84:87],a[232:235],v[180:183],v[84:87]\nbuffer_load_dwordx4 v252,s[8:11],s25 offen sc1 lds\nv_mfma_f32_16x16x32_bf16 v[92:95],a[236:239],v[180:183],v[92:95]\n"
"s_cmp_lg_u32 s39,0\nds_read_b128 a[232:235],v196 offset:17408\nv_mfma_f32_16x16x32_bf16 v[88:91],a[236:239],v[176:179],v[88:91]\ns_waitcnt lgkmcnt(5)\nv_exp_f32 v166,v134\nv_mfma_f32_16x16x32_bf16 v[96:99],a[240:243],v[176:179],v[96:99]\nds_read_b128 a[236:239],v196 offset:25600\ns_cbranch_scc1 L_fused_block_hook_25\nL_fused_block_hookr_26:\nv_add_f32 v195,v195,v166\nv_mfma_f32_16x16x32_bf16 v[100:103],a[240:243],v[180:183],v[100:103]\n"
"s_add_u32 s32,s32,0x4000\nv_exp_f32 v163,v131\nv_mfma_f32_16x16x32_bf16 v[108:111],a[244:247],v[180:183],v[108:111]\ns_and_b32 s32,s32,0xffff\nds_read_b128 a[240:243],v196 offset:18432\nv_add_f32 v194,v194,v163\nv_mfma_f32_16x16x32_bf16 v[104:107],a[244:247],v[176:179],v[104:107]\ns_waitcnt lgkmcnt(5)\nv_exp_f32 v167,v135\nv_mfma_f32_16x16x32_bf16 v[112:115],a[248:251],v[176:179],v[112:115]\nds_read_b128 a[244:247],v196 offset:26624\n"
"v_add_f32 v195,v195,v167\nv_mfma_f32_16x16x32_bf16 v[116:119],a[248:251],v[180:183],v[116:119]\nbuffer_load_dwordx4 v252,s[8:11],s25 offen offset:1024 sc1 lds\nv_mfma_f32_16x16x32_bf16 v[124:127],a[252:255],v[180:183],v[124:127]\nds_read_b128 a[248:251],v196 offset:19456\nv_mfma_f32_16x16x32_bf16 v[120:123],a[252:255],v[176:179],v[120:123]\ns_waitcnt lgkmcnt(5)\nL_fused_block_P0_18:\nv_mfma_f32_16x16x32_bf16 v[144:147],a[224:227],a[0:3],v[184:187]\n"
"ds_read_b128 a[252:255],v196 offset:27648\nv_cvt_pk_bf16_f32 v176,v160,v161\nv_mfma_f32_16x16x32_bf16 v[148:151],a[224:227],a[32:35],v[188:191]\nv_exp_f32 v168,v136\nv_mfma_f32_16x16x32_bf16 v[156:159],a[228:231],a[32:35],v[188:191]\nds_read_b128 a[224:227],v196 offset:20480\nv_add_f32 v194,v194,v168\nv_mfma_f32_16x16x32_bf16 v[152:155],a[228:231],a[0:3],v[184:187]\ns_waitcnt lgkmcnt(5)\nv_exp_f32 v172,v140\n"
"v_mfma_f32_16x16x32_bf16 v[144:147],a[232:235],a[4:7],v[144:147]\nds_read_b128 a[228:231],v196 offset:28672\nv_add_f32 v195,v195,v172\nv_mfma_f32_16x16x32_bf16 v[148:151],a[232:235],a[36:39],v[148:151]\nbuffer_load_dwordx4 v252,s[8:11],s25 offen offset:2048 sc1 lds\nv_mfma_f32_16x16x32_bf16 v[156:159],a[236:239],a[36:39],v[156:159]\nds_read_b128 a[232:235],v196 offset:21504\nv_cvt_pk_bf16_f32 v180,v164,v165\n"
"v_mfma_f32_16x16x32_bf16 v[152:155],a[236:239],a[4:7],v[152:155]\ns_waitcnt lgkmcnt(5)\nv_exp_f32 v169,v137\nv_mfma_f32_16x16x32_bf16 v[144:147],a[240:243],a[8:11],v[144:147]\nds_read_b128 a[236:239],v196 offset:29696\nv_add_f32 v194,v194,v169\nv_mfma_f32_16x16x32_bf16 v[148:151],a[240:243],a[40:43],v[148:151]\nv_exp_f32 v173,v141\nv_mfma_f32_16x16x32_bf16 v[156:159],a[244:247],a[40:43],v[156:159]\n"
"ds_read_b128 a[240:243],v196 offset:22528\nv_add_f32 v195,v195,v173\nv_mfma_f32_16x16x32_bf16 v[152:155],a[244:247],a[8:11],v[152:155]\ns_waitcnt lgkmcnt(5)\nv_exp_f32 v170,v138\nv_mfma_f32_16x16x32_bf16 v[144:147],a[248:251],a[12:15],v[144:147]\nds_read_b128 a[244:247],v196 offset:30720\nv_add_f32 v194,v194,v170\nv_mfma_f32_16x16x32_bf16 v[148:151],a[248:251],a[44:47],v[148:151]\nbuffer_load_dwordx4 v252,s[8:11],s25 offen offset:3072 sc1 lds\n"
"v_mfma_f32_16x16x32_bf16 v[156:159],a[252:255],a[44:47],v[156:159]\ns_add_u32 s84,s32,0x8000\nds_read_b128 a[248:251],v196 offset:23552\ns_and_b32 s84,s84,0xffff\nv_mfma_f32_16x16x32_bf16 v[152:155],a[252:255],a[12:15],v[152:155]\ns_waitcnt lgkmcnt(5)\ns_add_u32 s84,s84,s15\ns_add_u32 m0,s84,0x10000\nv_mfma_f32_16x16x32_bf16 v[144:147],a[224:227],a[16:19],v[144:147]\nds_read_b128 a[252:255],v196 offset:31744\n"
"v_cvt_pk_bf16_f32 v177,v162,v163\nv_mfma_f32_16x16x32_bf16 v[148:151],a[224:227],a[48:51],v[148:151]\nv_exp_f32 v174,v142\nv_mfma_f32_16x16x32_bf16 v[156:159],a[228:231],a[48:51],v[156:159]\nds_read_b128 a[224:227],v197 offset:0\nv_add_f32 v195,v195,v174\nv_mfma_f32_16x16x32_bf16 v[152:155],a[228:231],a[16:19],v[152:155]\ns_waitcnt lgkmcnt(5)\nv_exp_f32 v171,v139\nv_mfma_f32_16x16x32_bf16 v[144:147],a[232:235],a[20:23],v[144:147]\n"
"ds_read_b128 a[228:231],v197 offset:1024\nv_add_f32 v194,v194,v171\nv_mfma_f32_16x16x32_bf16 v[148:151],a[232:235],a[52:55],v[148:151]\nbuffer_load_dwordx4 v252,s[8:11],s26 offen sc1 lds\nv_mfma_f32_16x16x32_bf16 v[156:159],a[236:239],a[52:55],v[156:159]\nds_read_b128 a[232:235],v197 offset:2048\nv_cvt_pk_bf16_f32 v181,v166,v167\nv_mfma_f32_16x16x32_bf16 v[152:155],a[236:239],a[20:23],v[152:155]\ns_waitcnt lgkmcnt(5)\n"
"v_exp_f32 v175,v143\nv_mfma_f32_16x16x32_bf16 v[144:147],a[240:243],a[24:27],v[144:147]\nds_read_b128 a[236:239],v197 offset:3072\nv_add_f32 v195,v195,v175\nv_mfma_f32_16x16x32_bf16 v[148:151],a[240:243],a[56:59],v[148:151]\nv_cvt_pk_bf16_f32 v178,v168,v169\nv_cvt_pk_bf16_f32 v182,v172,v173\nv_mfma_f32_16x16x32_bf16 v[156:159],a[244:247],a[56:59],v[156:159]\nds_read_b128 a[240:243],v197 offset:4096\n"
"v_cvt_pk_bf16_f32 v179,v170,v171\nv_mfma_f32_16x16x32_bf16 v[152:155],a[244:247],a[24:27],v[152:155]\ns_waitcnt lgkmcnt(5)\nv_cvt_pk_bf16_f32 v183,v174,v175\nv_max_f32 v202,v194,v195\nv_mfma_f32_16x16x32_bf16 v[144:147],a[248:251],a[28:31],v[144:147]\nds_read_b128 a[244:247],v197 offset:5120\nv_cmp_nge_f32 vcc,s37,v202\nv_mfma_f32_16x16x32_bf16 v[148:151],a[248:251],a[60:63],v[148:151]\nbuffer_load_dwordx4 v252,s[8:11],s26 offen offset:1024 sc1 lds\n"
"v_mfma_f32_16x16x32_bf16 v[156:159],a[252:255],a[60:63],v[156:159]\nds_read_b128 a[248:251],v197 offset:6144\nv_mfma_f32_16x16x32_bf16 v[152:155],a[252:255],a[28:31],v[152:155]\ns_waitcnt lgkmcnt(5)\ns_cbranch_vccnz L_fused_block_rare_27\nL_fused_block_rarer_28:\nv_mfma_f32_16x16x32_bf16 v[0:3],a[224:227],v[176:179],v[0:3]\ns_add_u32 s33,s25,0x4000\nds_read_b128 a[252:255],v197 offset:7168\nv_add_f32 v192,v192,v194\n"
"v_mfma_f32_16x16x32_bf16 v[4:7],a[224:227],v[180:183],v[4:7]\ns_add_u32 s25,s25,0x8000\nv_add_f32 v193,v193,v195\nv_mfma_f32_16x16x32_bf16 v[12:15],a[228:231],v[180:183],v[12:15]\ns_cmp_eq_u32 s25,s27\nds_read_b128 a[224:227],v197 offset:8192\nv_mfma_f32_16x16x32_bf16 v[8:11],a[228:231],v[176:179],v[8:11]\ns_waitcnt lgkmcnt(5)\nv_exp_f32 v160,v144\nv_mfma_f32_16x16x32_bf16 v[16:19],a[232:235],v[176:179],v[16:19]\n"
"s_cselect_b32 s25,s28,s25\nds_read_b128 a[228:231],v197 offset:9216\nv_mfma_f32_16x16x32_bf16 v[20:23],a[232:235],v[180:183],v[20:23]\ns_cselect_b32 s27,s29,s27\nbuffer_load_dwordx4 v252,s[8:11],s26 offen offset:2048 sc1 lds\nv_mfma_f32_16x16x32_bf16 v[28:31],a[236:239],v[180:183],v[28:31]\ns_cselect_b32 s28,s30,s28\nds_read_b128 a[232:235],v197 offset:10240\nv_mfma_f32_16x16x32_bf16 v[24:27],a[236:239],v[176:179],v[24:27]\n"
"s_waitcnt lgkmcnt(5)\nv_exp_f32 v164,v148\nv_mfma_f32_16x16x32_bf16 v[32:35],a[240:243],v[176:179],v[32:35]\ns_cselect_b32 s29,s31,s29\nds_read_b128 a[236:239],v197 offset:11264\nv_mfma_f32_16x16x32_bf16 v[36:39],a[240:243],v[180:183],v[36:39]\ns_cselect_b32 s30,1073741824,s30\nv_exp_f32 v161,v145\nv_mfma_f32_16x16x32_bf16 v[44:47],a[244:247],v[180:183],v[44:47]\ns_sub_u32 s34,s34,1\nds_read_b128 a[240:243],v197 offset:12288\n"
"v_add_f32 v194,v160,v161\nv_mfma_f32_16x16x32_bf16 v[40:43],a[244:247],v[176:179],v[40:43]\ns_waitcnt lgkmcnt(5)\nv_exp_f32 v165,v149\nv_mfma_f32_16x16x32_bf16 v[48:51],a[248:251],v[176:179],v[48:51]\nds_read_b128 a[244:247],v197 offset:13312\nv_add_f32 v195,v164,v165\nv_mfma_f32_16x16x32_bf16 v[52:55],a[248:251],v[180:183],v[52:55]\nbuffer_load_dwordx4 v252,s[8:11],s26 offen offset:3072 sc1 lds\nv_mfma_f32_16x16x32_bf16 v[60:63],a[252:255],v[180:183],v[60:63]\n"
"s_add_u32 m0,s32,s15\nds_read_b128 a[248:251],v197 offset:14336\ns_mov_b32 s26,s33\nv_mfma_f32_16x16x32_bf16 v[56:59],a[252:255],v[176:179],v[56:59]\ns_waitcnt lgkmcnt(5)\nv_exp_f32 v162,v146\nv_mfma_f32_16x16x32_bf16 v[64:67],a[224:227],v[176:179],v[64:67]\nds_read_b128 a[252:255],v197 offset:15360\nv_add_f32 v194,v194,v162\nv_mfma_f32_16x16x32_bf16 v[68:71],a[224:227],v[180:183],v[68:71]\ns_waitcnt vmcnt(8)\n"
"s_barrier\nv_xor_b32 v196,0x8000,v196\nv_mfma_f32_16x16x32_bf16 v[76:79],a[228:231],v[180:183],v[76:79]\nds_read_b128 a[224:227],v196 offset:0\nv_mfma_f32_16x16x32_bf16 v[72:75],a[228:231],v[176:179],v[72:75]\ns_waitcnt lgkmcnt(5)\nv_exp_f32 v166,v150\nv_mfma_f32_16x16x32_bf16 v[80:83],a[232:235],v[176:179],v[80:83]\nds_read_b128 a[228:231],v196 offset:8192\nv_add_f32 v195,v195,v166\nv_mfma_f32_16x16x32_bf16 v[84:87],a[232:235],v[180:183],v[84:87]\n"
"buffer_load_dwordx4 v252,s[8:11],s25 offen sc1 lds\nv_mfma_f32_16x16x32_bf16 v[92:95],a[236:239],v[180:183],v[92:95]\nds_read_b128 a[232:235],v196 offset:1024\nv_mfma_f32_16x16x32_bf16 v[88:91],a[236:239],v[176:179],v[88:91]\ns_waitcnt lgkmcnt(5)\nv_exp_f32 v163,v147\nv_mfma_f32_16x16x32_bf16 v[96:99],a[240:243],v[176:179],v[96:99]\ns_add_u32 s32,s32,0x4000\nds_read_b128 a[236:239],v196 offset:9216\n"
"v_add_f32 v194,v194,v163\nv_mfma_f32_16x16x32_bf16 v[100:103],a[240:243],v[180:183],v[100:103]\ns_and_b32 s32,s32,0xffff\nv_exp_f32 v167,v151\nv_mfma_f32_16x16x32_bf16 v[108:111],a[244:247],v[180:183],v[108:111]\nds_read_b128 a[240:243],v196 offset:2048\nv_add_f32 v195,v195,v167\nv_mfma_f32_16x16x32_bf16 v[104:107],a[244:247],v[176:179],v[104:107]\ns_waitcnt lgkmcnt(5)\nv_mfma_f32_16x16x32_bf16 v[112:115],a[248:251],v[176:179],v[112:115]\n"
"ds_read_b128 a[244:247],v196 offset:10240\nv_mfma_f32_16x16x32_bf16 v[116:119],a[248:251],v[180:183],v[116:119]\nbuffer_load_dwordx4 v252,s[8:11],s25 offen offset:1024 sc1 lds\nv_mfma_f32_16x16x32_bf16 v[124:127],a[252:255],v[180:183],v[124:127]\nds_read_b128 a[248:251],v196 offset:3072\nv_mfma_f32_16x16x32_bf16 v[120:123],a[252:255],v[176:179],v[120:123]\ns_waitcnt lgkmcnt(5)\ns_cmp_lg_u32 s34,0\n"
"s_cbranch_scc1 L_fused_block_P1_19\ns_cmp_eq_u32 s23,0\ns_cbranch_scc1 L_fused_block_DR_22\nL_fused_block_M1_21:\nv_mfma_f32_16x16x32_bf16 v[128:131],a[224:227],a[0:3],v[184:187]\ns_cmp_gt_i32 s36,0\nds_read_b128 a[252:255],v196 offset:11264\nv_cvt_pk_bf16_f32 v176,v160,v161\nv_mfma_f32_16x16x32_bf16 v[132:135],a[224:227],a[32:35],v[188:191]\ns_cselect_b64 s[42:43],-1,0\nv_exp_f32 v168,v152\nv_mfma_f32_16x16x32_bf16 v[140:143],a[228:231],a[32:35],v[188:191]\n"
"s_cmp_eq_i32 s36,0\nds_read_b128 a[224:227],v196 offset:4096\nv_add_f32 v194,v194,v168\nv_mfma_f32_16x16x32_bf16 v[136:139],a[228:231],a[0:3],v[184:187]\ns_waitcnt lgkmcnt(5)\nv_exp_f32 v172,v156\nv_mfma_f32_16x16x32_bf16 v[128:131],a[232:235],a[4:7],v[128:131]\ns_cselect_b64 s[52:53],s[44:45],s[42:43]\nds_read_b128 a[228:231],v196 offset:12288\nv_add_f32 v195,v195,v172\nv_mfma_f32_16x16x32_bf16 v[132:135],a[232:235],a[36:39],v[132:135]\n"
"s_cselect_b64 s[54:55],s[46:47],s[42:43]\nbuffer_load_dwordx4 v252,s[8:11],s25 offen offset:2048 sc1 lds\nv_mfma_f32_16x16x32_bf16 v[140:143],a[236:239],a[36:39],v[140:143]\ns_cselect_b64 s[56:57],s[48:49],s[42:43]\nds_read_b128 a[232:235],v196 offset:5120\nv_cvt_pk_bf16_f32 v180,v164,v165\nv_mfma_f32_16x16x32_bf16 v[136:139],a[236:239],a[4:7],v[136:139]\ns_waitcnt lgkmcnt(5)\nv_exp_f32 v169,v153\n"
"v_mfma_f32_16x16x32_bf16 v[128:131],a[240:243],a[8:11],v[128:131]\ns_cselect_b64 s[58:59],s[50:51],s[42:43]\nds_read_b128 a[236:239],v196 offset:13312\nv_add_f32 v194,v194,v169\nv_mfma_f32_16x16x32_bf16 v[132:135],a[240:243],a[40:43],v[132:135]\nv_exp_f32 v173,v157\nv_mfma_f32_16x16x32_bf16 v[140:143],a[244:247],a[40:43],v[140:143]\nds_read_b128 a[240:243],v196 offset:6144\nv_add_f32 v195,v195,v173\n"
"v_mfma_f32_16x16x32_bf16 v[136:139],a[244:247],a[8:11],v[136:139]\ns_waitcnt lgkmcnt(5)\nv_exp_f32 v170,v154\nv_mfma_f32_16x16x32_bf16 v[128:131],a[248:251],a[12:15],v[128:131]\nds_read_b128 a[244:247],v196 offset:14336\nv_add_f32 v194,v194,v170\nv_mfma_f32_16x16x32_bf16 v[132:135],a[248:251],a[44:47],v[132:135]\nbuffer_load_dwordx4 v252,s[8:11],s25 offen offset:3072 sc1 lds\nv_mfma_f32_16x16x32_bf16 v[140:143],a[252:255],a[44:47],v[140:143]\n"
"s_add_u32 s84,s32,0x8000\nds_read_b128 a[248:251],v196 offset:7168\ns_and_b32 s84,s84,0xffff\nv_mfma_f32_16x16x32_bf16 v[136:139],a[252:255],a[12:15],v[136:139]\ns_waitcnt lgkmcnt(5)\ns_add_u32 s84,s84,s15\ns_add_u32 m0,s84,0x10000\nv_mfma_f32_16x16x32_bf16 v[128:131],a[224:227],a[16:19],v[128:131]\ns_cmp_gt_i32 s36,-64\nds_read_b128 a[252:255],v196 offset:15360\nv_cvt_pk_bf16_f32 v177,v162,v163\nv_mfma_f32_16x16x32_bf16 v[132:135],a[224:227],a[48:51],v[132:135]\n"
"s_cselect_b64 s[42:43],-1,0\nv_exp_f32 v174,v158\nv_mfma_f32_16x16x32_bf16 v[140:143],a[228:231],a[48:51],v[140:143]\ns_cmp_eq_i32 s36,-64\nds_read_b128 a[224:227],v197 offset:16384\nv_add_f32 v195,v195,v174\nv_mfma_f32_16x16x32_bf16 v[136:139],a[228:231],a[16:19],v[136:139]\ns_waitcnt lgkmcnt(5)\nv_exp_f32 v171,v155\nv_mfma_f32_16x16x32_bf16 v[128:131],a[232:235],a[20:23],v[128:131]\ns_cselect_b64 s[60:61],s[44:45],s[42:43]\n"
"ds_read_b128 a[228:231],v197 offset:17408\nv_add_f32 v194,v194,v171\nv_mfma_f32_16x16x32_bf16 v[132:135],a[232:235],a[52:55],v[132:135]\ns_cselect_b64 s[62:63],s[46:47],s[42:43]\nbuffer_load_dwordx4 v252,s[8:11],s26 offen sc1 lds\nv_mfma_f32_16x16x32_bf16 v[140:143],a[236:239],a[52:55],v[140:143]\ns_cselect_b64 s[64:65],s[48:49],s[42:43]\nds_read_b128 a[232:235],v197 offset:18432\nv_cvt_pk_bf16_f32 v181,v166,v167\n"
"v_mfma_f32_16x16x32_bf16 v[136:139],a[236:239],a[20:23],v[136:139]\ns_waitcnt lgkmcnt(5)\nv_exp_f32 v175,v159\nv_mfma_f32_16x16x32_bf16 v[128:131],a[240:243],a[24:27],v[128:131]\ns_cselect_b64 s[66:67],s[50:51],s[42:43]\nds_read_b128 a[236:239],v197 offset:19456\nv_add_f32 v195,v195,v175\ns_cmp_gt_i32 s36,16\ns_cselect_b64 s[42:43],-1,0\ns_cmp_eq_i32 s36,16\ns_cselect_b64 s[68:69],s[44:45],s[42:43]\n"
"s_cselect_b64 s[70:71],s[46:47],s[42:43]\ns_cselect_b64 s[72:73],s[48:49],s[42:43]\ns_cselect_b64 s[74:75],s[50:51],s[42:43]\ns_cmp_gt_i32 s36,-48\ns_cselect_b64 s[42:43],-1,0\ns_cmp_eq_i32 s36,-48\ns_cselect_b64 s[76:77],s[44:45],s[42:43]\ns_cselect_b64 s[78:79],s[46:47],s[42:43]\ns_cselect_b64 s[80:81],s[48:49],s[42:43]\ns_cselect_b64 s[82:83],s[50:51],s[42:43]\nv_mfma_f32_16x16x32_bf16 v[132:135],a[240:243],a[56:59],v[132:135]\n"
"s_sub_u32 s36,s36,32\nv_cvt_pk_bf16_f32 v178,v168,v169\nv_cvt_pk_bf16_f32 v182,v172,v173\nv_mfma_f32_16x16x32_bf16 v[140:143],a[244:247],a[56:59],v[140:143]\nds_read_b128 a[240:243],v197 offset:20480\nv_cvt_pk_bf16_f32 v179,v170,v171\nv_mfma_f32_16x16x32_bf16 v[136:139],a[244:247],a[24:27],v[136:139]\ns_waitcnt lgkmcnt(5)\nv_cvt_pk_bf16_f32 v183,v174,v175\nv_max_f32 v202,v194,v195\nv_mfma_f32_16x16x32_bf16 v[128:131],a[248:251],a[28:31],v[128:131]\n"
"ds_read_b128 a[244:247],v197 offset:21504\nv_cmp_nge_f32 vcc,s37,v202\nv_mfma_f32_16x16x32_bf16 v[132:135],a[248:251],a[60:63],v[132:135]\nbuffer_load_dwordx4 v252,s[8:11],s26 offen offset:1024 sc1 lds\nv_mfma_f32_16x16x32_bf16 v[140:143],a[252:255],a[60:63],v[140:143]\nds_read_b128 a[248:251],v197 offset:22528\nv_mfma_f32_16x16x32_bf16 v[136:139],a[252:255],a[28:31],v[136:139]\ns_waitcnt lgkmcnt(5)\n"
"s_cbranch_vccnz L_fused_block_rare_31\nL_fused_block_rarer_32:\nv_mfma_f32_16x16x32_bf16 v[0:3],a[224:227],v[176:179],v[0:3]\ns_add_u32 s33,s25,0x4000\nds_read_b128 a[252:255],v197 offset:23552\nv_add_f32 v192,v192,v194\nv_mfma_f32_16x16x32_bf16 v[4:7],a[224:227],v[180:183],v[4:7]\ns_add_u32 s25,s25,0x8000\nv_add_f32 v193,v193,v195\nv_cndmask_b32 v128,v198,v128,s[52:53]\nv_mfma_f32_16x16x32_bf16 v[12:15],a[228:231],v[180:183],v[12:15]\n"
"s_cmp_eq_u32 s25,s27\nds_read_b128 a[224:227],v197 offset:24576\nv_cndmask_b32 v129,v198,v129,s[54:55]\nv_mfma_f32_16x16x32_bf16 v[8:11],a[228:231],v[176:179],v[8:11]\ns_waitcnt lgkmcnt(5)\nv_cndmask_b32 v130,v198,v130,s[56:57]\nv_cndmask_b32 v131,v198,v131,s[58:59]\nv_mfma_f32_16x16x32_bf16 v[16:19],a[232:235],v[176:179],v[16:19]\ns_cselect_b32 s25,s28,s25\nds_read_b128 a[228:231],v197 offset:25600\n"
"v_cndmask_b32 v132,v198,v132,s[60:61]\nv_mfma_f32_16x16x32_bf16 v[20:23],a[232:235],v[180:183],v[20:23]\ns_cselect_b32 s27,s29,s27\nbuffer_load_dwordx4 v252,s[8:11],s26 offen offset:2048 sc1 lds\nv_mfma_f32_16x16x32_bf16 v[28:31],a[236:239],v[180:183],v[28:31]\ns_cselect_b32 s28,s30,s28\nds_read_b128 a[232:235],v197 offset:26624\nv_cndmask_b32 v133,v198,v133,s[62:63]\nv_mfma_f32_16x16x32_bf16 v[24:27],a[236:239],v[176:179],v[24:27]\n"
"s_waitcnt lgkmcnt(5)\nv_cndmask_b32 v134,v198,v134,s[64:65]\nv_cndmask_b32 v135,v198,v135,s[66:67]\nv_mfma_f32_16x16x32_bf16 v[32:35],a[240:243],v[176:179],v[32:35]\ns_cselect_b32 s29,s31,s29\nds_read_b128 a[236:239],v197 offset:27648\nv_cndmask_b32 v136,v198,v136,s[68:69]\nv_mfma_f32_16x16x32_bf16 v[36:39],a[240:243],v[180:183],v[36:39]\ns_cselect_b32 s30,1073741824,s30\nv_exp_f32 v160,v128\nv_mfma_f32_16x16x32_bf16 v[44:47],a[244:247],v[180:183],v[44:47]\n"
"ds_read_b128 a[240:243],v197 offset:28672\nv_cndmask_b32 v137,v198,v137,s[70:71]\nv_mfma_f32_16x16x32_bf16 v[40:43],a[244:247],v[176:179],v[40:43]\ns_waitcnt lgkmcnt(5)\nv_exp_f32 v164,v132\nv_mfma_f32_16x16x32_bf16 v[48:51],a[248:251],v[176:179],v[48:51]\nds_read_b128 a[244:247],v197 offset:29696\nv_cndmask_b32 v138,v198,v138,s[72:73]\nv_mfma_f32_16x16x32_bf16 v[52:55],a[248:251],v[180:183],v[52:55]\n"
"buffer_load_dwordx4 v252,s[8:11],s26 offen offset:3072 sc1 lds\nv_mfma_f32_16x16x32_bf16 v[60:63],a[252:255],v[180:183],v[60:63]\ns_add_u32 m0,s32,s15\nds_read_b128 a[248:251],v197 offset:30720\nv_cndmask_b32 v139,v198,v139,s[74:75]\nv_mfma_f32_16x16x32_bf16 v[56:59],a[252:255],v[176:179],v[56:59]\ns_waitcnt lgkmcnt(5)\nv_exp_f32 v161,v129\nv_mfma_f32_16x16x32_bf16 v[64:67],a[224:227],v[176:179],v[64:67]\n"
"s_mov_b32 s26,s33\nds_read_b128 a[252:255],v197 offset:31744\nv_add_f32 v194,v160,v161\nv_mfma_f32_16x16x32_bf16 v[68:71],a[224:227],v[180:183],v[68:71]\ns_waitcnt vmcnt(8)\ns_barrier\nv_cndmask_b32 v140,v198,v140,s[76:77]\nv_mfma_f32_16x16x32_bf16 v[76:79],a[228:231],v[180:183],v[76:79]\nds_read_b128 a[224:227],v196 offset:16384\nv_cndmask_b32 v141,v198,v141,s[78:79]\nv_mfma_f32_16x16x32_bf16 v[72:75],a[228:231],v[176:179],v[72:75]\n"
"s_waitcnt lgkmcnt(5)\nv_xor_b32 v197,0x8000,v197\nv_cndmask_b32 v142,v198,v142,s[80:81]\nv_mfma_f32_16x16x32_bf16 v[80:83],a[232:235],v[176:179],v[80:83]\nds_read_b128 a[228:231],v196 offset:24576\nv_cndmask_b32 v143,v198,v143,s[82:83]\nv_mfma_f32_16x16x32_bf16 v[84:87],a[232:235],v[180:183],v[84:87]\nbuffer_load_dwordx4 v252,s[8:11],s25 offen sc1 lds\nv_mfma_f32_16x16x32_bf16 v[92:95],a[236:239],v[180:183],v[92:95]\n"
"s_cmp_lg_u32 s39,0\nds_read_b128 a[232:235],v196 offset:17408\nv_mfma_f32_16x16x32_bf16 v[88:91],a[236:239],v[176:179],v[88:91]\ns_waitcnt lgkmcnt(5)\nv_exp_f32 v165,v133\nv_mfma_f32_16x16x32_bf16 v[96:99],a[240:243],v[176:179],v[96:99]\nds_read_b128 a[236:239],v196 offset:25600\ns_cbranch_scc1 L_fused_block_hook_33\nL_fused_block_hookr_34:\nv_add_f32 v195,v164,v165\nv_mfma_f32_16x16x32_bf16 v[100:103],a[240:243],v[180:183],v[100:103]\n"
"s_add_u32 s32,s32,0x4000\nv_exp_f32 v162,v130\nv_mfma_f32_16x16x32_bf16 v[108:111],a[244:247],v[180:183],v[108:111]\ns_and_b32 s32,s32,0xffff\nds_read_b128 a[240:243],v196 offset:18432\nv_add_f32 v194,v194,v162\nv_mfma_f32_16x16x32_bf16 v[104:107],a[244:247],v[176:179],v[104:107]\ns_waitcnt lgkmcnt(5)\nv_exp_f32 v166,v134\nv_mfma_f32_16x16x32_bf16 v[112:115],a[248:251],v[176:179],v[112:115]\nds_read_b128 a[244:247],v196 offset:26624\n"
"v_add_f32 v195,v195,v166\nv_mfma_f32_16x16x32_bf16 v[116:119],a[248:251],v[180:183],v[116:119]\nbuffer_load_dwordx4 v252,s[8:11],s25 offen offset:1024 sc1 lds\nv_mfma_f32_16x16x32_bf16 v[124:127],a[252:255],v[180:183],v[124:127]\nds_read_b128 a[248:251],v196 offset:19456\nv_mfma_f32_16x16x32_bf16 v[120:123],a[252:255],v[176:179],v[120:123]\ns_waitcnt lgkmcnt(5)\nv_exp_f32 v163,v131\nv_exp_f32 v167,v135\n"
"v_add_f32 v194,v194,v163\nv_add_f32 v195,v195,v167\nL_fused_block_M0_20:\nv_mfma_f32_16x16x32_bf16 v[144:147],a[224:227],a[0:3],v[184:187]\ns_cmp_gt_i32 s36,0\nds_read_b128 a[252:255],v196 offset:27648\nv_cvt_pk_bf16_f32 v176,v160,v161\nv_mfma_f32_16x16x32_bf16 v[148:151],a[224:227],a[32:35],v[188:191]\ns_cselect_b64 s[42:43],-1,0\nv_exp_f32 v168,v136\nv_mfma_f32_16x16x32_bf16 v[156:159],a[228:231],a[32:35],v[188:191]\n"
"s_cmp_eq_i32 s36,0\nds_read_b128 a[224:227],v196 offset:20480\nv_add_f32 v194,v194,v168\nv_mfma_f32_16x16x32_bf16 v[152:155],a[228:231],a[0:3],v[184:187]\ns_waitcnt lgkmcnt(5)\nv_exp_f32 v172,v140\nv_mfma_f32_16x16x32_bf16 v[144:147],a[232:235],a[4:7],v[144:147]\ns_cselect_b64 s[52:53],s[44:45],s[42:43]\nds_read_b128 a[228:231],v196 offset:28672\nv_add_f32 v195,v195,v172\nv_mfma_f32_16x16x32_bf16 v[148:151],a[232:235],a[36:39],v[148:151]\n"
"s_cselect_b64 s[54:55],s[46:47],s[42:43]\nbuffer_load_dwordx4 v252,s[8:11],s25 offen offset:2048 sc1 lds\nv_mfma_f32_16x16x32_bf16 v[156:159],a[236:239],a[36:39],v[156:159]\ns_cselect_b64 s[56:57],s[48:49],s[42:43]\nds_read_b128 a[232:235],v196 offset:21504\nv_cvt_pk_bf16_f32 v180,v164,v165\nv_mfma_f32_16x16x32_bf16 v[152:155],a[236:239],a[4:7],v[152:155]\ns_waitcnt lgkmcnt(5)\nv_exp_f32 v169,v137\n"
"v_mfma_f32_16x16x32_bf16 v[144:147],a[240:243],a[8:11],v[144:147]\ns_cselect_b64 s[58:59],s[50:51],s[42:43]\nds_read_b128 a[236:239],v196 offset:29696\nv_add_f32 v194,v194,v169\nv_mfma_f32_16x16x32_bf16 v[148:151],a[240:243],a[40:43],v[148:151]\nv_exp_f32 v173,v141\nv_mfma_f32_16x16x32_bf16 v[156:159],a[244:247],a[40:43],v[156:159]\nds_read_b128 a[240:243],v196 offset:22528\nv_add_f32 v195,v195,v173\n"
"v_mfma_f32_16x16x32_bf16 v[152:155],a[244:247],a[8:11],v[152:155]\ns_waitcnt lgkmcnt(5)\nv_exp_f32 v170,v138\nv_mfma_f32_16x16x32_bf16 v[144:147],a[248:251],a[12:15],v[144:147]\nds_read_b128 a[244:247],v196 offset:30720\nv_add_f32 v194,v194,v170\nv_mfma_f32_16x16x32_bf16 v[148:151],a[248:251],a[44:47],v[148:151]\nbuffer_load_dwordx4 v252,s[8:11],s25 offen offset:3072 sc1 lds\nv_mfma_f32_16x16x32_bf16 v[156:159],a[252:255],a[44:47],v[156:159]\n"
"s_add_u32 s84,s32,0x8000\nds_read_b128 a[248:251],v196 offset:23552\ns_and_b32 s84,s84,0xffff\nv_mfma_f32_16x16x32_bf16 v[152:155],a[252:255],a[12:15],v[152:155]\ns_waitcnt lgkmcnt(5)\ns_add_u32 s84,s84,s15\ns_add_u32 m0,s84,0x10000\nv_mfma_f32_16x16x32_bf16 v[144:147],a[224:227],a[16:19],v[144:147]\ns_cmp_gt_i32 s36,-64\nds_read_b128 a[252:255],v196 offset:31744\nv_cvt_pk_bf16_f32 v177,v162,v163\n"
"v_mfma_f32_16x16x32_bf16 v[148:151],a[224:227],a[48:51],v[148:151]\ns_cselect_b64 s[42:43],-1,0\nv_exp_f32 v174,v142\nv_mfma_f32_16x16x32_bf16 v[156:159],a[228:231],a[48:51],v[156:159]\ns_cmp_eq_i32 s36,-64\nds_read_b128 a[224:227],v197 offset:0\nv_add_f32 v195,v195,v174\nv_mfma_f32_16x16x32_bf16 v[152:155],a[228:231],a[16:19],v[152:155]\ns_waitcnt lgkmcnt(5)\nv_exp_f32 v171,v139\nv_mfma_f32_16x16x32_bf16 v[144:147],a[232:235],a[20:23],v[144:147]\n"
"s_cselect_b64 s[60:61],s[44:45],s[42:43]\nds_read_b128 a[228:231],v197 offset:1024\nv_add_f32 v194,v194,v171\nv_mfma_f32_16x16x32_bf16 v[148:151],a[232:235],a[52:55],v[148:151]\ns_cselect_b64 s[62:63],s[46:47],s[42:43]\nbuffer_load_dwordx4 v252,s[8:11],s26 offen sc1 lds\nv_mfma_f32_16x16x32_bf16 v[156:159],a[236:239],a[52:55],v[156:159]\ns_cselect_b64 s[64:65],s[48:49],s[42:43]\nds_read_b128 a[232:235],v197 offset:2048\n"
"v_cvt_pk_bf16_f32 v181,v166,v167\nv_mfma_f32_16x16x32_bf16 v[152:155],a[236:239],a[20:23],v[152:155]\ns_waitcnt lgkmcnt(5)\nv_exp_f32 v175,v143\nv_mfma_f32_16x16x32_bf16 v[144:147],a[240:243],a[24:27],v[144:147]\ns_cselect_b64 s[66:67],s[50:51],s[42:43]\nds_read_b128 a[236:239],v197 offset:3072\nv_add_f32 v195,v195,v175\ns_cmp_gt_i32 s36,16\ns_cselect_b64 s[42:43],-1,0\ns_cmp_eq_i32 s36,16\ns_cselect_b64 s[68:69],s[44:45],s[42:43]\n"
"s_cselect_b64 s[70:71],s[46:47],s[42:43]\ns_cselect_b64 s[72:73],s[48:49],s[42:43]\ns_cselect_b64 s[74:75],s[50:51],s[42:43]\ns_cmp_gt_i32 s36,-48\ns_cselect_b64 s[42:43],-1,0\ns_cmp_eq_i32 s36,-48\ns_cselect_b64 s[76:77],s[44:45],s[42:43]\ns_cselect_b64 s[78:79],s[46:47],s[42:43]\ns_cselect_b64 s[80:81],s[48:49],s[42:43]\ns_cselect_b64 s[82:83],s[50:51],s[42:43]\nv_mfma_f32_16x16x32_bf16 v[148:151],a[240:243],a[56:59],v[148:151]\n"
"s_sub_u32 s36,s36,32\nv_cvt_pk_bf16_f32 v178,v168,v169\nv_cvt_pk_bf16_f32 v182,v172,v173\nv_mfma_f32_16x16x32_bf16 v[156:159],a[244:247],a[56:59],v[156:159]\nds_read_b128 a[240:243],v197 offset:4096\nv_cvt_pk_bf16_f32 v179,v170,v171\nv_mfma_f32_16x16x32_bf16 v[152:155],a[244:247],a[24:27],v[152:155]\ns_waitcnt lgkmcnt(5)\nv_cvt_pk_bf16_f32 v183,v174,v175\nv_max_f32 v202,v194,v195\nv_mfma_f32_16x16x32_bf16 v[144:147],a[248:251],a[28:31],v[144:147]\n"
"ds_read_b128 a[244:247],v197 offset:5120\nv_cmp_nge_f32 vcc,s37,v202\nv_mfma_f32_16x16x32_bf16 v[148:151],a[248:251],a[60:63],v[148:151]\nbuffer_load_dwordx4 v252,s[8:11],s26 offen offset:1024 sc1 lds\nv_mfma_f32_16x16x32_bf16 v[156:159],a[252:255],a[60:63],v[156:159]\nds_read_b128 a[248:251],v197 offset:6144\nv_mfma_f32_16x16x32_bf16 v[152:155],a[252:255],a[28:31],v[152:155]\ns_waitcnt lgkmcnt(5)\n"
"s_cbranch_vccnz L_fused_block_rare_35\nL_fused_block_rarer_36:\nv_mfma_f32_16x16x32_bf16 v[0:3],a[224:227],v[176:179],v[0:3]\ns_add_u32 s33,s25,0x4000\nds_read_b128 a[252:255],v197 offset:7168\nv_add_f32 v192,v192,v194\nv_mfma_f32_16x16x32_bf16 v[4:7],a[224:227],v[180:183],v[4:7]\ns_add_u32 s25,s25,0x8000\nv_add_f32 v193,v193,v195\nv_cndmask_b32 v144,v198,v144,s[52:53]\nv_mfma_f32_16x16x32_bf16 v[12:15],a[228:231],v[180:183],v[12:15]\n"
"s_cmp_eq_u32 s25,s27\nds_read_b128 a[224:227],v197 offset:8192\nv_cndmask_b32 v145,v198,v145,s[54:55]\nv_mfma_f32_16x16x32_bf16 v[8:11],a[228:231],v[176:179],v[8:11]\ns_waitcnt lgkmcnt(5)\nv_cndmask_b32 v146,v198,v146,s[56:57]\nv_cndmask_b32 v147,v198,v147,s[58:59]\nv_mfma_f32_16x16x32_bf16 v[16:19],a[232:235],v[176:179],v[16:19]\ns_cselect_b32 s25,s28,s25\nds_read_b128 a[228:231],v197 offset:9216\n"
"v_cndmask_b32 v148,v198,v148,s[60:61]\nv_mfma_f32_16x16x32_bf16 v[20:23],a[232:235],v[180:183],v[20:23]\ns_cselect_b32 s27,s29,s27\nbuffer_load_dwordx4 v252,s[8:11],s26 offen offset:2048 sc1 lds\nv_mfma_f32_16x16x32_bf16 v[28:31],a[236:239],v[180:183],v[28:31]\ns_cselect_b32 s28,s30,s28\nds_read_b128 a[232:235],v197 offset:10240\nv_cndmask_b32 v149,v198,v149,s[62:63]\nv_mfma_f32_16x16x32_bf16 v[24:27],a[236:239],v[176:179],v[24:27]\n"
"s_waitcnt lgkmcnt(5)\nv_cndmask_b32 v150,v198,v150,s[64:65]\nv_cndmask_b32 v151,v198,v151,s[66:67]\nv_mfma_f32_16x16x32_bf16 v[32:35],a[240:243],v[176:179],v[32:35]\ns_cselect_b32 s29,s31,s29\nds_read_b128 a[236:239],v197 offset:11264\nv_cndmask_b32 v152,v198,v152,s[68:69]\nv_mfma_f32_16x16x32_bf16 v[36:39],a[240:243],v[180:183],v[36:39]\ns_cselect_b32 s30,1073741824,s30\nv_exp_f32 v160,v144\nv_mfma_f32_16x16x32_bf16 v[44:47],a[244:247],v[180:183],v[44:47]\n"
"s_sub_u32 s35,s35,1\nds_read_b128 a[240:243],v197 offset:12288\nv_cndmask_b32 v153,v198,v153,s[70:71]\nv_mfma_f32_16x16x32_bf16 v[40:43],a[244:247],v[176:179],v[40:43]\ns_waitcnt lgkmcnt(5)\nv_exp_f32 v164,v148\nv_mfma_f32_16x16x32_bf16 v[48:51],a[248:251],v[176:179],v[48:51]\nds_read_b128 a[244:247],v197 offset:13312\nv_cndmask_b32 v154,v198,v154,s[72:73]\nv_mfma_f32_16x16x32_bf16 v[52:55],a[248:251],v[180:183],v[52:55]\n"
"buffer_load_dwordx4 v252,s[8:11],s26 offen offset:3072 sc1 lds\nv_mfma_f32_16x16x32_bf16 v[60:63],a[252:255],v[180:183],v[60:63]\ns_add_u32 m0,s32,s15\nds_read_b128 a[248:251],v197 offset:14336\nv_cndmask_b32 v155,v198,v155,s[74:75]\nv_mfma_f32_16x16x32_bf16 v[56:59],a[252:255],v[176:179],v[56:59]\ns_waitcnt lgkmcnt(5)\nv_exp_f32 v161,v145\nv_mfma_f32_16x16x32_bf16 v[64:67],a[224:227],v[176:179],v[64:67]\n"
"s_mov_b32 s26,s33\nds_read_b128 a[252:255],v197 offset:15360\nv_add_f32 v194,v160,v161\nv_mfma_f32_16x16x32_bf16 v[68:71],a[224:227],v[180:183],v[68:71]\ns_waitcnt vmcnt(8)\ns_barrier\nv_xor_b32 v196,0x8000,v196\nv_mfma_f32_16x16x32_bf16 v[76:79],a[228:231],v[180:183],v[76:79]\nds_read_b128 a[224:227],v196 offset:0\nv_cndmask_b32 v156,v198,v156,s[76:77]\nv_mfma_f32_16x16x32_bf16 v[72:75],a[228:231],v[176:179],v[72:75]\n"
"s_waitcnt lgkmcnt(5)\nv_exp_f32 v165,v149\nv_mfma_f32_16x16x32_bf16 v[80:83],a[232:235],v[176:179],v[80:83]\nds_read_b128 a[228:231],v196 offset:8192\nv_add_f32 v195,v164,v165\nv_mfma_f32_16x16x32_bf16 v[84:87],a[232:235],v[180:183],v[84:87]\nbuffer_load_dwordx4 v252,s[8:11],s25 offen sc1 lds\nv_mfma_f32_16x16x32_bf16 v[92:95],a[236:239],v[180:183],v[92:95]\nds_read_b128 a[232:235],v196 offset:1024\n"
"v_cndmask_b32 v157,v198,v157,s[78:79]\nv_mfma_f32_16x16x32_bf16 v[88:91],a[236:239],v[176:179],v[88:91]\ns_waitcnt lgkmcnt(5)\nv_exp_f32 v162,v146\nv_mfma_f32_16x16x32_bf16 v[96:99],a[240:243],v[176:179],v[96:99]\ns_add_u32 s32,s32,0x4000\nds_read_b128 a[236:239],v196 offset:9216\nv_add_f32 v194,v194,v162\nv_mfma_f32_16x16x32_bf16 v[100:103],a[240:243],v[180:183],v[100:103]\ns_and_b32 s32,s32,0xffff\n"
"v_exp_f32 v166,v150\nv_mfma_f32_16x16x32_bf16 v[108:111],a[244:247],v[180:183],v[108:111]\nds_read_b128 a[240:243],v196 offset:2048\nv_add_f32 v195,v195,v166\nv_mfma_f32_16x16x32_bf16 v[104:107],a[244:247],v[176:179],v[104:107]\ns_waitcnt lgkmcnt(5)\nv_exp_f32 v163,v147\nv_mfma_f32_16x16x32_bf16 v[112:115],a[248:251],v[176:179],v[112:115]\nds_read_b128 a[244:247],v196 offset:10240\nv_add_f32 v194,v194,v163\n"
"v_mfma_f32_16x16x32_bf16 v[116:119],a[248:251],v[180:183],v[116:119]\nbuffer_load_dwordx4 v252,s[8:11],s25 offen offset:1024 sc1 lds\nv_mfma_f32_16x16x32_bf16 v[124:127],a[252:255],v[180:183],v[124:127]\nds_read_b128 a[248:251],v196 offset:3072\nv_cndmask_b32 v158,v198,v158,s[80:81]\nv_cndmask_b32 v159,v198,v159,s[82:83]\nv_mfma_f32_16x16x32_bf16 v[120:123],a[252:255],v[176:179],v[120:123]\ns_waitcnt lgkmcnt(5)\n"
"v_exp_f32 v167,v151\ns_nop 0\nv_add_f32 v195,v195,v167\ns_cmp_lg_u32 s35,0\ns_cbranch_scc1 L_fused_block_M1_21\nL_fused_block_DR_22:\nds_read_b128 a[224:227],v197 offset:16384\nds_read_b128 a[228:231],v197 offset:17408\ns_waitcnt lgkmcnt(6)\nds_read_b128 a[232:235],v197 offset:18432\ns_waitcnt lgkmcnt(6)\nds_read_b128 a[236:239],v197 offset:19456\ns_waitcnt lgkmcnt(6)\nds_read_b128 a[240:243],v197 offset:20480\n"
"s_waitcnt lgkmcnt(6)\nds_read_b128 a[244:247],v197 offset:21504\ns_waitcnt lgkmcnt(6)\nds_read_b128 a[248:251],v197 offset:22528\nbuffer_load_dwordx4 v252,s[8:11],s25 offen offset:2048 sc1 lds\nv_exp_f32 v168,v152\nv_cvt_pk_bf16_f32 v176,v160,v161\nv_exp_f32 v172,v156\nv_cvt_pk_bf16_f32 v180,v164,v165\nv_exp_f32 v169,v153\nv_cvt_pk_bf16_f32 v177,v162,v163\nv_exp_f32 v173,v157\nv_cvt_pk_bf16_f32 v181,v166,v167\n"
"buffer_load_dwordx4 v252,s[8:11],s25 offen offset:3072 sc1 lds\ns_add_u32 s84,s32,0x8000\ns_and_b32 s84,s84,0xffff\ns_add_u32 s84,s84,s15\ns_add_u32 m0,s84,0x10000\nv_exp_f32 v170,v154\nv_exp_f32 v174,v158\nv_exp_f32 v171,v155\nv_exp_f32 v175,v159\nbuffer_load_dwordx4 v252,s[8:11],s26 offen sc1 lds\nv_add_f32 v194,v194,v168\nv_add_f32 v195,v195,v172\nv_add_f32 v194,v194,v169\nv_add_f32 v195,v195,v173\n"
"v_add_f32 v194,v194,v170\nv_add_f32 v195,v195,v174\nv_add_f32 v194,v194,v171\nv_add_f32 v195,v195,v175\nbuffer_load_dwordx4 v252,s[8:11],s26 offen offset:1024 sc1 lds\nv_cvt_pk_bf16_f32 v178,v168,v169\nv_cvt_pk_bf16_f32 v182,v172,v173\nv_cvt_pk_bf16_f32 v179,v170,v171\nv_cvt_pk_bf16_f32 v183,v174,v175\nv_max_f32 v202,v194,v195\nv_cmp_nge_f32 vcc,s37,v202\ns_waitcnt lgkmcnt(5)\ns_nop 1\ns_cbranch_vccnz L_fused_block_rare_39\n"
"L_fused_block_rarer_40:\nv_mfma_f32_16x16x32_bf16 v[0:3],a[224:227],v[176:179],v[0:3]\ns_add_u32 s33,s25,0x4000\nds_read_b128 a[252:255],v197 offset:23552\nv_add_f32 v192,v192,v194\nv_mfma_f32_16x16x32_bf16 v[4:7],a[224:227],v[180:183],v[4:7]\ns_add_u32 s25,s25,0x8000\nv_add_f32 v193,v193,v195\nv_mfma_f32_16x16x32_bf16 v[12:15],a[228:231],v[180:183],v[12:15]\ns_cmp_eq_u32 s25,s27\nds_read_b128 a[224:227],v197 offset:24576\n"
"v_mfma_f32_16x16x32_bf16 v[8:11],a[228:231],v[176:179],v[8:11]\ns_waitcnt lgkmcnt(5)\nv_mov_b32 v204,v192\nv_mfma_f32_16x16x32_bf16 v[16:19],a[232:235],v[176:179],v[16:19]\ns_cselect_b32 s25,s28,s25\nds_read_b128 a[228:231],v197 offset:25600\nv_mfma_f32_16x16x32_bf16 v[20:23],a[232:235],v[180:183],v[20:23]\ns_cselect_b32 s27,s29,s27\nbuffer_load_dwordx4 v252,s[8:11],s26 offen offset:2048 sc1 lds\nv_mfma_f32_16x16x32_bf16 v[28:31],a[236:239],v[180:183],v[28:31]\n"
"s_cselect_b32 s28,s30,s28\nds_read_b128 a[232:235],v197 offset:26624\nv_permlane16_swap_b32 v204,v192\nv_mfma_f32_16x16x32_bf16 v[24:27],a[236:239],v[176:179],v[24:27]\ns_waitcnt lgkmcnt(5)\nv_add_f32 v192,v204,v192\nv_mfma_f32_16x16x32_bf16 v[32:35],a[240:243],v[176:179],v[32:35]\ns_cselect_b32 s29,s31,s29\nds_read_b128 a[236:239],v197 offset:27648\nv_mov_b32 v204,v192\nv_mfma_f32_16x16x32_bf16 v[36:39],a[240:243],v[180:183],v[36:39]\n"
"s_cselect_b32 s30,1073741824,s30\nv_mfma_f32_16x16x32_bf16 v[44:47],a[244:247],v[180:183],v[44:47]\nds_read_b128 a[240:243],v197 offset:28672\nv_permlane32_swap_b32 v204,v192\nv_mfma_f32_16x16x32_bf16 v[40:43],a[244:247],v[176:179],v[40:43]\ns_waitcnt lgkmcnt(5)\nv_add_f32 v192,v204,v192\nv_mfma_f32_16x16x32_bf16 v[48:51],a[248:251],v[176:179],v[48:51]\nds_read_b128 a[244:247],v197 offset:29696\nv_mfma_f32_16x16x32_bf16 v[52:55],a[248:251],v[180:183],v[52:55]\n"
"buffer_load_dwordx4 v252,s[8:11],s26 offen offset:3072 sc1 lds\nv_mfma_f32_16x16x32_bf16 v[60:63],a[252:255],v[180:183],v[60:63]\ns_add_u32 m0,s32,s15\nds_read_b128 a[248:251],v197 offset:30720\ns_mov_b32 s26,s33\nv_mfma_f32_16x16x32_bf16 v[56:59],a[252:255],v[176:179],v[56:59]\ns_waitcnt lgkmcnt(5)\nv_rcp_f32 v206,v192\nv_mfma_f32_16x16x32_bf16 v[64:67],a[224:227],v[176:179],v[64:67]\nds_read_b128 a[252:255],v197 offset:31744\n"
"v_mov_b32 v194,v184\nv_mfma_f32_16x16x32_bf16 v[68:71],a[224:227],v[180:183],v[68:71]\ns_waitcnt vmcnt(8)\ns_barrier\nv_mov_b32 v205,v193\nv_mfma_f32_16x16x32_bf16 v[76:79],a[228:231],v[180:183],v[76:79]\nds_read_b128 a[224:227],v196 offset:0\nv_mfma_f32_16x16x32_bf16 v[72:75],a[228:231],v[176:179],v[72:75]\ns_waitcnt lgkmcnt(5)\nv_xor_b32 v197,0x8000,v197\nv_permlane16_swap_b32 v205,v193\nv_mfma_f32_16x16x32_bf16 v[80:83],a[232:235],v[176:179],v[80:83]\n"
"ds_read_b128 a[228:231],v196 offset:8192\nv_add_f32 v193,v205,v193\nv_mfma_f32_16x16x32_bf16 v[84:87],a[232:235],v[180:183],v[84:87]\nbuffer_load_dwordx4 v252,s[8:11],s25 offen sc1 lds\nv_mfma_f32_16x16x32_bf16 v[92:95],a[236:239],v[180:183],v[92:95]\nds_read_b128 a[232:235],v196 offset:1024\nv_mov_b32 v205,v193\nv_mfma_f32_16x16x32_bf16 v[88:91],a[236:239],v[176:179],v[88:91]\ns_waitcnt lgkmcnt(5)\n"
"v_mfma_f32_16x16x32_bf16 v[96:99],a[240:243],v[176:179],v[96:99]\ns_add_u32 s32,s32,0x4000\nds_read_b128 a[236:239],v196 offset:9216\ns_and_b32 s32,s32,0xffff\nv_mfma_f32_16x16x32_bf16 v[100:103],a[240:243],v[180:183],v[100:103]\nv_permlane32_swap_b32 v205,v193\nv_mfma_f32_16x16x32_bf16 v[108:111],a[244:247],v[180:183],v[108:111]\nds_read_b128 a[240:243],v196 offset:2048\nv_add_f32 v193,v205,v193\n"
"v_mfma_f32_16x16x32_bf16 v[104:107],a[244:247],v[176:179],v[104:107]\ns_waitcnt lgkmcnt(5)\nv_rcp_f32 v207,v193\nv_mfma_f32_16x16x32_bf16 v[112:115],a[248:251],v[176:179],v[112:115]\nds_read_b128 a[244:247],v196 offset:10240\nv_mov_b32 v195,v188\nv_mfma_f32_16x16x32_bf16 v[116:119],a[248:251],v[180:183],v[116:119]\nbuffer_load_dwordx4 v252,s[8:11],s25 offen offset:1024 sc1 lds\nv_mfma_f32_16x16x32_bf16 v[124:127],a[252:255],v[180:183],v[124:127]\n"
"ds_read_b128 a[248:251],v196 offset:3072\nv_mfma_f32_16x16x32_bf16 v[120:123],a[252:255],v[176:179],v[120:123]\ns_waitcnt lgkmcnt(5)\ns_lshl3_add_u32 s7,s21,s14\ns_mul_i32 s7,s7,229376\ns_mul_i32 s84,s16,7168\ns_add_u32 s7,s7,50331648\ns_add_u32 s52,s7,s84\ns_add_u32 s53,s52,917504\ns_add_u32 s54,s52,4096\ns_add_u32 s55,s53,4096\ns_xor_b32 s7,s19,1\ns_lshl3_add_u32 s56,s7,s18\ns_and_b32 s7,s17,1\ns_lshl1_add_u32 s57,s20,s7\n"
"s_lshl4_add_u32 s58,s56,s57\ns_mul_i32 s59,s57,14336\ns_add_u32 s59,s59,s15\ns_add_u32 s59,s59,128974848\ns_add_u32 s60,s59,229376\ns_sub_u32 s7,s57,7\ns_cmp_ge_u32 s57,7\ns_cselect_b32 s63,s7,s57\ns_cselect_b32 s7,1,0\ns_lshl1_add_u32 s62,s56,s7\ns_mul_i32 s61,s63,229376\ns_mul_i32 s7,s14,57344\ns_add_u32 s61,s61,s7\ns_add_u32 s61,s61,127369216\ns_cmp_ge_u32 s57,14\ns_cselect_b32 s61,1073741824,s61\n"
"s_cmp_eq_u32 s24,2\ns_cbranch_scc1 L_fused_block_comb_43\ns_cmp_eq_u32 s24,3\ns_cbranch_scc1 L_fused_block_part_45\ns_cmp_lg_u32 s24,0\ns_cbranch_scc1 L_fused_block_nq_46\nv_lshlrev_b32 v214,7,v253\ns_mov_b64 exec,1\ns_mov_b32 s7,0\nL_fused_block_poll_47:\nbuffer_load_dwordx2 v[212:213],v214,s[8:11],s86 offen sc1\ns_add_u32 s7,s7,1\ns_waitcnt vmcnt(0)\nv_cmp_eq_u64 vcc,s[12:13],v[212:213]\ns_nop 1\n"
"s_cmp_eq_u64 vcc,exec\ns_cbranch_scc1 L_fused_block_polld_48\ns_cmp_lt_u32 s7,131072\ns_cbranch_scc1 L_fused_block_poll_47\nL_fused_block_polld_48:\ns_mov_b64 exec,-1\nbuffer_load_dwordx4 a[0:3],v252,s[8:11],s85 offen offset:0 sc1\nbuffer_load_dwordx4 a[4:7],v252,s[8:11],s85 offen offset:1024 sc1\nbuffer_load_dwordx4 a[8:11],v252,s[8:11],s85 offen offset:2048 sc1\nbuffer_load_dwordx4 a[12:15],v252,s[8:11],s85 offen offset:3072 sc1\n"
"s_add_u32 s85,s85,0x1000\nbuffer_load_dwordx4 a[16:19],v252,s[8:11],s85 offen offset:0 sc1\nbuffer_load_dwordx4 a[20:23],v252,s[8:11],s85 offen offset:1024 sc1\nbuffer_load_dwordx4 a[24:27],v252,s[8:11],s85 offen offset:2048 sc1\nbuffer_load_dwordx4 a[28:31],v252,s[8:11],s85 offen offset:3072 sc1\ns_add_u32 s85,s85,0x1000\nbuffer_load_dwordx4 a[32:35],v252,s[8:11],s85 offen offset:0 sc1\nbuffer_load_dwordx4 a[36:39],v252,s[8:11],s85 offen offset:1024 sc1\n"
"buffer_load_dwordx4 a[40:43],v252,s[8:11],s85 offen offset:2048 sc1\nbuffer_load_dwordx4 a[44:47],v252,s[8:11],s85 offen offset:3072 sc1\ns_add_u32 s85,s85,0x1000\nbuffer_load_dwordx4 a[48:51],v252,s[8:11],s85 offen offset:0 sc1\nbuffer_load_dwordx4 a[52:55],v252,s[8:11],s85 offen offset:1024 sc1\nbuffer_load_dwordx4 a[56:59],v252,s[8:11],s85 offen offset:2048 sc1\nbuffer_load_dwordx4 a[60:63],v252,s[8:11],s85 offen offset:3072 sc1\n"
"v_mul_f32 v0,v206,v0\nv_mul_f32 v1,v206,v1\nv_mul_f32 v2,v206,v2\nv_mul_f32 v3,v206,v3\nv_mul_f32 v8,v206,v8\nv_mul_f32 v9,v206,v9\nv_mul_f32 v10,v206,v10\nv_mul_f32 v11,v206,v11\nv_cvt_pk_bf16_f32 v128,v0,v1\nv_cvt_pk_bf16_f32 v129,v2,v3\nv_cvt_pk_bf16_f32 v130,v8,v9\nv_cvt_pk_bf16_f32 v131,v10,v11\nv_mul_f32 v4,v207,v4\nv_mul_f32 v5,v207,v5\nv_mul_f32 v6,v207,v6\nv_mul_f32 v7,v207,v7\nv_mul_f32 v12,v207,v12\n"
"v_mul_f32 v13,v207,v13\nv_mul_f32 v14,v207,v14\nv_mul_f32 v15,v207,v15\nv_cvt_pk_bf16_f32 v160,v4,v5\nv_cvt_pk_bf16_f32 v161,v6,v7\nv_cvt_pk_bf16_f32 v162,v12,v13\nv_cvt_pk_bf16_f32 v163,v14,v15\nv_mul_f32 v16,v206,v16\nv_mul_f32 v17,v206,v17\nv_mul_f32 v18,v206,v18\nv_mul_f32 v19,v206,v19\nv_mul_f32 v24,v206,v24\nv_mul_f32 v25,v206,v25\nv_mul_f32 v26,v206,v26\nv_mul_f32 v27,v206,v27\nv_cvt_pk_bf16_f32 v132,v16,v17\n"
"v_cvt_pk_bf16_f32 v133,v18,v19\nv_cvt_pk_bf16_f32 v134,v24,v25\nv_cvt_pk_bf16_f32 v135,v26,v27\nv_mul_f32 v20,v207,v20\nv_mul_f32 v21,v207,v21\nv_mul_f32 v22,v207,v22\nv_mul_f32 v23,v207,v23\nv_mul_f32 v28,v207,v28\nv_mul_f32 v29,v207,v29\nv_mul_f32 v30,v207,v30\nv_mul_f32 v31,v207,v31\nv_cvt_pk_bf16_f32 v164,v20,v21\nv_cvt_pk_bf16_f32 v165,v22,v23\nv_cvt_pk_bf16_f32 v166,v28,v29\nv_cvt_pk_bf16_f32 v167,v30,v31\n"
"v_mul_f32 v32,v206,v32\nv_mul_f32 v33,v206,v33\nv_mul_f32 v34,v206,v34\nv_mul_f32 v35,v206,v35\nv_mul_f32 v40,v206,v40\nv_mul_f32 v41,v206,v41\nv_mul_f32 v42,v206,v42\nv_mul_f32 v43,v206,v43\nv_cvt_pk_bf16_f32 v136,v32,v33\nv_cvt_pk_bf16_f32 v137,v34,v35\nv_cvt_pk_bf16_f32 v138,v40,v41\nv_cvt_pk_bf16_f32 v139,v42,v43\nv_mul_f32 v36,v207,v36\nv_mul_f32 v37,v207,v37\nv_mul_f32 v38,v207,v38\nv_mul_f32 v39,v207,v39\n"
"v_mul_f32 v44,v207,v44\nv_mul_f32 v45,v207,v45\nv_mul_f32 v46,v207,v46\nv_mul_f32 v47,v207,v47\nv_cvt_pk_bf16_f32 v168,v36,v37\nv_cvt_pk_bf16_f32 v169,v38,v39\nv_cvt_pk_bf16_f32 v170,v44,v45\nv_cvt_pk_bf16_f32 v171,v46,v47\nv_mul_f32 v48,v206,v48\nv_mul_f32 v49,v206,v49\nv_mul_f32 v50,v206,v50\nv_mul_f32 v51,v206,v51\nv_mul_f32 v56,v206,v56\nv_mul_f32 v57,v206,v57\nv_mul_f32 v58,v206,v58\nv_mul_f32 v59,v206,v59\n"
"v_cvt_pk_bf16_f32 v140,v48,v49\nv_cvt_pk_bf16_f32 v141,v50,v51\nv_cvt_pk_bf16_f32 v142,v56,v57\nv_cvt_pk_bf16_f32 v143,v58,v59\nv_mul_f32 v52,v207,v52\nv_mul_f32 v53,v207,v53\nv_mul_f32 v54,v207,v54\nv_mul_f32 v55,v207,v55\nv_mul_f32 v60,v207,v60\nv_mul_f32 v61,v207,v61\nv_mul_f32 v62,v207,v62\nv_mul_f32 v63,v207,v63\nv_cvt_pk_bf16_f32 v172,v52,v53\nv_cvt_pk_bf16_f32 v173,v54,v55\nv_cvt_pk_bf16_f32 v174,v60,v61\n"
"v_cvt_pk_bf16_f32 v175,v62,v63\nv_mul_f32 v64,v206,v64\nv_mul_f32 v65,v206,v65\nv_mul_f32 v66,v206,v66\nv_mul_f32 v67,v206,v67\nv_mul_f32 v72,v206,v72\nv_mul_f32 v73,v206,v73\nv_mul_f32 v74,v206,v74\nv_mul_f32 v75,v206,v75\nv_cvt_pk_bf16_f32 v144,v64,v65\nv_cvt_pk_bf16_f32 v145,v66,v67\nv_cvt_pk_bf16_f32 v146,v72,v73\nv_cvt_pk_bf16_f32 v147,v74,v75\nv_mul_f32 v68,v207,v68\nv_mul_f32 v69,v207,v69\nv_mul_f32 v70,v207,v70\n"
"v_mul_f32 v71,v207,v71\nv_mul_f32 v76,v207,v76\nv_mul_f32 v77,v207,v77\nv_mul_f32 v78,v207,v78\nv_mul_f32 v79,v207,v79\nv_cvt_pk_bf16_f32 v176,v68,v69\nv_cvt_pk_bf16_f32 v177,v70,v71\nv_cvt_pk_bf16_f32 v178,v76,v77\nv_cvt_pk_bf16_f32 v179,v78,v79\nv_mul_f32 v80,v206,v80\nv_mul_f32 v81,v206,v81\nv_mul_f32 v82,v206,v82\nv_mul_f32 v83,v206,v83\nv_mul_f32 v88,v206,v88\nv_mul_f32 v89,v206,v89\nv_mul_f32 v90,v206,v90\n"
"v_mul_f32 v91,v206,v91\nv_cvt_pk_bf16_f32 v148,v80,v81\nv_cvt_pk_bf16_f32 v149,v82,v83\nv_cvt_pk_bf16_f32 v150,v88,v89\nv_cvt_pk_bf16_f32 v151,v90,v91\nv_mul_f32 v84,v207,v84\nv_mul_f32 v85,v207,v85\nv_mul_f32 v86,v207,v86\nv_mul_f32 v87,v207,v87\nv_mul_f32 v92,v207,v92\nv_mul_f32 v93,v207,v93\nv_mul_f32 v94,v207,v94\nv_mul_f32 v95,v207,v95\nv_cvt_pk_bf16_f32 v180,v84,v85\nv_cvt_pk_bf16_f32 v181,v86,v87\n"
"v_cvt_pk_bf16_f32 v182,v92,v93\nv_cvt_pk_bf16_f32 v183,v94,v95\nv_mul_f32 v96,v206,v96\nv_mul_f32 v97,v206,v97\nv_mul_f32 v98,v206,v98\nv_mul_f32 v99,v206,v99\nv_mul_f32 v104,v206,v104\nv_mul_f32 v105,v206,v105\nv_mul_f32 v106,v206,v106\nv_mul_f32 v107,v206,v107\nv_cvt_pk_bf16_f32 v152,v96,v97\nv_cvt_pk_bf16_f32 v153,v98,v99\nv_cvt_pk_bf16_f32 v154,v104,v105\nv_cvt_pk_bf16_f32 v155,v106,v107\nv_mul_f32 v100,v207,v100\n"
"v_mul_f32 v101,v207,v101\nv_mul_f32 v102,v207,v102\nv_mul_f32 v103,v207,v103\nv_mul_f32 v108,v207,v108\nv_mul_f32 v109,v207,v109\nv_mul_f32 v110,v207,v110\nv_mul_f32 v111,v207,v111\nv_cvt_pk_bf16_f32 v184,v100,v101\nv_cvt_pk_bf16_f32 v185,v102,v103\nv_cvt_pk_bf16_f32 v186,v108,v109\nv_cvt_pk_bf16_f32 v187,v110,v111\nv_mul_f32 v112,v206,v112\nv_mul_f32 v113,v206,v113\nv_mul_f32 v114,v206,v114\nv_mul_f32 v115,v206,v115\n"
"v_mul_f32 v120,v206,v120\nv_mul_f32 v121,v206,v121\nv_mul_f32 v122,v206,v122\nv_mul_f32 v123,v206,v123\nv_cvt_pk_bf16_f32 v156,v112,v113\nv_cvt_pk_bf16_f32 v157,v114,v115\nv_cvt_pk_bf16_f32 v158,v120,v121\nv_cvt_pk_bf16_f32 v159,v122,v123\nv_mul_f32 v116,v207,v116\nv_mul_f32 v117,v207,v117\nv_mul_f32 v118,v207,v118\nv_mul_f32 v119,v207,v119\nv_mul_f32 v124,v207,v124\nv_mul_f32 v125,v207,v125\nv_mul_f32 v126,v207,v126\n"
"v_mul_f32 v127,v207,v127\nv_cvt_pk_bf16_f32 v188,v116,v117\nv_cvt_pk_bf16_f32 v189,v118,v119\nv_cvt_pk_bf16_f32 v190,v124,v125\nv_cvt_pk_bf16_f32 v191,v126,v127\nv_accvgpr_write_b32 a64,v128\nv_accvgpr_write_b32 a65,v129\nv_accvgpr_write_b32 a66,v130\nv_accvgpr_write_b32 a67,v131\nv_accvgpr_write_b32 a68,v132\nv_accvgpr_write_b32 a69,v133\nv_accvgpr_write_b32 a70,v134\nv_accvgpr_write_b32 a71,v135\n"
"v_accvgpr_write_b32 a72,v136\nv_accvgpr_write_b32 a73,v137\nv_accvgpr_write_b32 a74,v138\nv_accvgpr_write_b32 a75,v139\nv_accvgpr_write_b32 a76,v140\nv_accvgpr_write_b32 a77,v141\nv_accvgpr_write_b32 a78,v142\nv_accvgpr_write_b32 a79,v143\nv_accvgpr_write_b32 a80,v144\nv_accvgpr_write_b32 a81,v145\nv_accvgpr_write_b32 a82,v146\nv_accvgpr_write_b32 a83,v147\nv_accvgpr_write_b32 a84,v148\nv_accvgpr_write_b32 a85,v149\n"
"v_accvgpr_write_b32 a86,v150\nv_accvgpr_write_b32 a87,v151\nv_accvgpr_write_b32 a88,v152\nv_accvgpr_write_b32 a89,v153\nv_accvgpr_write_b32 a90,v154\nv_accvgpr_write_b32 a91,v155\nv_accvgpr_write_b32 a92,v156\nv_accvgpr_write_b32 a93,v157\nv_accvgpr_write_b32 a94,v158\nv_accvgpr_write_b32 a95,v159\nv_accvgpr_write_b32 a96,v160\nv_accvgpr_write_b32 a97,v161\nv_accvgpr_write_b32 a98,v162\nv_accvgpr_write_b32 a99,v163\n"
"v_accvgpr_write_b32 a100,v164\nv_accvgpr_write_b32 a101,v165\nv_accvgpr_write_b32 a102,v166\nv_accvgpr_write_b32 a103,v167\nv_accvgpr_write_b32 a104,v168\nv_accvgpr_write_b32 a105,v169\nv_accvgpr_write_b32 a106,v170\nv_accvgpr_write_b32 a107,v171\nv_accvgpr_write_b32 a108,v172\nv_accvgpr_write_b32 a109,v173\nv_accvgpr_write_b32 a110,v174\nv_accvgpr_write_b32 a111,v175\nv_accvgpr_write_b32 a112,v176\n"
"v_accvgpr_write_b32 a113,v177\nv_accvgpr_write_b32 a114,v178\nv_accvgpr_write_b32 a115,v179\nv_accvgpr_write_b32 a116,v180\nv_accvgpr_write_b32 a117,v181\nv_accvgpr_write_b32 a118,v182\nv_accvgpr_write_b32 a119,v183\nv_accvgpr_write_b32 a120,v184\nv_accvgpr_write_b32 a121,v185\nv_accvgpr_write_b32 a122,v186\nv_accvgpr_write_b32 a123,v187\nv_accvgpr_write_b32 a124,v188\nv_accvgpr_write_b32 a125,v189\n"
"v_accvgpr_write_b32 a126,v190\nv_accvgpr_write_b32 a127,v191\ns_mov_b32 s24,3\ns_lshl_b32 s22,s20,2\ns_sub_u32 s22,26,s22\nv_mov_b32 v0,0\nv_mov_b32 v1,0\nv_mov_b32 v2,0\nv_mov_b32 v3,0\nv_mov_b32 v4,0\nv_mov_b32 v5,0\nv_mov_b32 v6,0\nv_mov_b32 v7,0\nv_mov_b32 v8,0\nv_mov_b32 v9,0\nv_mov_b32 v10,0\nv_mov_b32 v11,0\nv_mov_b32 v12,0\nv_mov_b32 v13,0\nv_mov_b32 v14,0\nv_mov_b32 v15,0\nv_mov_b32 v16,0\n"
"v_mov_b32 v17,0\nv_mov_b32 v18,0\nv_mov_b32 v19,0\nv_mov_b32 v20,0\nv_mov_b32 v21,0\nv_mov_b32 v22,0\nv_mov_b32 v23,0\nv_mov_b32 v24,0\nv_mov_b32 v25,0\nv_mov_b32 v26,0\nv_mov_b32 v27,0\nv_mov_b32 v28,0\nv_mov_b32 v29,0\nv_mov_b32 v30,0\nv_mov_b32 v31,0\nv_mov_b32 v32,0\nv_mov_b32 v33,0\nv_mov_b32 v34,0\nv_mov_b32 v35,0\nv_mov_b32 v36,0\nv_mov_b32 v37,0\nv_mov_b32 v38,0\nv_mov_b32 v39,0\nv_mov_b32 v40,0\n"
"v_mov_b32 v41,0\nv_mov_b32 v42,0\nv_mov_b32 v43,0\nv_mov_b32 v44,0\nv_mov_b32 v45,0\nv_mov_b32 v46,0\nv_mov_b32 v47,0\nv_mov_b32 v48,0\nv_mov_b32 v49,0\nv_mov_b32 v50,0\nv_mov_b32 v51,0\nv_mov_b32 v52,0\nv_mov_b32 v53,0\nv_mov_b32 v54,0\nv_mov_b32 v55,0\nv_mov_b32 v56,0\nv_mov_b32 v57,0\nv_mov_b32 v58,0\nv_mov_b32 v59,0\nv_mov_b32 v60,0\nv_mov_b32 v61,0\nv_mov_b32 v62,0\nv_mov_b32 v63,0\nv_mov_b32 v64,0\n"
"v_mov_b32 v65,0\nv_mov_b32 v66,0\nv_mov_b32 v67,0\nv_mov_b32 v68,0\nv_mov_b32 v69,0\nv_mov_b32 v70,0\nv_mov_b32 v71,0\nv_mov_b32 v72,0\nv_mov_b32 v73,0\nv_mov_b32 v74,0\nv_mov_b32 v75,0\nv_mov_b32 v76,0\nv_mov_b32 v77,0\nv_mov_b32 v78,0\nv_mov_b32 v79,0\nv_mov_b32 v80,0\nv_mov_b32 v81,0\nv_mov_b32 v82,0\nv_mov_b32 v83,0\nv_mov_b32 v84,0\nv_mov_b32 v85,0\nv_mov_b32 v86,0\nv_mov_b32 v87,0\nv_mov_b32 v88,0\n"
"v_mov_b32 v89,0\nv_mov_b32 v90,0\nv_mov_b32 v91,0\nv_mov_b32 v92,0\nv_mov_b32 v93,0\nv_mov_b32 v94,0\nv_mov_b32 v95,0\nv_mov_b32 v96,0\nv_mov_b32 v97,0\nv_mov_b32 v98,0\nv_mov_b32 v99,0\nv_mov_b32 v100,0\nv_mov_b32 v101,0\nv_mov_b32 v102,0\nv_mov_b32 v103,0\nv_mov_b32 v104,0\nv_mov_b32 v105,0\nv_mov_b32 v106,0\nv_mov_b32 v107,0\nv_mov_b32 v108,0\nv_mov_b32 v109,0\nv_mov_b32 v110,0\nv_mov_b32 v111,0\n"
"v_mov_b32 v112,0\nv_mov_b32 v113,0\nv_mov_b32 v114,0\nv_mov_b32 v115,0\nv_mov_b32 v116,0\nv_mov_b32 v117,0\nv_mov_b32 v118,0\nv_mov_b32 v119,0\nv_mov_b32 v120,0\nv_mov_b32 v121,0\nv_mov_b32 v122,0\nv_mov_b32 v123,0\nv_mov_b32 v124,0\nv_mov_b32 v125,0\nv_mov_b32 v126,0\nv_mov_b32 v127,0\nv_mov_b32 v184,0\nv_mov_b32 v185,0\nv_mov_b32 v186,0\nv_mov_b32 v187,0\nv_mov_b32 v188,0\nv_mov_b32 v189,0\nv_mov_b32 v190,0\n"
"v_mov_b32 v191,0\nv_mov_b32 v192,0\nv_mov_b32 v193,0\nv_mov_b32 v194,0\nv_mov_b32 v195,0\ns_mov_b32 s37,0xbf800000\ns_mov_b32 s38,0xff7fffff\ns_waitcnt vmcnt(0)\ns_branch L_fused_block_pro_17\nL_fused_block_nq_46:\nv_mul_f32 v0,v206,v0\nv_mul_f32 v1,v206,v1\nv_mul_f32 v2,v206,v2\nv_mul_f32 v3,v206,v3\nv_mul_f32 v8,v206,v8\nv_mul_f32 v9,v206,v9\nv_mul_f32 v10,v206,v10\nv_mul_f32 v11,v206,v11\nv_cvt_pk_bf16_f32 v128,v0,v1\n"
"v_cvt_pk_bf16_f32 v129,v2,v3\nv_cvt_pk_bf16_f32 v130,v8,v9\nv_cvt_pk_bf16_f32 v131,v10,v11\nv_mul_f32 v4,v207,v4\nv_mul_f32 v5,v207,v5\nv_mul_f32 v6,v207,v6\nv_mul_f32 v7,v207,v7\nv_mul_f32 v12,v207,v12\nv_mul_f32 v13,v207,v13\nv_mul_f32 v14,v207,v14\nv_mul_f32 v15,v207,v15\nv_cvt_pk_bf16_f32 v160,v4,v5\nv_cvt_pk_bf16_f32 v161,v6,v7\nv_cvt_pk_bf16_f32 v162,v12,v13\nv_cvt_pk_bf16_f32 v163,v14,v15\nv_mul_f32 v16,v206,v16\n"
"v_mul_f32 v17,v206,v17\nv_mul_f32 v18,v206,v18\nv_mul_f32 v19,v206,v19\nv_mul_f32 v24,v206,v24\nv_mul_f32 v25,v206,v25\nv_mul_f32 v26,v206,v26\nv_mul_f32 v27,v206,v27\nv_cvt_pk_bf16_f32 v132,v16,v17\nv_cvt_pk_bf16_f32 v133,v18,v19\nv_cvt_pk_bf16_f32 v134,v24,v25\nv_cvt_pk_bf16_f32 v135,v26,v27\nv_mul_f32 v20,v207,v20\nv_mul_f32 v21,v207,v21\nv_mul_f32 v22,v207,v22\nv_mul_f32 v23,v207,v23\nv_mul_f32 v28,v207,v28\n"
"v_mul_f32 v29,v207,v29\nv_mul_f32 v30,v207,v30\nv_mul_f32 v31,v207,v31\nv_cvt_pk_bf16_f32 v164,v20,v21\nv_cvt_pk_bf16_f32 v165,v22,v23\nv_cvt_pk_bf16_f32 v166,v28,v29\nv_cvt_pk_bf16_f32 v167,v30,v31\nv_mul_f32 v32,v206,v32\nv_mul_f32 v33,v206,v33\nv_mul_f32 v34,v206,v34\nv_mul_f32 v35,v206,v35\nv_mul_f32 v40,v206,v40\nv_mul_f32 v41,v206,v41\nv_mul_f32 v42,v206,v42\nv_mul_f32 v43,v206,v43\nv_cvt_pk_bf16_f32 v136,v32,v33\n"
"v_cvt_pk_bf16_f32 v137,v34,v35\nv_cvt_pk_bf16_f32 v138,v40,v41\nv_cvt_pk_bf16_f32 v139,v42,v43\nv_mul_f32 v36,v207,v36\nv_mul_f32 v37,v207,v37\nv_mul_f32 v38,v207,v38\nv_mul_f32 v39,v207,v39\nv_mul_f32 v44,v207,v44\nv_mul_f32 v45,v207,v45\nv_mul_f32 v46,v207,v46\nv_mul_f32 v47,v207,v47\nv_cvt_pk_bf16_f32 v168,v36,v37\nv_cvt_pk_bf16_f32 v169,v38,v39\nv_cvt_pk_bf16_f32 v170,v44,v45\nv_cvt_pk_bf16_f32 v171,v46,v47\n"
"v_mul_f32 v48,v206,v48\nv_mul_f32 v49,v206,v49\nv_mul_f32 v50,v206,v50\nv_mul_f32 v51,v206,v51\nv_mul_f32 v56,v206,v56\nv_mul_f32 v57,v206,v57\nv_mul_f32 v58,v206,v58\nv_mul_f32 v59,v206,v59\nv_cvt_pk_bf16_f32 v140,v48,v49\nv_cvt_pk_bf16_f32 v141,v50,v51\nv_cvt_pk_bf16_f32 v142,v56,v57\nv_cvt_pk_bf16_f32 v143,v58,v59\nv_mul_f32 v52,v207,v52\nv_mul_f32 v53,v207,v53\nv_mul_f32 v54,v207,v54\nv_mul_f32 v55,v207,v55\n"
"v_mul_f32 v60,v207,v60\nv_mul_f32 v61,v207,v61\nv_mul_f32 v62,v207,v62\nv_mul_f32 v63,v207,v63\nv_cvt_pk_bf16_f32 v172,v52,v53\nv_cvt_pk_bf16_f32 v173,v54,v55\nv_cvt_pk_bf16_f32 v174,v60,v61\nv_cvt_pk_bf16_f32 v175,v62,v63\nv_mul_f32 v64,v206,v64\nv_mul_f32 v65,v206,v65\nv_mul_f32 v66,v206,v66\nv_mul_f32 v67,v206,v67\nv_mul_f32 v72,v206,v72\nv_mul_f32 v73,v206,v73\nv_mul_f32 v74,v206,v74\nv_mul_f32 v75,v206,v75\n"
"v_cvt_pk_bf16_f32 v144,v64,v65\nv_cvt_pk_bf16_f32 v145,v66,v67\nv_cvt_pk_bf16_f32 v146,v72,v73\nv_cvt_pk_bf16_f32 v147,v74,v75\nv_mul_f32 v68,v207,v68\nv_mul_f32 v69,v207,v69\nv_mul_f32 v70,v207,v70\nv_mul_f32 v71,v207,v71\nv_mul_f32 v76,v207,v76\nv_mul_f32 v77,v207,v77\nv_mul_f32 v78,v207,v78\nv_mul_f32 v79,v207,v79\nv_cvt_pk_bf16_f32 v176,v68,v69\nv_cvt_pk_bf16_f32 v177,v70,v71\nv_cvt_pk_bf16_f32 v178,v76,v77\n"
"v_cvt_pk_bf16_f32 v179,v78,v79\nv_mul_f32 v80,v206,v80\nv_mul_f32 v81,v206,v81\nv_mul_f32 v82,v206,v82\nv_mul_f32 v83,v206,v83\nv_mul_f32 v88,v206,v88\nv_mul_f32 v89,v206,v89\nv_mul_f32 v90,v206,v90\nv_mul_f32 v91,v206,v91\nv_cvt_pk_bf16_f32 v148,v80,v81\nv_cvt_pk_bf16_f32 v149,v82,v83\nv_cvt_pk_bf16_f32 v150,v88,v89\nv_cvt_pk_bf16_f32 v151,v90,v91\nv_mul_f32 v84,v207,v84\nv_mul_f32 v85,v207,v85\nv_mul_f32 v86,v207,v86\n"
"v_mul_f32 v87,v207,v87\nv_mul_f32 v92,v207,v92\nv_mul_f32 v93,v207,v93\nv_mul_f32 v94,v207,v94\nv_mul_f32 v95,v207,v95\nv_cvt_pk_bf16_f32 v180,v84,v85\nv_cvt_pk_bf16_f32 v181,v86,v87\nv_cvt_pk_bf16_f32 v182,v92,v93\nv_cvt_pk_bf16_f32 v183,v94,v95\nv_mul_f32 v96,v206,v96\nv_mul_f32 v97,v206,v97\nv_mul_f32 v98,v206,v98\nv_mul_f32 v99,v206,v99\nv_mul_f32 v104,v206,v104\nv_mul_f32 v105,v206,v105\nv_mul_f32 v106,v206,v106\n"
"v_mul_f32 v107,v206,v107\nv_cvt_pk_bf16_f32 v152,v96,v97\nv_cvt_pk_bf16_f32 v153,v98,v99\nv_cvt_pk_bf16_f32 v154,v104,v105\nv_cvt_pk_bf16_f32 v155,v106,v107\nv_mul_f32 v100,v207,v100\nv_mul_f32 v101,v207,v101\nv_mul_f32 v102,v207,v102\nv_mul_f32 v103,v207,v103\nv_mul_f32 v108,v207,v108\nv_mul_f32 v109,v207,v109\nv_mul_f32 v110,v207,v110\nv_mul_f32 v111,v207,v111\nv_cvt_pk_bf16_f32 v184,v100,v101\nv_cvt_pk_bf16_f32 v185,v102,v103\n"
"v_cvt_pk_bf16_f32 v186,v108,v109\nv_cvt_pk_bf16_f32 v187,v110,v111\nv_mul_f32 v112,v206,v112\nv_mul_f32 v113,v206,v113\nv_mul_f32 v114,v206,v114\nv_mul_f32 v115,v206,v115\nv_mul_f32 v120,v206,v120\nv_mul_f32 v121,v206,v121\nv_mul_f32 v122,v206,v122\nv_mul_f32 v123,v206,v123\nv_cvt_pk_bf16_f32 v156,v112,v113\nv_cvt_pk_bf16_f32 v157,v114,v115\nv_cvt_pk_bf16_f32 v158,v120,v121\nv_cvt_pk_bf16_f32 v159,v122,v123\n"
"v_mul_f32 v116,v207,v116\nv_mul_f32 v117,v207,v117\nv_mul_f32 v118,v207,v118\nv_mul_f32 v119,v207,v119\nv_mul_f32 v124,v207,v124\nv_mul_f32 v125,v207,v125\nv_mul_f32 v126,v207,v126\nv_mul_f32 v127,v207,v127\nv_cvt_pk_bf16_f32 v188,v116,v117\nv_cvt_pk_bf16_f32 v189,v118,v119\nv_cvt_pk_bf16_f32 v190,v124,v125\nv_cvt_pk_bf16_f32 v191,v126,v127\ns_branch L_fused_block_out_44\nL_fused_block_part_45:\ns_add_u32 s7,s40,0x4000\n"
"buffer_store_dwordx4 v[192:195],v252,s[8:11],s7 offen sc1\ns_mov_b32 s7,s40\nv_cvt_pk_bf16_f32 v0,v0,v1\nv_cvt_pk_bf16_f32 v1,v2,v3\nv_cvt_pk_bf16_f32 v2,v8,v9\nv_cvt_pk_bf16_f32 v3,v10,v11\nbuffer_store_dwordx4 v[0:3],v252,s[8:11],s7 offen offset:0 sc1\nv_cvt_pk_bf16_f32 v16,v16,v17\nv_cvt_pk_bf16_f32 v17,v18,v19\nv_cvt_pk_bf16_f32 v18,v24,v25\nv_cvt_pk_bf16_f32 v19,v26,v27\nbuffer_store_dwordx4 v[16:19],v252,s[8:11],s7 offen offset:1024 sc1\n"
"v_cvt_pk_bf16_f32 v32,v32,v33\nv_cvt_pk_bf16_f32 v33,v34,v35\nv_cvt_pk_bf16_f32 v34,v40,v41\nv_cvt_pk_bf16_f32 v35,v42,v43\nbuffer_store_dwordx4 v[32:35],v252,s[8:11],s7 offen offset:2048 sc1\nv_cvt_pk_bf16_f32 v48,v48,v49\nv_cvt_pk_bf16_f32 v49,v50,v51\nv_cvt_pk_bf16_f32 v50,v56,v57\nv_cvt_pk_bf16_f32 v51,v58,v59\nbuffer_store_dwordx4 v[48:51],v252,s[8:11],s7 offen offset:3072 sc1\nv_cvt_pk_bf16_f32 v64,v64,v65\n"
"v_cvt_pk_bf16_f32 v65,v66,v67\nv_cvt_pk_bf16_f32 v66,v72,v73\nv_cvt_pk_bf16_f32 v67,v74,v75\ns_add_u32 s7,s7,0x1000\nbuffer_store_dwordx4 v[64:67],v252,s[8:11],s7 offen offset:0 sc1\nv_cvt_pk_bf16_f32 v80,v80,v81\nv_cvt_pk_bf16_f32 v81,v82,v83\nv_cvt_pk_bf16_f32 v82,v88,v89\nv_cvt_pk_bf16_f32 v83,v90,v91\nbuffer_store_dwordx4 v[80:83],v252,s[8:11],s7 offen offset:1024 sc1\nv_cvt_pk_bf16_f32 v96,v96,v97\n"
"v_cvt_pk_bf16_f32 v97,v98,v99\nv_cvt_pk_bf16_f32 v98,v104,v105\nv_cvt_pk_bf16_f32 v99,v106,v107\nbuffer_store_dwordx4 v[96:99],v252,s[8:11],s7 offen offset:2048 sc1\nv_cvt_pk_bf16_f32 v112,v112,v113\nv_cvt_pk_bf16_f32 v113,v114,v115\nv_cvt_pk_bf16_f32 v114,v120,v121\nv_cvt_pk_bf16_f32 v115,v122,v123\nbuffer_store_dwordx4 v[112:115],v252,s[8:11],s7 offen offset:3072 sc1\nv_cvt_pk_bf16_f32 v4,v4,v5\n"
"v_cvt_pk_bf16_f32 v5,v6,v7\nv_cvt_pk_bf16_f32 v6,v12,v13\nv_cvt_pk_bf16_f32 v7,v14,v15\ns_add_u32 s7,s7,0x1000\nbuffer_store_dwordx4 v[4:7],v252,s[8:11],s7 offen offset:0 sc1\nv_cvt_pk_bf16_f32 v20,v20,v21\nv_cvt_pk_bf16_f32 v21,v22,v23\nv_cvt_pk_bf16_f32 v22,v28,v29\nv_cvt_pk_bf16_f32 v23,v30,v31\nbuffer_store_dwordx4 v[20:23],v252,s[8:11],s7 offen offset:1024 sc1\nv_cvt_pk_bf16_f32 v36,v36,v37\nv_cvt_pk_bf16_f32 v37,v38,v39\n"
"v_cvt_pk_bf16_f32 v38,v44,v45\nv_cvt_pk_bf16_f32 v39,v46,v47\nbuffer_store_dwordx4 v[36:39],v252,s[8:11],s7 offen offset:2048 sc1\nv_cvt_pk_bf16_f32 v52,v52,v53\nv_cvt_pk_bf16_f32 v53,v54,v55\nv_cvt_pk_bf16_f32 v54,v60,v61\nv_cvt_pk_bf16_f32 v55,v62,v63\nbuffer_store_dwordx4 v[52:55],v252,s[8:11],s7 offen offset:3072 sc1\nv_cvt_pk_bf16_f32 v68,v68,v69\nv_cvt_pk_bf16_f32 v69,v70,v71\nv_cvt_pk_bf16_f32 v70,v76,v77\n"
"v_cvt_pk_bf16_f32 v71,v78,v79\ns_add_u32 s7,s7,0x1000\nbuffer_store_dwordx4 v[68:71],v252,s[8:11],s7 offen offset:0 sc1\nv_cvt_pk_bf16_f32 v84,v84,v85\nv_cvt_pk_bf16_f32 v85,v86,v87\nv_cvt_pk_bf16_f32 v86,v92,v93\nv_cvt_pk_bf16_f32 v87,v94,v95\nbuffer_store_dwordx4 v[84:87],v252,s[8:11],s7 offen offset:1024 sc1\nv_cvt_pk_bf16_f32 v100,v100,v101\nv_cvt_pk_bf16_f32 v101,v102,v103\nv_cvt_pk_bf16_f32 v102,v108,v109\n"
"v_cvt_pk_bf16_f32 v103,v110,v111\nbuffer_store_dwordx4 v[100:103],v252,s[8:11],s7 offen offset:2048 sc1\nv_cvt_pk_bf16_f32 v116,v116,v117\nv_cvt_pk_bf16_f32 v117,v118,v119\nv_cvt_pk_bf16_f32 v118,v124,v125\nv_cvt_pk_bf16_f32 v119,v126,v127\nbuffer_store_dwordx4 v[116:119],v252,s[8:11],s7 offen offset:3072 sc1\nv_accvgpr_read_b32 v128,a64\nv_accvgpr_read_b32 v129,a65\nv_accvgpr_read_b32 v130,a66\nv_accvgpr_read_b32 v131,a67\n"
"v_accvgpr_read_b32 v132,a68\nv_accvgpr_read_b32 v133,a69\nv_accvgpr_read_b32 v134,a70\nv_accvgpr_read_b32 v135,a71\nv_accvgpr_read_b32 v136,a72\nv_accvgpr_read_b32 v137,a73\nv_accvgpr_read_b32 v138,a74\nv_accvgpr_read_b32 v139,a75\nv_accvgpr_read_b32 v140,a76\nv_accvgpr_read_b32 v141,a77\nv_accvgpr_read_b32 v142,a78\nv_accvgpr_read_b32 v143,a79\nv_accvgpr_read_b32 v144,a80\nv_accvgpr_read_b32 v145,a81\n"
"v_accvgpr_read_b32 v146,a82\nv_accvgpr_read_b32 v147,a83\nv_accvgpr_read_b32 v148,a84\nv_accvgpr_read_b32 v149,a85\nv_accvgpr_read_b32 v150,a86\nv_accvgpr_read_b32 v151,a87\nv_accvgpr_read_b32 v152,a88\nv_accvgpr_read_b32 v153,a89\nv_accvgpr_read_b32 v154,a90\nv_accvgpr_read_b32 v155,a91\nv_accvgpr_read_b32 v156,a92\nv_accvgpr_read_b32 v157,a93\nv_accvgpr_read_b32 v158,a94\nv_accvgpr_read_b32 v159,a95\n"
"v_accvgpr_read_b32 v160,a96\nv_accvgpr_read_b32 v161,a97\nv_accvgpr_read_b32 v162,a98\nv_accvgpr_read_b32 v163,a99\nv_accvgpr_read_b32 v164,a100\nv_accvgpr_read_b32 v165,a101\nv_accvgpr_read_b32 v166,a102\nv_accvgpr_read_b32 v167,a103\nv_accvgpr_read_b32 v168,a104\nv_accvgpr_read_b32 v169,a105\nv_accvgpr_read_b32 v170,a106\nv_accvgpr_read_b32 v171,a107\nv_accvgpr_read_b32 v172,a108\nv_accvgpr_read_b32 v173,a109\n"
"v_accvgpr_read_b32 v174,a110\nv_accvgpr_read_b32 v175,a111\nv_accvgpr_read_b32 v176,a112\nv_accvgpr_read_b32 v177,a113\nv_accvgpr_read_b32 v178,a114\nv_accvgpr_read_b32 v179,a115\nv_accvgpr_read_b32 v180,a116\nv_accvgpr_read_b32 v181,a117\nv_accvgpr_read_b32 v182,a118\nv_accvgpr_read_b32 v183,a119\nv_accvgpr_read_b32 v184,a120\nv_accvgpr_read_b32 v185,a121\nv_accvgpr_read_b32 v186,a122\nv_accvgpr_read_b32 v187,a123\n"
"v_accvgpr_read_b32 v188,a124\nv_accvgpr_read_b32 v189,a125\nv_accvgpr_read_b32 v190,a126\nv_accvgpr_read_b32 v191,a127\ns_waitcnt vmcnt(0)\nbuffer_store_dwordx2 v[250:251],off,s[8:11],s41 sc1\ns_branch L_fused_block_out_44\nL_fused_block_comb_43:\nv_lshlrev_b32 v214,7,v253\ns_mov_b64 exec,1\ns_mov_b32 s7,0\nL_fused_block_poll_49:\nbuffer_load_dwordx2 v[212:213],v214,s[8:11],s41 offen sc1\ns_add_u32 s7,s7,1\n"
"s_waitcnt vmcnt(0)\nv_cmp_eq_u64 vcc,s[12:13],v[212:213]\ns_nop 1\ns_cmp_eq_u64 vcc,exec\ns_cbranch_scc1 L_fused_block_polld_50\ns_cmp_lt_u32 s7,131072\ns_cbranch_scc1 L_fused_block_poll_49\nL_fused_block_polld_50:\ns_mov_b64 exec,-1\ns_add_u32 s7,s40,0x4000\nbuffer_load_dwordx4 v[208:211],v252,s[8:11],s7 offen sc1\ns_mov_b32 s7,s40\nbuffer_load_dwordx4 v[128:131],v252,s[8:11],s7 offen offset:0 sc1\n"
"buffer_load_dwordx4 v[132:135],v252,s[8:11],s7 offen offset:1024 sc1\nbuffer_load_dwordx4 v[136:139],v252,s[8:11],s7 offen offset:2048 sc1\nbuffer_load_dwordx4 v[140:143],v252,s[8:11],s7 offen offset:3072 sc1\ns_add_u32 s7,s7,0x1000\nbuffer_load_dwordx4 v[144:147],v252,s[8:11],s7 offen offset:0 sc1\nbuffer_load_dwordx4 v[148:151],v252,s[8:11],s7 offen offset:1024 sc1\nbuffer_load_dwordx4 v[152:155],v252,s[8:11],s7 offen offset:2048 sc1\n"
"buffer_load_dwordx4 v[156:159],v252,s[8:11],s7 offen offset:3072 sc1\ns_add_u32 s7,s7,0x1000\nbuffer_load_dwordx4 v[160:163],v252,s[8:11],s7 offen offset:0 sc1\nbuffer_load_dwordx4 v[164:167],v252,s[8:11],s7 offen offset:1024 sc1\nbuffer_load_dwordx4 v[168:171],v252,s[8:11],s7 offen offset:2048 sc1\nbuffer_load_dwordx4 v[172:175],v252,s[8:11],s7 offen offset:3072 sc1\ns_add_u32 s7,s7,0x1000\nbuffer_load_dwordx4 v[176:179],v252,s[8:11],s7 offen offset:0 sc1\n"
"buffer_load_dwordx4 v[180:183],v252,s[8:11],s7 offen offset:1024 sc1\nbuffer_load_dwordx4 v[184:187],v252,s[8:11],s7 offen offset:2048 sc1\nbuffer_load_dwordx4 v[188:191],v252,s[8:11],s7 offen offset:3072 sc1\ns_waitcnt vmcnt(16)\nv_min_f32 v204,v194,v210\nv_sub_f32 v206,v204,v194\nv_sub_f32 v210,v204,v210\nv_exp_f32 v206,v206\nv_exp_f32 v210,v210\ns_nop 0\nv_mul_f32 v204,v206,v192\nv_fmac_f32 v204,v210,v208\n"
"v_rcp_f32 v204,v204\ns_nop 0\nv_mul_f32 v206,v206,v204\nv_mul_f32 v210,v210,v204\nv_min_f32 v205,v195,v211\nv_sub_f32 v207,v205,v195\nv_sub_f32 v211,v205,v211\nv_exp_f32 v207,v207\nv_exp_f32 v211,v211\ns_nop 0\nv_mul_f32 v205,v207,v193\nv_fmac_f32 v205,v211,v209\nv_rcp_f32 v205,v205\ns_nop 0\nv_mul_f32 v207,v207,v205\nv_mul_f32 v211,v211,v205\ns_waitcnt vmcnt(15)\nv_lshlrev_b32 v212,16,v128\nv_and_b32 v213,0xffff0000,v128\n"
"v_mul_f32 v212,v210,v212\nv_mul_f32 v213,v210,v213\nv_fmac_f32 v212,v206,v0\nv_fmac_f32 v213,v206,v1\nv_cvt_pk_bf16_f32 v128,v212,v213\nv_lshlrev_b32 v214,16,v129\nv_and_b32 v215,0xffff0000,v129\nv_mul_f32 v214,v210,v214\nv_mul_f32 v215,v210,v215\nv_fmac_f32 v214,v206,v2\nv_fmac_f32 v215,v206,v3\nv_cvt_pk_bf16_f32 v129,v214,v215\nv_lshlrev_b32 v212,16,v130\nv_and_b32 v213,0xffff0000,v130\nv_mul_f32 v212,v210,v212\n"
"v_mul_f32 v213,v210,v213\nv_fmac_f32 v212,v206,v8\nv_fmac_f32 v213,v206,v9\nv_cvt_pk_bf16_f32 v130,v212,v213\nv_lshlrev_b32 v214,16,v131\nv_and_b32 v215,0xffff0000,v131\nv_mul_f32 v214,v210,v214\nv_mul_f32 v215,v210,v215\nv_fmac_f32 v214,v206,v10\nv_fmac_f32 v215,v206,v11\nv_cvt_pk_bf16_f32 v131,v214,v215\ns_waitcnt vmcnt(14)\nv_lshlrev_b32 v212,16,v132\nv_and_b32 v213,0xffff0000,v132\nv_mul_f32 v212,v210,v212\n"
"v_mul_f32 v213,v210,v213\nv_fmac_f32 v212,v206,v16\nv_fmac_f32 v213,v206,v17\nv_cvt_pk_bf16_f32 v132,v212,v213\nv_lshlrev_b32 v214,16,v133\nv_and_b32 v215,0xffff0000,v133\nv_mul_f32 v214,v210,v214\nv_mul_f32 v215,v210,v215\nv_fmac_f32 v214,v206,v18\nv_fmac_f32 v215,v206,v19\nv_cvt_pk_bf16_f32 v133,v214,v215\nv_lshlrev_b32 v212,16,v134\nv_and_b32 v213,0xffff0000,v134\nv_mul_f32 v212,v210,v212\nv_mul_f32 v213,v210,v213\n"
"v_fmac_f32 v212,v206,v24\nv_fmac_f32 v213,v206,v25\nv_cvt_pk_bf16_f32 v134,v212,v213\nv_lshlrev_b32 v214,16,v135\nv_and_b32 v215,0xffff0000,v135\nv_mul_f32 v214,v210,v214\nv_mul_f32 v215,v210,v215\nv_fmac_f32 v214,v206,v26\nv_fmac_f32 v215,v206,v27\nv_cvt_pk_bf16_f32 v135,v214,v215\ns_waitcnt vmcnt(13)\nv_lshlrev_b32 v212,16,v136\nv_and_b32 v213,0xffff0000,v136\nv_mul_f32 v212,v210,v212\nv_mul_f32 v213,v210,v213\n"
"v_fmac_f32 v212,v206,v32\nv_fmac_f32 v213,v206,v33\nv_cvt_pk_bf16_f32 v136,v212,v213\nv_lshlrev_b32 v214,16,v137\nv_and_b32 v215,0xffff0000,v137\nv_mul_f32 v214,v210,v214\nv_mul_f32 v215,v210,v215\nv_fmac_f32 v214,v206,v34\nv_fmac_f32 v215,v206,v35\nv_cvt_pk_bf16_f32 v137,v214,v215\nv_lshlrev_b32 v212,16,v138\nv_and_b32 v213,0xffff0000,v138\nv_mul_f32 v212,v210,v212\nv_mul_f32 v213,v210,v213\nv_fmac_f32 v212,v206,v40\n"
"v_fmac_f32 v213,v206,v41\nv_cvt_pk_bf16_f32 v138,v212,v213\nv_lshlrev_b32 v214,16,v139\nv_and_b32 v215,0xffff0000,v139\nv_mul_f32 v214,v210,v214\nv_mul_f32 v215,v210,v215\nv_fmac_f32 v214,v206,v42\nv_fmac_f32 v215,v206,v43\nv_cvt_pk_bf16_f32 v139,v214,v215\ns_waitcnt vmcnt(12)\nv_lshlrev_b32 v212,16,v140\nv_and_b32 v213,0xffff0000,v140\nv_mul_f32 v212,v210,v212\nv_mul_f32 v213,v210,v213\nv_fmac_f32 v212,v206,v48\n"
"v_fmac_f32 v213,v206,v49\nv_cvt_pk_bf16_f32 v140,v212,v213\nv_lshlrev_b32 v214,16,v141\nv_and_b32 v215,0xffff0000,v141\nv_mul_f32 v214,v210,v214\nv_mul_f32 v215,v210,v215\nv_fmac_f32 v214,v206,v50\nv_fmac_f32 v215,v206,v51\nv_cvt_pk_bf16_f32 v141,v214,v215\nv_lshlrev_b32 v212,16,v142\nv_and_b32 v213,0xffff0000,v142\nv_mul_f32 v212,v210,v212\nv_mul_f32 v213,v210,v213\nv_fmac_f32 v212,v206,v56\nv_fmac_f32 v213,v206,v57\n"
"v_cvt_pk_bf16_f32 v142,v212,v213\nv_lshlrev_b32 v214,16,v143\nv_and_b32 v215,0xffff0000,v143\nv_mul_f32 v214,v210,v214\nv_mul_f32 v215,v210,v215\nv_fmac_f32 v214,v206,v58\nv_fmac_f32 v215,v206,v59\nv_cvt_pk_bf16_f32 v143,v214,v215\ns_waitcnt vmcnt(11)\nv_lshlrev_b32 v212,16,v144\nv_and_b32 v213,0xffff0000,v144\nv_mul_f32 v212,v210,v212\nv_mul_f32 v213,v210,v213\nv_fmac_f32 v212,v206,v64\nv_fmac_f32 v213,v206,v65\n"
"v_cvt_pk_bf16_f32 v144,v212,v213\nv_lshlrev_b32 v214,16,v145\nv_and_b32 v215,0xffff0000,v145\nv_mul_f32 v214,v210,v214\nv_mul_f32 v215,v210,v215\nv_fmac_f32 v214,v206,v66\nv_fmac_f32 v215,v206,v67\nv_cvt_pk_bf16_f32 v145,v214,v215\nv_lshlrev_b32 v212,16,v146\nv_and_b32 v213,0xffff0000,v146\nv_mul_f32 v212,v210,v212\nv_mul_f32 v213,v210,v213\nv_fmac_f32 v212,v206,v72\nv_fmac_f32 v213,v206,v73\nv_cvt_pk_bf16_f32 v146,v212,v213\n"
"v_lshlrev_b32 v214,16,v147\nv_and_b32 v215,0xffff0000,v147\nv_mul_f32 v214,v210,v214\nv_mul_f32 v215,v210,v215\nv_fmac_f32 v214,v206,v74\nv_fmac_f32 v215,v206,v75\nv_cvt_pk_bf16_f32 v147,v214,v215\ns_waitcnt vmcnt(10)\nv_lshlrev_b32 v212,16,v148\nv_and_b32 v213,0xffff0000,v148\nv_mul_f32 v212,v210,v212\nv_mul_f32 v213,v210,v213\nv_fmac_f32 v212,v206,v80\nv_fmac_f32 v213,v206,v81\nv_cvt_pk_bf16_f32 v148,v212,v213\n"
"v_lshlrev_b32 v214,16,v149\nv_and_b32 v215,0xffff0000,v149\nv_mul_f32 v214,v210,v214\nv_mul_f32 v215,v210,v215\nv_fmac_f32 v214,v206,v82\nv_fmac_f32 v215,v206,v83\nv_cvt_pk_bf16_f32 v149,v214,v215\nv_lshlrev_b32 v212,16,v150\nv_and_b32 v213,0xffff0000,v150\nv_mul_f32 v212,v210,v212\nv_mul_f32 v213,v210,v213\nv_fmac_f32 v212,v206,v88\nv_fmac_f32 v213,v206,v89\nv_cvt_pk_bf16_f32 v150,v212,v213\nv_lshlrev_b32 v214,16,v151\n"
"v_and_b32 v215,0xffff0000,v151\nv_mul_f32 v214,v210,v214\nv_mul_f32 v215,v210,v215\nv_fmac_f32 v214,v206,v90\nv_fmac_f32 v215,v206,v91\nv_cvt_pk_bf16_f32 v151,v214,v215\ns_waitcnt vmcnt(9)\nv_lshlrev_b32 v212,16,v152\nv_and_b32 v213,0xffff0000,v152\nv_mul_f32 v212,v210,v212\nv_mul_f32 v213,v210,v213\nv_fmac_f32 v212,v206,v96\nv_fmac_f32 v213,v206,v97\nv_cvt_pk_bf16_f32 v152,v212,v213\nv_lshlrev_b32 v214,16,v153\n"
"v_and_b32 v215,0xffff0000,v153\nv_mul_f32 v214,v210,v214\nv_mul_f32 v215,v210,v215\nv_fmac_f32 v214,v206,v98\nv_fmac_f32 v215,v206,v99\nv_cvt_pk_bf16_f32 v153,v214,v215\nv_lshlrev_b32 v212,16,v154\nv_and_b32 v213,0xffff0000,v154\nv_mul_f32 v212,v210,v212\nv_mul_f32 v213,v210,v213\nv_fmac_f32 v212,v206,v104\nv_fmac_f32 v213,v206,v105\nv_cvt_pk_bf16_f32 v154,v212,v213\nv_lshlrev_b32 v214,16,v155\nv_and_b32 v215,0xffff0000,v155\n"
"v_mul_f32 v214,v210,v214\nv_mul_f32 v215,v210,v215\nv_fmac_f32 v214,v206,v106\nv_fmac_f32 v215,v206,v107\nv_cvt_pk_bf16_f32 v155,v214,v215\ns_waitcnt vmcnt(8)\nv_lshlrev_b32 v212,16,v156\nv_and_b32 v213,0xffff0000,v156\nv_mul_f32 v212,v210,v212\nv_mul_f32 v213,v210,v213\nv_fmac_f32 v212,v206,v112\nv_fmac_f32 v213,v206,v113\nv_cvt_pk_bf16_f32 v156,v212,v213\nv_lshlrev_b32 v214,16,v157\nv_and_b32 v215,0xffff0000,v157\n"
"v_mul_f32 v214,v210,v214\nv_mul_f32 v215,v210,v215\nv_fmac_f32 v214,v206,v114\nv_fmac_f32 v215,v206,v115\nv_cvt_pk_bf16_f32 v157,v214,v215\nv_lshlrev_b32 v212,16,v158\nv_and_b32 v213,0xffff0000,v158\nv_mul_f32 v212,v210,v212\nv_mul_f32 v213,v210,v213\nv_fmac_f32 v212,v206,v120\nv_fmac_f32 v213,v206,v121\nv_cvt_pk_bf16_f32 v158,v212,v213\nv_lshlrev_b32 v214,16,v159\nv_and_b32 v215,0xffff0000,v159\nv_mul_f32 v214,v210,v214\n"
"v_mul_f32 v215,v210,v215\nv_fmac_f32 v214,v206,v122\nv_fmac_f32 v215,v206,v123\nv_cvt_pk_bf16_f32 v159,v214,v215\ns_waitcnt vmcnt(7)\nv_lshlrev_b32 v212,16,v160\nv_and_b32 v213,0xffff0000,v160\nv_mul_f32 v212,v211,v212\nv_mul_f32 v213,v211,v213\nv_fmac_f32 v212,v207,v4\nv_fmac_f32 v213,v207,v5\nv_cvt_pk_bf16_f32 v160,v212,v213\nv_lshlrev_b32 v214,16,v161\nv_and_b32 v215,0xffff0000,v161\nv_mul_f32 v214,v211,v214\n"
"v_mul_f32 v215,v211,v215\nv_fmac_f32 v214,v207,v6\nv_fmac_f32 v215,v207,v7\nv_cvt_pk_bf16_f32 v161,v214,v215\nv_lshlrev_b32 v212,16,v162\nv_and_b32 v213,0xffff0000,v162\nv_mul_f32 v212,v211,v212\nv_mul_f32 v213,v211,v213\nv_fmac_f32 v212,v207,v12\nv_fmac_f32 v213,v207,v13\nv_cvt_pk_bf16_f32 v162,v212,v213\nv_lshlrev_b32 v214,16,v163\nv_and_b32 v215,0xffff0000,v163\nv_mul_f32 v214,v211,v214\nv_mul_f32 v215,v211,v215\n"
"v_fmac_f32 v214,v207,v14\nv_fmac_f32 v215,v207,v15\nv_cvt_pk_bf16_f32 v163,v214,v215\ns_waitcnt vmcnt(6)\nv_lshlrev_b32 v212,16,v164\nv_and_b32 v213,0xffff0000,v164\nv_mul_f32 v212,v211,v212\nv_mul_f32 v213,v211,v213\nv_fmac_f32 v212,v207,v20\nv_fmac_f32 v213,v207,v21\nv_cvt_pk_bf16_f32 v164,v212,v213\nv_lshlrev_b32 v214,16,v165\nv_and_b32 v215,0xffff0000,v165\nv_mul_f32 v214,v211,v214\nv_mul_f32 v215,v211,v215\n"
"v_fmac_f32 v214,v207,v22\nv_fmac_f32 v215,v207,v23\nv_cvt_pk_bf16_f32 v165,v214,v215\nv_lshlrev_b32 v212,16,v166\nv_and_b32 v213,0xffff0000,v166\nv_mul_f32 v212,v211,v212\nv_mul_f32 v213,v211,v213\nv_fmac_f32 v212,v207,v28\nv_fmac_f32 v213,v207,v29\nv_cvt_pk_bf16_f32 v166,v212,v213\nv_lshlrev_b32 v214,16,v167\nv_and_b32 v215,0xffff0000,v167\nv_mul_f32 v214,v211,v214\nv_mul_f32 v215,v211,v215\nv_fmac_f32 v214,v207,v30\n"
"v_fmac_f32 v215,v207,v31\nv_cvt_pk_bf16_f32 v167,v214,v215\ns_waitcnt vmcnt(5)\nv_lshlrev_b32 v212,16,v168\nv_and_b32 v213,0xffff0000,v168\nv_mul_f32 v212,v211,v212\nv_mul_f32 v213,v211,v213\nv_fmac_f32 v212,v207,v36\nv_fmac_f32 v213,v207,v37\nv_cvt_pk_bf16_f32 v168,v212,v213\nv_lshlrev_b32 v214,16,v169\nv_and_b32 v215,0xffff0000,v169\nv_mul_f32 v214,v211,v214\nv_mul_f32 v215,v211,v215\nv_fmac_f32 v214,v207,v38\n"
"v_fmac_f32 v215,v207,v39\nv_cvt_pk_bf16_f32 v169,v214,v215\nv_lshlrev_b32 v212,16,v170\nv_and_b32 v213,0xffff0000,v170\nv_mul_f32 v212,v211,v212\nv_mul_f32 v213,v211,v213\nv_fmac_f32 v212,v207,v44\nv_fmac_f32 v213,v207,v45\nv_cvt_pk_bf16_f32 v170,v212,v213\nv_lshlrev_b32 v214,16,v171\nv_and_b32 v215,0xffff0000,v171\nv_mul_f32 v214,v211,v214\nv_mul_f32 v215,v211,v215\nv_fmac_f32 v214,v207,v46\nv_fmac_f32 v215,v207,v47\n"
"v_cvt_pk_bf16_f32 v171,v214,v215\ns_waitcnt vmcnt(4)\nv_lshlrev_b32 v212,16,v172\nv_and_b32 v213,0xffff0000,v172\nv_mul_f32 v212,v211,v212\nv_mul_f32 v213,v211,v213\nv_fmac_f32 v212,v207,v52\nv_fmac_f32 v213,v207,v53\nv_cvt_pk_bf16_f32 v172,v212,v213\nv_lshlrev_b32 v214,16,v173\nv_and_b32 v215,0xffff0000,v173\nv_mul_f32 v214,v211,v214\nv_mul_f32 v215,v211,v215\nv_fmac_f32 v214,v207,v54\nv_fmac_f32 v215,v207,v55\n"
"v_cvt_pk_bf16_f32 v173,v214,v215\nv_lshlrev_b32 v212,16,v174\nv_and_b32 v213,0xffff0000,v174\nv_mul_f32 v212,v211,v212\nv_mul_f32 v213,v211,v213\nv_fmac_f32 v212,v207,v60\nv_fmac_f32 v213,v207,v61\nv_cvt_pk_bf16_f32 v174,v212,v213\nv_lshlrev_b32 v214,16,v175\nv_and_b32 v215,0xffff0000,v175\nv_mul_f32 v214,v211,v214\nv_mul_f32 v215,v211,v215\nv_fmac_f32 v214,v207,v62\nv_fmac_f32 v215,v207,v63\nv_cvt_pk_bf16_f32 v175,v214,v215\n"
"s_waitcnt vmcnt(3)\nv_lshlrev_b32 v212,16,v176\nv_and_b32 v213,0xffff0000,v176\nv_mul_f32 v212,v211,v212\nv_mul_f32 v213,v211,v213\nv_fmac_f32 v212,v207,v68\nv_fmac_f32 v213,v207,v69\nv_cvt_pk_bf16_f32 v176,v212,v213\nv_lshlrev_b32 v214,16,v177\nv_and_b32 v215,0xffff0000,v177\nv_mul_f32 v214,v211,v214\nv_mul_f32 v215,v211,v215\nv_fmac_f32 v214,v207,v70\nv_fmac_f32 v215,v207,v71\nv_cvt_pk_bf16_f32 v177,v214,v215\n"
"v_lshlrev_b32 v212,16,v178\nv_and_b32 v213,0xffff0000,v178\nv_mul_f32 v212,v211,v212\nv_mul_f32 v213,v211,v213\nv_fmac_f32 v212,v207,v76\nv_fmac_f32 v213,v207,v77\nv_cvt_pk_bf16_f32 v178,v212,v213\nv_lshlrev_b32 v214,16,v179\nv_and_b32 v215,0xffff0000,v179\nv_mul_f32 v214,v211,v214\nv_mul_f32 v215,v211,v215\nv_fmac_f32 v214,v207,v78\nv_fmac_f32 v215,v207,v79\nv_cvt_pk_bf16_f32 v179,v214,v215\ns_waitcnt vmcnt(2)\n"
"v_lshlrev_b32 v212,16,v180\nv_and_b32 v213,0xffff0000,v180\nv_mul_f32 v212,v211,v212\nv_mul_f32 v213,v211,v213\nv_fmac_f32 v212,v207,v84\nv_fmac_f32 v213,v207,v85\nv_cvt_pk_bf16_f32 v180,v212,v213\nv_lshlrev_b32 v214,16,v181\nv_and_b32 v215,0xffff0000,v181\nv_mul_f32 v214,v211,v214\nv_mul_f32 v215,v211,v215\nv_fmac_f32 v214,v207,v86\nv_fmac_f32 v215,v207,v87\nv_cvt_pk_bf16_f32 v181,v214,v215\nv_lshlrev_b32 v212,16,v182\n"
"v_and_b32 v213,0xffff0000,v182\nv_mul_f32 v212,v211,v212\nv_mul_f32 v213,v211,v213\nv_fmac_f32 v212,v207,v92\nv_fmac_f32 v213,v207,v93\nv_cvt_pk_bf16_f32 v182,v212,v213\nv_lshlrev_b32 v214,16,v183\nv_and_b32 v215,0xffff0000,v183\nv_mul_f32 v214,v211,v214\nv_mul_f32 v215,v211,v215\nv_fmac_f32 v214,v207,v94\nv_fmac_f32 v215,v207,v95\nv_cvt_pk_bf16_f32 v183,v214,v215\ns_waitcnt vmcnt(1)\nv_lshlrev_b32 v212,16,v184\n"
"v_and_b32 v213,0xffff0000,v184\nv_mul_f32 v212,v211,v212\nv_mul_f32 v213,v211,v213\nv_fmac_f32 v212,v207,v100\nv_fmac_f32 v213,v207,v101\nv_cvt_pk_bf16_f32 v184,v212,v213\nv_lshlrev_b32 v214,16,v185\nv_and_b32 v215,0xffff0000,v185\nv_mul_f32 v214,v211,v214\nv_mul_f32 v215,v211,v215\nv_fmac_f32 v214,v207,v102\nv_fmac_f32 v215,v207,v103\nv_cvt_pk_bf16_f32 v185,v214,v215\nv_lshlrev_b32 v212,16,v186\nv_and_b32 v213,0xffff0000,v186\n"
"v_mul_f32 v212,v211,v212\nv_mul_f32 v213,v211,v213\nv_fmac_f32 v212,v207,v108\nv_fmac_f32 v213,v207,v109\nv_cvt_pk_bf16_f32 v186,v212,v213\nv_lshlrev_b32 v214,16,v187\nv_and_b32 v215,0xffff0000,v187\nv_mul_f32 v214,v211,v214\nv_mul_f32 v215,v211,v215\nv_fmac_f32 v214,v207,v110\nv_fmac_f32 v215,v207,v111\nv_cvt_pk_bf16_f32 v187,v214,v215\ns_waitcnt vmcnt(0)\nv_lshlrev_b32 v212,16,v188\nv_and_b32 v213,0xffff0000,v188\n"
"v_mul_f32 v212,v211,v212\nv_mul_f32 v213,v211,v213\nv_fmac_f32 v212,v207,v116\nv_fmac_f32 v213,v207,v117\nv_cvt_pk_bf16_f32 v188,v212,v213\nv_lshlrev_b32 v214,16,v189\nv_and_b32 v215,0xffff0000,v189\nv_mul_f32 v214,v211,v214\nv_mul_f32 v215,v211,v215\nv_fmac_f32 v214,v207,v118\nv_fmac_f32 v215,v207,v119\nv_cvt_pk_bf16_f32 v189,v214,v215\nv_lshlrev_b32 v212,16,v190\nv_and_b32 v213,0xffff0000,v190\nv_mul_f32 v212,v211,v212\n"
"v_mul_f32 v213,v211,v213\nv_fmac_f32 v212,v207,v124\nv_fmac_f32 v213,v207,v125\nv_cvt_pk_bf16_f32 v190,v212,v213\nv_lshlrev_b32 v214,16,v191\nv_and_b32 v215,0xffff0000,v191\nv_mul_f32 v214,v211,v214\nv_mul_f32 v215,v211,v215\nv_fmac_f32 v214,v207,v126\nv_fmac_f32 v215,v207,v127\nv_cvt_pk_bf16_f32 v191,v214,v215\nL_fused_block_out_44:\nv_mfma_f32_16x16x32_bf16 v[0:3],a[224:227],v[128:131],0\nds_read_b128 a[252:255],v196 offset:11264\n"
"v_mfma_f32_16x16x32_bf16 v[4:7],a[224:227],v[160:163],0\nv_mfma_f32_16x16x32_bf16 v[12:15],a[228:231],v[160:163],0\nds_read_b128 a[224:227],v196 offset:4096\nv_mfma_f32_16x16x32_bf16 v[8:11],a[228:231],v[128:131],0\ns_waitcnt lgkmcnt(5)\nv_mfma_f32_16x16x32_bf16 v[0:3],a[232:235],v[132:135],v[0:3]\nds_read_b128 a[228:231],v196 offset:12288\nv_mfma_f32_16x16x32_bf16 v[4:7],a[232:235],v[164:167],v[4:7]\n"
"buffer_load_dwordx4 v252,s[8:11],s25 offen offset:2048 sc1 lds\nv_mfma_f32_16x16x32_bf16 v[12:15],a[236:239],v[164:167],v[12:15]\nds_read_b128 a[232:235],v196 offset:5120\nv_mfma_f32_16x16x32_bf16 v[8:11],a[236:239],v[132:135],v[8:11]\ns_waitcnt lgkmcnt(5)\nv_mfma_f32_16x16x32_bf16 v[0:3],a[240:243],v[136:139],v[0:3]\nds_read_b128 a[236:239],v196 offset:13312\nv_mfma_f32_16x16x32_bf16 v[4:7],a[240:243],v[168:171],v[4:7]\n"
"v_mfma_f32_16x16x32_bf16 v[12:15],a[244:247],v[168:171],v[12:15]\nds_read_b128 a[240:243],v196 offset:6144\nv_mfma_f32_16x16x32_bf16 v[8:11],a[244:247],v[136:139],v[8:11]\ns_waitcnt lgkmcnt(5)\nv_mfma_f32_16x16x32_bf16 v[0:3],a[248:251],v[140:143],v[0:3]\nds_read_b128 a[244:247],v196 offset:14336\nv_mfma_f32_16x16x32_bf16 v[4:7],a[248:251],v[172:175],v[4:7]\nbuffer_load_dwordx4 v252,s[8:11],s25 offen offset:3072 sc1 lds\n"
"v_mfma_f32_16x16x32_bf16 v[12:15],a[252:255],v[172:175],v[12:15]\ns_add_u32 s84,s32,0x8000\nds_read_b128 a[248:251],v196 offset:7168\ns_and_b32 s84,s84,0xffff\nv_mfma_f32_16x16x32_bf16 v[8:11],a[252:255],v[140:143],v[8:11]\ns_waitcnt lgkmcnt(5)\ns_add_u32 s84,s84,s15\ns_add_u32 m0,s84,0x10000\nv_mfma_f32_16x16x32_bf16 v[0:3],a[224:227],v[144:147],v[0:3]\nds_read_b128 a[252:255],v196 offset:15360\nv_mfma_f32_16x16x32_bf16 v[4:7],a[224:227],v[176:179],v[4:7]\n"
"v_mfma_f32_16x16x32_bf16 v[12:15],a[228:231],v[176:179],v[12:15]\nds_read_b128 a[224:227],v197 offset:0\nv_mfma_f32_16x16x32_bf16 v[8:11],a[228:231],v[144:147],v[8:11]\ns_waitcnt lgkmcnt(5)\nv_mfma_f32_16x16x32_bf16 v[0:3],a[232:235],v[148:151],v[0:3]\nds_read_b128 a[228:231],v197 offset:8192\nv_mfma_f32_16x16x32_bf16 v[4:7],a[232:235],v[180:183],v[4:7]\nbuffer_load_dwordx4 v252,s[8:11],s26 offen sc1 lds\n"
"v_mfma_f32_16x16x32_bf16 v[12:15],a[236:239],v[180:183],v[12:15]\nds_read_b128 a[232:235],v197 offset:1024\nv_mfma_f32_16x16x32_bf16 v[8:11],a[236:239],v[148:151],v[8:11]\ns_waitcnt lgkmcnt(5)\nv_mfma_f32_16x16x32_bf16 v[0:3],a[240:243],v[152:155],v[0:3]\nds_read_b128 a[236:239],v197 offset:9216\nv_mfma_f32_16x16x32_bf16 v[4:7],a[240:243],v[184:187],v[4:7]\nv_mfma_f32_16x16x32_bf16 v[12:15],a[244:247],v[184:187],v[12:15]\n"
"ds_read_b128 a[240:243],v197 offset:2048\nv_mfma_f32_16x16x32_bf16 v[8:11],a[244:247],v[152:155],v[8:11]\ns_waitcnt lgkmcnt(5)\nv_mfma_f32_16x16x32_bf16 v[0:3],a[248:251],v[156:159],v[0:3]\nds_read_b128 a[244:247],v197 offset:10240\nv_mfma_f32_16x16x32_bf16 v[4:7],a[248:251],v[188:191],v[4:7]\nbuffer_load_dwordx4 v252,s[8:11],s26 offen offset:1024 sc1 lds\nv_mfma_f32_16x16x32_bf16 v[12:15],a[252:255],v[188:191],v[12:15]\n"
"ds_read_b128 a[248:251],v197 offset:3072\nv_mfma_f32_16x16x32_bf16 v[8:11],a[252:255],v[156:159],v[8:11]\ns_waitcnt lgkmcnt(5)\nv_mfma_f32_16x16x32_bf16 v[16:19],a[224:227],v[128:131],0\ns_add_u32 s33,s25,0x4000\nds_read_b128 a[252:255],v197 offset:11264\nv_mfma_f32_16x16x32_bf16 v[20:23],a[224:227],v[160:163],0\ns_add_u32 s25,s25,0x8000\nv_mfma_f32_16x16x32_bf16 v[28:31],a[228:231],v[160:163],0\ns_cmp_eq_u32 s25,s27\n"
"ds_read_b128 a[224:227],v197 offset:4096\nv_mfma_f32_16x16x32_bf16 v[24:27],a[228:231],v[128:131],0\ns_waitcnt lgkmcnt(5)\nv_mfma_f32_16x16x32_bf16 v[16:19],a[232:235],v[132:135],v[16:19]\ns_cselect_b32 s25,s28,s25\nds_read_b128 a[228:231],v197 offset:12288\nv_mfma_f32_16x16x32_bf16 v[20:23],a[232:235],v[164:167],v[20:23]\ns_cselect_b32 s27,s29,s27\nbuffer_load_dwordx4 v252,s[8:11],s26 offen offset:2048 sc1 lds\n"
"v_mfma_f32_16x16x32_bf16 v[28:31],a[236:239],v[164:167],v[28:31]\ns_cselect_b32 s28,s30,s28\nds_read_b128 a[232:235],v197 offset:5120\nv_cvt_pk_bf16_f32 v32,v0,v1\nv_mfma_f32_16x16x32_bf16 v[24:27],a[236:239],v[132:135],v[24:27]\ns_waitcnt lgkmcnt(5)\nv_cvt_pk_bf16_f32 v33,v2,v3\nv_cvt_pk_bf16_f32 v34,v8,v9\nv_mfma_f32_16x16x32_bf16 v[16:19],a[240:243],v[136:139],v[16:19]\ns_cselect_b32 s29,s31,s29\n"
"ds_read_b128 a[236:239],v197 offset:13312\nv_cvt_pk_bf16_f32 v35,v10,v11\nv_mfma_f32_16x16x32_bf16 v[20:23],a[240:243],v[168:171],v[20:23]\ns_cselect_b32 s30,1073741824,s30\nbuffer_store_dwordx4 v[32:35],v199,s[8:11],s52 offen offset:0 sc1\nv_mfma_f32_16x16x32_bf16 v[28:31],a[244:247],v[168:171],v[28:31]\nds_read_b128 a[240:243],v197 offset:6144\nv_cvt_pk_bf16_f32 v36,v4,v5\nv_mfma_f32_16x16x32_bf16 v[24:27],a[244:247],v[136:139],v[24:27]\n"
"s_waitcnt lgkmcnt(5)\nv_cvt_pk_bf16_f32 v37,v6,v7\nv_cvt_pk_bf16_f32 v38,v12,v13\nv_mfma_f32_16x16x32_bf16 v[16:19],a[248:251],v[140:143],v[16:19]\nds_read_b128 a[244:247],v197 offset:14336\nv_cvt_pk_bf16_f32 v39,v14,v15\nv_mfma_f32_16x16x32_bf16 v[20:23],a[248:251],v[172:175],v[20:23]\nbuffer_load_dwordx4 v252,s[8:11],s26 offen offset:3072 sc1 lds\nv_mfma_f32_16x16x32_bf16 v[28:31],a[252:255],v[172:175],v[28:31]\n"
"s_add_u32 m0,s32,s15\nds_read_b128 a[248:251],v197 offset:7168\ns_mov_b32 s26,s33\nv_mfma_f32_16x16x32_bf16 v[24:27],a[252:255],v[140:143],v[24:27]\ns_waitcnt lgkmcnt(5)\nv_mfma_f32_16x16x32_bf16 v[16:19],a[224:227],v[144:147],v[16:19]\nds_read_b128 a[252:255],v197 offset:15360\nv_mfma_f32_16x16x32_bf16 v[20:23],a[224:227],v[176:179],v[20:23]\ns_waitcnt vmcnt(9)\ns_barrier\nv_mfma_f32_16x16x32_bf16 v[28:31],a[228:231],v[176:179],v[28:31]\n"
"ds_read_b128 a[224:227],v196 offset:16384\nv_mfma_f32_16x16x32_bf16 v[24:27],a[228:231],v[144:147],v[24:27]\ns_waitcnt lgkmcnt(5)\nv_mfma_f32_16x16x32_bf16 v[16:19],a[232:235],v[148:151],v[16:19]\nds_read_b128 a[228:231],v196 offset:24576\nv_mfma_f32_16x16x32_bf16 v[20:23],a[232:235],v[180:183],v[20:23]\nbuffer_load_dwordx4 v252,s[8:11],s25 offen sc1 lds\nv_mfma_f32_16x16x32_bf16 v[28:31],a[236:239],v[180:183],v[28:31]\n"
"ds_read_b128 a[232:235],v196 offset:17408\nv_mfma_f32_16x16x32_bf16 v[24:27],a[236:239],v[148:151],v[24:27]\ns_waitcnt lgkmcnt(5)\nv_mfma_f32_16x16x32_bf16 v[16:19],a[240:243],v[152:155],v[16:19]\ns_add_u32 s32,s32,0x4000\nds_read_b128 a[236:239],v196 offset:25600\ns_and_b32 s32,s32,0xffff\nv_mfma_f32_16x16x32_bf16 v[20:23],a[240:243],v[184:187],v[20:23]\nbuffer_store_dwordx4 v[36:39],v199,s[8:11],s53 offen offset:0 sc1\n"
"v_mfma_f32_16x16x32_bf16 v[28:31],a[244:247],v[184:187],v[28:31]\nds_read_b128 a[240:243],v196 offset:18432\nv_mfma_f32_16x16x32_bf16 v[24:27],a[244:247],v[152:155],v[24:27]\ns_waitcnt lgkmcnt(5)\nv_mfma_f32_16x16x32_bf16 v[16:19],a[248:251],v[156:159],v[16:19]\nds_read_b128 a[244:247],v196 offset:26624\nv_mfma_f32_16x16x32_bf16 v[20:23],a[248:251],v[188:191],v[20:23]\nbuffer_load_dwordx4 v252,s[8:11],s25 offen offset:1024 sc1 lds\n"
"v_mfma_f32_16x16x32_bf16 v[28:31],a[252:255],v[188:191],v[28:31]\nds_read_b128 a[248:251],v196 offset:19456\nv_mfma_f32_16x16x32_bf16 v[24:27],a[252:255],v[156:159],v[24:27]\ns_waitcnt lgkmcnt(5)\nv_mfma_f32_16x16x32_bf16 v[0:3],a[224:227],v[128:131],0\nds_read_b128 a[252:255],v196 offset:27648\nv_mfma_f32_16x16x32_bf16 v[4:7],a[224:227],v[160:163],0\nv_mfma_f32_16x16x32_bf16 v[12:15],a[228:231],v[160:163],0\n"
"ds_read_b128 a[224:227],v196 offset:20480\nv_mfma_f32_16x16x32_bf16 v[8:11],a[228:231],v[128:131],0\ns_waitcnt lgkmcnt(5)\nv_mfma_f32_16x16x32_bf16 v[0:3],a[232:235],v[132:135],v[0:3]\nds_read_b128 a[228:231],v196 offset:28672\nv_mfma_f32_16x16x32_bf16 v[4:7],a[232:235],v[164:167],v[4:7]\nbuffer_load_dwordx4 v252,s[8:11],s25 offen offset:2048 sc1 lds\nv_mfma_f32_16x16x32_bf16 v[12:15],a[236:239],v[164:167],v[12:15]\n"
"ds_read_b128 a[232:235],v196 offset:21504\nv_cvt_pk_bf16_f32 v40,v16,v17\nv_mfma_f32_16x16x32_bf16 v[8:11],a[236:239],v[132:135],v[8:11]\ns_waitcnt lgkmcnt(5)\nv_cvt_pk_bf16_f32 v41,v18,v19\nv_cvt_pk_bf16_f32 v42,v24,v25\nv_mfma_f32_16x16x32_bf16 v[0:3],a[240:243],v[136:139],v[0:3]\nds_read_b128 a[236:239],v196 offset:29696\nv_cvt_pk_bf16_f32 v43,v26,v27\nv_mfma_f32_16x16x32_bf16 v[4:7],a[240:243],v[168:171],v[4:7]\n"
"buffer_store_dwordx4 v[40:43],v199,s[8:11],s52 offen offset:512 sc1\nv_mfma_f32_16x16x32_bf16 v[12:15],a[244:247],v[168:171],v[12:15]\nds_read_b128 a[240:243],v196 offset:22528\nv_cvt_pk_bf16_f32 v44,v20,v21\nv_mfma_f32_16x16x32_bf16 v[8:11],a[244:247],v[136:139],v[8:11]\ns_waitcnt lgkmcnt(5)\nv_cvt_pk_bf16_f32 v45,v22,v23\nv_cvt_pk_bf16_f32 v46,v28,v29\nv_mfma_f32_16x16x32_bf16 v[0:3],a[248:251],v[140:143],v[0:3]\n"
"ds_read_b128 a[244:247],v196 offset:30720\nv_cvt_pk_bf16_f32 v47,v30,v31\nv_mfma_f32_16x16x32_bf16 v[4:7],a[248:251],v[172:175],v[4:7]\nbuffer_load_dwordx4 v252,s[8:11],s25 offen offset:3072 sc1 lds\nv_mfma_f32_16x16x32_bf16 v[12:15],a[252:255],v[172:175],v[12:15]\ns_add_u32 s84,s32,0x8000\nds_read_b128 a[248:251],v196 offset:23552\ns_and_b32 s84,s84,0xffff\nv_mfma_f32_16x16x32_bf16 v[8:11],a[252:255],v[140:143],v[8:11]\n"
"s_waitcnt lgkmcnt(5)\ns_add_u32 s84,s84,s15\ns_add_u32 m0,s84,0x10000\nv_mfma_f32_16x16x32_bf16 v[0:3],a[224:227],v[144:147],v[0:3]\nds_read_b128 a[252:255],v196 offset:31744\nv_mfma_f32_16x16x32_bf16 v[4:7],a[224:227],v[176:179],v[4:7]\nv_mfma_f32_16x16x32_bf16 v[12:15],a[228:231],v[176:179],v[12:15]\nds_read_b128 a[224:227],v197 offset:16384\nv_mfma_f32_16x16x32_bf16 v[8:11],a[228:231],v[144:147],v[8:11]\n"
"s_waitcnt lgkmcnt(5)\nv_mfma_f32_16x16x32_bf16 v[0:3],a[232:235],v[148:151],v[0:3]\nds_read_b128 a[228:231],v197 offset:24576\nv_mfma_f32_16x16x32_bf16 v[4:7],a[232:235],v[180:183],v[4:7]\nbuffer_load_dwordx4 v252,s[8:11],s26 offen sc1 lds\nv_mfma_f32_16x16x32_bf16 v[12:15],a[236:239],v[180:183],v[12:15]\nds_read_b128 a[232:235],v197 offset:17408\nv_mfma_f32_16x16x32_bf16 v[8:11],a[236:239],v[148:151],v[8:11]\n"
"s_waitcnt lgkmcnt(5)\nv_mfma_f32_16x16x32_bf16 v[0:3],a[240:243],v[152:155],v[0:3]\nds_read_b128 a[236:239],v197 offset:25600\nv_mfma_f32_16x16x32_bf16 v[4:7],a[240:243],v[184:187],v[4:7]\nbuffer_store_dwordx4 v[44:47],v199,s[8:11],s53 offen offset:512 sc1\nv_mfma_f32_16x16x32_bf16 v[12:15],a[244:247],v[184:187],v[12:15]\nds_read_b128 a[240:243],v197 offset:18432\nv_mfma_f32_16x16x32_bf16 v[8:11],a[244:247],v[152:155],v[8:11]\n"
"s_waitcnt lgkmcnt(5)\nv_mfma_f32_16x16x32_bf16 v[0:3],a[248:251],v[156:159],v[0:3]\nds_read_b128 a[244:247],v197 offset:26624\nv_mfma_f32_16x16x32_bf16 v[4:7],a[248:251],v[188:191],v[4:7]\nbuffer_load_dwordx4 v252,s[8:11],s26 offen offset:1024 sc1 lds\nv_mfma_f32_16x16x32_bf16 v[12:15],a[252:255],v[188:191],v[12:15]\nds_read_b128 a[248:251],v197 offset:19456\nv_mfma_f32_16x16x32_bf16 v[8:11],a[252:255],v[156:159],v[8:11]\n"
"s_waitcnt lgkmcnt(5)\nv_mfma_f32_16x16x32_bf16 v[16:19],a[224:227],v[128:131],0\ns_add_u32 s33,s25,0x4000\nds_read_b128 a[252:255],v197 offset:27648\nv_mfma_f32_16x16x32_bf16 v[20:23],a[224:227],v[160:163],0\ns_add_u32 s25,s25,0x8000\nv_mfma_f32_16x16x32_bf16 v[28:31],a[228:231],v[160:163],0\ns_cmp_eq_u32 s25,s27\nds_read_b128 a[224:227],v197 offset:20480\nv_mfma_f32_16x16x32_bf16 v[24:27],a[228:231],v[128:131],0\n"
"s_waitcnt lgkmcnt(5)\nv_mfma_f32_16x16x32_bf16 v[16:19],a[232:235],v[132:135],v[16:19]\ns_cselect_b32 s25,s28,s25\nds_read_b128 a[228:231],v197 offset:28672\nv_mfma_f32_16x16x32_bf16 v[20:23],a[232:235],v[164:167],v[20:23]\ns_cselect_b32 s27,s29,s27\nbuffer_load_dwordx4 v252,s[8:11],s26 offen offset:2048 sc1 lds\nv_mfma_f32_16x16x32_bf16 v[28:31],a[236:239],v[164:167],v[28:31]\ns_cselect_b32 s28,s30,s28\n"
"ds_read_b128 a[232:235],v197 offset:21504\nv_cvt_pk_bf16_f32 v32,v0,v1\nv_mfma_f32_16x16x32_bf16 v[24:27],a[236:239],v[132:135],v[24:27]\ns_waitcnt lgkmcnt(5)\nv_cvt_pk_bf16_f32 v33,v2,v3\nv_cvt_pk_bf16_f32 v34,v8,v9\nv_mfma_f32_16x16x32_bf16 v[16:19],a[240:243],v[136:139],v[16:19]\ns_cselect_b32 s29,s31,s29\nds_read_b128 a[236:239],v197 offset:29696\nv_cvt_pk_bf16_f32 v35,v10,v11\nv_mfma_f32_16x16x32_bf16 v[20:23],a[240:243],v[168:171],v[20:23]\n"
"s_cselect_b32 s30,1073741824,s30\nbuffer_store_dwordx4 v[32:35],v199,s[8:11],s52 offen offset:1024 sc1\nv_mfma_f32_16x16x32_bf16 v[28:31],a[244:247],v[168:171],v[28:31]\nds_read_b128 a[240:243],v197 offset:22528\nv_cvt_pk_bf16_f32 v36,v4,v5\nv_mfma_f32_16x16x32_bf16 v[24:27],a[244:247],v[136:139],v[24:27]\ns_waitcnt lgkmcnt(5)\nv_cvt_pk_bf16_f32 v37,v6,v7\nv_cvt_pk_bf16_f32 v38,v12,v13\nv_mfma_f32_16x16x32_bf16 v[16:19],a[248:251],v[140:143],v[16:19]\n"
"ds_read_b128 a[244:247],v197 offset:30720\nv_cvt_pk_bf16_f32 v39,v14,v15\nv_mfma_f32_16x16x32_bf16 v[20:23],a[248:251],v[172:175],v[20:23]\nbuffer_load_dwordx4 v252,s[8:11],s26 offen offset:3072 sc1 lds\nv_mfma_f32_16x16x32_bf16 v[28:31],a[252:255],v[172:175],v[28:31]\ns_add_u32 m0,s32,s15\nds_read_b128 a[248:251],v197 offset:23552\ns_mov_b32 s26,s33\nv_mfma_f32_16x16x32_bf16 v[24:27],a[252:255],v[140:143],v[24:27]\n"
"s_waitcnt lgkmcnt(5)\nv_mfma_f32_16x16x32_bf16 v[16:19],a[224:227],v[144:147],v[16:19]\nds_read_b128 a[252:255],v197 offset:31744\nv_mfma_f32_16x16x32_bf16 v[20:23],a[224:227],v[176:179],v[20:23]\ns_waitcnt vmcnt(12)\ns_barrier\nv_xor_b32 v196,0x8000,v196\nv_mfma_f32_16x16x32_bf16 v[28:31],a[228:231],v[176:179],v[28:31]\nds_read_b128 a[224:227],v196 offset:0\nv_mfma_f32_16x16x32_bf16 v[24:27],a[228:231],v[144:147],v[24:27]\n"
"s_waitcnt lgkmcnt(5)\nv_xor_b32 v197,0x8000,v197\nv_mfma_f32_16x16x32_bf16 v[16:19],a[232:235],v[148:151],v[16:19]\nds_read_b128 a[228:231],v196 offset:8192\nv_mfma_f32_16x16x32_bf16 v[20:23],a[232:235],v[180:183],v[20:23]\nbuffer_load_dwordx4 v252,s[8:11],s25 offen sc1 lds\nv_mfma_f32_16x16x32_bf16 v[28:31],a[236:239],v[180:183],v[28:31]\nds_read_b128 a[232:235],v196 offset:1024\nv_mfma_f32_16x16x32_bf16 v[24:27],a[236:239],v[148:151],v[24:27]\n"
"s_waitcnt lgkmcnt(5)\nv_mfma_f32_16x16x32_bf16 v[16:19],a[240:243],v[152:155],v[16:19]\ns_add_u32 s32,s32,0x4000\nds_read_b128 a[236:239],v196 offset:9216\ns_and_b32 s32,s32,0xffff\nv_mfma_f32_16x16x32_bf16 v[20:23],a[240:243],v[184:187],v[20:23]\nbuffer_store_dwordx4 v[36:39],v199,s[8:11],s53 offen offset:1024 sc1\nv_mfma_f32_16x16x32_bf16 v[28:31],a[244:247],v[184:187],v[28:31]\nds_read_b128 a[240:243],v196 offset:2048\n"
"v_mfma_f32_16x16x32_bf16 v[24:27],a[244:247],v[152:155],v[24:27]\ns_waitcnt lgkmcnt(5)\nv_mfma_f32_16x16x32_bf16 v[16:19],a[248:251],v[156:159],v[16:19]\nds_read_b128 a[244:247],v196 offset:10240\nv_mfma_f32_16x16x32_bf16 v[20:23],a[248:251],v[188:191],v[20:23]\nbuffer_load_dwordx4 v252,s[8:11],s25 offen offset:1024 sc1 lds\nv_mfma_f32_16x16x32_bf16 v[28:31],a[252:255],v[188:191],v[28:31]\nds_read_b128 a[248:251],v196 offset:3072\n"
"v_mfma_f32_16x16x32_bf16 v[24:27],a[252:255],v[156:159],v[24:27]\ns_waitcnt lgkmcnt(5)\nv_mfma_f32_16x16x32_bf16 v[0:3],a[224:227],v[128:131],0\nds_read_b128 a[252:255],v196 offset:11264\nv_mfma_f32_16x16x32_bf16 v[4:7],a[224:227],v[160:163],0\nv_mfma_f32_16x16x32_bf16 v[12:15],a[228:231],v[160:163],0\nds_read_b128 a[224:227],v196 offset:4096\nv_mfma_f32_16x16x32_bf16 v[8:11],a[228:231],v[128:131],0\n"
"s_waitcnt lgkmcnt(5)\nv_mfma_f32_16x16x32_bf16 v[0:3],a[232:235],v[132:135],v[0:3]\nds_read_b128 a[228:231],v196 offset:12288\nv_mfma_f32_16x16x32_bf16 v[4:7],a[232:235],v[164:167],v[4:7]\nbuffer_load_dwordx4 v252,s[8:11],s25 offen offset:2048 sc1 lds\nv_mfma_f32_16x16x32_bf16 v[12:15],a[236:239],v[164:167],v[12:15]\nds_read_b128 a[232:235],v196 offset:5120\nv_cvt_pk_bf16_f32 v40,v16,v17\nv_mfma_f32_16x16x32_bf16 v[8:11],a[236:239],v[132:135],v[8:11]\n"
"s_waitcnt lgkmcnt(5)\nv_cvt_pk_bf16_f32 v41,v18,v19\nv_cvt_pk_bf16_f32 v42,v24,v25\nv_mfma_f32_16x16x32_bf16 v[0:3],a[240:243],v[136:139],v[0:3]\nds_read_b128 a[236:239],v196 offset:13312\nv_cvt_pk_bf16_f32 v43,v26,v27\nv_mfma_f32_16x16x32_bf16 v[4:7],a[240:243],v[168:171],v[4:7]\nbuffer_store_dwordx4 v[40:43],v199,s[8:11],s52 offen offset:1536 sc1\nv_mfma_f32_16x16x32_bf16 v[12:15],a[244:247],v[168:171],v[12:15]\n"
"ds_read_b128 a[240:243],v196 offset:6144\nv_cvt_pk_bf16_f32 v44,v20,v21\nv_mfma_f32_16x16x32_bf16 v[8:11],a[244:247],v[136:139],v[8:11]\ns_waitcnt lgkmcnt(5)\nv_cvt_pk_bf16_f32 v45,v22,v23\nv_cvt_pk_bf16_f32 v46,v28,v29\nv_mfma_f32_16x16x32_bf16 v[0:3],a[248:251],v[140:143],v[0:3]\nds_read_b128 a[244:247],v196 offset:14336\nv_cvt_pk_bf16_f32 v47,v30,v31\nv_mfma_f32_16x16x32_bf16 v[4:7],a[248:251],v[172:175],v[4:7]\n"
"buffer_load_dwordx4 v252,s[8:11],s25 offen offset:3072 sc1 lds\nv_mfma_f32_16x16x32_bf16 v[12:15],a[252:255],v[172:175],v[12:15]\ns_add_u32 s84,s32,0x8000\nds_read_b128 a[248:251],v196 offset:7168\ns_and_b32 s84,s84,0xffff\nv_mfma_f32_16x16x32_bf16 v[8:11],a[252:255],v[140:143],v[8:11]\ns_waitcnt lgkmcnt(5)\ns_add_u32 s84,s84,s15\ns_add_u32 m0,s84,0x10000\nv_mfma_f32_16x16x32_bf16 v[0:3],a[224:227],v[144:147],v[0:3]\n"
"ds_read_b128 a[252:255],v196 offset:15360\nv_mfma_f32_16x16x32_bf16 v[4:7],a[224:227],v[176:179],v[4:7]\nv_mfma_f32_16x16x32_bf16 v[12:15],a[228:231],v[176:179],v[12:15]\nds_read_b128 a[224:227],v197 offset:0\nv_mfma_f32_16x16x32_bf16 v[8:11],a[228:231],v[144:147],v[8:11]\ns_waitcnt lgkmcnt(5)\nv_mfma_f32_16x16x32_bf16 v[0:3],a[232:235],v[148:151],v[0:3]\nds_read_b128 a[228:231],v197 offset:8192\nv_mfma_f32_16x16x32_bf16 v[4:7],a[232:235],v[180:183],v[4:7]\n"
"buffer_load_dwordx4 v252,s[8:11],s26 offen sc1 lds\nv_mfma_f32_16x16x32_bf16 v[12:15],a[236:239],v[180:183],v[12:15]\nds_read_b128 a[232:235],v197 offset:1024\nv_mfma_f32_16x16x32_bf16 v[8:11],a[236:239],v[148:151],v[8:11]\ns_waitcnt lgkmcnt(5)\nv_mfma_f32_16x16x32_bf16 v[0:3],a[240:243],v[152:155],v[0:3]\nds_read_b128 a[236:239],v197 offset:9216\nv_mfma_f32_16x16x32_bf16 v[4:7],a[240:243],v[184:187],v[4:7]\n"
"buffer_store_dwordx4 v[44:47],v199,s[8:11],s53 offen offset:1536 sc1\nv_mfma_f32_16x16x32_bf16 v[12:15],a[244:247],v[184:187],v[12:15]\nds_read_b128 a[240:243],v197 offset:2048\nv_mfma_f32_16x16x32_bf16 v[8:11],a[244:247],v[152:155],v[8:11]\ns_waitcnt lgkmcnt(5)\nv_mfma_f32_16x16x32_bf16 v[0:3],a[248:251],v[156:159],v[0:3]\nds_read_b128 a[244:247],v197 offset:10240\nv_mfma_f32_16x16x32_bf16 v[4:7],a[248:251],v[188:191],v[4:7]\n"
"buffer_load_dwordx4 v252,s[8:11],s26 offen offset:1024 sc1 lds\nv_mfma_f32_16x16x32_bf16 v[12:15],a[252:255],v[188:191],v[12:15]\nds_read_b128 a[248:251],v197 offset:3072\nv_mfma_f32_16x16x32_bf16 v[8:11],a[252:255],v[156:159],v[8:11]\ns_waitcnt lgkmcnt(5)\nv_mfma_f32_16x16x32_bf16 v[16:19],a[224:227],v[128:131],0\ns_add_u32 s33,s25,0x4000\nds_read_b128 a[252:255],v197 offset:11264\nv_mfma_f32_16x16x32_bf16 v[20:23],a[224:227],v[160:163],0\n"
"s_add_u32 s25,s25,0x8000\nv_mfma_f32_16x16x32_bf16 v[28:31],a[228:231],v[160:163],0\ns_cmp_eq_u32 s25,s27\nds_read_b128 a[224:227],v197 offset:4096\nv_mfma_f32_16x16x32_bf16 v[24:27],a[228:231],v[128:131],0\ns_waitcnt lgkmcnt(5)\nv_mfma_f32_16x16x32_bf16 v[16:19],a[232:235],v[132:135],v[16:19]\ns_cselect_b32 s25,s28,s25\nds_read_b128 a[228:231],v197 offset:12288\nv_mfma_f32_16x16x32_bf16 v[20:23],a[232:235],v[164:167],v[20:23]\n"
"s_cselect_b32 s27,s29,s27\nbuffer_load_dwordx4 v252,s[8:11],s26 offen offset:2048 sc1 lds\nv_mfma_f32_16x16x32_bf16 v[28:31],a[236:239],v[164:167],v[28:31]\ns_cselect_b32 s28,s30,s28\nds_read_b128 a[232:235],v197 offset:5120\nv_cvt_pk_bf16_f32 v32,v0,v1\nv_mfma_f32_16x16x32_bf16 v[24:27],a[236:239],v[132:135],v[24:27]\ns_waitcnt lgkmcnt(5)\nv_cvt_pk_bf16_f32 v33,v2,v3\nv_cvt_pk_bf16_f32 v34,v8,v9\n"
"v_mfma_f32_16x16x32_bf16 v[16:19],a[240:243],v[136:139],v[16:19]\ns_cselect_b32 s29,s31,s29\nds_read_b128 a[236:239],v197 offset:13312\nv_cvt_pk_bf16_f32 v35,v10,v11\nv_mfma_f32_16x16x32_bf16 v[20:23],a[240:243],v[168:171],v[20:23]\ns_cselect_b32 s30,1073741824,s30\nbuffer_store_dwordx4 v[32:35],v199,s[8:11],s52 offen offset:2048 sc1\nv_mfma_f32_16x16x32_bf16 v[28:31],a[244:247],v[168:171],v[28:31]\n"
"ds_read_b128 a[240:243],v197 offset:6144\nv_cvt_pk_bf16_f32 v36,v4,v5\nv_mfma_f32_16x16x32_bf16 v[24:27],a[244:247],v[136:139],v[24:27]\ns_waitcnt lgkmcnt(5)\nv_cvt_pk_bf16_f32 v37,v6,v7\nv_cvt_pk_bf16_f32 v38,v12,v13\nv_mfma_f32_16x16x32_bf16 v[16:19],a[248:251],v[140:143],v[16:19]\nds_read_b128 a[244:247],v197 offset:14336\nv_cvt_pk_bf16_f32 v39,v14,v15\nv_mfma_f32_16x16x32_bf16 v[20:23],a[248:251],v[172:175],v[20:23]\n"
"buffer_load_dwordx4 v252,s[8:11],s26 offen offset:3072 sc1 lds\nv_mfma_f32_16x16x32_bf16 v[28:31],a[252:255],v[172:175],v[28:31]\ns_add_u32 m0,s32,s15\nds_read_b128 a[248:251],v197 offset:7168\ns_mov_b32 s26,s33\nv_mfma_f32_16x16x32_bf16 v[24:27],a[252:255],v[140:143],v[24:27]\ns_waitcnt lgkmcnt(5)\nv_mfma_f32_16x16x32_bf16 v[16:19],a[224:227],v[144:147],v[16:19]\nds_read_b128 a[252:255],v197 offset:15360\n"
"v_mfma_f32_16x16x32_bf16 v[20:23],a[224:227],v[176:179],v[20:23]\ns_waitcnt vmcnt(12)\ns_barrier\nv_mfma_f32_16x16x32_bf16 v[28:31],a[228:231],v[176:179],v[28:31]\nds_read_b128 a[224:227],v196 offset:16384\nv_mfma_f32_16x16x32_bf16 v[24:27],a[228:231],v[144:147],v[24:27]\ns_waitcnt lgkmcnt(5)\nv_mfma_f32_16x16x32_bf16 v[16:19],a[232:235],v[148:151],v[16:19]\nds_read_b128 a[228:231],v196 offset:24576\n"
"v_mfma_f32_16x16x32_bf16 v[20:23],a[232:235],v[180:183],v[20:23]\nbuffer_load_dwordx4 v252,s[8:11],s25 offen sc1 lds\nv_mfma_f32_16x16x32_bf16 v[28:31],a[236:239],v[180:183],v[28:31]\nds_read_b128 a[232:235],v196 offset:17408\nv_mfma_f32_16x16x32_bf16 v[24:27],a[236:239],v[148:151],v[24:27]\ns_waitcnt lgkmcnt(5)\nv_mfma_f32_16x16x32_bf16 v[16:19],a[240:243],v[152:155],v[16:19]\ns_add_u32 s32,s32,0x4000\n"
"ds_read_b128 a[236:239],v196 offset:25600\ns_and_b32 s32,s32,0xffff\nv_mfma_f32_16x16x32_bf16 v[20:23],a[240:243],v[184:187],v[20:23]\nbuffer_store_dwordx4 v[36:39],v199,s[8:11],s53 offen offset:2048 sc1\nv_mfma_f32_16x16x32_bf16 v[28:31],a[244:247],v[184:187],v[28:31]\nds_read_b128 a[240:243],v196 offset:18432\nv_mfma_f32_16x16x32_bf16 v[24:27],a[244:247],v[152:155],v[24:27]\ns_waitcnt lgkmcnt(5)\n"
"v_mfma_f32_16x16x32_bf16 v[16:19],a[248:251],v[156:159],v[16:19]\nds_read_b128 a[244:247],v196 offset:26624\nv_mfma_f32_16x16x32_bf16 v[20:23],a[248:251],v[188:191],v[20:23]\nbuffer_load_dwordx4 v252,s[8:11],s25 offen offset:1024 sc1 lds\nv_mfma_f32_16x16x32_bf16 v[28:31],a[252:255],v[188:191],v[28:31]\nds_read_b128 a[248:251],v196 offset:19456\nv_mfma_f32_16x16x32_bf16 v[24:27],a[252:255],v[156:159],v[24:27]\n"
"s_waitcnt lgkmcnt(5)\nv_mfma_f32_16x16x32_bf16 v[0:3],a[224:227],v[128:131],0\nds_read_b128 a[252:255],v196 offset:27648\nv_mfma_f32_16x16x32_bf16 v[4:7],a[224:227],v[160:163],0\nv_mfma_f32_16x16x32_bf16 v[12:15],a[228:231],v[160:163],0\nds_read_b128 a[224:227],v196 offset:20480\nv_mfma_f32_16x16x32_bf16 v[8:11],a[228:231],v[128:131],0\ns_waitcnt lgkmcnt(5)\nv_mfma_f32_16x16x32_bf16 v[0:3],a[232:235],v[132:135],v[0:3]\n"
"ds_read_b128 a[228:231],v196 offset:28672\nv_mfma_f32_16x16x32_bf16 v[4:7],a[232:235],v[164:167],v[4:7]\nbuffer_load_dwordx4 v252,s[8:11],s25 offen offset:2048 sc1 lds\nv_mfma_f32_16x16x32_bf16 v[12:15],a[236:239],v[164:167],v[12:15]\nds_read_b128 a[232:235],v196 offset:21504\nv_cvt_pk_bf16_f32 v40,v16,v17\nv_mfma_f32_16x16x32_bf16 v[8:11],a[236:239],v[132:135],v[8:11]\ns_waitcnt lgkmcnt(5)\nv_cvt_pk_bf16_f32 v41,v18,v19\n"
"v_cvt_pk_bf16_f32 v42,v24,v25\nv_mfma_f32_16x16x32_bf16 v[0:3],a[240:243],v[136:139],v[0:3]\nds_read_b128 a[236:239],v196 offset:29696\nv_cvt_pk_bf16_f32 v43,v26,v27\nv_mfma_f32_16x16x32_bf16 v[4:7],a[240:243],v[168:171],v[4:7]\nbuffer_store_dwordx4 v[40:43],v199,s[8:11],s52 offen offset:2560 sc1\nv_mfma_f32_16x16x32_bf16 v[12:15],a[244:247],v[168:171],v[12:15]\nds_read_b128 a[240:243],v196 offset:22528\n"
"v_cvt_pk_bf16_f32 v44,v20,v21\nv_mfma_f32_16x16x32_bf16 v[8:11],a[244:247],v[136:139],v[8:11]\ns_waitcnt lgkmcnt(5)\nv_cvt_pk_bf16_f32 v45,v22,v23\nv_cvt_pk_bf16_f32 v46,v28,v29\nv_mfma_f32_16x16x32_bf16 v[0:3],a[248:251],v[140:143],v[0:3]\nds_read_b128 a[244:247],v196 offset:30720\nv_cvt_pk_bf16_f32 v47,v30,v31\nv_mfma_f32_16x16x32_bf16 v[4:7],a[248:251],v[172:175],v[4:7]\nbuffer_load_dwordx4 v252,s[8:11],s25 offen offset:3072 sc1 lds\n"
"v_mfma_f32_16x16x32_bf16 v[12:15],a[252:255],v[172:175],v[12:15]\ns_add_u32 s84,s32,0x8000\nds_read_b128 a[248:251],v196 offset:23552\ns_and_b32 s84,s84,0xffff\nv_mfma_f32_16x16x32_bf16 v[8:11],a[252:255],v[140:143],v[8:11]\ns_waitcnt lgkmcnt(5)\ns_add_u32 s84,s84,s15\ns_add_u32 m0,s84,0x10000\nv_mfma_f32_16x16x32_bf16 v[0:3],a[224:227],v[144:147],v[0:3]\nds_read_b128 a[252:255],v196 offset:31744\n"
"v_mfma_f32_16x16x32_bf16 v[4:7],a[224:227],v[176:179],v[4:7]\nv_mfma_f32_16x16x32_bf16 v[12:15],a[228:231],v[176:179],v[12:15]\nds_read_b128 a[224:227],v197 offset:16384\nv_mfma_f32_16x16x32_bf16 v[8:11],a[228:231],v[144:147],v[8:11]\ns_waitcnt lgkmcnt(5)\nv_mfma_f32_16x16x32_bf16 v[0:3],a[232:235],v[148:151],v[0:3]\nds_read_b128 a[228:231],v197 offset:24576\nv_mfma_f32_16x16x32_bf16 v[4:7],a[232:235],v[180:183],v[4:7]\n"
"buffer_load_dwordx4 v252,s[8:11],s26 offen sc1 lds\nv_mfma_f32_16x16x32_bf16 v[12:15],a[236:239],v[180:183],v[12:15]\nds_read_b128 a[232:235],v197 offset:17408\nv_mfma_f32_16x16x32_bf16 v[8:11],a[236:239],v[148:151],v[8:11]\ns_waitcnt lgkmcnt(5)\nv_mfma_f32_16x16x32_bf16 v[0:3],a[240:243],v[152:155],v[0:3]\nds_read_b128 a[236:239],v197 offset:25600\nv_mfma_f32_16x16x32_bf16 v[4:7],a[240:243],v[184:187],v[4:7]\n"
"buffer_store_dwordx4 v[44:47],v199,s[8:11],s53 offen offset:2560 sc1\nv_mfma_f32_16x16x32_bf16 v[12:15],a[244:247],v[184:187],v[12:15]\nds_read_b128 a[240:243],v197 offset:18432\nv_mfma_f32_16x16x32_bf16 v[8:11],a[244:247],v[152:155],v[8:11]\ns_waitcnt lgkmcnt(5)\nv_mfma_f32_16x16x32_bf16 v[0:3],a[248:251],v[156:159],v[0:3]\nds_read_b128 a[244:247],v197 offset:26624\nv_mfma_f32_16x16x32_bf16 v[4:7],a[248:251],v[188:191],v[4:7]\n"
"buffer_load_dwordx4 v252,s[8:11],s26 offen offset:1024 sc1 lds\nv_mfma_f32_16x16x32_bf16 v[12:15],a[252:255],v[188:191],v[12:15]\nds_read_b128 a[248:251],v197 offset:19456\nv_mfma_f32_16x16x32_bf16 v[8:11],a[252:255],v[156:159],v[8:11]\ns_waitcnt lgkmcnt(5)\nv_mfma_f32_16x16x32_bf16 v[16:19],a[224:227],v[128:131],0\ns_add_u32 s33,s25,0x4000\nds_read_b128 a[252:255],v197 offset:27648\nv_mfma_f32_16x16x32_bf16 v[20:23],a[224:227],v[160:163],0\n"
"s_add_u32 s25,s25,0x8000\nv_mfma_f32_16x16x32_bf16 v[28:31],a[228:231],v[160:163],0\ns_cmp_eq_u32 s25,s27\nds_read_b128 a[224:227],v197 offset:20480\nv_mfma_f32_16x16x32_bf16 v[24:27],a[228:231],v[128:131],0\ns_waitcnt lgkmcnt(5)\nv_mfma_f32_16x16x32_bf16 v[16:19],a[232:235],v[132:135],v[16:19]\ns_cselect_b32 s25,s28,s25\nds_read_b128 a[228:231],v197 offset:28672\nv_mfma_f32_16x16x32_bf16 v[20:23],a[232:235],v[164:167],v[20:23]\n"
"s_cselect_b32 s27,s29,s27\nbuffer_load_dwordx4 v252,s[8:11],s26 offen offset:2048 sc1 lds\nv_mfma_f32_16x16x32_bf16 v[28:31],a[236:239],v[164:167],v[28:31]\ns_cselect_b32 s28,s30,s28\nds_read_b128 a[232:235],v197 offset:21504\nv_cvt_pk_bf16_f32 v32,v0,v1\nv_mfma_f32_16x16x32_bf16 v[24:27],a[236:239],v[132:135],v[24:27]\ns_waitcnt lgkmcnt(5)\nv_cvt_pk_bf16_f32 v33,v2,v3\nv_cvt_pk_bf16_f32 v34,v8,v9\n"
"v_mfma_f32_16x16x32_bf16 v[16:19],a[240:243],v[136:139],v[16:19]\ns_cselect_b32 s29,s31,s29\nds_read_b128 a[236:239],v197 offset:29696\nv_cvt_pk_bf16_f32 v35,v10,v11\nv_mfma_f32_16x16x32_bf16 v[20:23],a[240:243],v[168:171],v[20:23]\ns_cselect_b32 s30,1073741824,s30\nbuffer_store_dwordx4 v[32:35],v199,s[8:11],s52 offen offset:3072 sc1\nv_mfma_f32_16x16x32_bf16 v[28:31],a[244:247],v[168:171],v[28:31]\n"
"ds_read_b128 a[240:243],v197 offset:22528\nv_cvt_pk_bf16_f32 v36,v4,v5\nv_mfma_f32_16x16x32_bf16 v[24:27],a[244:247],v[136:139],v[24:27]\ns_waitcnt lgkmcnt(5)\nv_cvt_pk_bf16_f32 v37,v6,v7\nv_cvt_pk_bf16_f32 v38,v12,v13\nv_mfma_f32_16x16x32_bf16 v[16:19],a[248:251],v[140:143],v[16:19]\nds_read_b128 a[244:247],v197 offset:30720\nv_cvt_pk_bf16_f32 v39,v14,v15\nv_mfma_f32_16x16x32_bf16 v[20:23],a[248:251],v[172:175],v[20:23]\n"
"buffer_load_dwordx4 v252,s[8:11],s26 offen offset:3072 sc1 lds\nv_mfma_f32_16x16x32_bf16 v[28:31],a[252:255],v[172:175],v[28:31]\ns_add_u32 m0,s32,s15\nds_read_b128 a[248:251],v197 offset:23552\ns_mov_b32 s26,s33\nv_mfma_f32_16x16x32_bf16 v[24:27],a[252:255],v[140:143],v[24:27]\ns_waitcnt lgkmcnt(5)\nv_mfma_f32_16x16x32_bf16 v[16:19],a[224:227],v[144:147],v[16:19]\nds_read_b128 a[252:255],v197 offset:31744\n"
"v_mfma_f32_16x16x32_bf16 v[20:23],a[224:227],v[176:179],v[20:23]\ns_waitcnt vmcnt(12)\ns_barrier\nv_xor_b32 v196,0x8000,v196\nv_mfma_f32_16x16x32_bf16 v[28:31],a[228:231],v[176:179],v[28:31]\nds_read_b128 a[224:227],v196 offset:0\nv_mfma_f32_16x16x32_bf16 v[24:27],a[228:231],v[144:147],v[24:27]\ns_waitcnt lgkmcnt(5)\nv_xor_b32 v197,0x8000,v197\nv_mfma_f32_16x16x32_bf16 v[16:19],a[232:235],v[148:151],v[16:19]\n"
"ds_read_b128 a[228:231],v196 offset:8192\nv_mfma_f32_16x16x32_bf16 v[20:23],a[232:235],v[180:183],v[20:23]\nbuffer_load_dwordx4 v252,s[8:11],s59 offen sc1 lds\nv_mfma_f32_16x16x32_bf16 v[28:31],a[236:239],v[180:183],v[28:31]\nds_read_b128 a[232:235],v196 offset:1024\nv_mfma_f32_16x16x32_bf16 v[24:27],a[236:239],v[148:151],v[24:27]\ns_waitcnt lgkmcnt(5)\nv_mfma_f32_16x16x32_bf16 v[16:19],a[240:243],v[152:155],v[16:19]\n"
"s_add_u32 s32,s32,0x4000\nds_read_b128 a[236:239],v196 offset:9216\ns_and_b32 s32,s32,0xffff\nv_mfma_f32_16x16x32_bf16 v[20:23],a[240:243],v[184:187],v[20:23]\nbuffer_store_dwordx4 v[36:39],v199,s[8:11],s53 offen offset:3072 sc1\nv_mfma_f32_16x16x32_bf16 v[28:31],a[244:247],v[184:187],v[28:31]\nds_read_b128 a[240:243],v196 offset:2048\nv_mfma_f32_16x16x32_bf16 v[24:27],a[244:247],v[152:155],v[24:27]\n"
"s_waitcnt lgkmcnt(5)\nv_mfma_f32_16x16x32_bf16 v[16:19],a[248:251],v[156:159],v[16:19]\nds_read_b128 a[244:247],v196 offset:10240\nv_mfma_f32_16x16x32_bf16 v[20:23],a[248:251],v[188:191],v[20:23]\nbuffer_load_dwordx4 v252,s[8:11],s59 offen offset:1024 sc1 lds\nv_mfma_f32_16x16x32_bf16 v[28:31],a[252:255],v[188:191],v[28:31]\nds_read_b128 a[248:251],v196 offset:3072\nv_mfma_f32_16x16x32_bf16 v[24:27],a[252:255],v[156:159],v[24:27]\n"
"s_waitcnt lgkmcnt(5)\nv_mfma_f32_16x16x32_bf16 v[0:3],a[224:227],v[128:131],0\nds_read_b128 a[252:255],v196 offset:11264\nv_mfma_f32_16x16x32_bf16 v[4:7],a[224:227],v[160:163],0\nv_mfma_f32_16x16x32_bf16 v[12:15],a[228:231],v[160:163],0\nds_read_b128 a[224:227],v196 offset:4096\nv_mfma_f32_16x16x32_bf16 v[8:11],a[228:231],v[128:131],0\ns_waitcnt lgkmcnt(5)\nv_mfma_f32_16x16x32_bf16 v[0:3],a[232:235],v[132:135],v[0:3]\n"
"ds_read_b128 a[228:231],v196 offset:12288\nv_mfma_f32_16x16x32_bf16 v[4:7],a[232:235],v[164:167],v[4:7]\nbuffer_load_dwordx4 v252,s[8:11],s59 offen offset:2048 sc1 lds\nv_mfma_f32_16x16x32_bf16 v[12:15],a[236:239],v[164:167],v[12:15]\nds_read_b128 a[232:235],v196 offset:5120\nv_cvt_pk_bf16_f32 v40,v16,v17\nv_mfma_f32_16x16x32_bf16 v[8:11],a[236:239],v[132:135],v[8:11]\ns_waitcnt lgkmcnt(5)\nv_cvt_pk_bf16_f32 v41,v18,v19\n"
"v_cvt_pk_bf16_f32 v42,v24,v25\nv_mfma_f32_16x16x32_bf16 v[0:3],a[240:243],v[136:139],v[0:3]\nds_read_b128 a[236:239],v196 offset:13312\nv_cvt_pk_bf16_f32 v43,v26,v27\nv_mfma_f32_16x16x32_bf16 v[4:7],a[240:243],v[168:171],v[4:7]\nbuffer_store_dwordx4 v[40:43],v199,s[8:11],s52 offen offset:3584 sc1\nv_mfma_f32_16x16x32_bf16 v[12:15],a[244:247],v[168:171],v[12:15]\nds_read_b128 a[240:243],v196 offset:6144\n"
"v_cvt_pk_bf16_f32 v44,v20,v21\nv_mfma_f32_16x16x32_bf16 v[8:11],a[244:247],v[136:139],v[8:11]\ns_waitcnt lgkmcnt(5)\nv_cvt_pk_bf16_f32 v45,v22,v23\nv_cvt_pk_bf16_f32 v46,v28,v29\nv_mfma_f32_16x16x32_bf16 v[0:3],a[248:251],v[140:143],v[0:3]\nds_read_b128 a[244:247],v196 offset:14336\nv_cvt_pk_bf16_f32 v47,v30,v31\nv_mfma_f32_16x16x32_bf16 v[4:7],a[248:251],v[172:175],v[4:7]\nbuffer_load_dwordx4 v252,s[8:11],s59 offen offset:3072 sc1 lds\n"
"v_mfma_f32_16x16x32_bf16 v[12:15],a[252:255],v[172:175],v[12:15]\ns_add_u32 s59,s59,458752\nds_read_b128 a[248:251],v196 offset:7168\ns_add_u32 s84,s32,0x8000\nv_mfma_f32_16x16x32_bf16 v[8:11],a[252:255],v[140:143],v[8:11]\ns_waitcnt lgkmcnt(5)\ns_and_b32 s84,s84,0xffff\ns_add_u32 s84,s84,s15\nv_mfma_f32_16x16x32_bf16 v[0:3],a[224:227],v[144:147],v[0:3]\ns_add_u32 m0,s84,0x10000\nds_read_b128 a[252:255],v196 offset:15360\n"
"v_mfma_f32_16x16x32_bf16 v[4:7],a[224:227],v[176:179],v[4:7]\nv_mfma_f32_16x16x32_bf16 v[12:15],a[228:231],v[176:179],v[12:15]\nds_read_b128 a[224:227],v197 offset:0\nv_mfma_f32_16x16x32_bf16 v[8:11],a[228:231],v[144:147],v[8:11]\ns_waitcnt lgkmcnt(5)\nv_mfma_f32_16x16x32_bf16 v[0:3],a[232:235],v[148:151],v[0:3]\nds_read_b128 a[228:231],v197 offset:8192\nv_mfma_f32_16x16x32_bf16 v[4:7],a[232:235],v[180:183],v[4:7]\n"
"buffer_load_dwordx4 v252,s[8:11],s26 offen sc1 lds\nv_mfma_f32_16x16x32_bf16 v[12:15],a[236:239],v[180:183],v[12:15]\nds_read_b128 a[232:235],v197 offset:1024\nv_mfma_f32_16x16x32_bf16 v[8:11],a[236:239],v[148:151],v[8:11]\ns_waitcnt lgkmcnt(5)\nv_mfma_f32_16x16x32_bf16 v[0:3],a[240:243],v[152:155],v[0:3]\nds_read_b128 a[236:239],v197 offset:9216\nv_mfma_f32_16x16x32_bf16 v[4:7],a[240:243],v[184:187],v[4:7]\n"
"buffer_store_dwordx4 v[44:47],v199,s[8:11],s53 offen offset:3584 sc1\nv_mfma_f32_16x16x32_bf16 v[12:15],a[244:247],v[184:187],v[12:15]\nds_read_b128 a[240:243],v197 offset:2048\nv_mfma_f32_16x16x32_bf16 v[8:11],a[244:247],v[152:155],v[8:11]\ns_waitcnt lgkmcnt(5)\nv_mfma_f32_16x16x32_bf16 v[0:3],a[248:251],v[156:159],v[0:3]\nds_read_b128 a[244:247],v197 offset:10240\nv_mfma_f32_16x16x32_bf16 v[4:7],a[248:251],v[188:191],v[4:7]\n"
"buffer_load_dwordx4 v252,s[8:11],s26 offen offset:1024 sc1 lds\nv_mfma_f32_16x16x32_bf16 v[12:15],a[252:255],v[188:191],v[12:15]\nds_read_b128 a[248:251],v197 offset:3072\nv_mfma_f32_16x16x32_bf16 v[8:11],a[252:255],v[156:159],v[8:11]\ns_waitcnt lgkmcnt(5)\nv_mfma_f32_16x16x32_bf16 v[16:19],a[224:227],v[128:131],0\ns_add_u32 s33,s25,0x4000\nds_read_b128 a[252:255],v197 offset:11264\nv_mfma_f32_16x16x32_bf16 v[20:23],a[224:227],v[160:163],0\n"
"s_add_u32 s25,s25,0x8000\nv_mfma_f32_16x16x32_bf16 v[28:31],a[228:231],v[160:163],0\ns_cmp_eq_u32 s25,s27\nds_read_b128 a[224:227],v197 offset:4096\nv_mfma_f32_16x16x32_bf16 v[24:27],a[228:231],v[128:131],0\ns_waitcnt lgkmcnt(5)\nv_mfma_f32_16x16x32_bf16 v[16:19],a[232:235],v[132:135],v[16:19]\ns_cselect_b32 s25,s28,s25\nds_read_b128 a[228:231],v197 offset:12288\nv_mfma_f32_16x16x32_bf16 v[20:23],a[232:235],v[164:167],v[20:23]\n"
"s_cselect_b32 s27,s29,s27\nbuffer_load_dwordx4 v252,s[8:11],s26 offen offset:2048 sc1 lds\nv_mfma_f32_16x16x32_bf16 v[28:31],a[236:239],v[164:167],v[28:31]\ns_cselect_b32 s28,s30,s28\nds_read_b128 a[232:235],v197 offset:5120\nv_cvt_pk_bf16_f32 v32,v0,v1\nv_mfma_f32_16x16x32_bf16 v[24:27],a[236:239],v[132:135],v[24:27]\ns_waitcnt lgkmcnt(5)\nv_cvt_pk_bf16_f32 v33,v2,v3\nv_cvt_pk_bf16_f32 v34,v8,v9\n"
"v_mfma_f32_16x16x32_bf16 v[16:19],a[240:243],v[136:139],v[16:19]\ns_cselect_b32 s29,s31,s29\nds_read_b128 a[236:239],v197 offset:13312\nv_cvt_pk_bf16_f32 v35,v10,v11\nv_mfma_f32_16x16x32_bf16 v[20:23],a[240:243],v[168:171],v[20:23]\ns_cselect_b32 s30,1073741824,s30\nbuffer_store_dwordx4 v[32:35],v199,s[8:11],s54 offen offset:0 sc1\nv_mfma_f32_16x16x32_bf16 v[28:31],a[244:247],v[168:171],v[28:31]\nds_read_b128 a[240:243],v197 offset:6144\n"
"v_cvt_pk_bf16_f32 v36,v4,v5\nv_mfma_f32_16x16x32_bf16 v[24:27],a[244:247],v[136:139],v[24:27]\ns_waitcnt lgkmcnt(5)\nv_cvt_pk_bf16_f32 v37,v6,v7\nv_cvt_pk_bf16_f32 v38,v12,v13\nv_mfma_f32_16x16x32_bf16 v[16:19],a[248:251],v[140:143],v[16:19]\nds_read_b128 a[244:247],v197 offset:14336\nv_cvt_pk_bf16_f32 v39,v14,v15\nv_mfma_f32_16x16x32_bf16 v[20:23],a[248:251],v[172:175],v[20:23]\nbuffer_load_dwordx4 v252,s[8:11],s26 offen offset:3072 sc1 lds\n"
"v_mfma_f32_16x16x32_bf16 v[28:31],a[252:255],v[172:175],v[28:31]\ns_add_u32 m0,s32,s15\nds_read_b128 a[248:251],v197 offset:7168\ns_mov_b32 s26,s33\nv_mfma_f32_16x16x32_bf16 v[24:27],a[252:255],v[140:143],v[24:27]\ns_waitcnt lgkmcnt(5)\nv_mfma_f32_16x16x32_bf16 v[16:19],a[224:227],v[144:147],v[16:19]\nds_read_b128 a[252:255],v197 offset:15360\nv_mfma_f32_16x16x32_bf16 v[20:23],a[224:227],v[176:179],v[20:23]\n"
"s_waitcnt vmcnt(12)\ns_barrier\nv_mfma_f32_16x16x32_bf16 v[28:31],a[228:231],v[176:179],v[28:31]\nds_read_b128 a[224:227],v196 offset:16384\nv_mfma_f32_16x16x32_bf16 v[24:27],a[228:231],v[144:147],v[24:27]\ns_waitcnt lgkmcnt(5)\nv_mfma_f32_16x16x32_bf16 v[16:19],a[232:235],v[148:151],v[16:19]\nds_read_b128 a[228:231],v196 offset:24576\nv_mfma_f32_16x16x32_bf16 v[20:23],a[232:235],v[180:183],v[20:23]\n"
"buffer_load_dwordx4 v252,s[8:11],s59 offen sc1 lds\nv_mfma_f32_16x16x32_bf16 v[28:31],a[236:239],v[180:183],v[28:31]\nds_read_b128 a[232:235],v196 offset:17408\nv_mfma_f32_16x16x32_bf16 v[24:27],a[236:239],v[148:151],v[24:27]\ns_waitcnt lgkmcnt(5)\nv_mfma_f32_16x16x32_bf16 v[16:19],a[240:243],v[152:155],v[16:19]\ns_add_u32 s32,s32,0x4000\nds_read_b128 a[236:239],v196 offset:25600\ns_and_b32 s32,s32,0xffff\n"
"v_mfma_f32_16x16x32_bf16 v[20:23],a[240:243],v[184:187],v[20:23]\nbuffer_store_dwordx4 v[36:39],v199,s[8:11],s55 offen offset:0 sc1\nv_mfma_f32_16x16x32_bf16 v[28:31],a[244:247],v[184:187],v[28:31]\nds_read_b128 a[240:243],v196 offset:18432\nv_mfma_f32_16x16x32_bf16 v[24:27],a[244:247],v[152:155],v[24:27]\ns_waitcnt lgkmcnt(5)\nv_mfma_f32_16x16x32_bf16 v[16:19],a[248:251],v[156:159],v[16:19]\nds_read_b128 a[244:247],v196 offset:26624\n"
"v_mfma_f32_16x16x32_bf16 v[20:23],a[248:251],v[188:191],v[20:23]\nbuffer_load_dwordx4 v252,s[8:11],s59 offen offset:1024 sc1 lds\nv_mfma_f32_16x16x32_bf16 v[28:31],a[252:255],v[188:191],v[28:31]\nds_read_b128 a[248:251],v196 offset:19456\nv_mfma_f32_16x16x32_bf16 v[24:27],a[252:255],v[156:159],v[24:27]\ns_waitcnt lgkmcnt(5)\nv_mfma_f32_16x16x32_bf16 v[0:3],a[224:227],v[128:131],0\nds_read_b128 a[252:255],v196 offset:27648\n"
"v_mfma_f32_16x16x32_bf16 v[4:7],a[224:227],v[160:163],0\nv_mfma_f32_16x16x32_bf16 v[12:15],a[228:231],v[160:163],0\nds_read_b128 a[224:227],v196 offset:20480\nv_mfma_f32_16x16x32_bf16 v[8:11],a[228:231],v[128:131],0\ns_waitcnt lgkmcnt(5)\nv_mfma_f32_16x16x32_bf16 v[0:3],a[232:235],v[132:135],v[0:3]\nds_read_b128 a[228:231],v196 offset:28672\nv_mfma_f32_16x16x32_bf16 v[4:7],a[232:235],v[164:167],v[4:7]\n"
"buffer_load_dwordx4 v252,s[8:11],s59 offen offset:2048 sc1 lds\nv_mfma_f32_16x16x32_bf16 v[12:15],a[236:239],v[164:167],v[12:15]\nds_read_b128 a[232:235],v196 offset:21504\nv_cvt_pk_bf16_f32 v40,v16,v17\nv_mfma_f32_16x16x32_bf16 v[8:11],a[236:239],v[132:135],v[8:11]\ns_waitcnt lgkmcnt(5)\nv_cvt_pk_bf16_f32 v41,v18,v19\nv_cvt_pk_bf16_f32 v42,v24,v25\nv_mfma_f32_16x16x32_bf16 v[0:3],a[240:243],v[136:139],v[0:3]\n"
"ds_read_b128 a[236:239],v196 offset:29696\nv_cvt_pk_bf16_f32 v43,v26,v27\nv_mfma_f32_16x16x32_bf16 v[4:7],a[240:243],v[168:171],v[4:7]\nbuffer_store_dwordx4 v[40:43],v199,s[8:11],s54 offen offset:512 sc1\nv_mfma_f32_16x16x32_bf16 v[12:15],a[244:247],v[168:171],v[12:15]\nds_read_b128 a[240:243],v196 offset:22528\nv_cvt_pk_bf16_f32 v44,v20,v21\nv_mfma_f32_16x16x32_bf16 v[8:11],a[244:247],v[136:139],v[8:11]\n"
"s_waitcnt lgkmcnt(5)\nv_cvt_pk_bf16_f32 v45,v22,v23\nv_cvt_pk_bf16_f32 v46,v28,v29\nv_mfma_f32_16x16x32_bf16 v[0:3],a[248:251],v[140:143],v[0:3]\nds_read_b128 a[244:247],v196 offset:30720\nv_cvt_pk_bf16_f32 v47,v30,v31\nv_mfma_f32_16x16x32_bf16 v[4:7],a[248:251],v[172:175],v[4:7]\nbuffer_load_dwordx4 v252,s[8:11],s59 offen offset:3072 sc1 lds\nv_mfma_f32_16x16x32_bf16 v[12:15],a[252:255],v[172:175],v[12:15]\n"
"s_add_u32 s59,s59,458752\nds_read_b128 a[248:251],v196 offset:23552\ns_add_u32 s84,s32,0x8000\nv_mfma_f32_16x16x32_bf16 v[8:11],a[252:255],v[140:143],v[8:11]\ns_waitcnt lgkmcnt(5)\ns_and_b32 s84,s84,0xffff\ns_add_u32 s84,s84,s15\nv_mfma_f32_16x16x32_bf16 v[0:3],a[224:227],v[144:147],v[0:3]\ns_add_u32 m0,s84,0x10000\nds_read_b128 a[252:255],v196 offset:31744\nv_mfma_f32_16x16x32_bf16 v[4:7],a[224:227],v[176:179],v[4:7]\n"
"v_mfma_f32_16x16x32_bf16 v[12:15],a[228:231],v[176:179],v[12:15]\nds_read_b128 a[224:227],v197 offset:16384\nv_mfma_f32_16x16x32_bf16 v[8:11],a[228:231],v[144:147],v[8:11]\ns_waitcnt lgkmcnt(5)\nv_mfma_f32_16x16x32_bf16 v[0:3],a[232:235],v[148:151],v[0:3]\nds_read_b128 a[228:231],v197 offset:24576\nv_mfma_f32_16x16x32_bf16 v[4:7],a[232:235],v[180:183],v[4:7]\nbuffer_load_dwordx4 v252,s[8:11],s60 offen sc1 lds\n"
"v_mfma_f32_16x16x32_bf16 v[12:15],a[236:239],v[180:183],v[12:15]\nds_read_b128 a[232:235],v197 offset:17408\nv_mfma_f32_16x16x32_bf16 v[8:11],a[236:239],v[148:151],v[8:11]\ns_waitcnt lgkmcnt(5)\nv_mfma_f32_16x16x32_bf16 v[0:3],a[240:243],v[152:155],v[0:3]\nds_read_b128 a[236:239],v197 offset:25600\nv_mfma_f32_16x16x32_bf16 v[4:7],a[240:243],v[184:187],v[4:7]\nbuffer_store_dwordx4 v[44:47],v199,s[8:11],s55 offen offset:512 sc1\n"
"v_mfma_f32_16x16x32_bf16 v[12:15],a[244:247],v[184:187],v[12:15]\nds_read_b128 a[240:243],v197 offset:18432\nv_mfma_f32_16x16x32_bf16 v[8:11],a[244:247],v[152:155],v[8:11]\ns_waitcnt lgkmcnt(5)\nv_mfma_f32_16x16x32_bf16 v[0:3],a[248:251],v[156:159],v[0:3]\nds_read_b128 a[244:247],v197 offset:26624\nv_mfma_f32_16x16x32_bf16 v[4:7],a[248:251],v[188:191],v[4:7]\nbuffer_load_dwordx4 v252,s[8:11],s60 offen offset:1024 sc1 lds\n"
"v_mfma_f32_16x16x32_bf16 v[12:15],a[252:255],v[188:191],v[12:15]\nds_read_b128 a[248:251],v197 offset:19456\nv_mfma_f32_16x16x32_bf16 v[8:11],a[252:255],v[156:159],v[8:11]\ns_waitcnt lgkmcnt(5)\nv_mfma_f32_16x16x32_bf16 v[16:19],a[224:227],v[128:131],0\ns_add_u32 s33,s25,0x4000\nds_read_b128 a[252:255],v197 offset:27648\nv_mfma_f32_16x16x32_bf16 v[20:23],a[224:227],v[160:163],0\ns_add_u32 s25,s25,0x8000\n"
"v_mfma_f32_16x16x32_bf16 v[28:31],a[228:231],v[160:163],0\ns_cmp_eq_u32 s25,s27\nds_read_b128 a[224:227],v197 offset:20480\nv_mfma_f32_16x16x32_bf16 v[24:27],a[228:231],v[128:131],0\ns_waitcnt lgkmcnt(5)\nv_mfma_f32_16x16x32_bf16 v[16:19],a[232:235],v[132:135],v[16:19]\ns_cselect_b32 s25,s28,s25\nds_read_b128 a[228:231],v197 offset:28672\nv_mfma_f32_16x16x32_bf16 v[20:23],a[232:235],v[164:167],v[20:23]\n"
"s_cselect_b32 s27,s29,s27\nbuffer_load_dwordx4 v252,s[8:11],s60 offen offset:2048 sc1 lds\nv_mfma_f32_16x16x32_bf16 v[28:31],a[236:239],v[164:167],v[28:31]\ns_cselect_b32 s28,s30,s28\nds_read_b128 a[232:235],v197 offset:21504\nv_cvt_pk_bf16_f32 v32,v0,v1\nv_mfma_f32_16x16x32_bf16 v[24:27],a[236:239],v[132:135],v[24:27]\ns_waitcnt lgkmcnt(5)\nv_cvt_pk_bf16_f32 v33,v2,v3\nv_cvt_pk_bf16_f32 v34,v8,v9\n"
"v_mfma_f32_16x16x32_bf16 v[16:19],a[240:243],v[136:139],v[16:19]\ns_cselect_b32 s29,s31,s29\nds_read_b128 a[236:239],v197 offset:29696\nv_cvt_pk_bf16_f32 v35,v10,v11\nv_mfma_f32_16x16x32_bf16 v[20:23],a[240:243],v[168:171],v[20:23]\ns_cselect_b32 s30,1073741824,s30\nbuffer_store_dwordx4 v[32:35],v199,s[8:11],s54 offen offset:1024 sc1\nv_mfma_f32_16x16x32_bf16 v[28:31],a[244:247],v[168:171],v[28:31]\n"
"ds_read_b128 a[240:243],v197 offset:22528\nv_cvt_pk_bf16_f32 v36,v4,v5\nv_mfma_f32_16x16x32_bf16 v[24:27],a[244:247],v[136:139],v[24:27]\ns_waitcnt lgkmcnt(5)\nv_cvt_pk_bf16_f32 v37,v6,v7\nv_cvt_pk_bf16_f32 v38,v12,v13\nv_mfma_f32_16x16x32_bf16 v[16:19],a[248:251],v[140:143],v[16:19]\nds_read_b128 a[244:247],v197 offset:30720\nv_cvt_pk_bf16_f32 v39,v14,v15\nv_mfma_f32_16x16x32_bf16 v[20:23],a[248:251],v[172:175],v[20:23]\n"
"buffer_load_dwordx4 v252,s[8:11],s60 offen offset:3072 sc1 lds\nv_mfma_f32_16x16x32_bf16 v[28:31],a[252:255],v[172:175],v[28:31]\ns_add_u32 s60,s60,458752\nds_read_b128 a[248:251],v197 offset:23552\ns_mov_b32 s26,s33\nv_mfma_f32_16x16x32_bf16 v[24:27],a[252:255],v[140:143],v[24:27]\ns_waitcnt lgkmcnt(5)\nv_mfma_f32_16x16x32_bf16 v[16:19],a[224:227],v[144:147],v[16:19]\ns_add_u32 m0,s32,s15\nds_read_b128 a[252:255],v197 offset:31744\n"
"v_mfma_f32_16x16x32_bf16 v[20:23],a[224:227],v[176:179],v[20:23]\ns_waitcnt vmcnt(12)\ns_barrier\nv_xor_b32 v196,0x8000,v196\nv_mfma_f32_16x16x32_bf16 v[28:31],a[228:231],v[176:179],v[28:31]\nds_read_b128 a[224:227],v196 offset:0\nv_mfma_f32_16x16x32_bf16 v[24:27],a[228:231],v[144:147],v[24:27]\ns_waitcnt lgkmcnt(5)\nv_xor_b32 v197,0x8000,v197\nv_mfma_f32_16x16x32_bf16 v[16:19],a[232:235],v[148:151],v[16:19]\n"
"ds_read_b128 a[228:231],v196 offset:8192\nv_mfma_f32_16x16x32_bf16 v[20:23],a[232:235],v[180:183],v[20:23]\nbuffer_load_dwordx4 v252,s[8:11],s59 offen sc1 lds\nv_mfma_f32_16x16x32_bf16 v[28:31],a[236:239],v[180:183],v[28:31]\nds_read_b128 a[232:235],v196 offset:1024\nv_mfma_f32_16x16x32_bf16 v[24:27],a[236:239],v[148:151],v[24:27]\ns_waitcnt lgkmcnt(5)\nv_mfma_f32_16x16x32_bf16 v[16:19],a[240:243],v[152:155],v[16:19]\n"
"s_add_u32 s32,s32,0x4000\nds_read_b128 a[236:239],v196 offset:9216\ns_and_b32 s32,s32,0xffff\nv_mfma_f32_16x16x32_bf16 v[20:23],a[240:243],v[184:187],v[20:23]\nbuffer_store_dwordx4 v[36:39],v199,s[8:11],s55 offen offset:1024 sc1\nv_mfma_f32_16x16x32_bf16 v[28:31],a[244:247],v[184:187],v[28:31]\nds_read_b128 a[240:243],v196 offset:2048\nv_mfma_f32_16x16x32_bf16 v[24:27],a[244:247],v[152:155],v[24:27]\n"
"s_waitcnt lgkmcnt(5)\nv_mfma_f32_16x16x32_bf16 v[16:19],a[248:251],v[156:159],v[16:19]\nds_read_b128 a[244:247],v196 offset:10240\nv_mfma_f32_16x16x32_bf16 v[20:23],a[248:251],v[188:191],v[20:23]\nbuffer_load_dwordx4 v252,s[8:11],s59 offen offset:1024 sc1 lds\nv_mfma_f32_16x16x32_bf16 v[28:31],a[252:255],v[188:191],v[28:31]\nds_read_b128 a[248:251],v196 offset:3072\nv_mfma_f32_16x16x32_bf16 v[24:27],a[252:255],v[156:159],v[24:27]\n"
"s_waitcnt lgkmcnt(5)\nv_mfma_f32_16x16x32_bf16 v[0:3],a[224:227],v[128:131],0\nds_read_b128 a[252:255],v196 offset:11264\nv_mfma_f32_16x16x32_bf16 v[4:7],a[224:227],v[160:163],0\nv_mfma_f32_16x16x32_bf16 v[12:15],a[228:231],v[160:163],0\nds_read_b128 a[224:227],v196 offset:4096\nv_mfma_f32_16x16x32_bf16 v[8:11],a[228:231],v[128:131],0\ns_waitcnt lgkmcnt(5)\nv_mfma_f32_16x16x32_bf16 v[0:3],a[232:235],v[132:135],v[0:3]\n"
"ds_read_b128 a[228:231],v196 offset:12288\nv_mfma_f32_16x16x32_bf16 v[4:7],a[232:235],v[164:167],v[4:7]\nbuffer_load_dwordx4 v252,s[8:11],s59 offen offset:2048 sc1 lds\nv_mfma_f32_16x16x32_bf16 v[12:15],a[236:239],v[164:167],v[12:15]\nds_read_b128 a[232:235],v196 offset:5120\nv_cvt_pk_bf16_f32 v40,v16,v17\nv_mfma_f32_16x16x32_bf16 v[8:11],a[236:239],v[132:135],v[8:11]\ns_waitcnt lgkmcnt(5)\nv_cvt_pk_bf16_f32 v41,v18,v19\n"
"v_cvt_pk_bf16_f32 v42,v24,v25\nv_mfma_f32_16x16x32_bf16 v[0:3],a[240:243],v[136:139],v[0:3]\nds_read_b128 a[236:239],v196 offset:13312\nv_cvt_pk_bf16_f32 v43,v26,v27\nv_mfma_f32_16x16x32_bf16 v[4:7],a[240:243],v[168:171],v[4:7]\nbuffer_store_dwordx4 v[40:43],v199,s[8:11],s54 offen offset:1536 sc1\nv_mfma_f32_16x16x32_bf16 v[12:15],a[244:247],v[168:171],v[12:15]\nds_read_b128 a[240:243],v196 offset:6144\n"
"v_cvt_pk_bf16_f32 v44,v20,v21\nv_mfma_f32_16x16x32_bf16 v[8:11],a[244:247],v[136:139],v[8:11]\ns_waitcnt lgkmcnt(5)\nv_cvt_pk_bf16_f32 v45,v22,v23\nv_cvt_pk_bf16_f32 v46,v28,v29\nv_mfma_f32_16x16x32_bf16 v[0:3],a[248:251],v[140:143],v[0:3]\nds_read_b128 a[244:247],v196 offset:14336\nv_cvt_pk_bf16_f32 v47,v30,v31\nv_mfma_f32_16x16x32_bf16 v[4:7],a[248:251],v[172:175],v[4:7]\nbuffer_load_dwordx4 v252,s[8:11],s59 offen offset:3072 sc1 lds\n"
"v_mfma_f32_16x16x32_bf16 v[12:15],a[252:255],v[172:175],v[12:15]\ns_add_u32 s59,s59,458752\nds_read_b128 a[248:251],v196 offset:7168\ns_add_u32 s84,s32,0x8000\nv_mfma_f32_16x16x32_bf16 v[8:11],a[252:255],v[140:143],v[8:11]\ns_waitcnt lgkmcnt(5)\ns_and_b32 s84,s84,0xffff\ns_add_u32 s84,s84,s15\nv_mfma_f32_16x16x32_bf16 v[0:3],a[224:227],v[144:147],v[0:3]\ns_add_u32 m0,s84,0x10000\nds_read_b128 a[252:255],v196 offset:15360\n"
"v_mfma_f32_16x16x32_bf16 v[4:7],a[224:227],v[176:179],v[4:7]\nv_mfma_f32_16x16x32_bf16 v[12:15],a[228:231],v[176:179],v[12:15]\nds_read_b128 a[224:227],v197 offset:0\nv_mfma_f32_16x16x32_bf16 v[8:11],a[228:231],v[144:147],v[8:11]\ns_waitcnt lgkmcnt(5)\nv_mfma_f32_16x16x32_bf16 v[0:3],a[232:235],v[148:151],v[0:3]\nds_read_b128 a[228:231],v197 offset:8192\nv_mfma_f32_16x16x32_bf16 v[4:7],a[232:235],v[180:183],v[4:7]\n"
"buffer_load_dwordx4 v252,s[8:11],s60 offen sc1 lds\nv_mfma_f32_16x16x32_bf16 v[12:15],a[236:239],v[180:183],v[12:15]\nds_read_b128 a[232:235],v197 offset:1024\nv_mfma_f32_16x16x32_bf16 v[8:11],a[236:239],v[148:151],v[8:11]\ns_waitcnt lgkmcnt(5)\nv_mfma_f32_16x16x32_bf16 v[0:3],a[240:243],v[152:155],v[0:3]\nds_read_b128 a[236:239],v197 offset:9216\nv_mfma_f32_16x16x32_bf16 v[4:7],a[240:243],v[184:187],v[4:7]\n"
"buffer_store_dwordx4 v[44:47],v199,s[8:11],s55 offen offset:1536 sc1\nv_mfma_f32_16x16x32_bf16 v[12:15],a[244:247],v[184:187],v[12:15]\nds_read_b128 a[240:243],v197 offset:2048\nv_mfma_f32_16x16x32_bf16 v[8:11],a[244:247],v[152:155],v[8:11]\ns_waitcnt lgkmcnt(5)\nv_mfma_f32_16x16x32_bf16 v[0:3],a[248:251],v[156:159],v[0:3]\nds_read_b128 a[244:247],v197 offset:10240\nv_mfma_f32_16x16x32_bf16 v[4:7],a[248:251],v[188:191],v[4:7]\n"
"buffer_load_dwordx4 v252,s[8:11],s60 offen offset:1024 sc1 lds\nv_mfma_f32_16x16x32_bf16 v[12:15],a[252:255],v[188:191],v[12:15]\nds_read_b128 a[248:251],v197 offset:3072\nv_mfma_f32_16x16x32_bf16 v[8:11],a[252:255],v[156:159],v[8:11]\ns_waitcnt lgkmcnt(5)\nv_mfma_f32_16x16x32_bf16 v[16:19],a[224:227],v[128:131],0\nds_read_b128 a[252:255],v197 offset:11264\nv_mfma_f32_16x16x32_bf16 v[20:23],a[224:227],v[160:163],0\n"
"v_mfma_f32_16x16x32_bf16 v[28:31],a[228:231],v[160:163],0\nds_read_b128 a[224:227],v197 offset:4096\nv_mfma_f32_16x16x32_bf16 v[24:27],a[228:231],v[128:131],0\ns_waitcnt lgkmcnt(5)\nv_mfma_f32_16x16x32_bf16 v[16:19],a[232:235],v[132:135],v[16:19]\nds_read_b128 a[228:231],v197 offset:12288\nv_mfma_f32_16x16x32_bf16 v[20:23],a[232:235],v[164:167],v[20:23]\nbuffer_load_dwordx4 v252,s[8:11],s60 offen offset:2048 sc1 lds\n"
"v_mfma_f32_16x16x32_bf16 v[28:31],a[236:239],v[164:167],v[28:31]\nds_read_b128 a[232:235],v197 offset:5120\nv_cvt_pk_bf16_f32 v32,v0,v1\nv_mfma_f32_16x16x32_bf16 v[24:27],a[236:239],v[132:135],v[24:27]\ns_waitcnt lgkmcnt(5)\nv_cvt_pk_bf16_f32 v33,v2,v3\nv_cvt_pk_bf16_f32 v34,v8,v9\nv_mfma_f32_16x16x32_bf16 v[16:19],a[240:243],v[136:139],v[16:19]\nds_read_b128 a[236:239],v197 offset:13312\nv_cvt_pk_bf16_f32 v35,v10,v11\n"
"v_mfma_f32_16x16x32_bf16 v[20:23],a[240:243],v[168:171],v[20:23]\nbuffer_store_dwordx4 v[32:35],v199,s[8:11],s54 offen offset:2048 sc1\nv_mfma_f32_16x16x32_bf16 v[28:31],a[244:247],v[168:171],v[28:31]\nds_read_b128 a[240:243],v197 offset:6144\nv_cvt_pk_bf16_f32 v36,v4,v5\nv_mfma_f32_16x16x32_bf16 v[24:27],a[244:247],v[136:139],v[24:27]\ns_waitcnt lgkmcnt(5)\nv_cvt_pk_bf16_f32 v37,v6,v7\nv_cvt_pk_bf16_f32 v38,v12,v13\n"
"v_mfma_f32_16x16x32_bf16 v[16:19],a[248:251],v[140:143],v[16:19]\nds_read_b128 a[244:247],v197 offset:14336\nv_cvt_pk_bf16_f32 v39,v14,v15\nv_mfma_f32_16x16x32_bf16 v[20:23],a[248:251],v[172:175],v[20:23]\nbuffer_load_dwordx4 v252,s[8:11],s60 offen offset:3072 sc1 lds\nv_mfma_f32_16x16x32_bf16 v[28:31],a[252:255],v[172:175],v[28:31]\ns_add_u32 s60,s60,458752\nds_read_b128 a[248:251],v197 offset:7168\n"
"v_mfma_f32_16x16x32_bf16 v[24:27],a[252:255],v[140:143],v[24:27]\ns_waitcnt lgkmcnt(5)\nv_mfma_f32_16x16x32_bf16 v[16:19],a[224:227],v[144:147],v[16:19]\nds_read_b128 a[252:255],v197 offset:15360\nv_mfma_f32_16x16x32_bf16 v[20:23],a[224:227],v[176:179],v[20:23]\nv_mfma_f32_16x16x32_bf16 v[28:31],a[228:231],v[176:179],v[28:31]\nv_mfma_f32_16x16x32_bf16 v[24:27],a[228:231],v[144:147],v[24:27]\ns_waitcnt lgkmcnt(4)\n"
"v_mfma_f32_16x16x32_bf16 v[16:19],a[232:235],v[148:151],v[16:19]\nv_mfma_f32_16x16x32_bf16 v[20:23],a[232:235],v[180:183],v[20:23]\nv_mfma_f32_16x16x32_bf16 v[28:31],a[236:239],v[180:183],v[28:31]\nv_mfma_f32_16x16x32_bf16 v[24:27],a[236:239],v[148:151],v[24:27]\ns_waitcnt lgkmcnt(2)\nv_mfma_f32_16x16x32_bf16 v[16:19],a[240:243],v[152:155],v[16:19]\nv_mfma_f32_16x16x32_bf16 v[20:23],a[240:243],v[184:187],v[20:23]\n"
"buffer_store_dwordx4 v[36:39],v199,s[8:11],s55 offen offset:2048 sc1\nv_mfma_f32_16x16x32_bf16 v[28:31],a[244:247],v[184:187],v[28:31]\nv_mfma_f32_16x16x32_bf16 v[24:27],a[244:247],v[152:155],v[24:27]\ns_waitcnt lgkmcnt(0)\nv_mfma_f32_16x16x32_bf16 v[16:19],a[248:251],v[156:159],v[16:19]\nv_mfma_f32_16x16x32_bf16 v[20:23],a[248:251],v[188:191],v[20:23]\nv_mfma_f32_16x16x32_bf16 v[28:31],a[252:255],v[188:191],v[28:31]\n"
"v_mfma_f32_16x16x32_bf16 v[24:27],a[252:255],v[156:159],v[24:27]\ns_nop 4\nv_cvt_pk_bf16_f32 v40,v16,v17\nv_cvt_pk_bf16_f32 v41,v18,v19\ns_nop 0\nv_cvt_pk_bf16_f32 v42,v24,v25\nv_cvt_pk_bf16_f32 v43,v26,v27\nbuffer_store_dwordx4 v[40:43],v199,s[8:11],s54 offen offset:2560 sc1\nv_cvt_pk_bf16_f32 v44,v20,v21\nv_cvt_pk_bf16_f32 v45,v22,v23\nv_cvt_pk_bf16_f32 v46,v28,v29\nv_cvt_pk_bf16_f32 v47,v30,v31\n"
"buffer_store_dwordx4 v[44:47],v199,s[8:11],s55 offen offset:2560 sc1\ns_load_dwordx2 s[68:69],s[4:5],0\ns_lshl4_add_u32 s7,s21,s16\ns_lshl_b32 s7,s7,7\ns_add_u32 s64,s7,117506048\ns_waitcnt vmcnt(0) lgkmcnt(0)\ns_barrier\ns_cmp_lg_u32 s14,0\ns_cbranch_scc1 L_fused_block_nf_51\nbuffer_store_dwordx2 v[250:251],off,s[8:11],s64 sc1\nL_fused_block_nf_51:\ns_add_u32 m0,s32,s15\ns_mov_b32 s70,1835008\ns_mov_b32 s71,0x20000\n"
"s_and_b32 s69,s69,0xffff\nbuffer_load_dwordx4 v252,s[8:11],s59 offen sc1 lds\nbuffer_load_dwordx4 v252,s[8:11],s59 offen offset:1024 sc1 lds\nbuffer_load_dwordx4 v252,s[8:11],s59 offen offset:2048 sc1 lds\nbuffer_load_dwordx4 v252,s[8:11],s59 offen offset:3072 sc1 lds\ns_add_u32 s59,s59,458752\ns_add_u32 s7,s32,49152\ns_and_b32 s7,s7,0xffff\ns_add_u32 s7,s7,s15\ns_add_u32 m0,s7,0x10000\ns_nop 0\nbuffer_load_dwordx4 v252,s[8:11],s60 offen sc1 lds\n"
"buffer_load_dwordx4 v252,s[8:11],s60 offen offset:1024 sc1 lds\nbuffer_load_dwordx4 v252,s[8:11],s60 offen offset:2048 sc1 lds\nbuffer_load_dwordx4 v252,s[8:11],s60 offen offset:3072 sc1 lds\ns_add_u32 s60,s60,458752\ns_add_u32 s7,s32,0\ns_and_b32 s7,s7,0xffff\ns_add_u32 s7,s7,s15\ns_add_u32 m0,s7,0x10000\ns_nop 0\nbuffer_load_dwordx4 v252,s[8:11],s60 offen sc1 lds\nbuffer_load_dwordx4 v252,s[8:11],s60 offen offset:1024 sc1 lds\n"
"buffer_load_dwordx4 v252,s[8:11],s60 offen offset:2048 sc1 lds\nbuffer_load_dwordx4 v252,s[8:11],s60 offen offset:3072 sc1 lds\ns_add_u32 s60,s60,458752\nbuffer_load_dwordx4 a[0:3],v252,s[8:11],s61 offen offset:0 sc1\nbuffer_load_dwordx4 a[4:7],v252,s[8:11],s61 offen offset:1024 sc1\nbuffer_load_dwordx4 a[8:11],v252,s[8:11],s61 offen offset:2048 sc1\nbuffer_load_dwordx4 a[12:15],v252,s[8:11],s61 offen offset:3072 sc1\n"
"s_add_u32 s61,s61,0x1000\nbuffer_load_dwordx4 a[16:19],v252,s[8:11],s61 offen offset:0 sc1\nbuffer_load_dwordx4 a[20:23],v252,s[8:11],s61 offen offset:1024 sc1\nbuffer_load_dwordx4 a[24:27],v252,s[8:11],s61 offen offset:2048 sc1\nbuffer_load_dwordx4 a[28:31],v252,s[8:11],s61 offen offset:3072 sc1\ns_add_u32 s61,s61,0x1000\nbuffer_load_dwordx4 a[32:35],v252,s[8:11],s61 offen offset:0 sc1\nbuffer_load_dwordx4 a[36:39],v252,s[8:11],s61 offen offset:1024 sc1\n"
"buffer_load_dwordx4 a[40:43],v252,s[8:11],s61 offen offset:2048 sc1\nbuffer_load_dwordx4 a[44:47],v252,s[8:11],s61 offen offset:3072 sc1\ns_add_u32 s61,s61,0x1000\nbuffer_load_dwordx4 a[48:51],v252,s[8:11],s61 offen offset:0 sc1\nbuffer_load_dwordx4 a[52:55],v252,s[8:11],s61 offen offset:1024 sc1\nbuffer_load_dwordx4 a[56:59],v252,s[8:11],s61 offen offset:2048 sc1\nbuffer_load_dwordx4 a[60:63],v252,s[8:11],s61 offen offset:3072 sc1\n"
"s_add_u32 s61,s61,0x1000\nbuffer_load_dwordx4 a[64:67],v252,s[8:11],s61 offen offset:0 sc1\nbuffer_load_dwordx4 a[68:71],v252,s[8:11],s61 offen offset:1024 sc1\nbuffer_load_dwordx4 a[72:75],v252,s[8:11],s61 offen offset:2048 sc1\nbuffer_load_dwordx4 a[76:79],v252,s[8:11],s61 offen offset:3072 sc1\ns_add_u32 s61,s61,0x1000\nbuffer_load_dwordx4 a[80:83],v252,s[8:11],s61 offen offset:0 sc1\nbuffer_load_dwordx4 a[84:87],v252,s[8:11],s61 offen offset:1024 sc1\n"
"buffer_load_dwordx4 a[88:91],v252,s[8:11],s61 offen offset:2048 sc1\nbuffer_load_dwordx4 a[92:95],v252,s[8:11],s61 offen offset:3072 sc1\ns_add_u32 s61,s61,0x1000\ns_add_u32 s32,s32,0x4000\ns_and_b32 s32,s32,0xffff\nv_and_b32 v160,7,v253\nv_bfe_u32 v161,v253,3,2\nv_lshrrev_b32 v162,5,v253\nv_mul_u32_u24 v166,896,v160\nv_lshl_add_u32 v166,v161,3,v166\nv_lshl_add_u32 v166,v162,6,v166\nv_lshlrev_b32 v167,4,v160\n"
"v_lshl_add_u32 v167,v161,8,v167\nv_lshl_add_u32 v167,v162,10,v167\nv_lshlrev_b32 v168,3,v160\nv_add_u32 v168,0x24000,v168\ns_mul_i32 s66,s58,7168\ns_lshl_b32 s7,s14,8\ns_add_u32 s66,s66,s7\ns_mul_i32 s65,s58,114688\ns_lshl_b32 s7,s14,11\ns_add_u32 s65,s65,s7\ns_add_u32 s65,s65,50331648\ns_lshr_b32 s67,s58,1\ns_mul_i32 s67,s67,14336\ns_add_u32 s67,s67,s15\ns_and_b32 s7,s58,1\ns_lshl_b32 s7,s7,7\ns_add_u32 s67,s67,s7\n"
"s_add_u32 s86,s65,1024\ns_add_u32 s87,s66,128\ns_add_u32 s88,s67,2048\ns_cmp_eq_u32 s14,3\ns_cselect_b32 s86,1073741824,s86\ns_cselect_b32 s87,1073741824,s87\ns_cselect_b32 s88,1073741824,s88\ns_mov_b32 s7,0x3c000000\ns_cselect_b32 s72,0x3c800000,s7\ns_cselect_b64 s[74:75],0,-1\nbuffer_load_dwordx2 v[144:145],v166,s[68:71],s66 offen offset:0\nbuffer_load_dwordx2 v[146:147],v166,s[68:71],s66 offen offset:32\n"
"buffer_load_dwordx2 v[148:149],v166,s[68:71],s87 offen offset:0\nbuffer_load_dwordx2 v[150:151],v166,s[68:71],s87 offen offset:32\ns_lshl_b32 s7,s56,11\ns_add_u32 s84,s7,117506048\ns_cmp_lg_u32 s14,0\ns_cbranch_scc1 L_fused_block_np_52\nv_lshlrev_b32 v204,7,v253\ns_mov_b64 exec,0xffff\ns_mov_b32 s7,0\nL_fused_block_poll_53:\nbuffer_load_dwordx2 v[202:203],v204,s[8:11],s84 offen sc1\ns_add_u32 s7,s7,1\n"
"s_waitcnt vmcnt(0)\nv_cmp_eq_u64 vcc,s[12:13],v[202:203]\ns_nop 1\ns_cmp_eq_u64 vcc,exec\ns_cbranch_scc1 L_fused_block_polld_54\ns_cmp_lt_u32 s7,131072\ns_cbranch_scc1 L_fused_block_poll_53\nL_fused_block_polld_54:\ns_mov_b64 exec,-1\nL_fused_block_np_52:\ns_barrier\nbuffer_load_dwordx4 v[0:3],v252,s[8:11],s65 offen sc1\ns_add_u32 s65,s65,7168\nbuffer_load_dwordx4 v[4:7],v252,s[8:11],s86 offen sc1\n"
"s_add_u32 s86,s86,7168\nbuffer_load_dwordx4 v[8:11],v252,s[8:11],s65 offen sc1\ns_add_u32 s65,s65,7168\nbuffer_load_dwordx4 v[12:15],v252,s[8:11],s86 offen sc1\ns_add_u32 s86,s86,7168\nbuffer_load_dwordx4 v[16:19],v252,s[8:11],s65 offen sc1\ns_add_u32 s65,s65,7168\nbuffer_load_dwordx4 v[20:23],v252,s[8:11],s86 offen sc1\ns_add_u32 s86,s86,7168\nbuffer_load_dwordx4 v[24:27],v252,s[8:11],s65 offen sc1\n"
"s_add_u32 s65,s65,7168\nbuffer_load_dwordx4 v[28:31],v252,s[8:11],s86 offen sc1\ns_add_u32 s86,s86,7168\nbuffer_load_dwordx4 v[32:35],v252,s[8:11],s65 offen sc1\ns_add_u32 s65,s65,7168\nbuffer_load_dwordx4 v[36:39],v252,s[8:11],s86 offen sc1\ns_add_u32 s86,s86,7168\nbuffer_load_dwordx4 v[40:43],v252,s[8:11],s65 offen sc1\ns_add_u32 s65,s65,7168\nbuffer_load_dwordx4 v[44:47],v252,s[8:11],s86 offen sc1\n"
"s_add_u32 s86,s86,7168\nbuffer_load_dwordx4 v[48:51],v252,s[8:11],s65 offen sc1\ns_add_u32 s65,s65,7168\nbuffer_load_dwordx4 v[52:55],v252,s[8:11],s86 offen sc1\ns_add_u32 s86,s86,7168\nbuffer_load_dwordx4 v[56:59],v252,s[8:11],s65 offen sc1\ns_add_u32 s65,s65,7168\nbuffer_load_dwordx4 v[60:63],v252,s[8:11],s86 offen sc1\ns_add_u32 s86,s86,7168\nbuffer_load_dwordx4 v[64:67],v252,s[8:11],s65 offen sc1\n"
"s_add_u32 s65,s65,7168\nbuffer_load_dwordx4 v[68:71],v252,s[8:11],s86 offen sc1\ns_add_u32 s86,s86,7168\nbuffer_load_dwordx4 v[72:75],v252,s[8:11],s65 offen sc1\ns_add_u32 s65,s65,7168\nbuffer_load_dwordx4 v[76:79],v252,s[8:11],s86 offen sc1\ns_add_u32 s86,s86,7168\nbuffer_load_dwordx4 v[80:83],v252,s[8:11],s65 offen sc1\ns_add_u32 s65,s65,7168\nbuffer_load_dwordx4 v[84:87],v252,s[8:11],s86 offen sc1\n"
"s_add_u32 s86,s86,7168\nbuffer_load_dwordx4 v[88:91],v252,s[8:11],s65 offen sc1\ns_add_u32 s65,s65,7168\nbuffer_load_dwordx4 v[92:95],v252,s[8:11],s86 offen sc1\ns_add_u32 s86,s86,7168\nbuffer_load_dwordx4 v[96:99],v252,s[8:11],s65 offen sc1\ns_add_u32 s65,s65,7168\nbuffer_load_dwordx4 v[100:103],v252,s[8:11],s86 offen sc1\ns_add_u32 s86,s86,7168\nbuffer_load_dwordx4 v[104:107],v252,s[8:11],s65 offen sc1\n"
"s_add_u32 s65,s65,7168\nbuffer_load_dwordx4 v[108:111],v252,s[8:11],s86 offen sc1\ns_add_u32 s86,s86,7168\nbuffer_load_dwordx4 v[112:115],v252,s[8:11],s65 offen sc1\ns_add_u32 s65,s65,7168\nbuffer_load_dwordx4 v[116:119],v252,s[8:11],s86 offen sc1\ns_add_u32 s86,s86,7168\nbuffer_load_dwordx4 v[120:123],v252,s[8:11],s65 offen sc1\nbuffer_load_dwordx4 v[124:127],v252,s[8:11],s86 offen sc1\ns_waitcnt vmcnt(32)\n"
"v_lshlrev_b32 v128,16,v144\nv_and_b32 v129,0xffff0000,v144\nv_lshlrev_b32 v130,16,v145\nv_and_b32 v131,0xffff0000,v145\nv_lshlrev_b32 v132,16,v146\nv_and_b32 v133,0xffff0000,v146\nv_lshlrev_b32 v134,16,v147\nv_and_b32 v135,0xffff0000,v147\nv_lshlrev_b32 v136,16,v148\nv_and_b32 v137,0xffff0000,v148\nv_lshlrev_b32 v138,16,v149\nv_and_b32 v139,0xffff0000,v149\nv_lshlrev_b32 v140,16,v150\nv_and_b32 v141,0xffff0000,v150\n"
"v_lshlrev_b32 v142,16,v151\nv_and_b32 v143,0xffff0000,v151\ns_waitcnt vmcnt(31)\nv_lshlrev_b32 v160,16,v0\nv_and_b32 v0,0xffff0000,v0\nv_add_f32 v128,v128,v160\nv_add_f32 v129,v129,v0\nv_lshlrev_b32 v161,16,v1\nv_and_b32 v1,0xffff0000,v1\nv_add_f32 v130,v130,v161\nv_add_f32 v131,v131,v1\nv_lshlrev_b32 v160,16,v2\nv_and_b32 v2,0xffff0000,v2\nv_add_f32 v132,v132,v160\nv_add_f32 v133,v133,v2\nv_lshlrev_b32 v161,16,v3\n"
"v_and_b32 v3,0xffff0000,v3\nv_add_f32 v134,v134,v161\nv_add_f32 v135,v135,v3\ns_waitcnt vmcnt(30)\nv_lshlrev_b32 v160,16,v4\nv_and_b32 v4,0xffff0000,v4\nv_add_f32 v136,v136,v160\nv_add_f32 v137,v137,v4\nv_lshlrev_b32 v161,16,v5\nv_and_b32 v5,0xffff0000,v5\nv_add_f32 v138,v138,v161\nv_add_f32 v139,v139,v5\nv_lshlrev_b32 v160,16,v6\nv_and_b32 v6,0xffff0000,v6\nv_add_f32 v140,v140,v160\nv_add_f32 v141,v141,v6\n"
"v_lshlrev_b32 v161,16,v7\nv_and_b32 v7,0xffff0000,v7\nv_add_f32 v142,v142,v161\nv_add_f32 v143,v143,v7\ns_waitcnt vmcnt(29)\nv_lshlrev_b32 v160,16,v8\nv_and_b32 v8,0xffff0000,v8\nv_add_f32 v128,v128,v160\nv_add_f32 v129,v129,v8\nv_lshlrev_b32 v161,16,v9\nv_and_b32 v9,0xffff0000,v9\nv_add_f32 v130,v130,v161\nv_add_f32 v131,v131,v9\nv_lshlrev_b32 v160,16,v10\nv_and_b32 v10,0xffff0000,v10\nv_add_f32 v132,v132,v160\n"
"v_add_f32 v133,v133,v10\nv_lshlrev_b32 v161,16,v11\nv_and_b32 v11,0xffff0000,v11\nv_add_f32 v134,v134,v161\nv_add_f32 v135,v135,v11\ns_waitcnt vmcnt(28)\nv_lshlrev_b32 v160,16,v12\nv_and_b32 v12,0xffff0000,v12\nv_add_f32 v136,v136,v160\nv_add_f32 v137,v137,v12\nv_lshlrev_b32 v161,16,v13\nv_and_b32 v13,0xffff0000,v13\nv_add_f32 v138,v138,v161\nv_add_f32 v139,v139,v13\nv_lshlrev_b32 v160,16,v14\nv_and_b32 v14,0xffff0000,v14\n"
"v_add_f32 v140,v140,v160\nv_add_f32 v141,v141,v14\nv_lshlrev_b32 v161,16,v15\nv_and_b32 v15,0xffff0000,v15\nv_add_f32 v142,v142,v161\nv_add_f32 v143,v143,v15\ns_waitcnt vmcnt(27)\nv_lshlrev_b32 v160,16,v16\nv_and_b32 v16,0xffff0000,v16\nv_add_f32 v128,v128,v160\nv_add_f32 v129,v129,v16\nv_lshlrev_b32 v161,16,v17\nv_and_b32 v17,0xffff0000,v17\nv_add_f32 v130,v130,v161\nv_add_f32 v131,v131,v17\nv_lshlrev_b32 v160,16,v18\n"
"v_and_b32 v18,0xffff0000,v18\nv_add_f32 v132,v132,v160\nv_add_f32 v133,v133,v18\nv_lshlrev_b32 v161,16,v19\nv_and_b32 v19,0xffff0000,v19\nv_add_f32 v134,v134,v161\nv_add_f32 v135,v135,v19\ns_waitcnt vmcnt(26)\nv_lshlrev_b32 v160,16,v20\nv_and_b32 v20,0xffff0000,v20\nv_add_f32 v136,v136,v160\nv_add_f32 v137,v137,v20\nv_lshlrev_b32 v161,16,v21\nv_and_b32 v21,0xffff0000,v21\nv_add_f32 v138,v138,v161\n"
"v_add_f32 v139,v139,v21\nv_lshlrev_b32 v160,16,v22\nv_and_b32 v22,0xffff0000,v22\nv_add_f32 v140,v140,v160\nv_add_f32 v141,v141,v22\nv_lshlrev_b32 v161,16,v23\nv_and_b32 v23,0xffff0000,v23\nv_add_f32 v142,v142,v161\nv_add_f32 v143,v143,v23\ns_waitcnt vmcnt(25)\nv_lshlrev_b32 v160,16,v24\nv_and_b32 v24,0xffff0000,v24\nv_add_f32 v128,v128,v160\nv_add_f32 v129,v129,v24\nv_lshlrev_b32 v161,16,v25\nv_and_b32 v25,0xffff0000,v25\n"
"v_add_f32 v130,v130,v161\nv_add_f32 v131,v131,v25\nv_lshlrev_b32 v160,16,v26\nv_and_b32 v26,0xffff0000,v26\nv_add_f32 v132,v132,v160\nv_add_f32 v133,v133,v26\nv_lshlrev_b32 v161,16,v27\nv_and_b32 v27,0xffff0000,v27\nv_add_f32 v134,v134,v161\nv_add_f32 v135,v135,v27\ns_waitcnt vmcnt(24)\nv_lshlrev_b32 v160,16,v28\nv_and_b32 v28,0xffff0000,v28\nv_add_f32 v136,v136,v160\nv_add_f32 v137,v137,v28\nv_lshlrev_b32 v161,16,v29\n"
"v_and_b32 v29,0xffff0000,v29\nv_add_f32 v138,v138,v161\nv_add_f32 v139,v139,v29\nv_lshlrev_b32 v160,16,v30\nv_and_b32 v30,0xffff0000,v30\nv_add_f32 v140,v140,v160\nv_add_f32 v141,v141,v30\nv_lshlrev_b32 v161,16,v31\nv_and_b32 v31,0xffff0000,v31\nv_add_f32 v142,v142,v161\nv_add_f32 v143,v143,v31\ns_waitcnt vmcnt(23)\nv_lshlrev_b32 v160,16,v32\nv_and_b32 v32,0xffff0000,v32\nv_add_f32 v128,v128,v160\n"
"v_add_f32 v129,v129,v32\nv_lshlrev_b32 v161,16,v33\nv_and_b32 v33,0xffff0000,v33\nv_add_f32 v130,v130,v161\nv_add_f32 v131,v131,v33\nv_lshlrev_b32 v160,16,v34\nv_and_b32 v34,0xffff0000,v34\nv_add_f32 v132,v132,v160\nv_add_f32 v133,v133,v34\nv_lshlrev_b32 v161,16,v35\nv_and_b32 v35,0xffff0000,v35\nv_add_f32 v134,v134,v161\nv_add_f32 v135,v135,v35\ns_waitcnt vmcnt(22)\nv_lshlrev_b32 v160,16,v36\nv_and_b32 v36,0xffff0000,v36\n"
"v_add_f32 v136,v136,v160\nv_add_f32 v137,v137,v36\nv_lshlrev_b32 v161,16,v37\nv_and_b32 v37,0xffff0000,v37\nv_add_f32 v138,v138,v161\nv_add_f32 v139,v139,v37\nv_lshlrev_b32 v160,16,v38\nv_and_b32 v38,0xffff0000,v38\nv_add_f32 v140,v140,v160\nv_add_f32 v141,v141,v38\nv_lshlrev_b32 v161,16,v39\nv_and_b32 v39,0xffff0000,v39\nv_add_f32 v142,v142,v161\nv_add_f32 v143,v143,v39\ns_waitcnt vmcnt(21)\nv_lshlrev_b32 v160,16,v40\n"
"v_and_b32 v40,0xffff0000,v40\nv_add_f32 v128,v128,v160\nv_add_f32 v129,v129,v40\nv_lshlrev_b32 v161,16,v41\nv_and_b32 v41,0xffff0000,v41\nv_add_f32 v130,v130,v161\nv_add_f32 v131,v131,v41\nv_lshlrev_b32 v160,16,v42\nv_and_b32 v42,0xffff0000,v42\nv_add_f32 v132,v132,v160\nv_add_f32 v133,v133,v42\nv_lshlrev_b32 v161,16,v43\nv_and_b32 v43,0xffff0000,v43\nv_add_f32 v134,v134,v161\nv_add_f32 v135,v135,v43\n"
"s_waitcnt vmcnt(20)\nv_lshlrev_b32 v160,16,v44\nv_and_b32 v44,0xffff0000,v44\nv_add_f32 v136,v136,v160\nv_add_f32 v137,v137,v44\nv_lshlrev_b32 v161,16,v45\nv_and_b32 v45,0xffff0000,v45\nv_add_f32 v138,v138,v161\nv_add_f32 v139,v139,v45\nv_lshlrev_b32 v160,16,v46\nv_and_b32 v46,0xffff0000,v46\nv_add_f32 v140,v140,v160\nv_add_f32 v141,v141,v46\nv_lshlrev_b32 v161,16,v47\nv_and_b32 v47,0xffff0000,v47\n"
"v_add_f32 v142,v142,v161\nv_add_f32 v143,v143,v47\ns_waitcnt vmcnt(19)\nv_lshlrev_b32 v160,16,v48\nv_and_b32 v48,0xffff0000,v48\nv_add_f32 v128,v128,v160\nv_add_f32 v129,v129,v48\nv_lshlrev_b32 v161,16,v49\nv_and_b32 v49,0xffff0000,v49\nv_add_f32 v130,v130,v161\nv_add_f32 v131,v131,v49\nv_lshlrev_b32 v160,16,v50\nv_and_b32 v50,0xffff0000,v50\nv_add_f32 v132,v132,v160\nv_add_f32 v133,v133,v50\nv_lshlrev_b32 v161,16,v51\n"
"v_and_b32 v51,0xffff0000,v51\nv_add_f32 v134,v134,v161\nv_add_f32 v135,v135,v51\ns_waitcnt vmcnt(18)\nv_lshlrev_b32 v160,16,v52\nv_and_b32 v52,0xffff0000,v52\nv_add_f32 v136,v136,v160\nv_add_f32 v137,v137,v52\nv_lshlrev_b32 v161,16,v53\nv_and_b32 v53,0xffff0000,v53\nv_add_f32 v138,v138,v161\nv_add_f32 v139,v139,v53\nv_lshlrev_b32 v160,16,v54\nv_and_b32 v54,0xffff0000,v54\nv_add_f32 v140,v140,v160\n"
"v_add_f32 v141,v141,v54\nv_lshlrev_b32 v161,16,v55\nv_and_b32 v55,0xffff0000,v55\nv_add_f32 v142,v142,v161\nv_add_f32 v143,v143,v55\ns_waitcnt vmcnt(17)\nv_lshlrev_b32 v160,16,v56\nv_and_b32 v56,0xffff0000,v56\nv_add_f32 v128,v128,v160\nv_add_f32 v129,v129,v56\nv_lshlrev_b32 v161,16,v57\nv_and_b32 v57,0xffff0000,v57\nv_add_f32 v130,v130,v161\nv_add_f32 v131,v131,v57\nv_lshlrev_b32 v160,16,v58\nv_and_b32 v58,0xffff0000,v58\n"
"v_add_f32 v132,v132,v160\nv_add_f32 v133,v133,v58\nv_lshlrev_b32 v161,16,v59\nv_and_b32 v59,0xffff0000,v59\nv_add_f32 v134,v134,v161\nv_add_f32 v135,v135,v59\ns_waitcnt vmcnt(16)\nv_lshlrev_b32 v160,16,v60\nv_and_b32 v60,0xffff0000,v60\nv_add_f32 v136,v136,v160\nv_add_f32 v137,v137,v60\nv_lshlrev_b32 v161,16,v61\nv_and_b32 v61,0xffff0000,v61\nv_add_f32 v138,v138,v161\nv_add_f32 v139,v139,v61\nv_lshlrev_b32 v160,16,v62\n"
"v_and_b32 v62,0xffff0000,v62\nv_add_f32 v140,v140,v160\nv_add_f32 v141,v141,v62\nv_lshlrev_b32 v161,16,v63\nv_and_b32 v63,0xffff0000,v63\nv_add_f32 v142,v142,v161\nv_add_f32 v143,v143,v63\ns_waitcnt vmcnt(15)\nv_lshlrev_b32 v160,16,v64\nv_and_b32 v64,0xffff0000,v64\nv_add_f32 v128,v128,v160\nv_add_f32 v129,v129,v64\nv_lshlrev_b32 v161,16,v65\nv_and_b32 v65,0xffff0000,v65\nv_add_f32 v130,v130,v161\n"
"v_add_f32 v131,v131,v65\nv_lshlrev_b32 v160,16,v66\nv_and_b32 v66,0xffff0000,v66\nv_add_f32 v132,v132,v160\nv_add_f32 v133,v133,v66\nv_lshlrev_b32 v161,16,v67\nv_and_b32 v67,0xffff0000,v67\nv_add_f32 v134,v134,v161\nv_add_f32 v135,v135,v67\ns_waitcnt vmcnt(14)\nv_lshlrev_b32 v160,16,v68\nv_and_b32 v68,0xffff0000,v68\nv_add_f32 v136,v136,v160\nv_add_f32 v137,v137,v68\nv_lshlrev_b32 v161,16,v69\nv_and_b32 v69,0xffff0000,v69\n"
"v_add_f32 v138,v138,v161\nv_add_f32 v139,v139,v69\nv_lshlrev_b32 v160,16,v70\nv_and_b32 v70,0xffff0000,v70\nv_add_f32 v140,v140,v160\nv_add_f32 v141,v141,v70\nv_lshlrev_b32 v161,16,v71\nv_and_b32 v71,0xffff0000,v71\nv_add_f32 v142,v142,v161\nv_add_f32 v143,v143,v71\ns_waitcnt vmcnt(13)\nv_lshlrev_b32 v160,16,v72\nv_and_b32 v72,0xffff0000,v72\nv_add_f32 v128,v128,v160\nv_add_f32 v129,v129,v72\nv_lshlrev_b32 v161,16,v73\n"
"v_and_b32 v73,0xffff0000,v73\nv_add_f32 v130,v130,v161\nv_add_f32 v131,v131,v73\nv_lshlrev_b32 v160,16,v74\nv_and_b32 v74,0xffff0000,v74\nv_add_f32 v132,v132,v160\nv_add_f32 v133,v133,v74\nv_lshlrev_b32 v161,16,v75\nv_and_b32 v75,0xffff0000,v75\nv_add_f32 v134,v134,v161\nv_add_f32 v135,v135,v75\ns_waitcnt vmcnt(12)\nv_lshlrev_b32 v160,16,v76\nv_and_b32 v76,0xffff0000,v76\nv_add_f32 v136,v136,v160\n"
"v_add_f32 v137,v137,v76\nv_lshlrev_b32 v161,16,v77\nv_and_b32 v77,0xffff0000,v77\nv_add_f32 v138,v138,v161\nv_add_f32 v139,v139,v77\nv_lshlrev_b32 v160,16,v78\nv_and_b32 v78,0xffff0000,v78\nv_add_f32 v140,v140,v160\nv_add_f32 v141,v141,v78\nv_lshlrev_b32 v161,16,v79\nv_and_b32 v79,0xffff0000,v79\nv_add_f32 v142,v142,v161\nv_add_f32 v143,v143,v79\ns_waitcnt vmcnt(11)\nv_lshlrev_b32 v160,16,v80\nv_and_b32 v80,0xffff0000,v80\n"
"v_add_f32 v128,v128,v160\nv_add_f32 v129,v129,v80\nv_lshlrev_b32 v161,16,v81\nv_and_b32 v81,0xffff0000,v81\nv_add_f32 v130,v130,v161\nv_add_f32 v131,v131,v81\nv_lshlrev_b32 v160,16,v82\nv_and_b32 v82,0xffff0000,v82\nv_add_f32 v132,v132,v160\nv_add_f32 v133,v133,v82\nv_lshlrev_b32 v161,16,v83\nv_and_b32 v83,0xffff0000,v83\nv_add_f32 v134,v134,v161\nv_add_f32 v135,v135,v83\ns_waitcnt vmcnt(10)\nv_lshlrev_b32 v160,16,v84\n"
"v_and_b32 v84,0xffff0000,v84\nv_add_f32 v136,v136,v160\nv_add_f32 v137,v137,v84\nv_lshlrev_b32 v161,16,v85\nv_and_b32 v85,0xffff0000,v85\nv_add_f32 v138,v138,v161\nv_add_f32 v139,v139,v85\nv_lshlrev_b32 v160,16,v86\nv_and_b32 v86,0xffff0000,v86\nv_add_f32 v140,v140,v160\nv_add_f32 v141,v141,v86\nv_lshlrev_b32 v161,16,v87\nv_and_b32 v87,0xffff0000,v87\nv_add_f32 v142,v142,v161\nv_add_f32 v143,v143,v87\n"
"s_waitcnt vmcnt(9)\nv_lshlrev_b32 v160,16,v88\nv_and_b32 v88,0xffff0000,v88\nv_add_f32 v128,v128,v160\nv_add_f32 v129,v129,v88\nv_lshlrev_b32 v161,16,v89\nv_and_b32 v89,0xffff0000,v89\nv_add_f32 v130,v130,v161\nv_add_f32 v131,v131,v89\nv_lshlrev_b32 v160,16,v90\nv_and_b32 v90,0xffff0000,v90\nv_add_f32 v132,v132,v160\nv_add_f32 v133,v133,v90\nv_lshlrev_b32 v161,16,v91\nv_and_b32 v91,0xffff0000,v91\n"
"v_add_f32 v134,v134,v161\nv_add_f32 v135,v135,v91\ns_waitcnt vmcnt(8)\nv_lshlrev_b32 v160,16,v92\nv_and_b32 v92,0xffff0000,v92\nv_add_f32 v136,v136,v160\nv_add_f32 v137,v137,v92\nv_lshlrev_b32 v161,16,v93\nv_and_b32 v93,0xffff0000,v93\nv_add_f32 v138,v138,v161\nv_add_f32 v139,v139,v93\nv_lshlrev_b32 v160,16,v94\nv_and_b32 v94,0xffff0000,v94\nv_add_f32 v140,v140,v160\nv_add_f32 v141,v141,v94\nv_lshlrev_b32 v161,16,v95\n"
"v_and_b32 v95,0xffff0000,v95\nv_add_f32 v142,v142,v161\nv_add_f32 v143,v143,v95\ns_waitcnt vmcnt(7)\nv_lshlrev_b32 v160,16,v96\nv_and_b32 v96,0xffff0000,v96\nv_add_f32 v128,v128,v160\nv_add_f32 v129,v129,v96\nv_lshlrev_b32 v161,16,v97\nv_and_b32 v97,0xffff0000,v97\nv_add_f32 v130,v130,v161\nv_add_f32 v131,v131,v97\nv_lshlrev_b32 v160,16,v98\nv_and_b32 v98,0xffff0000,v98\nv_add_f32 v132,v132,v160\nv_add_f32 v133,v133,v98\n"
"v_lshlrev_b32 v161,16,v99\nv_and_b32 v99,0xffff0000,v99\nv_add_f32 v134,v134,v161\nv_add_f32 v135,v135,v99\ns_waitcnt vmcnt(6)\nv_lshlrev_b32 v160,16,v100\nv_and_b32 v100,0xffff0000,v100\nv_add_f32 v136,v136,v160\nv_add_f32 v137,v137,v100\nv_lshlrev_b32 v161,16,v101\nv_and_b32 v101,0xffff0000,v101\nv_add_f32 v138,v138,v161\nv_add_f32 v139,v139,v101\nv_lshlrev_b32 v160,16,v102\nv_and_b32 v102,0xffff0000,v102\n"
"v_add_f32 v140,v140,v160\nv_add_f32 v141,v141,v102\nv_lshlrev_b32 v161,16,v103\nv_and_b32 v103,0xffff0000,v103\nv_add_f32 v142,v142,v161\nv_add_f32 v143,v143,v103\ns_waitcnt vmcnt(5)\nv_lshlrev_b32 v160,16,v104\nv_and_b32 v104,0xffff0000,v104\nv_add_f32 v128,v128,v160\nv_add_f32 v129,v129,v104\nv_lshlrev_b32 v161,16,v105\nv_and_b32 v105,0xffff0000,v105\nv_add_f32 v130,v130,v161\nv_add_f32 v131,v131,v105\n"
"v_lshlrev_b32 v160,16,v106\nv_and_b32 v106,0xffff0000,v106\nv_add_f32 v132,v132,v160\nv_add_f32 v133,v133,v106\nv_lshlrev_b32 v161,16,v107\nv_and_b32 v107,0xffff0000,v107\nv_add_f32 v134,v134,v161\nv_add_f32 v135,v135,v107\ns_waitcnt vmcnt(4)\nv_lshlrev_b32 v160,16,v108\nv_and_b32 v108,0xffff0000,v108\nv_add_f32 v136,v136,v160\nv_add_f32 v137,v137,v108\nv_lshlrev_b32 v161,16,v109\nv_and_b32 v109,0xffff0000,v109\n"
"v_add_f32 v138,v138,v161\nv_add_f32 v139,v139,v109\nv_lshlrev_b32 v160,16,v110\nv_and_b32 v110,0xffff0000,v110\nv_add_f32 v140,v140,v160\nv_add_f32 v141,v141,v110\nv_lshlrev_b32 v161,16,v111\nv_and_b32 v111,0xffff0000,v111\nv_add_f32 v142,v142,v161\nv_add_f32 v143,v143,v111\ns_waitcnt vmcnt(3)\nv_lshlrev_b32 v160,16,v112\nv_and_b32 v112,0xffff0000,v112\nv_add_f32 v128,v128,v160\nv_add_f32 v129,v129,v112\n"
"v_lshlrev_b32 v161,16,v113\nv_and_b32 v113,0xffff0000,v113\nv_add_f32 v130,v130,v161\nv_add_f32 v131,v131,v113\nv_lshlrev_b32 v160,16,v114\nv_and_b32 v114,0xffff0000,v114\nv_add_f32 v132,v132,v160\nv_add_f32 v133,v133,v114\nv_lshlrev_b32 v161,16,v115\nv_and_b32 v115,0xffff0000,v115\nv_add_f32 v134,v134,v161\nv_add_f32 v135,v135,v115\ns_waitcnt vmcnt(2)\nv_lshlrev_b32 v160,16,v116\nv_and_b32 v116,0xffff0000,v116\n"
"v_add_f32 v136,v136,v160\nv_add_f32 v137,v137,v116\nv_lshlrev_b32 v161,16,v117\nv_and_b32 v117,0xffff0000,v117\nv_add_f32 v138,v138,v161\nv_add_f32 v139,v139,v117\nv_lshlrev_b32 v160,16,v118\nv_and_b32 v118,0xffff0000,v118\nv_add_f32 v140,v140,v160\nv_add_f32 v141,v141,v118\nv_lshlrev_b32 v161,16,v119\nv_and_b32 v119,0xffff0000,v119\nv_add_f32 v142,v142,v161\nv_add_f32 v143,v143,v119\ns_waitcnt vmcnt(1)\n"
"v_lshlrev_b32 v160,16,v120\nv_and_b32 v120,0xffff0000,v120\nv_add_f32 v128,v128,v160\nv_add_f32 v129,v129,v120\nv_lshlrev_b32 v161,16,v121\nv_and_b32 v121,0xffff0000,v121\nv_add_f32 v130,v130,v161\nv_add_f32 v131,v131,v121\nv_lshlrev_b32 v160,16,v122\nv_and_b32 v122,0xffff0000,v122\nv_add_f32 v132,v132,v160\nv_add_f32 v133,v133,v122\nv_lshlrev_b32 v161,16,v123\nv_and_b32 v123,0xffff0000,v123\nv_add_f32 v134,v134,v161\n"
"v_add_f32 v135,v135,v123\ns_waitcnt vmcnt(0)\nv_lshlrev_b32 v160,16,v124\nv_and_b32 v124,0xffff0000,v124\nv_add_f32 v136,v136,v160\nv_add_f32 v137,v137,v124\nv_lshlrev_b32 v161,16,v125\nv_and_b32 v125,0xffff0000,v125\nv_add_f32 v138,v138,v161\nv_add_f32 v139,v139,v125\nv_lshlrev_b32 v160,16,v126\nv_and_b32 v126,0xffff0000,v126\nv_add_f32 v140,v140,v160\nv_add_f32 v141,v141,v126\nv_lshlrev_b32 v161,16,v127\n"
"v_and_b32 v127,0xffff0000,v127\nv_add_f32 v142,v142,v161\nv_add_f32 v143,v143,v127\nv_cvt_pk_bf16_f32 v144,v128,v129\nv_cvt_pk_bf16_f32 v145,v130,v131\nv_cvt_pk_bf16_f32 v146,v132,v133\nv_cvt_pk_bf16_f32 v147,v134,v135\nv_cvt_pk_bf16_f32 v148,v136,v137\nv_cvt_pk_bf16_f32 v149,v138,v139\nv_cvt_pk_bf16_f32 v150,v140,v141\nv_cvt_pk_bf16_f32 v151,v142,v143\ns_add_u32 s7,s67,104857600\nbuffer_store_dwordx4 v[144:147],v167,s[8:11],s7 offen sc1\n"
"s_add_u32 s7,s88,104857600\nbuffer_store_dwordx4 v[148:151],v167,s[8:11],s7 offen sc1\nv_lshlrev_b32 v128,16,v144\nv_and_b32 v129,0xffff0000,v144\nv_lshlrev_b32 v130,16,v145\nv_and_b32 v131,0xffff0000,v145\nv_lshlrev_b32 v132,16,v146\nv_and_b32 v133,0xffff0000,v146\nv_lshlrev_b32 v134,16,v147\nv_and_b32 v135,0xffff0000,v147\nv_lshlrev_b32 v136,16,v148\nv_and_b32 v137,0xffff0000,v148\nv_lshlrev_b32 v138,16,v149\n"
"v_and_b32 v139,0xffff0000,v149\nv_lshlrev_b32 v140,16,v150\nv_and_b32 v141,0xffff0000,v150\nv_lshlrev_b32 v142,16,v151\nv_and_b32 v143,0xffff0000,v151\nv_add_f32 v160,v128,v129\nv_add_f32 v160,v160,v130\nv_add_f32 v160,v160,v131\nv_add_f32 v160,v160,v132\nv_add_f32 v160,v160,v133\nv_add_f32 v160,v160,v134\nv_add_f32 v160,v160,v135\nv_add_f32 v160,v160,v136\nv_add_f32 v160,v160,v137\nv_add_f32 v160,v160,v138\n"
"v_add_f32 v160,v160,v139\nv_add_f32 v160,v160,v140\nv_add_f32 v160,v160,v141\nv_add_f32 v160,v160,v142\nv_add_f32 v160,v160,v143\ns_nop 1\nv_add_f32_dpp v160,v160,v160 row_ror:8 row_mask:0xf bank_mask:0xf\nv_mov_b32 v162,v160\ns_nop 1\nv_permlane16_swap_b32 v162,v160\nv_add_f32 v160,v160,v162\nv_mov_b32 v162,v160\ns_nop 1\nv_permlane32_swap_b32 v162,v160\nv_add_f32 v160,v160,v162\nv_mul_f32 v160,s72,v160\n"
"v_cndmask_b32 v163,0,v160,s[74:75]\nv_sub_f32 v0,v128,v160\nv_sub_f32 v1,v129,v160\nv_sub_f32 v2,v130,v160\nv_sub_f32 v3,v131,v160\nv_sub_f32 v4,v132,v160\nv_sub_f32 v5,v133,v160\nv_sub_f32 v6,v134,v160\nv_sub_f32 v7,v135,v160\nv_sub_f32 v8,v136,v163\nv_sub_f32 v9,v137,v163\nv_sub_f32 v10,v138,v163\nv_sub_f32 v11,v139,v163\nv_sub_f32 v12,v140,v163\nv_sub_f32 v13,v141,v163\nv_sub_f32 v14,v142,v163\n"
"v_sub_f32 v15,v143,v163\nv_mul_f32 v161,v0,v0\nv_fmac_f32 v161,v1,v1\nv_fmac_f32 v161,v2,v2\nv_fmac_f32 v161,v3,v3\nv_fmac_f32 v161,v4,v4\nv_fmac_f32 v161,v5,v5\nv_fmac_f32 v161,v6,v6\nv_fmac_f32 v161,v7,v7\nv_fmac_f32 v161,v8,v8\nv_fmac_f32 v161,v9,v9\nv_fmac_f32 v161,v10,v10\nv_fmac_f32 v161,v11,v11\nv_fmac_f32 v161,v12,v12\nv_fmac_f32 v161,v13,v13\nv_fmac_f32 v161,v14,v14\nv_fmac_f32 v161,v15,v15\n"
"s_nop 1\nv_add_f32_dpp v161,v161,v161 row_ror:8 row_mask:0xf bank_mask:0xf\nv_mov_b32 v162,v161\ns_nop 1\nv_permlane16_swap_b32 v162,v161\nv_add_f32 v161,v161,v162\nv_mov_b32 v162,v161\ns_nop 1\nv_permlane32_swap_b32 v162,v161\nv_add_f32 v161,v161,v162\ns_lshl_b32 s7,s14,6\nv_add_u32 v162,s7,v168\nds_write_b64 v162,v[160:161]\ns_waitcnt lgkmcnt(0)\ns_barrier\nds_read_b64 v[0:1],v168 offset:0\nds_read_b64 v[2:3],v168 offset:64\n"
"ds_read_b64 v[4:5],v168 offset:128\nds_read_b64 v[6:7],v168 offset:192\ns_waitcnt lgkmcnt(2)\nv_add_f32 v160,v0,v2\ns_waitcnt lgkmcnt(1)\nv_add_f32 v160,v160,v4\ns_waitcnt lgkmcnt(0)\nv_fmac_f32 v160,0.5,v6\nv_mul_f32 v160,0x3e924925,v160\nv_add_f32 v161,v1,v3\nv_add_f32 v162,v5,v7\nv_add_f32 v161,v161,v162\nv_sub_f32 v0,v0,v160\nv_sub_f32 v2,v2,v160\nv_sub_f32 v4,v4,v160\nv_sub_f32 v6,v6,v160\nv_mul_f32 v162,v6,v6\n"
"v_mul_f32 v162,0.5,v162\nv_fmac_f32 v162,v0,v0\nv_fmac_f32 v162,v2,v2\nv_fmac_f32 v162,v4,v4\nv_fmac_f32 v161,0x43000000,v162\nv_mov_b32 v162,0x3727c5ac\nv_fmac_f32 v162,0x3b124925,v161\nv_rsq_f32 v162,v162\nv_sub_f32 v128,v128,v160\nv_sub_f32 v129,v129,v160\nv_sub_f32 v130,v130,v160\nv_sub_f32 v131,v131,v160\nv_sub_f32 v132,v132,v160\nv_sub_f32 v133,v133,v160\nv_sub_f32 v134,v134,v160\nv_sub_f32 v135,v135,v160\n"
"v_sub_f32 v136,v136,v160\nv_sub_f32 v137,v137,v160\nv_sub_f32 v138,v138,v160\nv_sub_f32 v139,v139,v160\nv_sub_f32 v140,v140,v160\nv_sub_f32 v141,v141,v160\nv_sub_f32 v142,v142,v160\nv_sub_f32 v143,v143,v160\nv_mul_f32 v128,v162,v128\nv_mul_f32 v129,v162,v129\nv_mul_f32 v130,v162,v130\nv_mul_f32 v131,v162,v131\nv_mul_f32 v132,v162,v132\nv_mul_f32 v133,v162,v133\nv_mul_f32 v134,v162,v134\nv_mul_f32 v135,v162,v135\n"
"v_mul_f32 v136,v162,v136\nv_mul_f32 v137,v162,v137\nv_mul_f32 v138,v162,v138\nv_mul_f32 v139,v162,v139\nv_mul_f32 v140,v162,v140\nv_mul_f32 v141,v162,v141\nv_mul_f32 v142,v162,v142\nv_mul_f32 v143,v162,v143\nv_cvt_pk_bf16_f32 v152,v128,v129\nv_cvt_pk_bf16_f32 v153,v130,v131\nv_cvt_pk_bf16_f32 v154,v132,v133\nv_cvt_pk_bf16_f32 v155,v134,v135\nv_cvt_pk_bf16_f32 v156,v136,v137\nv_cvt_pk_bf16_f32 v157,v138,v139\n"
"v_cvt_pk_bf16_f32 v158,v140,v141\nv_cvt_pk_bf16_f32 v159,v142,v143\ns_add_u32 s7,s67,102760448\nbuffer_store_dwordx4 v[152:155],v167,s[8:11],s7 offen sc1\ns_add_u32 s7,s88,102760448\nbuffer_store_dwordx4 v[156:159],v167,s[8:11],s7 offen sc1\ns_lshl_b32 s7,s58,7\ns_add_u32 s64,s7,117538816\ns_lshl3_add_u32 s7,s56,s14\ns_mul_i32 s65,s7,14336\ns_add_u32 s65,s65,102760448\ns_add_u32 s66,s65,57344\ns_mul_i32 s67,s7,57344\n"
"s_mul_i32 s7,s57,3584\ns_add_u32 s67,s67,s7\ns_add_u32 s67,s67,106954752\ns_add_u32 s68,s67,229376\nv_lshlrev_b32 v200,3,v253\ns_waitcnt vmcnt(0)\ns_barrier\ns_cmp_lg_u32 s14,0\ns_cbranch_scc1 L_fused_block_nf_55\nbuffer_store_dwordx2 v[250:251],off,s[8:11],s64 sc1\nL_fused_block_nf_55:\nds_read_b128 a[224:227],v196 offset:16384\nds_read_b128 a[228:231],v196 offset:17408\nds_read_b128 a[232:235],v196 offset:18432\n"
"ds_read_b128 a[236:239],v196 offset:19456\nds_read_b128 a[240:243],v196 offset:20480\nds_read_b128 a[244:247],v196 offset:21504\nds_read_b128 a[248:251],v196 offset:22528\ns_lshl_b32 s7,s56,11\ns_add_u32 s84,s7,117538816\ns_cmp_lg_u32 s14,0\ns_cbranch_scc1 L_fused_block_np_56\nv_lshlrev_b32 v242,7,v253\ns_mov_b64 exec,0xffff\ns_mov_b32 s7,0\nL_fused_block_poll_57:\nbuffer_load_dwordx2 v[240:241],v242,s[8:11],s84 offen sc1\n"
"s_add_u32 s7,s7,1\ns_waitcnt vmcnt(0)\nv_cmp_eq_u64 vcc,s[12:13],v[240:241]\ns_nop 1\ns_cmp_eq_u64 vcc,exec\ns_cbranch_scc1 L_fused_block_polld_58\ns_cmp_lt_u32 s7,131072\ns_cbranch_scc1 L_fused_block_poll_57\nL_fused_block_polld_58:\ns_mov_b64 exec,-1\nL_fused_block_np_56:\ns_barrier\nbuffer_load_dwordx4 v[112:115],v252,s[8:11],s65 offen offset:0 sc1\nbuffer_load_dwordx4 v[116:119],v252,s[8:11],s66 offen offset:0 sc1\n"
"buffer_load_dwordx4 v[120:123],v252,s[8:11],s65 offen offset:1024 sc1\nbuffer_load_dwordx4 v[124:127],v252,s[8:11],s66 offen offset:1024 sc1\nbuffer_load_dwordx4 v[128:131],v252,s[8:11],s65 offen offset:2048 sc1\nbuffer_load_dwordx4 v[132:135],v252,s[8:11],s66 offen offset:2048 sc1\nbuffer_load_dwordx4 v[136:139],v252,s[8:11],s65 offen offset:3072 sc1\ns_add_u32 s65,s65,0x1000\nbuffer_load_dwordx4 v[140:143],v252,s[8:11],s66 offen offset:3072 sc1\n"
"s_add_u32 s66,s66,0x1000\nbuffer_load_dwordx4 v[144:147],v252,s[8:11],s65 offen offset:0 sc1\nbuffer_load_dwordx4 v[148:151],v252,s[8:11],s66 offen offset:0 sc1\ns_waitcnt vmcnt(8)\ns_waitcnt lgkmcnt(5)\nv_mfma_f32_16x16x32_bf16 v[0:3],a[224:227],v[112:115],0\nds_read_b128 a[252:255],v196 offset:23552\nv_mfma_f32_16x16x32_bf16 v[4:7],a[224:227],v[116:119],0\nbuffer_load_dwordx4 v[152:155],v252,s[8:11],s65 offen offset:1024 sc1\n"
"v_mfma_f32_16x16x32_bf16 v[12:15],a[228:231],v[116:119],0\nds_read_b128 a[224:227],v196 offset:24576\nv_mfma_f32_16x16x32_bf16 v[8:11],a[228:231],v[112:115],0\ns_waitcnt lgkmcnt(5)\nv_mfma_f32_16x16x32_bf16 v[16:19],a[232:235],v[112:115],0\nds_read_b128 a[228:231],v196 offset:25600\nv_mfma_f32_16x16x32_bf16 v[20:23],a[232:235],v[116:119],0\nbuffer_load_dwordx4 v[156:159],v252,s[8:11],s66 offen offset:1024 sc1\n"
"v_mfma_f32_16x16x32_bf16 v[28:31],a[236:239],v[116:119],0\nds_read_b128 a[232:235],v196 offset:26624\nv_mfma_f32_16x16x32_bf16 v[24:27],a[236:239],v[112:115],0\ns_waitcnt lgkmcnt(5)\nv_mfma_f32_16x16x32_bf16 v[32:35],a[240:243],v[112:115],0\nds_read_b128 a[236:239],v196 offset:27648\nv_mfma_f32_16x16x32_bf16 v[36:39],a[240:243],v[116:119],0\nbuffer_load_dwordx4 v[160:163],v252,s[8:11],s65 offen offset:2048 sc1\n"
"v_mfma_f32_16x16x32_bf16 v[44:47],a[244:247],v[116:119],0\nds_read_b128 a[240:243],v196 offset:28672\nv_mfma_f32_16x16x32_bf16 v[40:43],a[244:247],v[112:115],0\ns_waitcnt lgkmcnt(5)\nv_mfma_f32_16x16x32_bf16 v[48:51],a[248:251],v[112:115],0\nds_read_b128 a[244:247],v196 offset:29696\nv_mfma_f32_16x16x32_bf16 v[52:55],a[248:251],v[116:119],0\nbuffer_load_dwordx4 v[164:167],v252,s[8:11],s66 offen offset:2048 sc1\n"
"v_mfma_f32_16x16x32_bf16 v[60:63],a[252:255],v[116:119],0\nds_read_b128 a[248:251],v197 offset:16384\nv_mfma_f32_16x16x32_bf16 v[56:59],a[252:255],v[112:115],0\ns_waitcnt lgkmcnt(5)\nv_mfma_f32_16x16x32_bf16 v[64:67],a[224:227],v[112:115],0\nds_read_b128 a[252:255],v197 offset:17408\nv_mfma_f32_16x16x32_bf16 v[68:71],a[224:227],v[116:119],0\nbuffer_load_dwordx4 v[168:171],v252,s[8:11],s65 offen offset:3072 sc1\n"
"v_mfma_f32_16x16x32_bf16 v[76:79],a[228:231],v[116:119],0\nds_read_b128 a[224:227],v197 offset:18432\nv_mfma_f32_16x16x32_bf16 v[72:75],a[228:231],v[112:115],0\ns_waitcnt lgkmcnt(5)\nv_mfma_f32_16x16x32_bf16 v[80:83],a[232:235],v[112:115],0\ns_add_u32 s65,s65,0x1000\nds_read_b128 a[228:231],v197 offset:19456\nv_mfma_f32_16x16x32_bf16 v[84:87],a[232:235],v[116:119],0\nbuffer_load_dwordx4 v[172:175],v252,s[8:11],s66 offen offset:3072 sc1\n"
"v_mfma_f32_16x16x32_bf16 v[92:95],a[236:239],v[116:119],0\nds_read_b128 a[232:235],v197 offset:20480\nv_mfma_f32_16x16x32_bf16 v[88:91],a[236:239],v[112:115],0\ns_waitcnt lgkmcnt(5)\nv_mfma_f32_16x16x32_bf16 v[96:99],a[240:243],v[112:115],0\ns_add_u32 s66,s66,0x1000\nds_read_b128 a[236:239],v197 offset:21504\nv_mfma_f32_16x16x32_bf16 v[100:103],a[240:243],v[116:119],0\nbuffer_load_dwordx4 v[176:179],v252,s[8:11],s65 offen offset:0 sc1\n"
"v_mfma_f32_16x16x32_bf16 v[108:111],a[244:247],v[116:119],0\ns_waitcnt vmcnt(13)\nds_read_b128 a[240:243],v197 offset:22528\nv_mfma_f32_16x16x32_bf16 v[104:107],a[244:247],v[112:115],0\ns_waitcnt lgkmcnt(5)\nv_mfma_f32_16x16x32_bf16 v[0:3],a[248:251],v[120:123],v[0:3]\nds_read_b128 a[244:247],v197 offset:23552\nv_mfma_f32_16x16x32_bf16 v[4:7],a[248:251],v[124:127],v[4:7]\nbuffer_load_dwordx4 v[180:183],v252,s[8:11],s66 offen offset:0 sc1\n"
"v_mfma_f32_16x16x32_bf16 v[12:15],a[252:255],v[124:127],v[12:15]\nds_read_b128 a[248:251],v197 offset:24576\nv_mfma_f32_16x16x32_bf16 v[8:11],a[252:255],v[120:123],v[8:11]\ns_waitcnt lgkmcnt(5)\nv_mfma_f32_16x16x32_bf16 v[16:19],a[224:227],v[120:123],v[16:19]\nds_read_b128 a[252:255],v197 offset:25600\nv_mfma_f32_16x16x32_bf16 v[20:23],a[224:227],v[124:127],v[20:23]\nbuffer_load_dwordx4 v[184:187],v252,s[8:11],s65 offen offset:1024 sc1\n"
"v_mfma_f32_16x16x32_bf16 v[28:31],a[228:231],v[124:127],v[28:31]\nds_read_b128 a[224:227],v197 offset:26624\nv_mfma_f32_16x16x32_bf16 v[24:27],a[228:231],v[120:123],v[24:27]\ns_waitcnt lgkmcnt(5)\nv_mfma_f32_16x16x32_bf16 v[32:35],a[232:235],v[120:123],v[32:35]\nds_read_b128 a[228:231],v197 offset:27648\nv_mfma_f32_16x16x32_bf16 v[36:39],a[232:235],v[124:127],v[36:39]\nbuffer_load_dwordx4 v[188:191],v252,s[8:11],s66 offen offset:1024 sc1\n"
"v_mfma_f32_16x16x32_bf16 v[44:47],a[236:239],v[124:127],v[44:47]\nds_read_b128 a[232:235],v197 offset:28672\nv_mfma_f32_16x16x32_bf16 v[40:43],a[236:239],v[120:123],v[40:43]\ns_waitcnt lgkmcnt(5)\nv_mfma_f32_16x16x32_bf16 v[48:51],a[240:243],v[120:123],v[48:51]\nds_read_b128 a[236:239],v197 offset:29696\nv_mfma_f32_16x16x32_bf16 v[52:55],a[240:243],v[124:127],v[52:55]\ns_barrier\nbuffer_load_dwordx4 v[202:205],v252,s[8:11],s65 offen offset:2048 sc1\n"
"v_xor_b32 v196,0x8000,v196\nv_mfma_f32_16x16x32_bf16 v[60:63],a[244:247],v[124:127],v[60:63]\ns_add_u32 m0,s32,s15\nds_read_b128 a[240:243],v196 offset:0\nv_mfma_f32_16x16x32_bf16 v[56:59],a[244:247],v[120:123],v[56:59]\ns_waitcnt lgkmcnt(5)\nv_xor_b32 v197,0x8000,v197\nv_mfma_f32_16x16x32_bf16 v[64:67],a[248:251],v[120:123],v[64:67]\nds_read_b128 a[244:247],v196 offset:1024\nv_mfma_f32_16x16x32_bf16 v[68:71],a[248:251],v[124:127],v[68:71]\n"
"buffer_load_dwordx4 v[206:209],v252,s[8:11],s66 offen offset:2048 sc1\nv_mfma_f32_16x16x32_bf16 v[76:79],a[252:255],v[124:127],v[76:79]\nds_read_b128 a[248:251],v196 offset:2048\nv_mfma_f32_16x16x32_bf16 v[72:75],a[252:255],v[120:123],v[72:75]\ns_waitcnt lgkmcnt(5)\nbuffer_load_dwordx4 v252,s[8:11],s59 offen sc1 lds\nv_mfma_f32_16x16x32_bf16 v[80:83],a[224:227],v[120:123],v[80:83]\nds_read_b128 a[252:255],v196 offset:3072\n"
"v_mfma_f32_16x16x32_bf16 v[84:87],a[224:227],v[124:127],v[84:87]\nbuffer_load_dwordx4 v[210:213],v252,s[8:11],s65 offen offset:3072 sc1\nv_mfma_f32_16x16x32_bf16 v[92:95],a[228:231],v[124:127],v[92:95]\nds_read_b128 a[224:227],v196 offset:4096\nv_mfma_f32_16x16x32_bf16 v[88:91],a[228:231],v[120:123],v[88:91]\ns_waitcnt lgkmcnt(5)\nv_mfma_f32_16x16x32_bf16 v[96:99],a[232:235],v[120:123],v[96:99]\ns_add_u32 s65,s65,0x1000\n"
"ds_read_b128 a[228:231],v196 offset:5120\nv_mfma_f32_16x16x32_bf16 v[100:103],a[232:235],v[124:127],v[100:103]\nbuffer_load_dwordx4 v[214:217],v252,s[8:11],s66 offen offset:3072 sc1\nv_mfma_f32_16x16x32_bf16 v[108:111],a[236:239],v[124:127],v[108:111]\ns_waitcnt vmcnt(19)\nds_read_b128 a[232:235],v196 offset:6144\nv_mfma_f32_16x16x32_bf16 v[104:107],a[236:239],v[120:123],v[104:107]\ns_waitcnt lgkmcnt(5)\n"
"buffer_load_dwordx4 v252,s[8:11],s59 offen offset:1024 sc1 lds\nv_mfma_f32_16x16x32_bf16 v[0:3],a[240:243],v[128:131],v[0:3]\ns_add_u32 s66,s66,0x1000\nds_read_b128 a[236:239],v196 offset:7168\nv_mfma_f32_16x16x32_bf16 v[4:7],a[240:243],v[132:135],v[4:7]\nbuffer_load_dwordx4 v[218:221],v252,s[8:11],s65 offen offset:0 sc1\nv_mfma_f32_16x16x32_bf16 v[12:15],a[244:247],v[132:135],v[12:15]\nds_read_b128 a[240:243],v196 offset:8192\n"
"v_mfma_f32_16x16x32_bf16 v[8:11],a[244:247],v[128:131],v[8:11]\ns_waitcnt lgkmcnt(5)\nv_mfma_f32_16x16x32_bf16 v[16:19],a[248:251],v[128:131],v[16:19]\nds_read_b128 a[244:247],v196 offset:9216\nv_mfma_f32_16x16x32_bf16 v[20:23],a[248:251],v[132:135],v[20:23]\nbuffer_load_dwordx4 v[222:225],v252,s[8:11],s66 offen offset:0 sc1\nv_mfma_f32_16x16x32_bf16 v[28:31],a[252:255],v[132:135],v[28:31]\nds_read_b128 a[248:251],v196 offset:10240\n"
"v_mfma_f32_16x16x32_bf16 v[24:27],a[252:255],v[128:131],v[24:27]\ns_waitcnt lgkmcnt(5)\nbuffer_load_dwordx4 v252,s[8:11],s59 offen offset:2048 sc1 lds\nv_mfma_f32_16x16x32_bf16 v[32:35],a[224:227],v[128:131],v[32:35]\nds_read_b128 a[252:255],v196 offset:11264\nv_mfma_f32_16x16x32_bf16 v[36:39],a[224:227],v[132:135],v[36:39]\nbuffer_load_dwordx4 v[226:229],v252,s[8:11],s65 offen offset:1024 sc1\nv_mfma_f32_16x16x32_bf16 v[44:47],a[228:231],v[132:135],v[44:47]\n"
"ds_read_b128 a[224:227],v196 offset:12288\nv_mfma_f32_16x16x32_bf16 v[40:43],a[228:231],v[128:131],v[40:43]\ns_waitcnt lgkmcnt(5)\nv_mfma_f32_16x16x32_bf16 v[48:51],a[232:235],v[128:131],v[48:51]\nds_read_b128 a[228:231],v196 offset:13312\nv_mfma_f32_16x16x32_bf16 v[52:55],a[232:235],v[132:135],v[52:55]\nbuffer_load_dwordx4 v[230:233],v252,s[8:11],s66 offen offset:1024 sc1\nv_mfma_f32_16x16x32_bf16 v[60:63],a[236:239],v[132:135],v[60:63]\n"
"ds_read_b128 a[232:235],v197 offset:0\nv_mfma_f32_16x16x32_bf16 v[56:59],a[236:239],v[128:131],v[56:59]\ns_waitcnt lgkmcnt(5)\nbuffer_load_dwordx4 v252,s[8:11],s59 offen offset:3072 sc1 lds\nv_mfma_f32_16x16x32_bf16 v[64:67],a[240:243],v[128:131],v[64:67]\ns_add_u32 s59,s59,458752\nds_read_b128 a[236:239],v197 offset:1024\nv_mfma_f32_16x16x32_bf16 v[68:71],a[240:243],v[132:135],v[68:71]\nv_mfma_f32_16x16x32_bf16 v[76:79],a[244:247],v[132:135],v[76:79]\n"
"ds_read_b128 a[240:243],v197 offset:2048\nv_mfma_f32_16x16x32_bf16 v[72:75],a[244:247],v[128:131],v[72:75]\ns_waitcnt lgkmcnt(5)\nv_mfma_f32_16x16x32_bf16 v[80:83],a[248:251],v[128:131],v[80:83]\ns_add_u32 s32,s32,0x4000\nds_read_b128 a[244:247],v197 offset:3072\ns_and_b32 s32,s32,0xffff\nv_mfma_f32_16x16x32_bf16 v[84:87],a[248:251],v[132:135],v[84:87]\nv_mfma_f32_16x16x32_bf16 v[92:95],a[252:255],v[132:135],v[92:95]\n"
"ds_read_b128 a[248:251],v197 offset:4096\nv_mfma_f32_16x16x32_bf16 v[88:91],a[252:255],v[128:131],v[88:91]\ns_waitcnt lgkmcnt(5)\nv_mfma_f32_16x16x32_bf16 v[96:99],a[224:227],v[128:131],v[96:99]\nds_read_b128 a[252:255],v197 offset:5120\nv_mfma_f32_16x16x32_bf16 v[100:103],a[224:227],v[132:135],v[100:103]\nbuffer_load_dwordx4 a[96:99],v252,s[8:11],s61 offen offset:0 sc1\nv_mfma_f32_16x16x32_bf16 v[108:111],a[228:231],v[132:135],v[108:111]\n"
"s_waitcnt vmcnt(25)\nds_read_b128 a[224:227],v197 offset:6144\nv_mfma_f32_16x16x32_bf16 v[104:107],a[228:231],v[128:131],v[104:107]\ns_waitcnt lgkmcnt(5)\nv_mfma_f32_16x16x32_bf16 v[0:3],a[232:235],v[136:139],v[0:3]\nds_read_b128 a[228:231],v197 offset:7168\nv_mfma_f32_16x16x32_bf16 v[4:7],a[232:235],v[140:143],v[4:7]\nv_mfma_f32_16x16x32_bf16 v[12:15],a[236:239],v[140:143],v[12:15]\nds_read_b128 a[232:235],v197 offset:8192\n"
"v_mfma_f32_16x16x32_bf16 v[8:11],a[236:239],v[136:139],v[8:11]\ns_waitcnt lgkmcnt(5)\nv_mfma_f32_16x16x32_bf16 v[16:19],a[240:243],v[136:139],v[16:19]\nds_read_b128 a[236:239],v197 offset:9216\nv_mfma_f32_16x16x32_bf16 v[20:23],a[240:243],v[140:143],v[20:23]\nbuffer_load_dwordx4 a[100:103],v252,s[8:11],s61 offen offset:1024 sc1\nv_mfma_f32_16x16x32_bf16 v[28:31],a[244:247],v[140:143],v[28:31]\nds_read_b128 a[240:243],v197 offset:10240\n"
"v_mfma_f32_16x16x32_bf16 v[24:27],a[244:247],v[136:139],v[24:27]\ns_waitcnt lgkmcnt(5)\nv_mfma_f32_16x16x32_bf16 v[32:35],a[248:251],v[136:139],v[32:35]\nds_read_b128 a[244:247],v197 offset:11264\nv_mfma_f32_16x16x32_bf16 v[36:39],a[248:251],v[140:143],v[36:39]\nv_mfma_f32_16x16x32_bf16 v[44:47],a[252:255],v[140:143],v[44:47]\nds_read_b128 a[248:251],v197 offset:12288\nv_mfma_f32_16x16x32_bf16 v[40:43],a[252:255],v[136:139],v[40:43]\n"
"s_waitcnt lgkmcnt(5)\nv_mfma_f32_16x16x32_bf16 v[48:51],a[224:227],v[136:139],v[48:51]\nds_read_b128 a[252:255],v197 offset:13312\nv_mfma_f32_16x16x32_bf16 v[52:55],a[224:227],v[140:143],v[52:55]\ns_barrier\nbuffer_load_dwordx4 a[104:107],v252,s[8:11],s61 offen offset:2048 sc1\nv_mfma_f32_16x16x32_bf16 v[60:63],a[228:231],v[140:143],v[60:63]\ns_add_u32 m0,s32,s15\nds_read_b128 a[224:227],v196 offset:16384\n"
"v_mfma_f32_16x16x32_bf16 v[56:59],a[228:231],v[136:139],v[56:59]\ns_waitcnt lgkmcnt(5)\nbuffer_load_dwordx4 v252,s[8:11],s59 offen sc1 lds\nv_mfma_f32_16x16x32_bf16 v[64:67],a[232:235],v[136:139],v[64:67]\nds_read_b128 a[228:231],v196 offset:17408\nv_mfma_f32_16x16x32_bf16 v[68:71],a[232:235],v[140:143],v[68:71]\nv_mfma_f32_16x16x32_bf16 v[76:79],a[236:239],v[140:143],v[76:79]\nds_read_b128 a[232:235],v196 offset:18432\n"
"v_mfma_f32_16x16x32_bf16 v[72:75],a[236:239],v[136:139],v[72:75]\ns_waitcnt lgkmcnt(5)\nv_mfma_f32_16x16x32_bf16 v[80:83],a[240:243],v[136:139],v[80:83]\nds_read_b128 a[236:239],v196 offset:19456\nv_mfma_f32_16x16x32_bf16 v[84:87],a[240:243],v[140:143],v[84:87]\nbuffer_load_dwordx4 v252,s[8:11],s59 offen offset:1024 sc1 lds\nv_mfma_f32_16x16x32_bf16 v[92:95],a[244:247],v[140:143],v[92:95]\nds_read_b128 a[240:243],v196 offset:20480\n"
"v_mfma_f32_16x16x32_bf16 v[88:91],a[244:247],v[136:139],v[88:91]\ns_waitcnt lgkmcnt(5)\nbuffer_load_dwordx4 a[108:111],v252,s[8:11],s61 offen offset:3072 sc1\nv_mfma_f32_16x16x32_bf16 v[96:99],a[248:251],v[136:139],v[96:99]\ns_add_u32 s61,s61,0x1000\nds_read_b128 a[244:247],v196 offset:21504\nv_mfma_f32_16x16x32_bf16 v[100:103],a[248:251],v[140:143],v[100:103]\nv_mfma_f32_16x16x32_bf16 v[108:111],a[252:255],v[140:143],v[108:111]\n"
"s_waitcnt vmcnt(28)\nds_read_b128 a[248:251],v196 offset:22528\nv_mfma_f32_16x16x32_bf16 v[104:107],a[252:255],v[136:139],v[104:107]\ns_waitcnt lgkmcnt(5)\nbuffer_load_dwordx4 v252,s[8:11],s59 offen offset:2048 sc1 lds\nv_mfma_f32_16x16x32_bf16 v[0:3],a[224:227],v[144:147],v[0:3]\nds_read_b128 a[252:255],v196 offset:23552\nv_mfma_f32_16x16x32_bf16 v[4:7],a[224:227],v[148:151],v[4:7]\nv_mfma_f32_16x16x32_bf16 v[12:15],a[228:231],v[148:151],v[12:15]\n"
"ds_read_b128 a[224:227],v196 offset:24576\nv_mfma_f32_16x16x32_bf16 v[8:11],a[228:231],v[144:147],v[8:11]\ns_waitcnt lgkmcnt(5)\nbuffer_load_dwordx4 a[112:115],v252,s[8:11],s61 offen offset:0 sc1\nv_mfma_f32_16x16x32_bf16 v[16:19],a[232:235],v[144:147],v[16:19]\nds_read_b128 a[228:231],v196 offset:25600\nv_mfma_f32_16x16x32_bf16 v[20:23],a[232:235],v[148:151],v[20:23]\nbuffer_load_dwordx4 v252,s[8:11],s59 offen offset:3072 sc1 lds\n"
"v_mfma_f32_16x16x32_bf16 v[28:31],a[236:239],v[148:151],v[28:31]\ns_add_u32 s59,s59,458752\nds_read_b128 a[232:235],v196 offset:26624\nv_mfma_f32_16x16x32_bf16 v[24:27],a[236:239],v[144:147],v[24:27]\ns_waitcnt lgkmcnt(5)\nv_mfma_f32_16x16x32_bf16 v[32:35],a[240:243],v[144:147],v[32:35]\nds_read_b128 a[236:239],v196 offset:27648\nv_mfma_f32_16x16x32_bf16 v[36:39],a[240:243],v[148:151],v[36:39]\nv_mfma_f32_16x16x32_bf16 v[44:47],a[244:247],v[148:151],v[44:47]\n"
"s_add_u32 s7,s32,0xc000\nds_read_b128 a[240:243],v196 offset:28672\ns_and_b32 s7,s7,0xffff\nv_mfma_f32_16x16x32_bf16 v[40:43],a[244:247],v[144:147],v[40:43]\ns_waitcnt lgkmcnt(5)\ns_add_u32 s7,s7,s15\ns_add_u32 m0,s7,0x10000\nv_mfma_f32_16x16x32_bf16 v[48:51],a[248:251],v[144:147],v[48:51]\nds_read_b128 a[244:247],v196 offset:29696\nv_mfma_f32_16x16x32_bf16 v[52:55],a[248:251],v[148:151],v[52:55]\n"
"buffer_load_dwordx4 v252,s[8:11],s60 offen sc1 lds\nv_mfma_f32_16x16x32_bf16 v[60:63],a[252:255],v[148:151],v[60:63]\nds_read_b128 a[248:251],v197 offset:16384\nv_mfma_f32_16x16x32_bf16 v[56:59],a[252:255],v[144:147],v[56:59]\ns_waitcnt lgkmcnt(5)\nbuffer_load_dwordx4 a[116:119],v252,s[8:11],s61 offen offset:1024 sc1\nv_mfma_f32_16x16x32_bf16 v[64:67],a[224:227],v[144:147],v[64:67]\nds_read_b128 a[252:255],v197 offset:17408\n"
"v_mfma_f32_16x16x32_bf16 v[68:71],a[224:227],v[148:151],v[68:71]\nv_mfma_f32_16x16x32_bf16 v[76:79],a[228:231],v[148:151],v[76:79]\nds_read_b128 a[224:227],v197 offset:18432\nv_mfma_f32_16x16x32_bf16 v[72:75],a[228:231],v[144:147],v[72:75]\ns_waitcnt lgkmcnt(5)\nbuffer_load_dwordx4 v252,s[8:11],s60 offen offset:1024 sc1 lds\nv_mfma_f32_16x16x32_bf16 v[80:83],a[232:235],v[144:147],v[80:83]\nds_read_b128 a[228:231],v197 offset:19456\n"
"v_mfma_f32_16x16x32_bf16 v[84:87],a[232:235],v[148:151],v[84:87]\nv_mfma_f32_16x16x32_bf16 v[92:95],a[236:239],v[148:151],v[92:95]\nds_read_b128 a[232:235],v197 offset:20480\nv_mfma_f32_16x16x32_bf16 v[88:91],a[236:239],v[144:147],v[88:91]\ns_waitcnt lgkmcnt(5)\nbuffer_load_dwordx4 a[120:123],v252,s[8:11],s61 offen offset:2048 sc1\nv_mfma_f32_16x16x32_bf16 v[96:99],a[240:243],v[144:147],v[96:99]\nds_read_b128 a[236:239],v197 offset:21504\n"
"v_mfma_f32_16x16x32_bf16 v[100:103],a[240:243],v[148:151],v[100:103]\nbuffer_load_dwordx4 v252,s[8:11],s60 offen offset:2048 sc1 lds\nv_mfma_f32_16x16x32_bf16 v[108:111],a[244:247],v[148:151],v[108:111]\ns_waitcnt vmcnt(34)\nds_read_b128 a[240:243],v197 offset:22528\nv_mfma_f32_16x16x32_bf16 v[104:107],a[244:247],v[144:147],v[104:107]\ns_waitcnt lgkmcnt(5)\nv_mfma_f32_16x16x32_bf16 v[0:3],a[248:251],v[152:155],v[0:3]\n"
"ds_read_b128 a[244:247],v197 offset:23552\nv_mfma_f32_16x16x32_bf16 v[4:7],a[248:251],v[156:159],v[4:7]\nv_mfma_f32_16x16x32_bf16 v[12:15],a[252:255],v[156:159],v[12:15]\nds_read_b128 a[248:251],v197 offset:24576\nv_mfma_f32_16x16x32_bf16 v[8:11],a[252:255],v[152:155],v[8:11]\ns_waitcnt lgkmcnt(5)\nbuffer_load_dwordx4 v252,s[8:11],s60 offen offset:3072 sc1 lds\nv_mfma_f32_16x16x32_bf16 v[16:19],a[224:227],v[152:155],v[16:19]\n"
"s_add_u32 s60,s60,458752\nds_read_b128 a[252:255],v197 offset:25600\nv_mfma_f32_16x16x32_bf16 v[20:23],a[224:227],v[156:159],v[20:23]\nbuffer_load_dwordx4 a[124:127],v252,s[8:11],s61 offen offset:3072 sc1\ns_add_u32 s61,s61,0x1000\nv_mfma_f32_16x16x32_bf16 v[28:31],a[228:231],v[156:159],v[28:31]\nds_read_b128 a[224:227],v197 offset:26624\nv_mfma_f32_16x16x32_bf16 v[24:27],a[228:231],v[152:155],v[24:27]\n"
"s_waitcnt lgkmcnt(5)\nv_mfma_f32_16x16x32_bf16 v[32:35],a[232:235],v[152:155],v[32:35]\ns_add_u32 s32,s32,0x4000\nds_read_b128 a[228:231],v197 offset:27648\ns_and_b32 s32,s32,0xffff\nv_mfma_f32_16x16x32_bf16 v[36:39],a[232:235],v[156:159],v[36:39]\nv_mfma_f32_16x16x32_bf16 v[44:47],a[236:239],v[156:159],v[44:47]\nds_read_b128 a[232:235],v197 offset:28672\nv_mfma_f32_16x16x32_bf16 v[40:43],a[236:239],v[152:155],v[40:43]\n"
"s_waitcnt lgkmcnt(5)\nv_mfma_f32_16x16x32_bf16 v[48:51],a[240:243],v[152:155],v[48:51]\nds_read_b128 a[236:239],v197 offset:29696\nv_mfma_f32_16x16x32_bf16 v[52:55],a[240:243],v[156:159],v[52:55]\ns_barrier\nv_xor_b32 v196,0x8000,v196\nv_mfma_f32_16x16x32_bf16 v[60:63],a[244:247],v[156:159],v[60:63]\ns_add_u32 m0,s32,s15\nds_read_b128 a[240:243],v196 offset:0\nv_mfma_f32_16x16x32_bf16 v[56:59],a[244:247],v[152:155],v[56:59]\n"
"s_waitcnt lgkmcnt(5)\nv_xor_b32 v197,0x8000,v197\nv_mfma_f32_16x16x32_bf16 v[64:67],a[248:251],v[152:155],v[64:67]\nds_read_b128 a[244:247],v196 offset:1024\nv_mfma_f32_16x16x32_bf16 v[68:71],a[248:251],v[156:159],v[68:71]\nbuffer_load_dwordx4 v252,s[8:11],s59 offen sc1 lds\nv_mfma_f32_16x16x32_bf16 v[76:79],a[252:255],v[156:159],v[76:79]\nds_read_b128 a[248:251],v196 offset:2048\nv_mfma_f32_16x16x32_bf16 v[72:75],a[252:255],v[152:155],v[72:75]\n"
"s_waitcnt lgkmcnt(5)\nbuffer_load_dwordx4 a[128:131],v252,s[8:11],s61 offen offset:0 sc1\nv_mfma_f32_16x16x32_bf16 v[80:83],a[224:227],v[152:155],v[80:83]\nds_read_b128 a[252:255],v196 offset:3072\nv_mfma_f32_16x16x32_bf16 v[84:87],a[224:227],v[156:159],v[84:87]\nv_mfma_f32_16x16x32_bf16 v[92:95],a[228:231],v[156:159],v[92:95]\nds_read_b128 a[224:227],v196 offset:4096\nv_mfma_f32_16x16x32_bf16 v[88:91],a[228:231],v[152:155],v[88:91]\n"
"s_waitcnt lgkmcnt(5)\nbuffer_load_dwordx4 v252,s[8:11],s59 offen offset:1024 sc1 lds\nv_mfma_f32_16x16x32_bf16 v[96:99],a[232:235],v[152:155],v[96:99]\nds_read_b128 a[228:231],v196 offset:5120\nv_mfma_f32_16x16x32_bf16 v[100:103],a[232:235],v[156:159],v[100:103]\nv_mfma_f32_16x16x32_bf16 v[108:111],a[236:239],v[156:159],v[108:111]\ns_waitcnt vmcnt(37)\nds_read_b128 a[232:235],v196 offset:6144\nv_mfma_f32_16x16x32_bf16 v[104:107],a[236:239],v[152:155],v[104:107]\n"
"s_waitcnt lgkmcnt(5)\nbuffer_load_dwordx4 a[132:135],v252,s[8:11],s61 offen offset:1024 sc1\nv_mfma_f32_16x16x32_bf16 v[0:3],a[240:243],v[160:163],v[0:3]\nds_read_b128 a[236:239],v196 offset:7168\nv_mfma_f32_16x16x32_bf16 v[4:7],a[240:243],v[164:167],v[4:7]\nbuffer_load_dwordx4 v252,s[8:11],s59 offen offset:2048 sc1 lds\nv_mfma_f32_16x16x32_bf16 v[12:15],a[244:247],v[164:167],v[12:15]\nds_read_b128 a[240:243],v196 offset:8192\n"
"v_mfma_f32_16x16x32_bf16 v[8:11],a[244:247],v[160:163],v[8:11]\ns_waitcnt lgkmcnt(5)\nv_mfma_f32_16x16x32_bf16 v[16:19],a[248:251],v[160:163],v[16:19]\nds_read_b128 a[244:247],v196 offset:9216\nv_mfma_f32_16x16x32_bf16 v[20:23],a[248:251],v[164:167],v[20:23]\nv_mfma_f32_16x16x32_bf16 v[28:31],a[252:255],v[164:167],v[28:31]\nds_read_b128 a[248:251],v196 offset:10240\nv_mfma_f32_16x16x32_bf16 v[24:27],a[252:255],v[160:163],v[24:27]\n"
"s_waitcnt lgkmcnt(5)\nbuffer_load_dwordx4 v252,s[8:11],s59 offen offset:3072 sc1 lds\nv_mfma_f32_16x16x32_bf16 v[32:35],a[224:227],v[160:163],v[32:35]\ns_add_u32 s59,s59,458752\nds_read_b128 a[252:255],v196 offset:11264\nv_mfma_f32_16x16x32_bf16 v[36:39],a[224:227],v[164:167],v[36:39]\nbuffer_load_dwordx4 a[136:139],v252,s[8:11],s61 offen offset:2048 sc1\nv_mfma_f32_16x16x32_bf16 v[44:47],a[228:231],v[164:167],v[44:47]\n"
"ds_read_b128 a[224:227],v196 offset:12288\nv_mfma_f32_16x16x32_bf16 v[40:43],a[228:231],v[160:163],v[40:43]\ns_waitcnt lgkmcnt(5)\nv_mfma_f32_16x16x32_bf16 v[48:51],a[232:235],v[160:163],v[48:51]\ns_add_u32 s7,s32,0xc000\nds_read_b128 a[228:231],v196 offset:13312\ns_and_b32 s7,s7,0xffff\nv_mfma_f32_16x16x32_bf16 v[52:55],a[232:235],v[164:167],v[52:55]\ns_add_u32 s7,s7,s15\ns_add_u32 m0,s7,0x10000\n"
"v_mfma_f32_16x16x32_bf16 v[60:63],a[236:239],v[164:167],v[60:63]\nds_read_b128 a[232:235],v197 offset:0\nv_mfma_f32_16x16x32_bf16 v[56:59],a[236:239],v[160:163],v[56:59]\ns_waitcnt lgkmcnt(5)\nbuffer_load_dwordx4 v252,s[8:11],s60 offen sc1 lds\nv_mfma_f32_16x16x32_bf16 v[64:67],a[240:243],v[160:163],v[64:67]\nds_read_b128 a[236:239],v197 offset:1024\nv_mfma_f32_16x16x32_bf16 v[68:71],a[240:243],v[164:167],v[68:71]\n"
"buffer_load_dwordx4 a[140:143],v252,s[8:11],s61 offen offset:3072 sc1\ns_add_u32 s61,s61,0x1000\nv_mfma_f32_16x16x32_bf16 v[76:79],a[244:247],v[164:167],v[76:79]\nds_read_b128 a[240:243],v197 offset:2048\nv_mfma_f32_16x16x32_bf16 v[72:75],a[244:247],v[160:163],v[72:75]\ns_waitcnt lgkmcnt(5)\nv_mfma_f32_16x16x32_bf16 v[80:83],a[248:251],v[160:163],v[80:83]\nds_read_b128 a[244:247],v197 offset:3072\n"
"v_mfma_f32_16x16x32_bf16 v[84:87],a[248:251],v[164:167],v[84:87]\nbuffer_load_dwordx4 v252,s[8:11],s60 offen offset:1024 sc1 lds\nv_mfma_f32_16x16x32_bf16 v[92:95],a[252:255],v[164:167],v[92:95]\nds_read_b128 a[248:251],v197 offset:4096\nv_mfma_f32_16x16x32_bf16 v[88:91],a[252:255],v[160:163],v[88:91]\ns_waitcnt lgkmcnt(5)\nv_mfma_f32_16x16x32_bf16 v[96:99],a[224:227],v[160:163],v[96:99]\nds_read_b128 a[252:255],v197 offset:5120\n"
"v_mfma_f32_16x16x32_bf16 v[100:103],a[224:227],v[164:167],v[100:103]\nbuffer_load_dwordx4 a[144:147],v252,s[8:11],s61 offen offset:0 sc1\nv_mfma_f32_16x16x32_bf16 v[108:111],a[228:231],v[164:167],v[108:111]\ns_waitcnt vmcnt(43)\nds_read_b128 a[224:227],v197 offset:6144\nv_mfma_f32_16x16x32_bf16 v[104:107],a[228:231],v[160:163],v[104:107]\ns_waitcnt lgkmcnt(5)\nbuffer_load_dwordx4 v252,s[8:11],s60 offen offset:2048 sc1 lds\n"
"v_mfma_f32_16x16x32_bf16 v[0:3],a[232:235],v[168:171],v[0:3]\nds_read_b128 a[228:231],v197 offset:7168\nv_mfma_f32_16x16x32_bf16 v[4:7],a[232:235],v[172:175],v[4:7]\nv_mfma_f32_16x16x32_bf16 v[12:15],a[236:239],v[172:175],v[12:15]\nds_read_b128 a[232:235],v197 offset:8192\nv_mfma_f32_16x16x32_bf16 v[8:11],a[236:239],v[168:171],v[8:11]\ns_waitcnt lgkmcnt(5)\nv_mfma_f32_16x16x32_bf16 v[16:19],a[240:243],v[168:171],v[16:19]\n"
"ds_read_b128 a[236:239],v197 offset:9216\nv_mfma_f32_16x16x32_bf16 v[20:23],a[240:243],v[172:175],v[20:23]\nbuffer_load_dwordx4 v252,s[8:11],s60 offen offset:3072 sc1 lds\nv_mfma_f32_16x16x32_bf16 v[28:31],a[244:247],v[172:175],v[28:31]\ns_add_u32 s60,s60,458752\nds_read_b128 a[240:243],v197 offset:10240\nv_mfma_f32_16x16x32_bf16 v[24:27],a[244:247],v[168:171],v[24:27]\ns_waitcnt lgkmcnt(5)\nbuffer_load_dwordx4 a[148:151],v252,s[8:11],s61 offen offset:1024 sc1\n"
"v_mfma_f32_16x16x32_bf16 v[32:35],a[248:251],v[168:171],v[32:35]\nds_read_b128 a[244:247],v197 offset:11264\nv_mfma_f32_16x16x32_bf16 v[36:39],a[248:251],v[172:175],v[36:39]\nv_mfma_f32_16x16x32_bf16 v[44:47],a[252:255],v[172:175],v[44:47]\ns_add_u32 s32,s32,0x4000\nds_read_b128 a[248:251],v197 offset:12288\ns_and_b32 s32,s32,0xffff\nv_mfma_f32_16x16x32_bf16 v[40:43],a[252:255],v[168:171],v[40:43]\n"
"s_waitcnt lgkmcnt(5)\nv_mfma_f32_16x16x32_bf16 v[48:51],a[224:227],v[168:171],v[48:51]\nds_read_b128 a[252:255],v197 offset:13312\nv_mfma_f32_16x16x32_bf16 v[52:55],a[224:227],v[172:175],v[52:55]\ns_waitcnt vmcnt(15)\ns_barrier\nv_mfma_f32_16x16x32_bf16 v[60:63],a[228:231],v[172:175],v[60:63]\ns_add_u32 s7,s32,0xc000\nds_read_b128 a[224:227],v196 offset:16384\ns_and_b32 s7,s7,0xffff\nv_mfma_f32_16x16x32_bf16 v[56:59],a[228:231],v[168:171],v[56:59]\n"
"s_waitcnt lgkmcnt(5)\ns_add_u32 s7,s7,s15\ns_add_u32 m0,s7,0x10000\nv_mfma_f32_16x16x32_bf16 v[64:67],a[232:235],v[168:171],v[64:67]\nds_read_b128 a[228:231],v196 offset:17408\nv_mfma_f32_16x16x32_bf16 v[68:71],a[232:235],v[172:175],v[68:71]\nbuffer_load_dwordx4 v252,s[8:11],s60 offen sc1 lds\nv_mfma_f32_16x16x32_bf16 v[76:79],a[236:239],v[172:175],v[76:79]\nds_read_b128 a[232:235],v196 offset:18432\n"
"v_mfma_f32_16x16x32_bf16 v[72:75],a[236:239],v[168:171],v[72:75]\ns_waitcnt lgkmcnt(5)\nbuffer_load_dwordx4 a[152:155],v252,s[8:11],s61 offen offset:2048 sc1\nv_mfma_f32_16x16x32_bf16 v[80:83],a[240:243],v[168:171],v[80:83]\nds_read_b128 a[236:239],v196 offset:19456\nv_mfma_f32_16x16x32_bf16 v[84:87],a[240:243],v[172:175],v[84:87]\nv_mfma_f32_16x16x32_bf16 v[92:95],a[244:247],v[172:175],v[92:95]\nds_read_b128 a[240:243],v196 offset:20480\n"
"v_mfma_f32_16x16x32_bf16 v[88:91],a[244:247],v[168:171],v[88:91]\ns_waitcnt lgkmcnt(5)\nbuffer_load_dwordx4 v252,s[8:11],s60 offen offset:1024 sc1 lds\nv_mfma_f32_16x16x32_bf16 v[96:99],a[248:251],v[168:171],v[96:99]\nds_read_b128 a[244:247],v196 offset:21504\nv_mfma_f32_16x16x32_bf16 v[100:103],a[248:251],v[172:175],v[100:103]\nv_mfma_f32_16x16x32_bf16 v[108:111],a[252:255],v[172:175],v[108:111]\n"
"ds_read_b128 a[248:251],v196 offset:22528\nv_mfma_f32_16x16x32_bf16 v[104:107],a[252:255],v[168:171],v[104:107]\ns_waitcnt lgkmcnt(5)\nbuffer_load_dwordx4 a[156:159],v252,s[8:11],s61 offen offset:3072 sc1\nv_mfma_f32_16x16x32_bf16 v[0:3],a[224:227],v[176:179],v[0:3]\ns_add_u32 s61,s61,0x1000\nds_read_b128 a[252:255],v196 offset:23552\nv_mfma_f32_16x16x32_bf16 v[4:7],a[224:227],v[180:183],v[4:7]\nbuffer_load_dwordx4 v252,s[8:11],s60 offen offset:2048 sc1 lds\n"
"v_mfma_f32_16x16x32_bf16 v[12:15],a[228:231],v[180:183],v[12:15]\nds_read_b128 a[224:227],v196 offset:24576\nv_mfma_f32_16x16x32_bf16 v[8:11],a[228:231],v[176:179],v[8:11]\ns_waitcnt lgkmcnt(5)\nv_mfma_f32_16x16x32_bf16 v[16:19],a[232:235],v[176:179],v[16:19]\nds_read_b128 a[228:231],v196 offset:25600\nv_mfma_f32_16x16x32_bf16 v[20:23],a[232:235],v[180:183],v[20:23]\nv_mfma_f32_16x16x32_bf16 v[28:31],a[236:239],v[180:183],v[28:31]\n"
"ds_read_b128 a[232:235],v196 offset:26624\nv_mfma_f32_16x16x32_bf16 v[24:27],a[236:239],v[176:179],v[24:27]\ns_waitcnt lgkmcnt(5)\nbuffer_load_dwordx4 v252,s[8:11],s60 offen offset:3072 sc1 lds\nv_mfma_f32_16x16x32_bf16 v[32:35],a[240:243],v[176:179],v[32:35]\ns_add_u32 s60,s60,458752\nds_read_b128 a[236:239],v196 offset:27648\nv_mfma_f32_16x16x32_bf16 v[36:39],a[240:243],v[180:183],v[36:39]\nbuffer_load_dwordx4 a[160:163],v252,s[8:11],s61 offen offset:0 sc1\n"
"v_mfma_f32_16x16x32_bf16 v[44:47],a[244:247],v[180:183],v[44:47]\nds_read_b128 a[240:243],v196 offset:28672\nv_mfma_f32_16x16x32_bf16 v[40:43],a[244:247],v[176:179],v[40:43]\ns_waitcnt lgkmcnt(5)\nv_mfma_f32_16x16x32_bf16 v[48:51],a[248:251],v[176:179],v[48:51]\ns_add_u32 s32,s32,0x4000\nds_read_b128 a[244:247],v196 offset:29696\ns_and_b32 s32,s32,0xffff\nv_mfma_f32_16x16x32_bf16 v[52:55],a[248:251],v[180:183],v[52:55]\n"
"v_mfma_f32_16x16x32_bf16 v[60:63],a[252:255],v[180:183],v[60:63]\nds_read_b128 a[248:251],v197 offset:16384\nv_mfma_f32_16x16x32_bf16 v[56:59],a[252:255],v[176:179],v[56:59]\ns_waitcnt lgkmcnt(5)\nv_mfma_f32_16x16x32_bf16 v[64:67],a[224:227],v[176:179],v[64:67]\nds_read_b128 a[252:255],v197 offset:17408\nv_mfma_f32_16x16x32_bf16 v[68:71],a[224:227],v[180:183],v[68:71]\nbuffer_load_dwordx4 a[164:167],v252,s[8:11],s61 offen offset:1024 sc1\n"
"v_mfma_f32_16x16x32_bf16 v[76:79],a[228:231],v[180:183],v[76:79]\nds_read_b128 a[224:227],v197 offset:18432\nv_mfma_f32_16x16x32_bf16 v[72:75],a[228:231],v[176:179],v[72:75]\ns_waitcnt lgkmcnt(5)\nv_mfma_f32_16x16x32_bf16 v[80:83],a[232:235],v[176:179],v[80:83]\nds_read_b128 a[228:231],v197 offset:19456\nv_mfma_f32_16x16x32_bf16 v[84:87],a[232:235],v[180:183],v[84:87]\nv_mfma_f32_16x16x32_bf16 v[92:95],a[236:239],v[180:183],v[92:95]\n"
"ds_read_b128 a[232:235],v197 offset:20480\nv_mfma_f32_16x16x32_bf16 v[88:91],a[236:239],v[176:179],v[88:91]\ns_waitcnt lgkmcnt(5)\nv_mfma_f32_16x16x32_bf16 v[96:99],a[240:243],v[176:179],v[96:99]\nds_read_b128 a[236:239],v197 offset:21504\nv_mfma_f32_16x16x32_bf16 v[100:103],a[240:243],v[180:183],v[100:103]\nbuffer_load_dwordx4 a[168:171],v252,s[8:11],s61 offen offset:2048 sc1\nv_mfma_f32_16x16x32_bf16 v[108:111],a[244:247],v[180:183],v[108:111]\n"
"ds_read_b128 a[240:243],v197 offset:22528\nv_mfma_f32_16x16x32_bf16 v[104:107],a[244:247],v[176:179],v[104:107]\ns_waitcnt lgkmcnt(5)\nv_mfma_f32_16x16x32_bf16 v[0:3],a[248:251],v[184:187],v[0:3]\nds_read_b128 a[244:247],v197 offset:23552\nv_mfma_f32_16x16x32_bf16 v[4:7],a[248:251],v[188:191],v[4:7]\nv_mfma_f32_16x16x32_bf16 v[12:15],a[252:255],v[188:191],v[12:15]\nds_read_b128 a[248:251],v197 offset:24576\n"
"v_mfma_f32_16x16x32_bf16 v[8:11],a[252:255],v[184:187],v[8:11]\ns_waitcnt lgkmcnt(5)\nv_mfma_f32_16x16x32_bf16 v[16:19],a[224:227],v[184:187],v[16:19]\nds_read_b128 a[252:255],v197 offset:25600\nv_mfma_f32_16x16x32_bf16 v[20:23],a[224:227],v[188:191],v[20:23]\nbuffer_load_dwordx4 a[172:175],v252,s[8:11],s61 offen offset:3072 sc1\ns_add_u32 s61,s61,0x1000\nv_mfma_f32_16x16x32_bf16 v[28:31],a[228:231],v[188:191],v[28:31]\n"
"ds_read_b128 a[224:227],v197 offset:26624\nv_mfma_f32_16x16x32_bf16 v[24:27],a[228:231],v[184:187],v[24:27]\ns_waitcnt lgkmcnt(5)\nv_mfma_f32_16x16x32_bf16 v[32:35],a[232:235],v[184:187],v[32:35]\nds_read_b128 a[228:231],v197 offset:27648\nv_mfma_f32_16x16x32_bf16 v[36:39],a[232:235],v[188:191],v[36:39]\nv_mfma_f32_16x16x32_bf16 v[44:47],a[236:239],v[188:191],v[44:47]\nds_read_b128 a[232:235],v197 offset:28672\n"
"v_mfma_f32_16x16x32_bf16 v[40:43],a[236:239],v[184:187],v[40:43]\ns_waitcnt lgkmcnt(5)\nv_mfma_f32_16x16x32_bf16 v[48:51],a[240:243],v[184:187],v[48:51]\nds_read_b128 a[236:239],v197 offset:29696\nv_mfma_f32_16x16x32_bf16 v[52:55],a[240:243],v[188:191],v[52:55]\ns_waitcnt vmcnt(11)\ns_barrier\nv_xor_b32 v196,0x8000,v196\nv_mfma_f32_16x16x32_bf16 v[60:63],a[244:247],v[188:191],v[60:63]\ns_add_u32 s32,s32,0x4000\n"
"ds_read_b128 a[240:243],v196 offset:0\ns_and_b32 s32,s32,0xffff\nv_mfma_f32_16x16x32_bf16 v[56:59],a[244:247],v[184:187],v[56:59]\ns_waitcnt lgkmcnt(5)\nv_xor_b32 v197,0x8000,v197\nv_mfma_f32_16x16x32_bf16 v[64:67],a[248:251],v[184:187],v[64:67]\nds_read_b128 a[244:247],v196 offset:1024\nv_mfma_f32_16x16x32_bf16 v[68:71],a[248:251],v[188:191],v[68:71]\nbuffer_load_dwordx4 a[176:179],v252,s[8:11],s61 offen offset:0 sc1\n"
"v_mfma_f32_16x16x32_bf16 v[76:79],a[252:255],v[188:191],v[76:79]\nds_read_b128 a[248:251],v196 offset:2048\nv_mfma_f32_16x16x32_bf16 v[72:75],a[252:255],v[184:187],v[72:75]\ns_waitcnt lgkmcnt(5)\nv_mfma_f32_16x16x32_bf16 v[80:83],a[224:227],v[184:187],v[80:83]\nds_read_b128 a[252:255],v196 offset:3072\nv_mfma_f32_16x16x32_bf16 v[84:87],a[224:227],v[188:191],v[84:87]\nv_mfma_f32_16x16x32_bf16 v[92:95],a[228:231],v[188:191],v[92:95]\n"
"ds_read_b128 a[224:227],v196 offset:4096\nv_mfma_f32_16x16x32_bf16 v[88:91],a[228:231],v[184:187],v[88:91]\ns_waitcnt lgkmcnt(5)\nv_mfma_f32_16x16x32_bf16 v[96:99],a[232:235],v[184:187],v[96:99]\nds_read_b128 a[228:231],v196 offset:5120\nv_mfma_f32_16x16x32_bf16 v[100:103],a[232:235],v[188:191],v[100:103]\nbuffer_load_dwordx4 a[180:183],v252,s[8:11],s61 offen offset:1024 sc1\nv_mfma_f32_16x16x32_bf16 v[108:111],a[236:239],v[188:191],v[108:111]\n"
"ds_read_b128 a[232:235],v196 offset:6144\nv_mfma_f32_16x16x32_bf16 v[104:107],a[236:239],v[184:187],v[104:107]\ns_waitcnt lgkmcnt(5)\nv_mfma_f32_16x16x32_bf16 v[0:3],a[240:243],v[202:205],v[0:3]\nds_read_b128 a[236:239],v196 offset:7168\nv_mfma_f32_16x16x32_bf16 v[4:7],a[240:243],v[206:209],v[4:7]\nv_mfma_f32_16x16x32_bf16 v[12:15],a[244:247],v[206:209],v[12:15]\nds_read_b128 a[240:243],v196 offset:8192\n"
"v_mfma_f32_16x16x32_bf16 v[8:11],a[244:247],v[202:205],v[8:11]\ns_waitcnt lgkmcnt(5)\nv_mfma_f32_16x16x32_bf16 v[16:19],a[248:251],v[202:205],v[16:19]\nds_read_b128 a[244:247],v196 offset:9216\nv_mfma_f32_16x16x32_bf16 v[20:23],a[248:251],v[206:209],v[20:23]\nbuffer_load_dwordx4 a[184:187],v252,s[8:11],s61 offen offset:2048 sc1\nv_mfma_f32_16x16x32_bf16 v[28:31],a[252:255],v[206:209],v[28:31]\nds_read_b128 a[248:251],v196 offset:10240\n"
"v_mfma_f32_16x16x32_bf16 v[24:27],a[252:255],v[202:205],v[24:27]\ns_waitcnt lgkmcnt(5)\nv_mfma_f32_16x16x32_bf16 v[32:35],a[224:227],v[202:205],v[32:35]\nds_read_b128 a[252:255],v196 offset:11264\nv_mfma_f32_16x16x32_bf16 v[36:39],a[224:227],v[206:209],v[36:39]\nv_mfma_f32_16x16x32_bf16 v[44:47],a[228:231],v[206:209],v[44:47]\nds_read_b128 a[224:227],v196 offset:12288\nv_mfma_f32_16x16x32_bf16 v[40:43],a[228:231],v[202:205],v[40:43]\n"
"s_waitcnt lgkmcnt(5)\nv_mfma_f32_16x16x32_bf16 v[48:51],a[232:235],v[202:205],v[48:51]\nds_read_b128 a[228:231],v196 offset:13312\nv_mfma_f32_16x16x32_bf16 v[52:55],a[232:235],v[206:209],v[52:55]\nbuffer_load_dwordx4 a[188:191],v252,s[8:11],s61 offen offset:3072 sc1\ns_add_u32 s61,s61,0x1000\nv_mfma_f32_16x16x32_bf16 v[60:63],a[236:239],v[206:209],v[60:63]\nds_read_b128 a[232:235],v197 offset:0\nv_mfma_f32_16x16x32_bf16 v[56:59],a[236:239],v[202:205],v[56:59]\n"
"s_waitcnt lgkmcnt(5)\nv_mfma_f32_16x16x32_bf16 v[64:67],a[240:243],v[202:205],v[64:67]\nds_read_b128 a[236:239],v197 offset:1024\nv_mfma_f32_16x16x32_bf16 v[68:71],a[240:243],v[206:209],v[68:71]\nv_mfma_f32_16x16x32_bf16 v[76:79],a[244:247],v[206:209],v[76:79]\nds_read_b128 a[240:243],v197 offset:2048\nv_mfma_f32_16x16x32_bf16 v[72:75],a[244:247],v[202:205],v[72:75]\ns_waitcnt lgkmcnt(5)\nv_mfma_f32_16x16x32_bf16 v[80:83],a[248:251],v[202:205],v[80:83]\n"
"ds_read_b128 a[244:247],v197 offset:3072\nv_mfma_f32_16x16x32_bf16 v[84:87],a[248:251],v[206:209],v[84:87]\nbuffer_load_dwordx4 a[192:195],v252,s[8:11],s61 offen offset:0 sc1\nv_mfma_f32_16x16x32_bf16 v[92:95],a[252:255],v[206:209],v[92:95]\nds_read_b128 a[248:251],v197 offset:4096\nv_mfma_f32_16x16x32_bf16 v[88:91],a[252:255],v[202:205],v[88:91]\ns_waitcnt lgkmcnt(5)\nv_mfma_f32_16x16x32_bf16 v[96:99],a[224:227],v[202:205],v[96:99]\n"
"ds_read_b128 a[252:255],v197 offset:5120\nv_mfma_f32_16x16x32_bf16 v[100:103],a[224:227],v[206:209],v[100:103]\nv_mfma_f32_16x16x32_bf16 v[108:111],a[228:231],v[206:209],v[108:111]\nds_read_b128 a[224:227],v197 offset:6144\nv_mfma_f32_16x16x32_bf16 v[104:107],a[228:231],v[202:205],v[104:107]\ns_waitcnt lgkmcnt(5)\nv_mfma_f32_16x16x32_bf16 v[0:3],a[232:235],v[210:213],v[0:3]\nds_read_b128 a[228:231],v197 offset:7168\n"
"v_mfma_f32_16x16x32_bf16 v[4:7],a[232:235],v[214:217],v[4:7]\nbuffer_load_dwordx4 a[196:199],v252,s[8:11],s61 offen offset:1024 sc1\nv_mfma_f32_16x16x32_bf16 v[12:15],a[236:239],v[214:217],v[12:15]\nds_read_b128 a[232:235],v197 offset:8192\nv_mfma_f32_16x16x32_bf16 v[8:11],a[236:239],v[210:213],v[8:11]\ns_waitcnt lgkmcnt(5)\nv_mfma_f32_16x16x32_bf16 v[16:19],a[240:243],v[210:213],v[16:19]\nds_read_b128 a[236:239],v197 offset:9216\n"
"v_mfma_f32_16x16x32_bf16 v[20:23],a[240:243],v[214:217],v[20:23]\nv_mfma_f32_16x16x32_bf16 v[28:31],a[244:247],v[214:217],v[28:31]\nds_read_b128 a[240:243],v197 offset:10240\nv_mfma_f32_16x16x32_bf16 v[24:27],a[244:247],v[210:213],v[24:27]\ns_waitcnt lgkmcnt(5)\nv_mfma_f32_16x16x32_bf16 v[32:35],a[248:251],v[210:213],v[32:35]\nds_read_b128 a[244:247],v197 offset:11264\nv_mfma_f32_16x16x32_bf16 v[36:39],a[248:251],v[214:217],v[36:39]\n"
"buffer_load_dwordx4 a[200:203],v252,s[8:11],s61 offen offset:2048 sc1\nv_mfma_f32_16x16x32_bf16 v[44:47],a[252:255],v[214:217],v[44:47]\nds_read_b128 a[248:251],v197 offset:12288\nv_mfma_f32_16x16x32_bf16 v[40:43],a[252:255],v[210:213],v[40:43]\ns_waitcnt lgkmcnt(5)\nv_mfma_f32_16x16x32_bf16 v[48:51],a[224:227],v[210:213],v[48:51]\nds_read_b128 a[252:255],v197 offset:13312\nv_mfma_f32_16x16x32_bf16 v[52:55],a[224:227],v[214:217],v[52:55]\n"
"s_waitcnt vmcnt(11)\ns_barrier\nv_mfma_f32_16x16x32_bf16 v[60:63],a[228:231],v[214:217],v[60:63]\ns_add_u32 s32,s32,0x4000\nds_read_b128 a[224:227],v196 offset:16384\ns_and_b32 s32,s32,0xffff\nv_mfma_f32_16x16x32_bf16 v[56:59],a[228:231],v[210:213],v[56:59]\ns_waitcnt lgkmcnt(5)\nv_mfma_f32_16x16x32_bf16 v[64:67],a[232:235],v[210:213],v[64:67]\nds_read_b128 a[228:231],v196 offset:17408\nv_mfma_f32_16x16x32_bf16 v[68:71],a[232:235],v[214:217],v[68:71]\n"
"buffer_load_dwordx4 a[204:207],v252,s[8:11],s61 offen offset:3072 sc1\ns_add_u32 s61,s61,0x1000\nv_mfma_f32_16x16x32_bf16 v[76:79],a[236:239],v[214:217],v[76:79]\nds_read_b128 a[232:235],v196 offset:18432\nv_mfma_f32_16x16x32_bf16 v[72:75],a[236:239],v[210:213],v[72:75]\ns_waitcnt lgkmcnt(5)\nv_mfma_f32_16x16x32_bf16 v[80:83],a[240:243],v[210:213],v[80:83]\nds_read_b128 a[236:239],v196 offset:19456\n"
"v_mfma_f32_16x16x32_bf16 v[84:87],a[240:243],v[214:217],v[84:87]\nv_mfma_f32_16x16x32_bf16 v[92:95],a[244:247],v[214:217],v[92:95]\nds_read_b128 a[240:243],v196 offset:20480\nv_mfma_f32_16x16x32_bf16 v[88:91],a[244:247],v[210:213],v[88:91]\ns_waitcnt lgkmcnt(5)\nv_mfma_f32_16x16x32_bf16 v[96:99],a[248:251],v[210:213],v[96:99]\nds_read_b128 a[244:247],v196 offset:21504\nv_mfma_f32_16x16x32_bf16 v[100:103],a[248:251],v[214:217],v[100:103]\n"
"buffer_load_dwordx4 a[208:211],v252,s[8:11],s61 offen offset:0 sc1\nv_mfma_f32_16x16x32_bf16 v[108:111],a[252:255],v[214:217],v[108:111]\nds_read_b128 a[248:251],v196 offset:22528\nv_mfma_f32_16x16x32_bf16 v[104:107],a[252:255],v[210:213],v[104:107]\ns_waitcnt lgkmcnt(5)\nv_mfma_f32_16x16x32_bf16 v[0:3],a[224:227],v[218:221],v[0:3]\nds_read_b128 a[252:255],v196 offset:23552\nv_mfma_f32_16x16x32_bf16 v[4:7],a[224:227],v[222:225],v[4:7]\n"
"v_mfma_f32_16x16x32_bf16 v[12:15],a[228:231],v[222:225],v[12:15]\nds_read_b128 a[224:227],v196 offset:24576\nv_mfma_f32_16x16x32_bf16 v[8:11],a[228:231],v[218:221],v[8:11]\ns_waitcnt lgkmcnt(5)\nv_mfma_f32_16x16x32_bf16 v[16:19],a[232:235],v[218:221],v[16:19]\nds_read_b128 a[228:231],v196 offset:25600\nv_mfma_f32_16x16x32_bf16 v[20:23],a[232:235],v[222:225],v[20:23]\nbuffer_load_dwordx4 a[212:215],v252,s[8:11],s61 offen offset:1024 sc1\n"
"v_mfma_f32_16x16x32_bf16 v[28:31],a[236:239],v[222:225],v[28:31]\nds_read_b128 a[232:235],v196 offset:26624\nv_mfma_f32_16x16x32_bf16 v[24:27],a[236:239],v[218:221],v[24:27]\ns_waitcnt lgkmcnt(5)\nv_mfma_f32_16x16x32_bf16 v[32:35],a[240:243],v[218:221],v[32:35]\nds_read_b128 a[236:239],v196 offset:27648\nv_mfma_f32_16x16x32_bf16 v[36:39],a[240:243],v[222:225],v[36:39]\nv_mfma_f32_16x16x32_bf16 v[44:47],a[244:247],v[222:225],v[44:47]\n"
"ds_read_b128 a[240:243],v196 offset:28672\nv_mfma_f32_16x16x32_bf16 v[40:43],a[244:247],v[218:221],v[40:43]\ns_waitcnt lgkmcnt(5)\nv_mfma_f32_16x16x32_bf16 v[48:51],a[248:251],v[218:221],v[48:51]\nds_read_b128 a[244:247],v196 offset:29696\nv_mfma_f32_16x16x32_bf16 v[52:55],a[248:251],v[222:225],v[52:55]\nbuffer_load_dwordx4 a[216:219],v252,s[8:11],s61 offen offset:2048 sc1\nv_mfma_f32_16x16x32_bf16 v[60:63],a[252:255],v[222:225],v[60:63]\n"
"ds_read_b128 a[248:251],v197 offset:16384\nv_mfma_f32_16x16x32_bf16 v[56:59],a[252:255],v[218:221],v[56:59]\ns_waitcnt lgkmcnt(5)\nv_mfma_f32_16x16x32_bf16 v[64:67],a[224:227],v[218:221],v[64:67]\nds_read_b128 a[252:255],v197 offset:17408\nv_mfma_f32_16x16x32_bf16 v[68:71],a[224:227],v[222:225],v[68:71]\nv_mfma_f32_16x16x32_bf16 v[76:79],a[228:231],v[222:225],v[76:79]\nds_read_b128 a[224:227],v197 offset:18432\n"
"v_mfma_f32_16x16x32_bf16 v[72:75],a[228:231],v[218:221],v[72:75]\ns_waitcnt lgkmcnt(5)\nv_mfma_f32_16x16x32_bf16 v[80:83],a[232:235],v[218:221],v[80:83]\nds_read_b128 a[228:231],v197 offset:19456\nv_mfma_f32_16x16x32_bf16 v[84:87],a[232:235],v[222:225],v[84:87]\nbuffer_load_dwordx4 a[220:223],v252,s[8:11],s61 offen offset:3072 sc1\ns_add_u32 s61,s61,0x1000\nv_mfma_f32_16x16x32_bf16 v[92:95],a[236:239],v[222:225],v[92:95]\n"
"ds_read_b128 a[232:235],v197 offset:20480\nv_mfma_f32_16x16x32_bf16 v[88:91],a[236:239],v[218:221],v[88:91]\ns_waitcnt lgkmcnt(5)\nv_mfma_f32_16x16x32_bf16 v[96:99],a[240:243],v[218:221],v[96:99]\nds_read_b128 a[236:239],v197 offset:21504\nv_mfma_f32_16x16x32_bf16 v[100:103],a[240:243],v[222:225],v[100:103]\nv_mfma_f32_16x16x32_bf16 v[108:111],a[244:247],v[222:225],v[108:111]\nds_read_b128 a[240:243],v197 offset:22528\n"
"v_mfma_f32_16x16x32_bf16 v[104:107],a[244:247],v[218:221],v[104:107]\ns_waitcnt lgkmcnt(5)\nv_mfma_f32_16x16x32_bf16 v[0:3],a[248:251],v[226:229],v[0:3]\nds_read_b128 a[244:247],v197 offset:23552\nv_mfma_f32_16x16x32_bf16 v[4:7],a[248:251],v[230:233],v[4:7]\nv_mfma_f32_16x16x32_bf16 v[12:15],a[252:255],v[230:233],v[12:15]\nds_read_b128 a[248:251],v197 offset:24576\nv_mfma_f32_16x16x32_bf16 v[8:11],a[252:255],v[226:229],v[8:11]\n"
"s_waitcnt lgkmcnt(5)\nv_mfma_f32_16x16x32_bf16 v[16:19],a[224:227],v[226:229],v[16:19]\nds_read_b128 a[252:255],v197 offset:25600\nv_mfma_f32_16x16x32_bf16 v[20:23],a[224:227],v[230:233],v[20:23]\nv_mfma_f32_16x16x32_bf16 v[28:31],a[228:231],v[230:233],v[28:31]\nds_read_b128 a[224:227],v197 offset:26624\nv_mfma_f32_16x16x32_bf16 v[24:27],a[228:231],v[226:229],v[24:27]\ns_waitcnt lgkmcnt(5)\nv_mfma_f32_16x16x32_bf16 v[32:35],a[232:235],v[226:229],v[32:35]\n"
"ds_read_b128 a[228:231],v197 offset:27648\nv_mfma_f32_16x16x32_bf16 v[36:39],a[232:235],v[230:233],v[36:39]\nv_mul_f32 v0,v0,v8\nv_mul_f32 v1,v1,v9\nv_mfma_f32_16x16x32_bf16 v[44:47],a[236:239],v[230:233],v[44:47]\nds_read_b128 a[232:235],v197 offset:28672\nv_mul_f32 v2,v2,v10\nv_mfma_f32_16x16x32_bf16 v[40:43],a[236:239],v[226:229],v[40:43]\ns_waitcnt lgkmcnt(5)\nv_mul_f32 v3,v3,v11\nv_cvt_pk_bf16_f32 v8,v0,v1\n"
"v_mfma_f32_16x16x32_bf16 v[48:51],a[240:243],v[226:229],v[48:51]\nds_read_b128 a[236:239],v197 offset:29696\nv_cvt_pk_bf16_f32 v9,v2,v3\nv_mfma_f32_16x16x32_bf16 v[52:55],a[240:243],v[230:233],v[52:55]\nv_mul_f32 v4,v4,v12\nv_mul_f32 v5,v5,v13\nv_mfma_f32_16x16x32_bf16 v[60:63],a[244:247],v[230:233],v[60:63]\nv_mul_f32 v6,v6,v14\nv_mul_f32 v7,v7,v15\nv_mfma_f32_16x16x32_bf16 v[56:59],a[244:247],v[226:229],v[56:59]\n"
"s_waitcnt lgkmcnt(4)\nv_cvt_pk_bf16_f32 v12,v4,v5\nv_cvt_pk_bf16_f32 v13,v6,v7\nv_mfma_f32_16x16x32_bf16 v[64:67],a[248:251],v[226:229],v[64:67]\nv_mul_f32 v16,v16,v24\nv_mul_f32 v17,v17,v25\nv_mfma_f32_16x16x32_bf16 v[68:71],a[248:251],v[230:233],v[68:71]\nv_mul_f32 v18,v18,v26\nv_mul_f32 v19,v19,v27\nv_mfma_f32_16x16x32_bf16 v[76:79],a[252:255],v[230:233],v[76:79]\nv_cvt_pk_bf16_f32 v10,v16,v17\n"
"v_cvt_pk_bf16_f32 v11,v18,v19\nv_mfma_f32_16x16x32_bf16 v[72:75],a[252:255],v[226:229],v[72:75]\ns_waitcnt lgkmcnt(2)\nv_mul_f32 v20,v20,v28\nv_mul_f32 v21,v21,v29\nv_mfma_f32_16x16x32_bf16 v[80:83],a[224:227],v[226:229],v[80:83]\nv_mul_f32 v22,v22,v30\nv_mul_f32 v23,v23,v31\nv_mfma_f32_16x16x32_bf16 v[84:87],a[224:227],v[230:233],v[84:87]\nv_cvt_pk_bf16_f32 v14,v20,v21\nv_cvt_pk_bf16_f32 v15,v22,v23\n"
"v_mfma_f32_16x16x32_bf16 v[92:95],a[228:231],v[230:233],v[92:95]\nv_mul_f32 v32,v32,v40\nv_mul_f32 v33,v33,v41\nv_mfma_f32_16x16x32_bf16 v[88:91],a[228:231],v[226:229],v[88:91]\ns_waitcnt lgkmcnt(0)\nv_mul_f32 v34,v34,v42\nv_mul_f32 v35,v35,v43\nv_mfma_f32_16x16x32_bf16 v[96:99],a[232:235],v[226:229],v[96:99]\nv_cvt_pk_bf16_f32 v40,v32,v33\nv_cvt_pk_bf16_f32 v41,v34,v35\nv_mfma_f32_16x16x32_bf16 v[100:103],a[232:235],v[230:233],v[100:103]\n"
"v_mul_f32 v36,v36,v44\nv_mul_f32 v37,v37,v45\nv_mfma_f32_16x16x32_bf16 v[108:111],a[236:239],v[230:233],v[108:111]\nv_mul_f32 v38,v38,v46\nv_mul_f32 v39,v39,v47\nv_mfma_f32_16x16x32_bf16 v[104:107],a[236:239],v[226:229],v[104:107]\nv_cvt_pk_bf16_f32 v44,v36,v37\nv_cvt_pk_bf16_f32 v45,v38,v39\nbuffer_store_dwordx4 v[8:11],v252,s[8:11],s67 offen offset:0 sc1\nbuffer_store_dwordx4 v[12:15],v252,s[8:11],s68 offen offset:0 sc1\n"
"v_mul_f32 v48,v48,v56\nv_mul_f32 v49,v49,v57\nv_mul_f32 v50,v50,v58\nv_mul_f32 v51,v51,v59\nv_cvt_pk_bf16_f32 v42,v48,v49\nv_cvt_pk_bf16_f32 v43,v50,v51\nbuffer_store_dwordx4 v[40:43],v252,s[8:11],s67 offen offset:1024 sc1\nv_mul_f32 v52,v52,v60\nv_mul_f32 v53,v53,v61\nv_mul_f32 v54,v54,v62\nv_mul_f32 v55,v55,v63\nv_cvt_pk_bf16_f32 v46,v52,v53\nv_cvt_pk_bf16_f32 v47,v54,v55\nbuffer_store_dwordx4 v[44:47],v252,s[8:11],s68 offen offset:1024 sc1\n"
"v_mul_f32 v64,v64,v72\nv_mul_f32 v65,v65,v73\nv_mul_f32 v66,v66,v74\nv_mul_f32 v67,v67,v75\nv_cvt_pk_bf16_f32 v72,v64,v65\nv_cvt_pk_bf16_f32 v73,v66,v67\nv_mul_f32 v68,v68,v76\nv_mul_f32 v69,v69,v77\nv_mul_f32 v70,v70,v78\nv_mul_f32 v71,v71,v79\nv_cvt_pk_bf16_f32 v76,v68,v69\nv_cvt_pk_bf16_f32 v77,v70,v71\nv_mul_f32 v80,v80,v88\nv_mul_f32 v81,v81,v89\nv_mul_f32 v82,v82,v90\nv_mul_f32 v83,v83,v91\nv_cvt_pk_bf16_f32 v74,v80,v81\n"
"v_cvt_pk_bf16_f32 v75,v82,v83\nbuffer_store_dwordx4 v[72:75],v252,s[8:11],s67 offen offset:2048 sc1\nv_mul_f32 v84,v84,v92\nv_mul_f32 v85,v85,v93\nv_mul_f32 v86,v86,v94\nv_mul_f32 v87,v87,v95\nv_cvt_pk_bf16_f32 v78,v84,v85\nv_cvt_pk_bf16_f32 v79,v86,v87\nbuffer_store_dwordx4 v[76:79],v252,s[8:11],s68 offen offset:2048 sc1\nv_mul_f32 v96,v96,v104\nv_mul_f32 v97,v97,v105\nv_mul_f32 v98,v98,v106\nv_mul_f32 v99,v99,v107\n"
"v_cvt_pk_bf16_f32 v104,v96,v97\nv_cvt_pk_bf16_f32 v105,v98,v99\nbuffer_store_dwordx2 v[104:105],v200,s[8:11],s67 offen offset:3072 sc1\nv_mul_f32 v100,v100,v108\nv_mul_f32 v101,v101,v109\nv_mul_f32 v102,v102,v110\nv_mul_f32 v103,v103,v111\nv_cvt_pk_bf16_f32 v108,v100,v101\nv_cvt_pk_bf16_f32 v109,v102,v103\nbuffer_store_dwordx2 v[108:109],v200,s[8:11],s68 offen offset:3072 sc1\ns_load_dwordx2 s[68:69],s[4:5],72\n"
"s_lshl_b32 s7,s58,7\ns_add_u32 s64,s7,117571584\ns_mul_i32 s72,s62,229376\ns_mul_i32 s7,s14,14336\ns_add_u32 s72,s72,s7\ns_add_u32 s72,s72,106954752\ns_add_u32 s73,s72,57344\ns_add_u32 s74,s73,57344\ns_add_u32 s75,s74,57344\ns_lshl2_add_u32 s7,s62,s14\ns_mul_i32 s65,s7,14336\ns_mul_i32 s66,s7,14336\ns_lshl_b32 s7,s63,11\ns_add_u32 s65,s65,s7\ns_add_u32 s65,s65,104857600\ns_lshl_b32 s7,s63,7\ns_add_u32 s66,s66,s7\n"
"s_waitcnt vmcnt(0) lgkmcnt(0)\ns_barrier\ns_cmp_lg_u32 s14,0\ns_cbranch_scc1 L_fused_block_nf_60\nbuffer_store_dwordx2 v[250:251],off,s[8:11],s64 sc1\nL_fused_block_nf_60:\ns_cmp_ge_u32 s57,14\ns_cbranch_scc1 L_fused_block_fin_59\ns_and_b32 s69,s69,0xffff\ns_mov_b32 s70,1835008\ns_mov_b32 s71,0x20000\nv_lshlrev_b32 v227,3,v253\nv_add_u32 v228,0xe00,v227\nv_add_u32 v229,0xe00,v252\nv_add_u32 v225,s15,v252\n"
"s_lshl_b32 s7,s14,14\nv_add_u32 v226,s7,v252\nv_and_b32 v240,15,v253\nv_mul_u32_u24 v240,896,v240\nv_lshrrev_b32 v241,4,v253\nv_lshl_add_u32 v224,v241,3,v240\nbuffer_load_dwordx4 v[232:235],v252,s[8:11],s65 offen offset:0 sc1\nbuffer_load_dwordx4 v[236:239],v252,s[8:11],s65 offen offset:1024 sc1\ns_lshl_b32 s7,s56,11\ns_add_u32 s84,s7,117571584\ns_cmp_lg_u32 s14,0\ns_cbranch_scc1 L_fused_block_np_61\n"
"v_lshlrev_b32 v242,7,v253\ns_mov_b64 exec,0xffff\ns_mov_b32 s7,0\nL_fused_block_poll_62:\nbuffer_load_dwordx2 v[240:241],v242,s[8:11],s84 offen sc1\ns_add_u32 s7,s7,1\ns_waitcnt vmcnt(0)\nv_cmp_eq_u64 vcc,s[12:13],v[240:241]\ns_nop 1\ns_cmp_eq_u64 vcc,exec\ns_cbranch_scc1 L_fused_block_polld_63\ns_cmp_lt_u32 s7,131072\ns_cbranch_scc1 L_fused_block_poll_62\nL_fused_block_polld_63:\ns_mov_b64 exec,-1\n"
"L_fused_block_np_61:\ns_barrier\nbuffer_load_dwordx4 v[64:67],v252,s[8:11],s72 offen offset:0 sc1\nbuffer_load_dwordx4 v[68:71],v252,s[8:11],s73 offen offset:0 sc1\nbuffer_load_dwordx4 v[72:75],v252,s[8:11],s74 offen offset:0 sc1\nbuffer_load_dwordx4 v[76:79],v252,s[8:11],s75 offen offset:0 sc1\nbuffer_load_dwordx4 v[80:83],v252,s[8:11],s72 offen offset:1024 sc1\nbuffer_load_dwordx4 v[84:87],v252,s[8:11],s73 offen offset:1024 sc1\n"
"buffer_load_dwordx4 v[88:91],v252,s[8:11],s74 offen offset:1024 sc1\nbuffer_load_dwordx4 v[92:95],v252,s[8:11],s75 offen offset:1024 sc1\nbuffer_load_dwordx4 v[96:99],v252,s[8:11],s72 offen offset:2048 sc1\nbuffer_load_dwordx4 v[100:103],v252,s[8:11],s73 offen offset:2048 sc1\nbuffer_load_dwordx4 v[104:107],v252,s[8:11],s74 offen offset:2048 sc1\nbuffer_load_dwordx4 v[108:111],v252,s[8:11],s75 offen offset:2048 sc1\n"
"buffer_load_dwordx4 v[112:115],v229,s[8:11],s72 offen offset:0 sc1\nbuffer_load_dwordx4 v[116:119],v229,s[8:11],s73 offen offset:0 sc1\nbuffer_load_dwordx4 v[120:123],v229,s[8:11],s74 offen offset:0 sc1\nbuffer_load_dwordx4 v[124:127],v229,s[8:11],s75 offen offset:0 sc1\nbuffer_load_dwordx4 v[128:131],v229,s[8:11],s72 offen offset:1024 sc1\nbuffer_load_dwordx4 v[132:135],v229,s[8:11],s73 offen offset:1024 sc1\n"
"buffer_load_dwordx4 v[136:139],v229,s[8:11],s74 offen offset:1024 sc1\nbuffer_load_dwordx4 v[140:143],v229,s[8:11],s75 offen offset:1024 sc1\nbuffer_load_dwordx4 v[144:147],v229,s[8:11],s72 offen offset:2048 sc1\nbuffer_load_dwordx4 v[148:151],v229,s[8:11],s73 offen offset:2048 sc1\nbuffer_load_dwordx4 v[152:155],v229,s[8:11],s74 offen offset:2048 sc1\nbuffer_load_dwordx4 v[156:159],v229,s[8:11],s75 offen offset:2048 sc1\n"
"buffer_load_dwordx2 v[160:161],v227,s[8:11],s72 offen offset:3072 sc1\nbuffer_load_dwordx2 v[162:163],v228,s[8:11],s72 offen offset:3072 sc1\ns_add_u32 s72,s72,7168\nbuffer_load_dwordx2 v[164:165],v227,s[8:11],s73 offen offset:3072 sc1\nbuffer_load_dwordx2 v[166:167],v228,s[8:11],s73 offen offset:3072 sc1\ns_add_u32 s73,s73,7168\nbuffer_load_dwordx2 v[168:169],v227,s[8:11],s74 offen offset:3072 sc1\n"
"buffer_load_dwordx2 v[170:171],v228,s[8:11],s74 offen offset:3072 sc1\ns_add_u32 s74,s74,7168\nbuffer_load_dwordx2 v[172:173],v227,s[8:11],s75 offen offset:3072 sc1\nbuffer_load_dwordx2 v[174:175],v228,s[8:11],s75 offen offset:3072 sc1\ns_add_u32 s75,s75,7168\nbuffer_load_dwordx4 v[176:179],v252,s[8:11],s72 offen offset:0 sc1\nbuffer_load_dwordx4 v[180:183],v252,s[8:11],s73 offen offset:0 sc1\nbuffer_load_dwordx4 v[184:187],v252,s[8:11],s74 offen offset:0 sc1\n"
"buffer_load_dwordx4 v[188:191],v252,s[8:11],s75 offen offset:0 sc1\nbuffer_load_dwordx4 v[192:195],v252,s[8:11],s72 offen offset:1024 sc1\nbuffer_load_dwordx4 v[196:199],v252,s[8:11],s73 offen offset:1024 sc1\nbuffer_load_dwordx4 v[200:203],v252,s[8:11],s74 offen offset:1024 sc1\nbuffer_load_dwordx4 v[204:207],v252,s[8:11],s75 offen offset:1024 sc1\ns_waitcnt vmcnt(36)\nv_mfma_f32_16x16x32_bf16 v[0:3],a[0:3],v[64:67],0\n"
"buffer_load_dwordx4 v[208:211],v252,s[8:11],s72 offen offset:2048 sc1\nv_mfma_f32_16x16x32_bf16 v[16:19],a[0:3],v[68:71],0\nv_mfma_f32_16x16x32_bf16 v[32:35],a[0:3],v[72:75],0\nbuffer_load_dwordx4 v[212:215],v252,s[8:11],s73 offen offset:2048 sc1\nv_mfma_f32_16x16x32_bf16 v[48:51],a[0:3],v[76:79],0\nv_mfma_f32_16x16x32_bf16 v[52:55],a[4:7],v[76:79],0\nbuffer_load_dwordx4 v[216:219],v252,s[8:11],s74 offen offset:2048 sc1\n"
"v_mfma_f32_16x16x32_bf16 v[36:39],a[4:7],v[72:75],0\nv_mfma_f32_16x16x32_bf16 v[20:23],a[4:7],v[68:71],0\nbuffer_load_dwordx4 v[220:223],v252,s[8:11],s75 offen offset:2048 sc1\nv_mfma_f32_16x16x32_bf16 v[4:7],a[4:7],v[64:67],0\nv_mfma_f32_16x16x32_bf16 v[8:11],a[8:11],v[64:67],0\nv_mfma_f32_16x16x32_bf16 v[24:27],a[8:11],v[68:71],0\nv_mfma_f32_16x16x32_bf16 v[40:43],a[8:11],v[72:75],0\nv_mfma_f32_16x16x32_bf16 v[56:59],a[8:11],v[76:79],0\n"
"v_mfma_f32_16x16x32_bf16 v[60:63],a[12:15],v[76:79],0\nv_mfma_f32_16x16x32_bf16 v[44:47],a[12:15],v[72:75],0\nv_mfma_f32_16x16x32_bf16 v[28:31],a[12:15],v[68:71],0\nv_mfma_f32_16x16x32_bf16 v[12:15],a[12:15],v[64:67],0\ns_waitcnt vmcnt(36)\nv_mfma_f32_16x16x32_bf16 v[0:3],a[16:19],v[80:83],v[0:3]\nbuffer_load_dwordx4 v[64:67],v229,s[8:11],s72 offen offset:0 sc1\nv_mfma_f32_16x16x32_bf16 v[16:19],a[16:19],v[84:87],v[16:19]\n"
"v_mfma_f32_16x16x32_bf16 v[32:35],a[16:19],v[88:91],v[32:35]\nbuffer_load_dwordx4 v[68:71],v229,s[8:11],s73 offen offset:0 sc1\nv_mfma_f32_16x16x32_bf16 v[48:51],a[16:19],v[92:95],v[48:51]\nv_mfma_f32_16x16x32_bf16 v[52:55],a[20:23],v[92:95],v[52:55]\nbuffer_load_dwordx4 v[72:75],v229,s[8:11],s74 offen offset:0 sc1\nv_mfma_f32_16x16x32_bf16 v[36:39],a[20:23],v[88:91],v[36:39]\nv_mfma_f32_16x16x32_bf16 v[20:23],a[20:23],v[84:87],v[20:23]\n"
"buffer_load_dwordx4 v[76:79],v229,s[8:11],s75 offen offset:0 sc1\nv_mfma_f32_16x16x32_bf16 v[4:7],a[20:23],v[80:83],v[4:7]\nv_mfma_f32_16x16x32_bf16 v[8:11],a[24:27],v[80:83],v[8:11]\nv_mfma_f32_16x16x32_bf16 v[24:27],a[24:27],v[84:87],v[24:27]\nv_mfma_f32_16x16x32_bf16 v[40:43],a[24:27],v[88:91],v[40:43]\nv_mfma_f32_16x16x32_bf16 v[56:59],a[24:27],v[92:95],v[56:59]\nv_mfma_f32_16x16x32_bf16 v[60:63],a[28:31],v[92:95],v[60:63]\n"
"v_mfma_f32_16x16x32_bf16 v[44:47],a[28:31],v[88:91],v[44:47]\nv_mfma_f32_16x16x32_bf16 v[28:31],a[28:31],v[84:87],v[28:31]\nv_mfma_f32_16x16x32_bf16 v[12:15],a[28:31],v[80:83],v[12:15]\ns_waitcnt vmcnt(36)\nv_mfma_f32_16x16x32_bf16 v[0:3],a[32:35],v[96:99],v[0:3]\nbuffer_load_dwordx4 v[80:83],v229,s[8:11],s72 offen offset:1024 sc1\nv_mfma_f32_16x16x32_bf16 v[16:19],a[32:35],v[100:103],v[16:19]\nv_mfma_f32_16x16x32_bf16 v[32:35],a[32:35],v[104:107],v[32:35]\n"
"buffer_load_dwordx4 v[84:87],v229,s[8:11],s73 offen offset:1024 sc1\nv_mfma_f32_16x16x32_bf16 v[48:51],a[32:35],v[108:111],v[48:51]\nv_mfma_f32_16x16x32_bf16 v[52:55],a[36:39],v[108:111],v[52:55]\nbuffer_load_dwordx4 v[88:91],v229,s[8:11],s74 offen offset:1024 sc1\nv_mfma_f32_16x16x32_bf16 v[36:39],a[36:39],v[104:107],v[36:39]\nv_mfma_f32_16x16x32_bf16 v[20:23],a[36:39],v[100:103],v[20:23]\nbuffer_load_dwordx4 v[92:95],v229,s[8:11],s75 offen offset:1024 sc1\n"
"v_mfma_f32_16x16x32_bf16 v[4:7],a[36:39],v[96:99],v[4:7]\nv_mfma_f32_16x16x32_bf16 v[8:11],a[40:43],v[96:99],v[8:11]\nv_mfma_f32_16x16x32_bf16 v[24:27],a[40:43],v[100:103],v[24:27]\nv_mfma_f32_16x16x32_bf16 v[40:43],a[40:43],v[104:107],v[40:43]\nv_mfma_f32_16x16x32_bf16 v[56:59],a[40:43],v[108:111],v[56:59]\nv_mfma_f32_16x16x32_bf16 v[60:63],a[44:47],v[108:111],v[60:63]\nv_mfma_f32_16x16x32_bf16 v[44:47],a[44:47],v[104:107],v[44:47]\n"
"v_mfma_f32_16x16x32_bf16 v[28:31],a[44:47],v[100:103],v[28:31]\nv_mfma_f32_16x16x32_bf16 v[12:15],a[44:47],v[96:99],v[12:15]\ns_waitcnt vmcnt(36)\nv_mfma_f32_16x16x32_bf16 v[0:3],a[48:51],v[112:115],v[0:3]\nbuffer_load_dwordx4 v[96:99],v229,s[8:11],s72 offen offset:2048 sc1\nv_mfma_f32_16x16x32_bf16 v[16:19],a[48:51],v[116:119],v[16:19]\nv_mfma_f32_16x16x32_bf16 v[32:35],a[48:51],v[120:123],v[32:35]\n"
"buffer_load_dwordx4 v[100:103],v229,s[8:11],s73 offen offset:2048 sc1\nv_mfma_f32_16x16x32_bf16 v[48:51],a[48:51],v[124:127],v[48:51]\nv_mfma_f32_16x16x32_bf16 v[52:55],a[52:55],v[124:127],v[52:55]\nbuffer_load_dwordx4 v[104:107],v229,s[8:11],s74 offen offset:2048 sc1\nv_mfma_f32_16x16x32_bf16 v[36:39],a[52:55],v[120:123],v[36:39]\nv_mfma_f32_16x16x32_bf16 v[20:23],a[52:55],v[116:119],v[20:23]\nbuffer_load_dwordx4 v[108:111],v229,s[8:11],s75 offen offset:2048 sc1\n"
"v_mfma_f32_16x16x32_bf16 v[4:7],a[52:55],v[112:115],v[4:7]\nv_mfma_f32_16x16x32_bf16 v[8:11],a[56:59],v[112:115],v[8:11]\nv_mfma_f32_16x16x32_bf16 v[24:27],a[56:59],v[116:119],v[24:27]\nv_mfma_f32_16x16x32_bf16 v[40:43],a[56:59],v[120:123],v[40:43]\nv_mfma_f32_16x16x32_bf16 v[56:59],a[56:59],v[124:127],v[56:59]\nv_mfma_f32_16x16x32_bf16 v[60:63],a[60:63],v[124:127],v[60:63]\nv_mfma_f32_16x16x32_bf16 v[44:47],a[60:63],v[120:123],v[44:47]\n"
"v_mfma_f32_16x16x32_bf16 v[28:31],a[60:63],v[116:119],v[28:31]\nv_mfma_f32_16x16x32_bf16 v[12:15],a[60:63],v[112:115],v[12:15]\ns_waitcnt vmcnt(36)\nv_mfma_f32_16x16x32_bf16 v[0:3],a[64:67],v[128:131],v[0:3]\nbuffer_load_dwordx2 v[112:113],v227,s[8:11],s72 offen offset:3072 sc1\nv_mfma_f32_16x16x32_bf16 v[16:19],a[64:67],v[132:135],v[16:19]\nv_mfma_f32_16x16x32_bf16 v[32:35],a[64:67],v[136:139],v[32:35]\n"
"buffer_load_dwordx2 v[114:115],v228,s[8:11],s72 offen offset:3072 sc1\nv_mfma_f32_16x16x32_bf16 v[48:51],a[64:67],v[140:143],v[48:51]\nv_mfma_f32_16x16x32_bf16 v[52:55],a[68:71],v[140:143],v[52:55]\nbuffer_load_dwordx2 v[116:117],v227,s[8:11],s73 offen offset:3072 sc1\nv_mfma_f32_16x16x32_bf16 v[36:39],a[68:71],v[136:139],v[36:39]\nv_mfma_f32_16x16x32_bf16 v[20:23],a[68:71],v[132:135],v[20:23]\nbuffer_load_dwordx2 v[118:119],v228,s[8:11],s73 offen offset:3072 sc1\n"
"v_mfma_f32_16x16x32_bf16 v[4:7],a[68:71],v[128:131],v[4:7]\nv_mfma_f32_16x16x32_bf16 v[8:11],a[72:75],v[128:131],v[8:11]\nbuffer_load_dwordx2 v[120:121],v227,s[8:11],s74 offen offset:3072 sc1\nv_mfma_f32_16x16x32_bf16 v[24:27],a[72:75],v[132:135],v[24:27]\nv_mfma_f32_16x16x32_bf16 v[40:43],a[72:75],v[136:139],v[40:43]\nbuffer_load_dwordx2 v[122:123],v228,s[8:11],s74 offen offset:3072 sc1\nv_mfma_f32_16x16x32_bf16 v[56:59],a[72:75],v[140:143],v[56:59]\n"
"v_mfma_f32_16x16x32_bf16 v[60:63],a[76:79],v[140:143],v[60:63]\nbuffer_load_dwordx2 v[124:125],v227,s[8:11],s75 offen offset:3072 sc1\nv_mfma_f32_16x16x32_bf16 v[44:47],a[76:79],v[136:139],v[44:47]\nv_mfma_f32_16x16x32_bf16 v[28:31],a[76:79],v[132:135],v[28:31]\nbuffer_load_dwordx2 v[126:127],v228,s[8:11],s75 offen offset:3072 sc1\nv_mfma_f32_16x16x32_bf16 v[12:15],a[76:79],v[128:131],v[12:15]\ns_waitcnt vmcnt(40)\n"
"v_mfma_f32_16x16x32_bf16 v[0:3],a[80:83],v[144:147],v[0:3]\nv_mfma_f32_16x16x32_bf16 v[16:19],a[80:83],v[148:151],v[16:19]\nv_mfma_f32_16x16x32_bf16 v[32:35],a[80:83],v[152:155],v[32:35]\nv_mfma_f32_16x16x32_bf16 v[48:51],a[80:83],v[156:159],v[48:51]\nv_mfma_f32_16x16x32_bf16 v[52:55],a[84:87],v[156:159],v[52:55]\nv_mfma_f32_16x16x32_bf16 v[36:39],a[84:87],v[152:155],v[36:39]\nv_mfma_f32_16x16x32_bf16 v[20:23],a[84:87],v[148:151],v[20:23]\n"
"v_mfma_f32_16x16x32_bf16 v[4:7],a[84:87],v[144:147],v[4:7]\nv_mfma_f32_16x16x32_bf16 v[8:11],a[88:91],v[144:147],v[8:11]\nv_mfma_f32_16x16x32_bf16 v[24:27],a[88:91],v[148:151],v[24:27]\nv_mfma_f32_16x16x32_bf16 v[40:43],a[88:91],v[152:155],v[40:43]\nv_mfma_f32_16x16x32_bf16 v[56:59],a[88:91],v[156:159],v[56:59]\nv_mfma_f32_16x16x32_bf16 v[60:63],a[92:95],v[156:159],v[60:63]\nv_mfma_f32_16x16x32_bf16 v[44:47],a[92:95],v[152:155],v[44:47]\n"
"v_mfma_f32_16x16x32_bf16 v[28:31],a[92:95],v[148:151],v[28:31]\nv_mfma_f32_16x16x32_bf16 v[12:15],a[92:95],v[144:147],v[12:15]\ns_waitcnt vmcnt(32)\nv_mfma_f32_16x16x32_bf16 v[0:3],a[96:99],v[160:163],v[0:3]\nv_mfma_f32_16x16x32_bf16 v[16:19],a[96:99],v[164:167],v[16:19]\nv_mfma_f32_16x16x32_bf16 v[32:35],a[96:99],v[168:171],v[32:35]\nv_mfma_f32_16x16x32_bf16 v[48:51],a[96:99],v[172:175],v[48:51]\n"
"v_mfma_f32_16x16x32_bf16 v[52:55],a[100:103],v[172:175],v[52:55]\nv_mfma_f32_16x16x32_bf16 v[36:39],a[100:103],v[168:171],v[36:39]\nv_mfma_f32_16x16x32_bf16 v[20:23],a[100:103],v[164:167],v[20:23]\nv_mfma_f32_16x16x32_bf16 v[4:7],a[100:103],v[160:163],v[4:7]\nv_mfma_f32_16x16x32_bf16 v[8:11],a[104:107],v[160:163],v[8:11]\nv_mfma_f32_16x16x32_bf16 v[24:27],a[104:107],v[164:167],v[24:27]\nv_mfma_f32_16x16x32_bf16 v[40:43],a[104:107],v[168:171],v[40:43]\n"
"v_mfma_f32_16x16x32_bf16 v[56:59],a[104:107],v[172:175],v[56:59]\nv_mfma_f32_16x16x32_bf16 v[60:63],a[108:111],v[172:175],v[60:63]\nv_mfma_f32_16x16x32_bf16 v[44:47],a[108:111],v[168:171],v[44:47]\nv_mfma_f32_16x16x32_bf16 v[28:31],a[108:111],v[164:167],v[28:31]\nv_mfma_f32_16x16x32_bf16 v[12:15],a[108:111],v[160:163],v[12:15]\ns_waitcnt vmcnt(28)\nv_mfma_f32_16x16x32_bf16 v[0:3],a[112:115],v[176:179],v[0:3]\n"
"v_mfma_f32_16x16x32_bf16 v[16:19],a[112:115],v[180:183],v[16:19]\nv_mfma_f32_16x16x32_bf16 v[32:35],a[112:115],v[184:187],v[32:35]\nv_mfma_f32_16x16x32_bf16 v[48:51],a[112:115],v[188:191],v[48:51]\nv_mfma_f32_16x16x32_bf16 v[52:55],a[116:119],v[188:191],v[52:55]\nv_mfma_f32_16x16x32_bf16 v[36:39],a[116:119],v[184:187],v[36:39]\nv_mfma_f32_16x16x32_bf16 v[20:23],a[116:119],v[180:183],v[20:23]\nv_mfma_f32_16x16x32_bf16 v[4:7],a[116:119],v[176:179],v[4:7]\n"
"v_mfma_f32_16x16x32_bf16 v[8:11],a[120:123],v[176:179],v[8:11]\nv_mfma_f32_16x16x32_bf16 v[24:27],a[120:123],v[180:183],v[24:27]\nv_mfma_f32_16x16x32_bf16 v[40:43],a[120:123],v[184:187],v[40:43]\nv_mfma_f32_16x16x32_bf16 v[56:59],a[120:123],v[188:191],v[56:59]\nv_mfma_f32_16x16x32_bf16 v[60:63],a[124:127],v[188:191],v[60:63]\nv_mfma_f32_16x16x32_bf16 v[44:47],a[124:127],v[184:187],v[44:47]\nv_mfma_f32_16x16x32_bf16 v[28:31],a[124:127],v[180:183],v[28:31]\n"
"v_mfma_f32_16x16x32_bf16 v[12:15],a[124:127],v[176:179],v[12:15]\ns_waitcnt vmcnt(24)\nv_mfma_f32_16x16x32_bf16 v[0:3],a[128:131],v[192:195],v[0:3]\nv_mfma_f32_16x16x32_bf16 v[16:19],a[128:131],v[196:199],v[16:19]\nv_mfma_f32_16x16x32_bf16 v[32:35],a[128:131],v[200:203],v[32:35]\nv_mfma_f32_16x16x32_bf16 v[48:51],a[128:131],v[204:207],v[48:51]\nv_mfma_f32_16x16x32_bf16 v[52:55],a[132:135],v[204:207],v[52:55]\n"
"v_mfma_f32_16x16x32_bf16 v[36:39],a[132:135],v[200:203],v[36:39]\nv_mfma_f32_16x16x32_bf16 v[20:23],a[132:135],v[196:199],v[20:23]\nv_mfma_f32_16x16x32_bf16 v[4:7],a[132:135],v[192:195],v[4:7]\nv_mfma_f32_16x16x32_bf16 v[8:11],a[136:139],v[192:195],v[8:11]\nv_mfma_f32_16x16x32_bf16 v[24:27],a[136:139],v[196:199],v[24:27]\nv_mfma_f32_16x16x32_bf16 v[40:43],a[136:139],v[200:203],v[40:43]\nv_mfma_f32_16x16x32_bf16 v[56:59],a[136:139],v[204:207],v[56:59]\n"
"v_mfma_f32_16x16x32_bf16 v[60:63],a[140:143],v[204:207],v[60:63]\nv_mfma_f32_16x16x32_bf16 v[44:47],a[140:143],v[200:203],v[44:47]\nv_mfma_f32_16x16x32_bf16 v[28:31],a[140:143],v[196:199],v[28:31]\nv_mfma_f32_16x16x32_bf16 v[12:15],a[140:143],v[192:195],v[12:15]\ns_waitcnt vmcnt(20)\nv_mfma_f32_16x16x32_bf16 v[0:3],a[144:147],v[208:211],v[0:3]\nv_mfma_f32_16x16x32_bf16 v[16:19],a[144:147],v[212:215],v[16:19]\n"
"v_mfma_f32_16x16x32_bf16 v[32:35],a[144:147],v[216:219],v[32:35]\nv_mfma_f32_16x16x32_bf16 v[48:51],a[144:147],v[220:223],v[48:51]\nv_mfma_f32_16x16x32_bf16 v[52:55],a[148:151],v[220:223],v[52:55]\nv_mfma_f32_16x16x32_bf16 v[36:39],a[148:151],v[216:219],v[36:39]\nv_mfma_f32_16x16x32_bf16 v[20:23],a[148:151],v[212:215],v[20:23]\nv_mfma_f32_16x16x32_bf16 v[4:7],a[148:151],v[208:211],v[4:7]\nv_mfma_f32_16x16x32_bf16 v[8:11],a[152:155],v[208:211],v[8:11]\n"
"v_mfma_f32_16x16x32_bf16 v[24:27],a[152:155],v[212:215],v[24:27]\nv_mfma_f32_16x16x32_bf16 v[40:43],a[152:155],v[216:219],v[40:43]\nv_mfma_f32_16x16x32_bf16 v[56:59],a[152:155],v[220:223],v[56:59]\nv_mfma_f32_16x16x32_bf16 v[60:63],a[156:159],v[220:223],v[60:63]\nv_mfma_f32_16x16x32_bf16 v[44:47],a[156:159],v[216:219],v[44:47]\nv_mfma_f32_16x16x32_bf16 v[28:31],a[156:159],v[212:215],v[28:31]\nv_mfma_f32_16x16x32_bf16 v[12:15],a[156:159],v[208:211],v[12:15]\n"
"s_waitcnt vmcnt(16)\nv_mfma_f32_16x16x32_bf16 v[0:3],a[160:163],v[64:67],v[0:3]\nv_mfma_f32_16x16x32_bf16 v[16:19],a[160:163],v[68:71],v[16:19]\nv_mfma_f32_16x16x32_bf16 v[32:35],a[160:163],v[72:75],v[32:35]\nv_mfma_f32_16x16x32_bf16 v[48:51],a[160:163],v[76:79],v[48:51]\nv_mfma_f32_16x16x32_bf16 v[52:55],a[164:167],v[76:79],v[52:55]\nv_mfma_f32_16x16x32_bf16 v[36:39],a[164:167],v[72:75],v[36:39]\n"
"v_mfma_f32_16x16x32_bf16 v[20:23],a[164:167],v[68:71],v[20:23]\nv_mfma_f32_16x16x32_bf16 v[4:7],a[164:167],v[64:67],v[4:7]\nv_mfma_f32_16x16x32_bf16 v[8:11],a[168:171],v[64:67],v[8:11]\nv_mfma_f32_16x16x32_bf16 v[24:27],a[168:171],v[68:71],v[24:27]\nv_mfma_f32_16x16x32_bf16 v[40:43],a[168:171],v[72:75],v[40:43]\nv_mfma_f32_16x16x32_bf16 v[56:59],a[168:171],v[76:79],v[56:59]\nv_mfma_f32_16x16x32_bf16 v[60:63],a[172:175],v[76:79],v[60:63]\n"
"v_mfma_f32_16x16x32_bf16 v[44:47],a[172:175],v[72:75],v[44:47]\nv_mfma_f32_16x16x32_bf16 v[28:31],a[172:175],v[68:71],v[28:31]\nv_mfma_f32_16x16x32_bf16 v[12:15],a[172:175],v[64:67],v[12:15]\ns_waitcnt vmcnt(12)\nv_mfma_f32_16x16x32_bf16 v[0:3],a[176:179],v[80:83],v[0:3]\nv_mfma_f32_16x16x32_bf16 v[16:19],a[176:179],v[84:87],v[16:19]\nv_mfma_f32_16x16x32_bf16 v[32:35],a[176:179],v[88:91],v[32:35]\n"
"v_mfma_f32_16x16x32_bf16 v[48:51],a[176:179],v[92:95],v[48:51]\nv_mfma_f32_16x16x32_bf16 v[52:55],a[180:183],v[92:95],v[52:55]\nv_mfma_f32_16x16x32_bf16 v[36:39],a[180:183],v[88:91],v[36:39]\nv_mfma_f32_16x16x32_bf16 v[20:23],a[180:183],v[84:87],v[20:23]\nv_mfma_f32_16x16x32_bf16 v[4:7],a[180:183],v[80:83],v[4:7]\nv_mfma_f32_16x16x32_bf16 v[8:11],a[184:187],v[80:83],v[8:11]\nv_mfma_f32_16x16x32_bf16 v[24:27],a[184:187],v[84:87],v[24:27]\n"
"v_mfma_f32_16x16x32_bf16 v[40:43],a[184:187],v[88:91],v[40:43]\nv_mfma_f32_16x16x32_bf16 v[56:59],a[184:187],v[92:95],v[56:59]\nv_mfma_f32_16x16x32_bf16 v[60:63],a[188:191],v[92:95],v[60:63]\nv_mfma_f32_16x16x32_bf16 v[44:47],a[188:191],v[88:91],v[44:47]\nv_mfma_f32_16x16x32_bf16 v[28:31],a[188:191],v[84:87],v[28:31]\nv_mfma_f32_16x16x32_bf16 v[12:15],a[188:191],v[80:83],v[12:15]\ns_waitcnt vmcnt(8)\n"
"v_mfma_f32_16x16x32_bf16 v[0:3],a[192:195],v[96:99],v[0:3]\nv_mfma_f32_16x16x32_bf16 v[16:19],a[192:195],v[100:103],v[16:19]\nv_mfma_f32_16x16x32_bf16 v[32:35],a[192:195],v[104:107],v[32:35]\nv_mfma_f32_16x16x32_bf16 v[48:51],a[192:195],v[108:111],v[48:51]\nv_mfma_f32_16x16x32_bf16 v[52:55],a[196:199],v[108:111],v[52:55]\nv_mfma_f32_16x16x32_bf16 v[36:39],a[196:199],v[104:107],v[36:39]\nv_mfma_f32_16x16x32_bf16 v[20:23],a[196:199],v[100:103],v[20:23]\n"
"v_mfma_f32_16x16x32_bf16 v[4:7],a[196:199],v[96:99],v[4:7]\nv_mfma_f32_16x16x32_bf16 v[8:11],a[200:203],v[96:99],v[8:11]\nv_mfma_f32_16x16x32_bf16 v[24:27],a[200:203],v[100:103],v[24:27]\nv_mfma_f32_16x16x32_bf16 v[40:43],a[200:203],v[104:107],v[40:43]\nv_mfma_f32_16x16x32_bf16 v[56:59],a[200:203],v[108:111],v[56:59]\nv_mfma_f32_16x16x32_bf16 v[60:63],a[204:207],v[108:111],v[60:63]\nv_mfma_f32_16x16x32_bf16 v[44:47],a[204:207],v[104:107],v[44:47]\n"
"v_mfma_f32_16x16x32_bf16 v[28:31],a[204:207],v[100:103],v[28:31]\nv_mfma_f32_16x16x32_bf16 v[12:15],a[204:207],v[96:99],v[12:15]\ns_waitcnt vmcnt(0)\nv_mfma_f32_16x16x32_bf16 v[0:3],a[208:211],v[112:115],v[0:3]\nv_mfma_f32_16x16x32_bf16 v[16:19],a[208:211],v[116:119],v[16:19]\nv_mfma_f32_16x16x32_bf16 v[32:35],a[208:211],v[120:123],v[32:35]\nv_mfma_f32_16x16x32_bf16 v[48:51],a[208:211],v[124:127],v[48:51]\n"
"v_mfma_f32_16x16x32_bf16 v[52:55],a[212:215],v[124:127],v[52:55]\nv_mfma_f32_16x16x32_bf16 v[36:39],a[212:215],v[120:123],v[36:39]\nv_mfma_f32_16x16x32_bf16 v[20:23],a[212:215],v[116:119],v[20:23]\nv_mfma_f32_16x16x32_bf16 v[4:7],a[212:215],v[112:115],v[4:7]\nv_mfma_f32_16x16x32_bf16 v[8:11],a[216:219],v[112:115],v[8:11]\nv_mfma_f32_16x16x32_bf16 v[24:27],a[216:219],v[116:119],v[24:27]\nv_mfma_f32_16x16x32_bf16 v[40:43],a[216:219],v[120:123],v[40:43]\n"
"v_mfma_f32_16x16x32_bf16 v[56:59],a[216:219],v[124:127],v[56:59]\nv_mfma_f32_16x16x32_bf16 v[60:63],a[220:223],v[124:127],v[60:63]\nv_mfma_f32_16x16x32_bf16 v[44:47],a[220:223],v[120:123],v[44:47]\nv_mfma_f32_16x16x32_bf16 v[28:31],a[220:223],v[116:119],v[28:31]\nv_mfma_f32_16x16x32_bf16 v[12:15],a[220:223],v[112:115],v[12:15]\nds_write_b128 v225,v[0:3] offset:0\nds_write_b128 v225,v[4:7] offset:1024\n"
"ds_write_b128 v225,v[8:11] offset:2048\ns_nop 4\nds_write_b128 v225,v[12:15] offset:3072\nds_write_b128 v225,v[16:19] offset:16384\nds_write_b128 v225,v[20:23] offset:17408\nds_write_b128 v225,v[24:27] offset:18432\nds_write_b128 v225,v[28:31] offset:19456\nds_write_b128 v225,v[32:35] offset:32768\nds_write_b128 v225,v[36:39] offset:33792\nds_write_b128 v225,v[40:43] offset:34816\nds_write_b128 v225,v[44:47] offset:35840\n"
"ds_write_b128 v225,v[48:51] offset:49152\nds_write_b128 v225,v[52:55] offset:50176\nds_write_b128 v225,v[56:59] offset:51200\nds_write_b128 v225,v[60:63] offset:52224\ns_waitcnt lgkmcnt(0)\ns_barrier\nds_read_b128 v[64:67],v226 offset:0\nds_read_b128 v[68:71],v226 offset:1024\nds_read_b128 v[72:75],v226 offset:2048\nds_read_b128 v[76:79],v226 offset:3072\nds_read_b128 v[80:83],v226 offset:4096\nds_read_b128 v[84:87],v226 offset:5120\n"
"ds_read_b128 v[88:91],v226 offset:6144\nds_read_b128 v[92:95],v226 offset:7168\nds_read_b128 v[96:99],v226 offset:8192\nds_read_b128 v[100:103],v226 offset:9216\nds_read_b128 v[104:107],v226 offset:10240\nds_read_b128 v[108:111],v226 offset:11264\ns_waitcnt lgkmcnt(8)\nds_read_b128 v[112:115],v226 offset:12288\nds_read_b128 v[116:119],v226 offset:13312\nds_read_b128 v[120:123],v226 offset:14336\nds_read_b128 v[124:127],v226 offset:15360\n"
"v_lshlrev_b32 v0,16,v232\nv_and_b32 v1,0xffff0000,v232\nv_lshlrev_b32 v2,16,v233\nv_and_b32 v3,0xffff0000,v233\nv_lshlrev_b32 v4,16,v234\nv_and_b32 v5,0xffff0000,v234\nv_lshlrev_b32 v6,16,v235\nv_and_b32 v7,0xffff0000,v235\nv_lshlrev_b32 v8,16,v236\nv_and_b32 v9,0xffff0000,v236\nv_lshlrev_b32 v10,16,v237\nv_and_b32 v11,0xffff0000,v237\nv_lshlrev_b32 v12,16,v238\nv_and_b32 v13,0xffff0000,v238\nv_lshlrev_b32 v14,16,v239\n"
"v_and_b32 v15,0xffff0000,v239\ns_waitcnt lgkmcnt(11)\nv_add_f32 v64,v64,v80\nv_add_f32 v65,v65,v81\nv_add_f32 v66,v66,v82\nv_add_f32 v67,v67,v83\ns_waitcnt lgkmcnt(10)\nv_add_f32 v68,v68,v84\nv_add_f32 v69,v69,v85\nv_add_f32 v70,v70,v86\nv_add_f32 v71,v71,v87\ns_waitcnt lgkmcnt(9)\nv_add_f32 v72,v72,v88\nv_add_f32 v73,v73,v89\nv_add_f32 v74,v74,v90\nv_add_f32 v75,v75,v91\ns_waitcnt lgkmcnt(8)\nv_add_f32 v76,v76,v92\n"
"v_add_f32 v77,v77,v93\nv_add_f32 v78,v78,v94\nv_add_f32 v79,v79,v95\ns_waitcnt lgkmcnt(7)\nv_add_f32 v64,v64,v96\nv_add_f32 v65,v65,v97\nv_add_f32 v66,v66,v98\nv_add_f32 v67,v67,v99\ns_waitcnt lgkmcnt(6)\nv_add_f32 v68,v68,v100\nv_add_f32 v69,v69,v101\nv_add_f32 v70,v70,v102\nv_add_f32 v71,v71,v103\ns_waitcnt lgkmcnt(5)\nv_add_f32 v72,v72,v104\nv_add_f32 v73,v73,v105\nv_add_f32 v74,v74,v106\nv_add_f32 v75,v75,v107\n"
"s_waitcnt lgkmcnt(4)\nv_add_f32 v76,v76,v108\nv_add_f32 v77,v77,v109\nv_add_f32 v78,v78,v110\nv_add_f32 v79,v79,v111\ns_waitcnt lgkmcnt(3)\nv_add_f32 v64,v64,v112\nv_add_f32 v65,v65,v113\nv_add_f32 v66,v66,v114\nv_add_f32 v67,v67,v115\ns_waitcnt lgkmcnt(2)\nv_add_f32 v68,v68,v116\nv_add_f32 v69,v69,v117\nv_add_f32 v70,v70,v118\nv_add_f32 v71,v71,v119\ns_waitcnt lgkmcnt(1)\nv_add_f32 v72,v72,v120\nv_add_f32 v73,v73,v121\n"
"v_add_f32 v74,v74,v122\nv_add_f32 v75,v75,v123\ns_waitcnt lgkmcnt(0)\nv_add_f32 v76,v76,v124\nv_add_f32 v77,v77,v125\nv_add_f32 v78,v78,v126\nv_add_f32 v79,v79,v127\nv_add_f32 v0,v0,v64\nv_add_f32 v1,v1,v65\nv_add_f32 v2,v2,v66\nv_add_f32 v3,v3,v67\nv_cvt_pk_bf16_f32 v16,v0,v1\nv_cvt_pk_bf16_f32 v17,v2,v3\nbuffer_store_dwordx2 v[16:17],v224,s[68:71],s66 offen offset:0\nv_add_f32 v4,v4,v68\nv_add_f32 v5,v5,v69\n"
"v_add_f32 v6,v6,v70\nv_add_f32 v7,v7,v71\nv_cvt_pk_bf16_f32 v18,v4,v5\nv_cvt_pk_bf16_f32 v19,v6,v7\nbuffer_store_dwordx2 v[18:19],v224,s[68:71],s66 offen offset:32\nv_add_f32 v8,v8,v72\nv_add_f32 v9,v9,v73\nv_add_f32 v10,v10,v74\nv_add_f32 v11,v11,v75\nv_cvt_pk_bf16_f32 v20,v8,v9\nv_cvt_pk_bf16_f32 v21,v10,v11\nbuffer_store_dwordx2 v[20:21],v224,s[68:71],s66 offen offset:64\nv_add_f32 v12,v12,v76\n"
"v_add_f32 v13,v13,v77\nv_add_f32 v14,v14,v78\nv_add_f32 v15,v15,v79\nv_cvt_pk_bf16_f32 v22,v12,v13\nv_cvt_pk_bf16_f32 v23,v14,v15\nbuffer_store_dwordx2 v[22:23],v224,s[68:71],s66 offen offset:96\nL_fused_block_fin_59:\ns_branch L_fused_block_end_2\nL_fused_block_rare_23:\nv_max3_f32 v202,v144,v145,v146\nv_max3_f32 v204,v152,v153,v154\nv_max3_f32 v202,v202,v147,v155\nv_max_f32 v202,v202,v204\nv_max3_f32 v203,v148,v149,v150\n"
"v_max3_f32 v205,v156,v157,v158\nv_max3_f32 v203,v203,v151,v159\nv_max_f32 v203,v203,v205\nv_mov_b32 v204,v202\nv_mov_b32 v205,v203\ns_nop 0\nv_permlane16_swap_b32 v204,v202\nv_permlane16_swap_b32 v205,v203\nv_max_f32 v202,v202,v204\nv_max_f32 v203,v203,v205\nv_mov_b32 v204,v202\nv_mov_b32 v205,v203\ns_nop 0\nv_permlane32_swap_b32 v204,v202\nv_permlane32_swap_b32 v205,v203\nv_max_f32 v202,v202,v204\n"
"v_max_f32 v203,v203,v205\nv_max_f32 v202,s38,v202\nv_max_f32 v203,s38,v203\nv_sub_f32 v144,v144,v202\nv_sub_f32 v145,v145,v202\nv_sub_f32 v146,v146,v202\nv_sub_f32 v147,v147,v202\nv_sub_f32 v152,v152,v202\nv_sub_f32 v153,v153,v202\nv_sub_f32 v154,v154,v202\nv_sub_f32 v155,v155,v202\nv_sub_f32 v148,v148,v203\nv_sub_f32 v149,v149,v203\nv_sub_f32 v150,v150,v203\nv_sub_f32 v151,v151,v203\nv_sub_f32 v156,v156,v203\n"
"v_sub_f32 v157,v157,v203\nv_sub_f32 v158,v158,v203\nv_sub_f32 v159,v159,v203\nv_exp_f32 v160,v144\nv_exp_f32 v164,v148\nv_exp_f32 v161,v145\nv_exp_f32 v165,v149\nv_exp_f32 v162,v146\nv_exp_f32 v166,v150\nv_exp_f32 v163,v147\nv_exp_f32 v167,v151\nv_exp_f32 v168,v152\nv_exp_f32 v172,v156\nv_exp_f32 v169,v153\nv_exp_f32 v173,v157\nv_exp_f32 v170,v154\nv_exp_f32 v174,v158\nv_exp_f32 v171,v155\nv_exp_f32 v175,v159\n"
"v_sub_f32 v184,v184,v202\nv_sub_f32 v185,v185,v202\nv_sub_f32 v186,v186,v202\nv_sub_f32 v187,v187,v202\nv_sub_f32 v188,v188,v203\nv_sub_f32 v189,v189,v203\nv_sub_f32 v190,v190,v203\nv_sub_f32 v191,v191,v203\nv_add_f32 v194,v160,v161\nv_add_f32 v195,v164,v165\nv_add_f32 v194,v194,v162\nv_add_f32 v195,v195,v166\nv_add_f32 v194,v194,v163\nv_add_f32 v195,v195,v167\nv_add_f32 v194,v194,v168\nv_add_f32 v195,v195,v172\n"
"v_add_f32 v194,v194,v169\nv_add_f32 v195,v195,v173\nv_add_f32 v194,v194,v170\nv_add_f32 v195,v195,v174\nv_add_f32 v194,v194,v171\nv_add_f32 v195,v195,v175\nv_cvt_pk_bf16_f32 v176,v160,v161\nv_cvt_pk_bf16_f32 v180,v164,v165\nv_cvt_pk_bf16_f32 v177,v162,v163\nv_cvt_pk_bf16_f32 v181,v166,v167\nv_cvt_pk_bf16_f32 v178,v168,v169\nv_cvt_pk_bf16_f32 v182,v172,v173\nv_cvt_pk_bf16_f32 v179,v170,v171\nv_cvt_pk_bf16_f32 v183,v174,v175\n"
"v_sub_f32 v128,v128,v202\nv_sub_f32 v129,v129,v202\nv_sub_f32 v130,v130,v202\nv_sub_f32 v131,v131,v202\nv_sub_f32 v136,v136,v202\nv_sub_f32 v137,v137,v202\nv_sub_f32 v138,v138,v202\nv_sub_f32 v139,v139,v202\nv_sub_f32 v132,v132,v203\nv_sub_f32 v133,v133,v203\nv_sub_f32 v134,v134,v203\nv_sub_f32 v135,v135,v203\nv_sub_f32 v140,v140,v203\nv_sub_f32 v141,v141,v203\nv_sub_f32 v142,v142,v203\nv_sub_f32 v143,v143,v203\n"
"s_cmp_lg_u32 s38,0\ns_mov_b32 s38,0\ns_mov_b32 s37,0x49800000\ns_cbranch_scc1 L_fused_block_rarer_24\nv_exp_f32 v206,-v202\nv_exp_f32 v207,-v203\ns_nop 0\nv_mul_f32 v192,v206,v192\nv_mul_f32 v193,v207,v193\nv_mul_f32 v0,v206,v0\nv_mul_f32 v1,v206,v1\nv_mul_f32 v2,v206,v2\nv_mul_f32 v3,v206,v3\nv_mul_f32 v4,v207,v4\nv_mul_f32 v5,v207,v5\nv_mul_f32 v6,v207,v6\nv_mul_f32 v7,v207,v7\nv_mul_f32 v8,v206,v8\n"
"v_mul_f32 v9,v206,v9\nv_mul_f32 v10,v206,v10\nv_mul_f32 v11,v206,v11\nv_mul_f32 v12,v207,v12\nv_mul_f32 v13,v207,v13\nv_mul_f32 v14,v207,v14\nv_mul_f32 v15,v207,v15\nv_mul_f32 v16,v206,v16\nv_mul_f32 v17,v206,v17\nv_mul_f32 v18,v206,v18\nv_mul_f32 v19,v206,v19\nv_mul_f32 v20,v207,v20\nv_mul_f32 v21,v207,v21\nv_mul_f32 v22,v207,v22\nv_mul_f32 v23,v207,v23\nv_mul_f32 v24,v206,v24\nv_mul_f32 v25,v206,v25\n"
"v_mul_f32 v26,v206,v26\nv_mul_f32 v27,v206,v27\nv_mul_f32 v28,v207,v28\nv_mul_f32 v29,v207,v29\nv_mul_f32 v30,v207,v30\nv_mul_f32 v31,v207,v31\nv_mul_f32 v32,v206,v32\nv_mul_f32 v33,v206,v33\nv_mul_f32 v34,v206,v34\nv_mul_f32 v35,v206,v35\nv_mul_f32 v36,v207,v36\nv_mul_f32 v37,v207,v37\nv_mul_f32 v38,v207,v38\nv_mul_f32 v39,v207,v39\nv_mul_f32 v40,v206,v40\nv_mul_f32 v41,v206,v41\nv_mul_f32 v42,v206,v42\n"
"v_mul_f32 v43,v206,v43\nv_mul_f32 v44,v207,v44\nv_mul_f32 v45,v207,v45\nv_mul_f32 v46,v207,v46\nv_mul_f32 v47,v207,v47\nv_mul_f32 v48,v206,v48\nv_mul_f32 v49,v206,v49\nv_mul_f32 v50,v206,v50\nv_mul_f32 v51,v206,v51\nv_mul_f32 v52,v207,v52\nv_mul_f32 v53,v207,v53\nv_mul_f32 v54,v207,v54\nv_mul_f32 v55,v207,v55\nv_mul_f32 v56,v206,v56\nv_mul_f32 v57,v206,v57\nv_mul_f32 v58,v206,v58\nv_mul_f32 v59,v206,v59\n"
"v_mul_f32 v60,v207,v60\nv_mul_f32 v61,v207,v61\nv_mul_f32 v62,v207,v62\nv_mul_f32 v63,v207,v63\nv_mul_f32 v64,v206,v64\nv_mul_f32 v65,v206,v65\nv_mul_f32 v66,v206,v66\nv_mul_f32 v67,v206,v67\nv_mul_f32 v68,v207,v68\nv_mul_f32 v69,v207,v69\nv_mul_f32 v70,v207,v70\nv_mul_f32 v71,v207,v71\nv_mul_f32 v72,v206,v72\nv_mul_f32 v73,v206,v73\nv_mul_f32 v74,v206,v74\nv_mul_f32 v75,v206,v75\nv_mul_f32 v76,v207,v76\n"
"v_mul_f32 v77,v207,v77\nv_mul_f32 v78,v207,v78\nv_mul_f32 v79,v207,v79\nv_mul_f32 v80,v206,v80\nv_mul_f32 v81,v206,v81\nv_mul_f32 v82,v206,v82\nv_mul_f32 v83,v206,v83\nv_mul_f32 v84,v207,v84\nv_mul_f32 v85,v207,v85\nv_mul_f32 v86,v207,v86\nv_mul_f32 v87,v207,v87\nv_mul_f32 v88,v206,v88\nv_mul_f32 v89,v206,v89\nv_mul_f32 v90,v206,v90\nv_mul_f32 v91,v206,v91\nv_mul_f32 v92,v207,v92\nv_mul_f32 v93,v207,v93\n"
"v_mul_f32 v94,v207,v94\nv_mul_f32 v95,v207,v95\nv_mul_f32 v96,v206,v96\nv_mul_f32 v97,v206,v97\nv_mul_f32 v98,v206,v98\nv_mul_f32 v99,v206,v99\nv_mul_f32 v100,v207,v100\nv_mul_f32 v101,v207,v101\nv_mul_f32 v102,v207,v102\nv_mul_f32 v103,v207,v103\nv_mul_f32 v104,v206,v104\nv_mul_f32 v105,v206,v105\nv_mul_f32 v106,v206,v106\nv_mul_f32 v107,v206,v107\nv_mul_f32 v108,v207,v108\nv_mul_f32 v109,v207,v109\n"
"v_mul_f32 v110,v207,v110\nv_mul_f32 v111,v207,v111\nv_mul_f32 v112,v206,v112\nv_mul_f32 v113,v206,v113\nv_mul_f32 v114,v206,v114\nv_mul_f32 v115,v206,v115\nv_mul_f32 v116,v207,v116\nv_mul_f32 v117,v207,v117\nv_mul_f32 v118,v207,v118\nv_mul_f32 v119,v207,v119\nv_mul_f32 v120,v206,v120\nv_mul_f32 v121,v206,v121\nv_mul_f32 v122,v206,v122\nv_mul_f32 v123,v206,v123\nv_mul_f32 v124,v207,v124\nv_mul_f32 v125,v207,v125\n"
"v_mul_f32 v126,v207,v126\nv_mul_f32 v127,v207,v127\ns_branch L_fused_block_rarer_24\nL_fused_block_rare_27:\nv_max3_f32 v202,v128,v129,v130\nv_max3_f32 v204,v136,v137,v138\nv_max3_f32 v202,v202,v131,v139\nv_max_f32 v202,v202,v204\nv_max3_f32 v203,v132,v133,v134\nv_max3_f32 v205,v140,v141,v142\nv_max3_f32 v203,v203,v135,v143\nv_max_f32 v203,v203,v205\nv_mov_b32 v204,v202\nv_mov_b32 v205,v203\ns_nop 0\n"
"v_permlane16_swap_b32 v204,v202\nv_permlane16_swap_b32 v205,v203\nv_max_f32 v202,v202,v204\nv_max_f32 v203,v203,v205\nv_mov_b32 v204,v202\nv_mov_b32 v205,v203\ns_nop 0\nv_permlane32_swap_b32 v204,v202\nv_permlane32_swap_b32 v205,v203\nv_max_f32 v202,v202,v204\nv_max_f32 v203,v203,v205\nv_max_f32 v202,s38,v202\nv_max_f32 v203,s38,v203\nv_sub_f32 v128,v128,v202\nv_sub_f32 v129,v129,v202\nv_sub_f32 v130,v130,v202\n"
"v_sub_f32 v131,v131,v202\nv_sub_f32 v136,v136,v202\nv_sub_f32 v137,v137,v202\nv_sub_f32 v138,v138,v202\nv_sub_f32 v139,v139,v202\nv_sub_f32 v132,v132,v203\nv_sub_f32 v133,v133,v203\nv_sub_f32 v134,v134,v203\nv_sub_f32 v135,v135,v203\nv_sub_f32 v140,v140,v203\nv_sub_f32 v141,v141,v203\nv_sub_f32 v142,v142,v203\nv_sub_f32 v143,v143,v203\nv_exp_f32 v160,v128\nv_exp_f32 v164,v132\nv_exp_f32 v161,v129\n"
"v_exp_f32 v165,v133\nv_exp_f32 v162,v130\nv_exp_f32 v166,v134\nv_exp_f32 v163,v131\nv_exp_f32 v167,v135\nv_exp_f32 v168,v136\nv_exp_f32 v172,v140\nv_exp_f32 v169,v137\nv_exp_f32 v173,v141\nv_exp_f32 v170,v138\nv_exp_f32 v174,v142\nv_exp_f32 v171,v139\nv_exp_f32 v175,v143\nv_sub_f32 v184,v184,v202\nv_sub_f32 v185,v185,v202\nv_sub_f32 v186,v186,v202\nv_sub_f32 v187,v187,v202\nv_sub_f32 v188,v188,v203\n"
"v_sub_f32 v189,v189,v203\nv_sub_f32 v190,v190,v203\nv_sub_f32 v191,v191,v203\nv_add_f32 v194,v160,v161\nv_add_f32 v195,v164,v165\nv_add_f32 v194,v194,v162\nv_add_f32 v195,v195,v166\nv_add_f32 v194,v194,v163\nv_add_f32 v195,v195,v167\nv_add_f32 v194,v194,v168\nv_add_f32 v195,v195,v172\nv_add_f32 v194,v194,v169\nv_add_f32 v195,v195,v173\nv_add_f32 v194,v194,v170\nv_add_f32 v195,v195,v174\nv_add_f32 v194,v194,v171\n"
"v_add_f32 v195,v195,v175\nv_cvt_pk_bf16_f32 v176,v160,v161\nv_cvt_pk_bf16_f32 v180,v164,v165\nv_cvt_pk_bf16_f32 v177,v162,v163\nv_cvt_pk_bf16_f32 v181,v166,v167\nv_cvt_pk_bf16_f32 v178,v168,v169\nv_cvt_pk_bf16_f32 v182,v172,v173\nv_cvt_pk_bf16_f32 v179,v170,v171\nv_cvt_pk_bf16_f32 v183,v174,v175\nv_sub_f32 v144,v144,v202\nv_sub_f32 v145,v145,v202\nv_sub_f32 v146,v146,v202\nv_sub_f32 v147,v147,v202\n"
"v_sub_f32 v152,v152,v202\nv_sub_f32 v153,v153,v202\nv_sub_f32 v154,v154,v202\nv_sub_f32 v155,v155,v202\nv_sub_f32 v148,v148,v203\nv_sub_f32 v149,v149,v203\nv_sub_f32 v150,v150,v203\nv_sub_f32 v151,v151,v203\nv_sub_f32 v156,v156,v203\nv_sub_f32 v157,v157,v203\nv_sub_f32 v158,v158,v203\nv_sub_f32 v159,v159,v203\ns_cmp_lg_u32 s38,0\ns_mov_b32 s38,0\ns_mov_b32 s37,0x49800000\ns_cbranch_scc1 L_fused_block_rarer_28\n"
"v_exp_f32 v206,-v202\nv_exp_f32 v207,-v203\ns_nop 0\nv_mul_f32 v192,v206,v192\nv_mul_f32 v193,v207,v193\nv_mul_f32 v0,v206,v0\nv_mul_f32 v1,v206,v1\nv_mul_f32 v2,v206,v2\nv_mul_f32 v3,v206,v3\nv_mul_f32 v4,v207,v4\nv_mul_f32 v5,v207,v5\nv_mul_f32 v6,v207,v6\nv_mul_f32 v7,v207,v7\nv_mul_f32 v8,v206,v8\nv_mul_f32 v9,v206,v9\nv_mul_f32 v10,v206,v10\nv_mul_f32 v11,v206,v11\nv_mul_f32 v12,v207,v12\nv_mul_f32 v13,v207,v13\n"
"v_mul_f32 v14,v207,v14\nv_mul_f32 v15,v207,v15\nv_mul_f32 v16,v206,v16\nv_mul_f32 v17,v206,v17\nv_mul_f32 v18,v206,v18\nv_mul_f32 v19,v206,v19\nv_mul_f32 v20,v207,v20\nv_mul_f32 v21,v207,v21\nv_mul_f32 v22,v207,v22\nv_mul_f32 v23,v207,v23\nv_mul_f32 v24,v206,v24\nv_mul_f32 v25,v206,v25\nv_mul_f32 v26,v206,v26\nv_mul_f32 v27,v206,v27\nv_mul_f32 v28,v207,v28\nv_mul_f32 v29,v207,v29\nv_mul_f32 v30,v207,v30\n"
"v_mul_f32 v31,v207,v31\nv_mul_f32 v32,v206,v32\nv_mul_f32 v33,v206,v33\nv_mul_f32 v34,v206,v34\nv_mul_f32 v35,v206,v35\nv_mul_f32 v36,v207,v36\nv_mul_f32 v37,v207,v37\nv_mul_f32 v38,v207,v38\nv_mul_f32 v39,v207,v39\nv_mul_f32 v40,v206,v40\nv_mul_f32 v41,v206,v41\nv_mul_f32 v42,v206,v42\nv_mul_f32 v43,v206,v43\nv_mul_f32 v44,v207,v44\nv_mul_f32 v45,v207,v45\nv_mul_f32 v46,v207,v46\nv_mul_f32 v47,v207,v47\n"
"v_mul_f32 v48,v206,v48\nv_mul_f32 v49,v206,v49\nv_mul_f32 v50,v206,v50\nv_mul_f32 v51,v206,v51\nv_mul_f32 v52,v207,v52\nv_mul_f32 v53,v207,v53\nv_mul_f32 v54,v207,v54\nv_mul_f32 v55,v207,v55\nv_mul_f32 v56,v206,v56\nv_mul_f32 v57,v206,v57\nv_mul_f32 v58,v206,v58\nv_mul_f32 v59,v206,v59\nv_mul_f32 v60,v207,v60\nv_mul_f32 v61,v207,v61\nv_mul_f32 v62,v207,v62\nv_mul_f32 v63,v207,v63\nv_mul_f32 v64,v206,v64\n"
"v_mul_f32 v65,v206,v65\nv_mul_f32 v66,v206,v66\nv_mul_f32 v67,v206,v67\nv_mul_f32 v68,v207,v68\nv_mul_f32 v69,v207,v69\nv_mul_f32 v70,v207,v70\nv_mul_f32 v71,v207,v71\nv_mul_f32 v72,v206,v72\nv_mul_f32 v73,v206,v73\nv_mul_f32 v74,v206,v74\nv_mul_f32 v75,v206,v75\nv_mul_f32 v76,v207,v76\nv_mul_f32 v77,v207,v77\nv_mul_f32 v78,v207,v78\nv_mul_f32 v79,v207,v79\nv_mul_f32 v80,v206,v80\nv_mul_f32 v81,v206,v81\n"
"v_mul_f32 v82,v206,v82\nv_mul_f32 v83,v206,v83\nv_mul_f32 v84,v207,v84\nv_mul_f32 v85,v207,v85\nv_mul_f32 v86,v207,v86\nv_mul_f32 v87,v207,v87\nv_mul_f32 v88,v206,v88\nv_mul_f32 v89,v206,v89\nv_mul_f32 v90,v206,v90\nv_mul_f32 v91,v206,v91\nv_mul_f32 v92,v207,v92\nv_mul_f32 v93,v207,v93\nv_mul_f32 v94,v207,v94\nv_mul_f32 v95,v207,v95\nv_mul_f32 v96,v206,v96\nv_mul_f32 v97,v206,v97\nv_mul_f32 v98,v206,v98\n"
"v_mul_f32 v99,v206,v99\nv_mul_f32 v100,v207,v100\nv_mul_f32 v101,v207,v101\nv_mul_f32 v102,v207,v102\nv_mul_f32 v103,v207,v103\nv_mul_f32 v104,v206,v104\nv_mul_f32 v105,v206,v105\nv_mul_f32 v106,v206,v106\nv_mul_f32 v107,v206,v107\nv_mul_f32 v108,v207,v108\nv_mul_f32 v109,v207,v109\nv_mul_f32 v110,v207,v110\nv_mul_f32 v111,v207,v111\nv_mul_f32 v112,v206,v112\nv_mul_f32 v113,v206,v113\nv_mul_f32 v114,v206,v114\n"
"v_mul_f32 v115,v206,v115\nv_mul_f32 v116,v207,v116\nv_mul_f32 v117,v207,v117\nv_mul_f32 v118,v207,v118\nv_mul_f32 v119,v207,v119\nv_mul_f32 v120,v206,v120\nv_mul_f32 v121,v206,v121\nv_mul_f32 v122,v206,v122\nv_mul_f32 v123,v206,v123\nv_mul_f32 v124,v207,v124\nv_mul_f32 v125,v207,v125\nv_mul_f32 v126,v207,v126\nv_mul_f32 v127,v207,v127\ns_branch L_fused_block_rarer_28\nL_fused_block_rare_31:\nv_max3_f32 v202,v144,v145,v146\n"
"v_max3_f32 v204,v152,v153,v154\nv_max3_f32 v202,v202,v147,v155\nv_max_f32 v202,v202,v204\nv_max3_f32 v203,v148,v149,v150\nv_max3_f32 v205,v156,v157,v158\nv_max3_f32 v203,v203,v151,v159\nv_max_f32 v203,v203,v205\nv_mov_b32 v204,v202\nv_mov_b32 v205,v203\ns_nop 0\nv_permlane16_swap_b32 v204,v202\nv_permlane16_swap_b32 v205,v203\nv_max_f32 v202,v202,v204\nv_max_f32 v203,v203,v205\nv_mov_b32 v204,v202\n"
"v_mov_b32 v205,v203\ns_nop 0\nv_permlane32_swap_b32 v204,v202\nv_permlane32_swap_b32 v205,v203\nv_max_f32 v202,v202,v204\nv_max_f32 v203,v203,v205\nv_max_f32 v202,s38,v202\nv_max_f32 v203,s38,v203\nv_sub_f32 v144,v144,v202\nv_sub_f32 v145,v145,v202\nv_sub_f32 v146,v146,v202\nv_sub_f32 v147,v147,v202\nv_sub_f32 v152,v152,v202\nv_sub_f32 v153,v153,v202\nv_sub_f32 v154,v154,v202\nv_sub_f32 v155,v155,v202\n"
"v_sub_f32 v148,v148,v203\nv_sub_f32 v149,v149,v203\nv_sub_f32 v150,v150,v203\nv_sub_f32 v151,v151,v203\nv_sub_f32 v156,v156,v203\nv_sub_f32 v157,v157,v203\nv_sub_f32 v158,v158,v203\nv_sub_f32 v159,v159,v203\nv_exp_f32 v160,v144\nv_exp_f32 v164,v148\nv_exp_f32 v161,v145\nv_exp_f32 v165,v149\nv_exp_f32 v162,v146\nv_exp_f32 v166,v150\nv_exp_f32 v163,v147\nv_exp_f32 v167,v151\nv_exp_f32 v168,v152\nv_exp_f32 v172,v156\n"
"v_exp_f32 v169,v153\nv_exp_f32 v173,v157\nv_exp_f32 v170,v154\nv_exp_f32 v174,v158\nv_exp_f32 v171,v155\nv_exp_f32 v175,v159\nv_sub_f32 v184,v184,v202\nv_sub_f32 v185,v185,v202\nv_sub_f32 v186,v186,v202\nv_sub_f32 v187,v187,v202\nv_sub_f32 v188,v188,v203\nv_sub_f32 v189,v189,v203\nv_sub_f32 v190,v190,v203\nv_sub_f32 v191,v191,v203\nv_add_f32 v194,v160,v161\nv_add_f32 v195,v164,v165\nv_add_f32 v194,v194,v162\n"
"v_add_f32 v195,v195,v166\nv_add_f32 v194,v194,v163\nv_add_f32 v195,v195,v167\nv_add_f32 v194,v194,v168\nv_add_f32 v195,v195,v172\nv_add_f32 v194,v194,v169\nv_add_f32 v195,v195,v173\nv_add_f32 v194,v194,v170\nv_add_f32 v195,v195,v174\nv_add_f32 v194,v194,v171\nv_add_f32 v195,v195,v175\nv_cvt_pk_bf16_f32 v176,v160,v161\nv_cvt_pk_bf16_f32 v180,v164,v165\nv_cvt_pk_bf16_f32 v177,v162,v163\nv_cvt_pk_bf16_f32 v181,v166,v167\n"
"v_cvt_pk_bf16_f32 v178,v168,v169\nv_cvt_pk_bf16_f32 v182,v172,v173\nv_cvt_pk_bf16_f32 v179,v170,v171\nv_cvt_pk_bf16_f32 v183,v174,v175\nv_sub_f32 v128,v128,v202\nv_sub_f32 v129,v129,v202\nv_sub_f32 v130,v130,v202\nv_sub_f32 v131,v131,v202\nv_sub_f32 v136,v136,v202\nv_sub_f32 v137,v137,v202\nv_sub_f32 v138,v138,v202\nv_sub_f32 v139,v139,v202\nv_sub_f32 v132,v132,v203\nv_sub_f32 v133,v133,v203\nv_sub_f32 v134,v134,v203\n"
"v_sub_f32 v135,v135,v203\nv_sub_f32 v140,v140,v203\nv_sub_f32 v141,v141,v203\nv_sub_f32 v142,v142,v203\nv_sub_f32 v143,v143,v203\ns_cmp_lg_u32 s38,0\ns_mov_b32 s38,0\ns_mov_b32 s37,0x49800000\ns_cbranch_scc1 L_fused_block_rarer_32\nv_exp_f32 v206,-v202\nv_exp_f32 v207,-v203\ns_nop 0\nv_mul_f32 v192,v206,v192\nv_mul_f32 v193,v207,v193\nv_mul_f32 v0,v206,v0\nv_mul_f32 v1,v206,v1\nv_mul_f32 v2,v206,v2\n"
"v_mul_f32 v3,v206,v3\nv_mul_f32 v4,v207,v4\nv_mul_f32 v5,v207,v5\nv_mul_f32 v6,v207,v6\nv_mul_f32 v7,v207,v7\nv_mul_f32 v8,v206,v8\nv_mul_f32 v9,v206,v9\nv_mul_f32 v10,v206,v10\nv_mul_f32 v11,v206,v11\nv_mul_f32 v12,v207,v12\nv_mul_f32 v13,v207,v13\nv_mul_f32 v14,v207,v14\nv_mul_f32 v15,v207,v15\nv_mul_f32 v16,v206,v16\nv_mul_f32 v17,v206,v17\nv_mul_f32 v18,v206,v18\nv_mul_f32 v19,v206,v19\nv_mul_f32 v20,v207,v20\n"
"v_mul_f32 v21,v207,v21\nv_mul_f32 v22,v207,v22\nv_mul_f32 v23,v207,v23\nv_mul_f32 v24,v206,v24\nv_mul_f32 v25,v206,v25\nv_mul_f32 v26,v206,v26\nv_mul_f32 v27,v206,v27\nv_mul_f32 v28,v207,v28\nv_mul_f32 v29,v207,v29\nv_mul_f32 v30,v207,v30\nv_mul_f32 v31,v207,v31\nv_mul_f32 v32,v206,v32\nv_mul_f32 v33,v206,v33\nv_mul_f32 v34,v206,v34\nv_mul_f32 v35,v206,v35\nv_mul_f32 v36,v207,v36\nv_mul_f32 v37,v207,v37\n"
"v_mul_f32 v38,v207,v38\nv_mul_f32 v39,v207,v39\nv_mul_f32 v40,v206,v40\nv_mul_f32 v41,v206,v41\nv_mul_f32 v42,v206,v42\nv_mul_f32 v43,v206,v43\nv_mul_f32 v44,v207,v44\nv_mul_f32 v45,v207,v45\nv_mul_f32 v46,v207,v46\nv_mul_f32 v47,v207,v47\nv_mul_f32 v48,v206,v48\nv_mul_f32 v49,v206,v49\nv_mul_f32 v50,v206,v50\nv_mul_f32 v51,v206,v51\nv_mul_f32 v52,v207,v52\nv_mul_f32 v53,v207,v53\nv_mul_f32 v54,v207,v54\n"
"v_mul_f32 v55,v207,v55\nv_mul_f32 v56,v206,v56\nv_mul_f32 v57,v206,v57\nv_mul_f32 v58,v206,v58\nv_mul_f32 v59,v206,v59\nv_mul_f32 v60,v207,v60\nv_mul_f32 v61,v207,v61\nv_mul_f32 v62,v207,v62\nv_mul_f32 v63,v207,v63\nv_mul_f32 v64,v206,v64\nv_mul_f32 v65,v206,v65\nv_mul_f32 v66,v206,v66\nv_mul_f32 v67,v206,v67\nv_mul_f32 v68,v207,v68\nv_mul_f32 v69,v207,v69\nv_mul_f32 v70,v207,v70\nv_mul_f32 v71,v207,v71\n"
"v_mul_f32 v72,v206,v72\nv_mul_f32 v73,v206,v73\nv_mul_f32 v74,v206,v74\nv_mul_f32 v75,v206,v75\nv_mul_f32 v76,v207,v76\nv_mul_f32 v77,v207,v77\nv_mul_f32 v78,v207,v78\nv_mul_f32 v79,v207,v79\nv_mul_f32 v80,v206,v80\nv_mul_f32 v81,v206,v81\nv_mul_f32 v82,v206,v82\nv_mul_f32 v83,v206,v83\nv_mul_f32 v84,v207,v84\nv_mul_f32 v85,v207,v85\nv_mul_f32 v86,v207,v86\nv_mul_f32 v87,v207,v87\nv_mul_f32 v88,v206,v88\n"
"v_mul_f32 v89,v206,v89\nv_mul_f32 v90,v206,v90\nv_mul_f32 v91,v206,v91\nv_mul_f32 v92,v207,v92\nv_mul_f32 v93,v207,v93\nv_mul_f32 v94,v207,v94\nv_mul_f32 v95,v207,v95\nv_mul_f32 v96,v206,v96\nv_mul_f32 v97,v206,v97\nv_mul_f32 v98,v206,v98\nv_mul_f32 v99,v206,v99\nv_mul_f32 v100,v207,v100\nv_mul_f32 v101,v207,v101\nv_mul_f32 v102,v207,v102\nv_mul_f32 v103,v207,v103\nv_mul_f32 v104,v206,v104\nv_mul_f32 v105,v206,v105\n"
"v_mul_f32 v106,v206,v106\nv_mul_f32 v107,v206,v107\nv_mul_f32 v108,v207,v108\nv_mul_f32 v109,v207,v109\nv_mul_f32 v110,v207,v110\nv_mul_f32 v111,v207,v111\nv_mul_f32 v112,v206,v112\nv_mul_f32 v113,v206,v113\nv_mul_f32 v114,v206,v114\nv_mul_f32 v115,v206,v115\nv_mul_f32 v116,v207,v116\nv_mul_f32 v117,v207,v117\nv_mul_f32 v118,v207,v118\nv_mul_f32 v119,v207,v119\nv_mul_f32 v120,v206,v120\nv_mul_f32 v121,v206,v121\n"
"v_mul_f32 v122,v206,v122\nv_mul_f32 v123,v206,v123\nv_mul_f32 v124,v207,v124\nv_mul_f32 v125,v207,v125\nv_mul_f32 v126,v207,v126\nv_mul_f32 v127,v207,v127\ns_branch L_fused_block_rarer_32\nL_fused_block_rare_35:\nv_max3_f32 v202,v128,v129,v130\nv_max3_f32 v204,v136,v137,v138\nv_max3_f32 v202,v202,v131,v139\nv_max_f32 v202,v202,v204\nv_max3_f32 v203,v132,v133,v134\nv_max3_f32 v205,v140,v141,v142\nv_max3_f32 v203,v203,v135,v143\n"
"v_max_f32 v203,v203,v205\nv_mov_b32 v204,v202\nv_mov_b32 v205,v203\ns_nop 0\nv_permlane16_swap_b32 v204,v202\nv_permlane16_swap_b32 v205,v203\nv_max_f32 v202,v202,v204\nv_max_f32 v203,v203,v205\nv_mov_b32 v204,v202\nv_mov_b32 v205,v203\ns_nop 0\nv_permlane32_swap_b32 v204,v202\nv_permlane32_swap_b32 v205,v203\nv_max_f32 v202,v202,v204\nv_max_f32 v203,v203,v205\nv_max_f32 v202,s38,v202\nv_max_f32 v203,s38,v203\n"
"v_sub_f32 v128,v128,v202\nv_sub_f32 v129,v129,v202\nv_sub_f32 v130,v130,v202\nv_sub_f32 v131,v131,v202\nv_sub_f32 v136,v136,v202\nv_sub_f32 v137,v137,v202\nv_sub_f32 v138,v138,v202\nv_sub_f32 v139,v139,v202\nv_sub_f32 v132,v132,v203\nv_sub_f32 v133,v133,v203\nv_sub_f32 v134,v134,v203\nv_sub_f32 v135,v135,v203\nv_sub_f32 v140,v140,v203\nv_sub_f32 v141,v141,v203\nv_sub_f32 v142,v142,v203\nv_sub_f32 v143,v143,v203\n"
"v_exp_f32 v160,v128\nv_exp_f32 v164,v132\nv_exp_f32 v161,v129\nv_exp_f32 v165,v133\nv_exp_f32 v162,v130\nv_exp_f32 v166,v134\nv_exp_f32 v163,v131\nv_exp_f32 v167,v135\nv_exp_f32 v168,v136\nv_exp_f32 v172,v140\nv_exp_f32 v169,v137\nv_exp_f32 v173,v141\nv_exp_f32 v170,v138\nv_exp_f32 v174,v142\nv_exp_f32 v171,v139\nv_exp_f32 v175,v143\nv_sub_f32 v184,v184,v202\nv_sub_f32 v185,v185,v202\nv_sub_f32 v186,v186,v202\n"
"v_sub_f32 v187,v187,v202\nv_sub_f32 v188,v188,v203\nv_sub_f32 v189,v189,v203\nv_sub_f32 v190,v190,v203\nv_sub_f32 v191,v191,v203\nv_add_f32 v194,v160,v161\nv_add_f32 v195,v164,v165\nv_add_f32 v194,v194,v162\nv_add_f32 v195,v195,v166\nv_add_f32 v194,v194,v163\nv_add_f32 v195,v195,v167\nv_add_f32 v194,v194,v168\nv_add_f32 v195,v195,v172\nv_add_f32 v194,v194,v169\nv_add_f32 v195,v195,v173\nv_add_f32 v194,v194,v170\n"
"v_add_f32 v195,v195,v174\nv_add_f32 v194,v194,v171\nv_add_f32 v195,v195,v175\nv_cvt_pk_bf16_f32 v176,v160,v161\nv_cvt_pk_bf16_f32 v180,v164,v165\nv_cvt_pk_bf16_f32 v177,v162,v163\nv_cvt_pk_bf16_f32 v181,v166,v167\nv_cvt_pk_bf16_f32 v178,v168,v169\nv_cvt_pk_bf16_f32 v182,v172,v173\nv_cvt_pk_bf16_f32 v179,v170,v171\nv_cvt_pk_bf16_f32 v183,v174,v175\nv_sub_f32 v144,v144,v202\nv_sub_f32 v145,v145,v202\n"
"v_sub_f32 v146,v146,v202\nv_sub_f32 v147,v147,v202\nv_sub_f32 v152,v152,v202\nv_sub_f32 v153,v153,v202\nv_sub_f32 v154,v154,v202\nv_sub_f32 v155,v155,v202\nv_sub_f32 v148,v148,v203\nv_sub_f32 v149,v149,v203\nv_sub_f32 v150,v150,v203\nv_sub_f32 v151,v151,v203\nv_sub_f32 v156,v156,v203\nv_sub_f32 v157,v157,v203\nv_sub_f32 v158,v158,v203\nv_sub_f32 v159,v159,v203\ns_cmp_lg_u32 s38,0\ns_mov_b32 s38,0\n"
"s_mov_b32 s37,0x49800000\ns_cbranch_scc1 L_fused_block_rarer_36\nv_exp_f32 v206,-v202\nv_exp_f32 v207,-v203\ns_nop 0\nv_mul_f32 v192,v206,v192\nv_mul_f32 v193,v207,v193\nv_mul_f32 v0,v206,v0\nv_mul_f32 v1,v206,v1\nv_mul_f32 v2,v206,v2\nv_mul_f32 v3,v206,v3\nv_mul_f32 v4,v207,v4\nv_mul_f32 v5,v207,v5\nv_mul_f32 v6,v207,v6\nv_mul_f32 v7,v207,v7\nv_mul_f32 v8,v206,v8\nv_mul_f32 v9,v206,v9\nv_mul_f32 v10,v206,v10\n"
"v_mul_f32 v11,v206,v11\nv_mul_f32 v12,v207,v12\nv_mul_f32 v13,v207,v13\nv_mul_f32 v14,v207,v14\nv_mul_f32 v15,v207,v15\nv_mul_f32 v16,v206,v16\nv_mul_f32 v17,v206,v17\nv_mul_f32 v18,v206,v18\nv_mul_f32 v19,v206,v19\nv_mul_f32 v20,v207,v20\nv_mul_f32 v21,v207,v21\nv_mul_f32 v22,v207,v22\nv_mul_f32 v23,v207,v23\nv_mul_f32 v24,v206,v24\nv_mul_f32 v25,v206,v25\nv_mul_f32 v26,v206,v26\nv_mul_f32 v27,v206,v27\n"
"v_mul_f32 v28,v207,v28\nv_mul_f32 v29,v207,v29\nv_mul_f32 v30,v207,v30\nv_mul_f32 v31,v207,v31\nv_mul_f32 v32,v206,v32\nv_mul_f32 v33,v206,v33\nv_mul_f32 v34,v206,v34\nv_mul_f32 v35,v206,v35\nv_mul_f32 v36,v207,v36\nv_mul_f32 v37,v207,v37\nv_mul_f32 v38,v207,v38\nv_mul_f32 v39,v207,v39\nv_mul_f32 v40,v206,v40\nv_mul_f32 v41,v206,v41\nv_mul_f32 v42,v206,v42\nv_mul_f32 v43,v206,v43\nv_mul_f32 v44,v207,v44\n"
"v_mul_f32 v45,v207,v45\nv_mul_f32 v46,v207,v46\nv_mul_f32 v47,v207,v47\nv_mul_f32 v48,v206,v48\nv_mul_f32 v49,v206,v49\nv_mul_f32 v50,v206,v50\nv_mul_f32 v51,v206,v51\nv_mul_f32 v52,v207,v52\nv_mul_f32 v53,v207,v53\nv_mul_f32 v54,v207,v54\nv_mul_f32 v55,v207,v55\nv_mul_f32 v56,v206,v56\nv_mul_f32 v57,v206,v57\nv_mul_f32 v58,v206,v58\nv_mul_f32 v59,v206,v59\nv_mul_f32 v60,v207,v60\nv_mul_f32 v61,v207,v61\n"
"v_mul_f32 v62,v207,v62\nv_mul_f32 v63,v207,v63\nv_mul_f32 v64,v206,v64\nv_mul_f32 v65,v206,v65\nv_mul_f32 v66,v206,v66\nv_mul_f32 v67,v206,v67\nv_mul_f32 v68,v207,v68\nv_mul_f32 v69,v207,v69\nv_mul_f32 v70,v207,v70\nv_mul_f32 v71,v207,v71\nv_mul_f32 v72,v206,v72\nv_mul_f32 v73,v206,v73\nv_mul_f32 v74,v206,v74\nv_mul_f32 v75,v206,v75\nv_mul_f32 v76,v207,v76\nv_mul_f32 v77,v207,v77\nv_mul_f32 v78,v207,v78\n"
"v_mul_f32 v79,v207,v79\nv_mul_f32 v80,v206,v80\nv_mul_f32 v81,v206,v81\nv_mul_f32 v82,v206,v82\nv_mul_f32 v83,v206,v83\nv_mul_f32 v84,v207,v84\nv_mul_f32 v85,v207,v85\nv_mul_f32 v86,v207,v86\nv_mul_f32 v87,v207,v87\nv_mul_f32 v88,v206,v88\nv_mul_f32 v89,v206,v89\nv_mul_f32 v90,v206,v90\nv_mul_f32 v91,v206,v91\nv_mul_f32 v92,v207,v92\nv_mul_f32 v93,v207,v93\nv_mul_f32 v94,v207,v94\nv_mul_f32 v95,v207,v95\n"
"v_mul_f32 v96,v206,v96\nv_mul_f32 v97,v206,v97\nv_mul_f32 v98,v206,v98\nv_mul_f32 v99,v206,v99\nv_mul_f32 v100,v207,v100\nv_mul_f32 v101,v207,v101\nv_mul_f32 v102,v207,v102\nv_mul_f32 v103,v207,v103\nv_mul_f32 v104,v206,v104\nv_mul_f32 v105,v206,v105\nv_mul_f32 v106,v206,v106\nv_mul_f32 v107,v206,v107\nv_mul_f32 v108,v207,v108\nv_mul_f32 v109,v207,v109\nv_mul_f32 v110,v207,v110\nv_mul_f32 v111,v207,v111\n"
"v_mul_f32 v112,v206,v112\nv_mul_f32 v113,v206,v113\nv_mul_f32 v114,v206,v114\nv_mul_f32 v115,v206,v115\nv_mul_f32 v116,v207,v116\nv_mul_f32 v117,v207,v117\nv_mul_f32 v118,v207,v118\nv_mul_f32 v119,v207,v119\nv_mul_f32 v120,v206,v120\nv_mul_f32 v121,v206,v121\nv_mul_f32 v122,v206,v122\nv_mul_f32 v123,v206,v123\nv_mul_f32 v124,v207,v124\nv_mul_f32 v125,v207,v125\nv_mul_f32 v126,v207,v126\nv_mul_f32 v127,v207,v127\n"
"s_branch L_fused_block_rarer_36\nL_fused_block_rare_39:\nv_max3_f32 v202,v144,v145,v146\nv_max3_f32 v204,v152,v153,v154\nv_max3_f32 v202,v202,v147,v155\nv_max_f32 v202,v202,v204\nv_max3_f32 v203,v148,v149,v150\nv_max3_f32 v205,v156,v157,v158\nv_max3_f32 v203,v203,v151,v159\nv_max_f32 v203,v203,v205\nv_mov_b32 v204,v202\nv_mov_b32 v205,v203\ns_nop 0\nv_permlane16_swap_b32 v204,v202\nv_permlane16_swap_b32 v205,v203\n"
"v_max_f32 v202,v202,v204\nv_max_f32 v203,v203,v205\nv_mov_b32 v204,v202\nv_mov_b32 v205,v203\ns_nop 0\nv_permlane32_swap_b32 v204,v202\nv_permlane32_swap_b32 v205,v203\nv_max_f32 v202,v202,v204\nv_max_f32 v203,v203,v205\nv_max_f32 v202,s38,v202\nv_max_f32 v203,s38,v203\nv_sub_f32 v144,v144,v202\nv_sub_f32 v145,v145,v202\nv_sub_f32 v146,v146,v202\nv_sub_f32 v147,v147,v202\nv_sub_f32 v152,v152,v202\n"
"v_sub_f32 v153,v153,v202\nv_sub_f32 v154,v154,v202\nv_sub_f32 v155,v155,v202\nv_sub_f32 v148,v148,v203\nv_sub_f32 v149,v149,v203\nv_sub_f32 v150,v150,v203\nv_sub_f32 v151,v151,v203\nv_sub_f32 v156,v156,v203\nv_sub_f32 v157,v157,v203\nv_sub_f32 v158,v158,v203\nv_sub_f32 v159,v159,v203\nv_exp_f32 v160,v144\nv_exp_f32 v164,v148\nv_exp_f32 v161,v145\nv_exp_f32 v165,v149\nv_exp_f32 v162,v146\nv_exp_f32 v166,v150\n"
"v_exp_f32 v163,v147\nv_exp_f32 v167,v151\nv_exp_f32 v168,v152\nv_exp_f32 v172,v156\nv_exp_f32 v169,v153\nv_exp_f32 v173,v157\nv_exp_f32 v170,v154\nv_exp_f32 v174,v158\nv_exp_f32 v171,v155\nv_exp_f32 v175,v159\nv_sub_f32 v184,v184,v202\nv_sub_f32 v185,v185,v202\nv_sub_f32 v186,v186,v202\nv_sub_f32 v187,v187,v202\nv_sub_f32 v188,v188,v203\nv_sub_f32 v189,v189,v203\nv_sub_f32 v190,v190,v203\nv_sub_f32 v191,v191,v203\n"
"v_add_f32 v194,v160,v161\nv_add_f32 v195,v164,v165\nv_add_f32 v194,v194,v162\nv_add_f32 v195,v195,v166\nv_add_f32 v194,v194,v163\nv_add_f32 v195,v195,v167\nv_add_f32 v194,v194,v168\nv_add_f32 v195,v195,v172\nv_add_f32 v194,v194,v169\nv_add_f32 v195,v195,v173\nv_add_f32 v194,v194,v170\nv_add_f32 v195,v195,v174\nv_add_f32 v194,v194,v171\nv_add_f32 v195,v195,v175\nv_cvt_pk_bf16_f32 v176,v160,v161\nv_cvt_pk_bf16_f32 v180,v164,v165\n"
"v_cvt_pk_bf16_f32 v177,v162,v163\nv_cvt_pk_bf16_f32 v181,v166,v167\nv_cvt_pk_bf16_f32 v178,v168,v169\nv_cvt_pk_bf16_f32 v182,v172,v173\nv_cvt_pk_bf16_f32 v179,v170,v171\nv_cvt_pk_bf16_f32 v183,v174,v175\ns_cmp_lg_u32 s38,0\ns_mov_b32 s38,0\ns_mov_b32 s37,0x49800000\ns_cbranch_scc1 L_fused_block_rarer_40\nv_exp_f32 v206,-v202\nv_exp_f32 v207,-v203\ns_nop 0\nv_mul_f32 v192,v206,v192\nv_mul_f32 v193,v207,v193\n"
"v_mul_f32 v0,v206,v0\nv_mul_f32 v1,v206,v1\nv_mul_f32 v2,v206,v2\nv_mul_f32 v3,v206,v3\nv_mul_f32 v4,v207,v4\nv_mul_f32 v5,v207,v5\nv_mul_f32 v6,v207,v6\nv_mul_f32 v7,v207,v7\nv_mul_f32 v8,v206,v8\nv_mul_f32 v9,v206,v9\nv_mul_f32 v10,v206,v10\nv_mul_f32 v11,v206,v11\nv_mul_f32 v12,v207,v12\nv_mul_f32 v13,v207,v13\nv_mul_f32 v14,v207,v14\nv_mul_f32 v15,v207,v15\nv_mul_f32 v16,v206,v16\nv_mul_f32 v17,v206,v17\n"
"v_mul_f32 v18,v206,v18\nv_mul_f32 v19,v206,v19\nv_mul_f32 v20,v207,v20\nv_mul_f32 v21,v207,v21\nv_mul_f32 v22,v207,v22\nv_mul_f32 v23,v207,v23\nv_mul_f32 v24,v206,v24\nv_mul_f32 v25,v206,v25\nv_mul_f32 v26,v206,v26\nv_mul_f32 v27,v206,v27\nv_mul_f32 v28,v207,v28\nv_mul_f32 v29,v207,v29\nv_mul_f32 v30,v207,v30\nv_mul_f32 v31,v207,v31\nv_mul_f32 v32,v206,v32\nv_mul_f32 v33,v206,v33\nv_mul_f32 v34,v206,v34\n"
"v_mul_f32 v35,v206,v35\nv_mul_f32 v36,v207,v36\nv_mul_f32 v37,v207,v37\nv_mul_f32 v38,v207,v38\nv_mul_f32 v39,v207,v39\nv_mul_f32 v40,v206,v40\nv_mul_f32 v41,v206,v41\nv_mul_f32 v42,v206,v42\nv_mul_f32 v43,v206,v43\nv_mul_f32 v44,v207,v44\nv_mul_f32 v45,v207,v45\nv_mul_f32 v46,v207,v46\nv_mul_f32 v47,v207,v47\nv_mul_f32 v48,v206,v48\nv_mul_f32 v49,v206,v49\nv_mul_f32 v50,v206,v50\nv_mul_f32 v51,v206,v51\n"
"v_mul_f32 v52,v207,v52\nv_mul_f32 v53,v207,v53\nv_mul_f32 v54,v207,v54\nv_mul_f32 v55,v207,v55\nv_mul_f32 v56,v206,v56\nv_mul_f32 v57,v206,v57\nv_mul_f32 v58,v206,v58\nv_mul_f32 v59,v206,v59\nv_mul_f32 v60,v207,v60\nv_mul_f32 v61,v207,v61\nv_mul_f32 v62,v207,v62\nv_mul_f32 v63,v207,v63\nv_mul_f32 v64,v206,v64\nv_mul_f32 v65,v206,v65\nv_mul_f32 v66,v206,v66\nv_mul_f32 v67,v206,v67\nv_mul_f32 v68,v207,v68\n"
"v_mul_f32 v69,v207,v69\nv_mul_f32 v70,v207,v70\nv_mul_f32 v71,v207,v71\nv_mul_f32 v72,v206,v72\nv_mul_f32 v73,v206,v73\nv_mul_f32 v74,v206,v74\nv_mul_f32 v75,v206,v75\nv_mul_f32 v76,v207,v76\nv_mul_f32 v77,v207,v77\nv_mul_f32 v78,v207,v78\nv_mul_f32 v79,v207,v79\nv_mul_f32 v80,v206,v80\nv_mul_f32 v81,v206,v81\nv_mul_f32 v82,v206,v82\nv_mul_f32 v83,v206,v83\nv_mul_f32 v84,v207,v84\nv_mul_f32 v85,v207,v85\n"
"v_mul_f32 v86,v207,v86\nv_mul_f32 v87,v207,v87\nv_mul_f32 v88,v206,v88\nv_mul_f32 v89,v206,v89\nv_mul_f32 v90,v206,v90\nv_mul_f32 v91,v206,v91\nv_mul_f32 v92,v207,v92\nv_mul_f32 v93,v207,v93\nv_mul_f32 v94,v207,v94\nv_mul_f32 v95,v207,v95\nv_mul_f32 v96,v206,v96\nv_mul_f32 v97,v206,v97\nv_mul_f32 v98,v206,v98\nv_mul_f32 v99,v206,v99\nv_mul_f32 v100,v207,v100\nv_mul_f32 v101,v207,v101\nv_mul_f32 v102,v207,v102\n"
"v_mul_f32 v103,v207,v103\nv_mul_f32 v104,v206,v104\nv_mul_f32 v105,v206,v105\nv_mul_f32 v106,v206,v106\nv_mul_f32 v107,v206,v107\nv_mul_f32 v108,v207,v108\nv_mul_f32 v109,v207,v109\nv_mul_f32 v110,v207,v110\nv_mul_f32 v111,v207,v111\nv_mul_f32 v112,v206,v112\nv_mul_f32 v113,v206,v113\nv_mul_f32 v114,v206,v114\nv_mul_f32 v115,v206,v115\nv_mul_f32 v116,v207,v116\nv_mul_f32 v117,v207,v117\nv_mul_f32 v118,v207,v118\n"
"v_mul_f32 v119,v207,v119\nv_mul_f32 v120,v206,v120\nv_mul_f32 v121,v206,v121\nv_mul_f32 v122,v206,v122\nv_mul_f32 v123,v206,v123\nv_mul_f32 v124,v207,v124\nv_mul_f32 v125,v207,v125\nv_mul_f32 v126,v207,v126\nv_mul_f32 v127,v207,v127\ns_branch L_fused_block_rarer_40\nL_fused_block_pub0_6:\ns_cmp_lg_u32 s14,0\ns_cbranch_scc1 L_fused_block_pub0r_7\ns_lshl_b32 s7,s6,7\ns_add_u32 s7,s7,117735424\nbuffer_store_dwordx2 v[250:251],off,s[8:11],s7 sc1\n"
"s_branch L_fused_block_pub0r_7\nL_fused_block_slow0_8:\nv_lshlrev_b32 v226,7,v253\ns_mov_b64 exec,-1\ns_mov_b32 s7,0\nL_fused_block_poll_64:\nbuffer_load_dwordx2 v[224:225],v226,s[8:11],s75 offen sc1\ns_add_u32 s7,s7,1\ns_waitcnt vmcnt(0)\nv_cmp_eq_u64 vcc,s[12:13],v[224:225]\ns_nop 1\ns_cmp_eq_u64 vcc,exec\ns_cbranch_scc1 L_fused_block_polld_65\ns_cmp_lt_u32 s7,131072\ns_cbranch_scc1 L_fused_block_poll_64\n"
"L_fused_block_polld_65:\ns_mov_b64 exec,-1\ns_branch L_fused_block_slow0r_9\nL_fused_block_pub0_11:\ns_cmp_lg_u32 s14,0\ns_cbranch_scc1 L_fused_block_pub0r_12\ns_lshl_b32 s7,s6,7\ns_add_u32 s7,s7,117735424\nbuffer_store_dwordx2 v[250:251],off,s[8:11],s7 sc1\ns_branch L_fused_block_pub0r_12\nL_fused_block_slow0_13:\nv_lshlrev_b32 v226,7,v253\ns_mov_b64 exec,-1\ns_mov_b32 s7,0\nL_fused_block_poll_66:\n"
"buffer_load_dwordx2 v[224:225],v226,s[8:11],s75 offen sc1\ns_add_u32 s7,s7,1\ns_waitcnt vmcnt(0)\nv_cmp_eq_u64 vcc,s[12:13],v[224:225]\ns_nop 1\ns_cmp_eq_u64 vcc,exec\ns_cbranch_scc1 L_fused_block_polld_67\ns_cmp_lt_u32 s7,131072\ns_cbranch_scc1 L_fused_block_poll_66\nL_fused_block_polld_67:\ns_mov_b64 exec,-1\ns_branch L_fused_block_slow0r_14\nL_fused_block_hook_25:\ns_mov_b32 s39,0\nbuffer_store_dwordx2 v[250:251],off,s[8:11],s94 sc1\n"
"s_branch L_fused_block_hookr_26\nL_fused_block_hook_33:\ns_mov_b32 s39,0\nbuffer_store_dwordx2 v[250:251],off,s[8:11],s94 sc1\ns_branch L_fused_block_hookr_34\nL_fused_block_end_2:\n"
    :: "s"(__builtin_amdgcn_kernarg_segment_ptr()), "s"(blockIdx.x), "v"(threadIdx.x), "s"((unsigned)(size_t)lds)
    : "v1","v2","v3","v4","v5","v6","v7","v8","v9","v10","v11","v12","v13","v14","v15","v16","v17","v18","v19","v20","v21","v22","v23","v24","v25","v26","v27","v28","v29","v30","v31","v32","v33","v34","v35","v36","v37","v38","v39","v40","v41","v42","v43","v44","v45","v46","v47","v48","v49","v50","v51","v52","v53","v54","v55","v56","v57","v58","v59","v60","v61","v62","v63","v64","v65","v66","v67","v68","v69","v70","v71","v72","v73","v74","v75","v76","v77","v78","v79","v80","v81","v82","v83","v84","v85","v86","v87","v88","v89","v90","v91","v92","v93","v94","v95","v96","v97","v98","v99","v100","v101","v102","v103","v104","v105","v106","v107","v108","v109","v110","v111","v112","v113","v114","v115","v116","v117","v118","v119","v120","v121","v122","v123","v124","v125","v126","v127","v128","v129","v130","v131","v132","v133","v134","v135","v136","v137","v138","v139","v140","v141","v142","v143","v144","v145","v146","v147","v148","v149","v150","v151","v152","v153","v154","v155","v156","v157","v158","v159","v160","v161","v162","v163","v164","v165","v166","v167","v168","v169","v170","v171","v172","v173","v174","v175","v176","v177","v178","v179","v180","v181","v182","v183","v184","v185","v186","v187","v188","v189","v190","v191","v192","v193","v194","v195","v196","v197","v198","v199","v200","v201","v202","v203","v204","v205","v206","v207","v208","v209","v210","v211","v212","v213","v214","v215","v216","v217","v218","v219","v220","v221","v222","v223","v224","v225","v226","v227","v228","v229","v230","v231","v232","v233","v234","v235","v236","v237","v238","v239","v240","v241","v242","v243","v244","v245","v246","v247","v248","v249","v250","v251","v252","v253","v254","v255","a0","a1","a2","a3","a4","a5","a6","a7","a8","a9","a10","a11","a12","a13","a14","a15","a16","a17","a18","a19","a20","a21","a22","a23","a24","a25","a26","a27","a28","a29","a30","a31","a32","a33","a34","a35","a36","a37","a38","a39","a40","a41","a42","a43","a44","a45","a46","a47","a48","a49","a50","a51","a52","a53","a54","a55","a56","a57","a58","a59","a60","a61","a62","a63","a64","a65","a66","a67","a68","a69","a70","a71","a72","a73","a74","a75","a76","a77","a78","a79","a80","a81","a82","a83","a84","a85","a86","a87","a88","a89","a90","a91","a92","a93","a94","a95","a96","a97","a98","a99","a100","a101","a102","a103","a104","a105","a106","a107","a108","a109","a110","a111","a112","a113","a114","a115","a116","a117","a118","a119","a120","a121","a122","a123","a124","a125","a126","a127","a128","a129","a130","a131","a132","a133","a134","a135","a136","a137","a138","a139","a140","a141","a142","a143","a144","a145","a146","a147","a148","a149","a150","a151","a152","a153","a154","a155","a156","a157","a158","a159","a160","a161","a162","a163","a164","a165","a166","a167","a168","a169","a170","a171","a172","a173","a174","a175","a176","a177","a178","a179","a180","a181","a182","a183","a184","a185","a186","a187","a188","a189","a190","a191","a192","a193","a194","a195","a196","a197","a198","a199","a200","a201","a202","a203","a204","a205","a206","a207","a208","a209","a210","a211","a212","a213","a214","a215","a216","a217","a218","a219","a220","a221","a222","a223","a224","a225","a226","a227","a228","a229","a230","a231","a232","a233","a234","a235","a236","a237","a238","a239","a240","a241","a242","a243","a244","a245","a246","a247","a248","a249","a250","a251","a252","a253","a254","a255","s4","s5","s6","s7","s8","s9","s10","s11","s12","s13","s14","s15","s16","s17","s18","s19","s20","s21","s22","s23","s24","s25","s26","s27","s28","s29","s30","s31","s32","s33","s34","s35","s36","s37","s38","s39","s40","s41","s42","s43","s44","s45","s46","s47","s48","s49","s50","s51","s52","s53","s54","s55","s56","s57","s58","s59","s60","s61","s62","s63","s64","s65","s66","s67","s68","s69","s70","s71","s72","s73","s74","s75","s76","s77","s78","s79","s80","s81","s82","s83","s84","s85","s86","s87","s88","s89","s90","s91","s92","s93","s94","s95","s96","s97","s98","s99","s100","s101","vcc","scc","memory");
}
extern "C" void kernel_launch(void* const* d_in, const int* in_sizes, int n_in, void* d_out, int out_size, void* d_ws, size_t ws_size, hipStream_t stream) {
  Params p; p.x=d_in[0]; p.ckv=d_in[1]; p.wq=d_in[2]; p.wuk=d_in[3]; p.wuv=d_in[4]; p.wo=d_in[5]; p.wg=d_in[7]; p.wu=d_in[8]; p.wd=d_in[9]; p.out=d_out; p.ws=d_ws;
  fused_block<<<256,256,0,stream>>>(p);
}
```
